```python
import torch

def _softmax(x, axis=-1):
    return torch.softmax(x, dim=axis)
def _gelu(x):
    return torch.nn.functional.gelu(x, approximate="tanh")
def _silu(x):
    return torch.nn.functional.silu(x)
def _sigmoid(x):
    return torch.sigmoid(x)
def _layernorm(x, axis=-1, eps=1e-5):
    m = x.mean(dim=axis, keepdim=True)
    v = x.var(dim=axis, keepdim=True, unbiased=False)
    return (x - m) / torch.sqrt(v + eps)
def _scatter_add(tgt, idx, src, axis=0):
    return tgt.clone().index_add_(axis, idx, src)
def _segment_sum(src, seg, n):
    out = torch.zeros((n,) + src.shape[1:], dtype=src.dtype)
    return out.index_add_(0, seg, src)
def _pad(x, pw):
    flat = tuple(v for pair in reversed(pw) for v in pair)
    return torch.nn.functional.pad(x, flat)

kernel_name = 'sequential_preLN__compressed_kv_cla__glu_gated_bilinear__prompt'

def setup_inputs():

    _w = (lambda a: (lambda t: t.to(torch.bfloat16) if t.is_floating_point() else t)(torch.as_tensor(a)))
    def _A_setup():
        B, H, Sq, Sk, D, M = 1, 16, 2048, 2048, 256, 448
        Dc = 4 * D
        return {
            "x": _w((lambda *s: torch.randn(*s, dtype=torch.bfloat16) * 0.05)(B, Sq, M)),
            "c_kv": _w((lambda *s: torch.randn(*s, dtype=torch.bfloat16) * 0.4)(B, Sk, Dc)),
            "w_q": _w((lambda *s: torch.randn(*s, dtype=torch.bfloat16) * 1.6)(M, H, D) * (M ** -0.5)),
            "w_uk": _w((lambda *s: torch.randn(*s, dtype=torch.bfloat16) * 1.6)(Dc, H, D) * (Dc ** -0.5)),
            "w_uv": _w((lambda *s: torch.randn(*s, dtype=torch.bfloat16) * 0.4)(Dc, H, D) * (Dc ** -0.5)),
            "w_o": _w((lambda *s: torch.randn(*s, dtype=torch.bfloat16) * 0.4)(H, D, M) * ((H * D) ** -0.5)),
        }
    _A_in = _A_setup()

    def _B_setup():
        B, S, D, F = 1, 2048, 448, 1792
        return {
            "x": _w((lambda *s: torch.randn(*s, dtype=torch.bfloat16) * 0.05)(B, S, D)),
            "w_gate": _w((lambda *s: torch.randn(*s, dtype=torch.bfloat16) * 0.4)(D, F) * (D ** -0.5)),
            "w_up": _w((lambda *s: torch.randn(*s, dtype=torch.bfloat16) * 0.4)(D, F) * (D ** -0.5)),
            "w_down": _w((lambda *s: torch.randn(*s, dtype=torch.bfloat16) * 0.4)(F, D) * (F ** -0.5)),
        }
    _B_in = _B_setup()

    return {
        "A_x": _A_in["x"],
        "A_c_kv": _A_in["c_kv"],
        "A_w_q": _A_in["w_q"],
        "A_w_uk": _A_in["w_uk"],
        "A_w_uv": _A_in["w_uv"],
        "A_w_o": _A_in["w_o"],
        "B_x": _B_in["x"],
        "B_w_gate": _B_in["w_gate"],
        "B_w_up": _B_in["w_up"],
        "B_w_down": _B_in["w_down"],
    }

def reference(A_x, A_c_kv, A_w_q, A_w_uk, A_w_uv, A_w_o, B_x, B_w_gate, B_w_up, B_w_down):
    def _A_ref(x, c_kv, w_q, w_uk, w_uv, w_o):
        B, Sq, M = x.shape[0], x.shape[1], x.shape[2]
        Sk = c_kv.shape[1]
        H, D = w_q.shape[1], w_q.shape[2]
        q = torch.einsum("bqm,mhd->bhqd", x, w_q)
        k = torch.einsum("bkc,chd->bhkd", c_kv, w_uk)
        v = torch.einsum("bkc,chd->bhkd", c_kv, w_uv)
        scores = torch.einsum("bhqd,bhkd->bhqk", q, k) * (D ** -0.5)
        off = Sk - Sq
        qi = torch.arange(Sq)[:, None]
        kj = torch.arange(Sk)[None, :]
        cmask = kj <= (qi + off)
        scores = torch.where(cmask[None, None], scores, -1e9)
        p = _softmax(scores, axis=-1)
        ctx = torch.einsum("bhqk,bhkd->bhqd", p, v)
        return torch.einsum("bhqd,hdm->bqm", ctx, w_o)

    def _B_ref(x, w_gate, w_up, w_down):
        g = torch.einsum("bsd,df->bsf", x, w_gate)
        u = torch.einsum("bsd,df->bsf", x, w_up)
        h = g * u
        return torch.einsum("bsf,fd->bsd", h, w_down)

    x = A_x
    h = x + _A_ref(_layernorm(x, axis=-1), A_c_kv, A_w_q, A_w_uk, A_w_uv, A_w_o)
    h2 = h + _B_ref(_layernorm(h, axis=-1), B_w_gate, B_w_up, B_w_down)
    return h2
```

```cpp
#include <hip/hip_runtime.h>
#include <hip/hip_bf16.h>
typedef unsigned short u16;
__device__ __forceinline__ float bf2f(u16 v){ return __uint_as_float(((unsigned)v)<<16); }
__device__ __forceinline__ u16 f2bf(float f){ unsigned u=__float_as_uint(f); u += 0x7FFF + ((u>>16)&1); return (u16)(u>>16); }
constexpr int S=2048, M=448, H=16, D=256, DC=1024, F=1792;

__global__ void ln_kernel(const u16* x, u16* y){
  int row=blockIdx.x; int lane=threadIdx.x;
  float v[7]; float s=0;
  for(int i=0;i<7;i++){ v[i]=bf2f(x[row*M+lane+64*i]); s+=v[i]; }
  for(int o=32;o;o>>=1) s+=__shfl_xor(s,o);
  float m=s/M; float q=0;
  for(int i=0;i<7;i++){ float d=v[i]-m; q+=d*d; }
  for(int o=32;o;o>>=1) q+=__shfl_xor(q,o);
  float r=rsqrtf(q/M+1e-5f);
  for(int i=0;i<7;i++) y[row*M+lane+64*i]=f2bf((v[i]-m)*r);
}
template<int MODE>
__global__ void gemm_kernel(const u16* A, const u16* B, const u16* B2, const u16* res, u16* C, int Md, int N, int K){
  __shared__ float As[32][33], Bs[32][33], Bs2[32][33];
  int tx=threadIdx.x, ty=threadIdx.y; int m=blockIdx.y*32+ty, n=blockIdx.x*32+tx;
  float acc=0, acc2=0;
  for(int k0=0;k0<K;k0+=32){
    As[ty][tx]=bf2f(A[(size_t)m*K+k0+tx]);
    Bs[ty][tx]=bf2f(B[(size_t)(k0+ty)*N+n]);
    if(MODE==2) Bs2[ty][tx]=bf2f(B2[(size_t)(k0+ty)*N+n]);
    __syncthreads();
    for(int k=0;k<32;k++){ acc+=As[ty][k]*Bs[k][tx]; if(MODE==2) acc2+=As[ty][k]*Bs2[k][tx]; }
    __syncthreads();
  }
  float o=acc;
  if(MODE==1) o=bf2f(f2bf(acc))+bf2f(res[(size_t)m*N+n]);
  if(MODE==2) o=bf2f(f2bf(acc))*bf2f(f2bf(acc2));
  C[(size_t)m*N+n]=f2bf(o);
}
__global__ void attn_kernel(const u16* Q,const u16* Kk,const u16* V,u16* ctx){
  int q=blockIdx.x, h=blockIdx.y, t=threadIdx.x;
  __shared__ float sq[256]; __shared__ float p[2048]; __shared__ float red[256];
  sq[t]=bf2f(Q[(size_t)q*4096+h*256+t]); __syncthreads();
  float mx=-1e30f;
  for(int k=t;k<=q;k+=256){ float s=0; const u16* kr=Kk+(size_t)k*4096+h*256; for(int d=0;d<256;d++) s+=sq[d]*bf2f(kr[d]); s*=0.0625f; p[k]=s; mx=fmaxf(mx,s);}
  red[t]=mx; __syncthreads(); for(int o=128;o;o>>=1){ if(t<o) red[t]=fmaxf(red[t],red[t+o]); __syncthreads(); }
  mx=red[0]; __syncthreads();
  float sum=0; for(int k=t;k<=q;k+=256){ float e=__expf(p[k]-mx); p[k]=e; sum+=e; }
  red[t]=sum; __syncthreads(); for(int o=128;o;o>>=1){ if(t<o) red[t]+=red[t+o]; __syncthreads(); }
  sum=red[0]; float acc=0;
  for(int k=0;k<=q;k++) acc+=p[k]*bf2f(V[(size_t)k*4096+h*256+t]);
  ctx[(size_t)q*4096+h*256+t]=f2bf(acc/sum);
}
extern "C" void kernel_launch(void* const* d_in, const int* in_sizes, int n_in, void* d_out, int out_size, void* d_ws, size_t ws_size, hipStream_t stream){
  const u16* x=(const u16*)d_in[0]; const u16* ckv=(const u16*)d_in[1]; const u16* wq=(const u16*)d_in[2]; const u16* wuk=(const u16*)d_in[3];
  const u16* wuv=(const u16*)d_in[4]; const u16* wo=(const u16*)d_in[5]; const u16* wg=(const u16*)d_in[7]; const u16* wu=(const u16*)d_in[8]; const u16* wd=(const u16*)d_in[9];
  char* ws=(char*)d_ws; size_t MB=1<<20;
  u16* xn=(u16*)(ws); u16* q=(u16*)(ws+2*MB); u16* k=(u16*)(ws+18*MB); u16* v=(u16*)(ws+34*MB); u16* ctx=(u16*)(ws+50*MB); u16* h=(u16*)(ws+66*MB); u16* hn=(u16*)(ws+68*MB); u16* hh=(u16*)(ws+70*MB);
  dim3 b(32,32);
  ln_kernel<<<S,64,0,stream>>>(x,xn);
  gemm_kernel<0><<<dim3(4096/32,S/32),b,0,stream>>>(xn,wq,0,0,q,S,4096,M);
  gemm_kernel<0><<<dim3(4096/32,S/32),b,0,stream>>>(ckv,wuk,0,0,k,S,4096,DC);
  gemm_kernel<0><<<dim3(4096/32,S/32),b,0,stream>>>(ckv,wuv,0,0,v,S,4096,DC);
  attn_kernel<<<dim3(S,H),256,0,stream>>>(q,k,v,ctx);
  gemm_kernel<1><<<dim3(M/32,S/32),b,0,stream>>>(ctx,wo,0,x,h,S,M,4096);
  ln_kernel<<<S,64,0,stream>>>(h,hn);
  gemm_kernel<2><<<dim3(F/32,S/32),b,0,stream>>>(hn,wg,wu,0,hh,S,F,M);
  gemm_kernel<1><<<dim3(M/32,S/32),b,0,stream>>>(hh,wd,0,h,(u16*)d_out,S,M,F);
}
```

```cpp
#include <hip/hip_runtime.h>
#pragma clang diagnostic ignored "-Winline-asm"
__global__ __launch_bounds__(256, 1) void fused_block(const void* x, const void* ckv, const void* wq, const void* wuk, const void* wuv,
    const void* wo, const void* wg, const void* wu, const void* wd, void* out, void* ws) {
  asm volatile(
"s_mov_b64 s[8:9],%0\n"
"s_mov_b32 s10,%1\n"
"s_mov_b32 s11,%2\n"
"s_load_dwordx16 s[12:27],s[8:9],0x0\n"
"s_load_dwordx4 s[28:31],s[8:9],0x40\n"
"s_load_dwordx2 s[32:33],s[8:9],0x50\n"
"v_mbcnt_lo_u32_b32 v0,-1,0\n"
"v_mbcnt_hi_u32_b32 v0,-1,v0\n"
"s_getpc_b64 s[76:77]\n"
"L_pc_%=:\n"
"s_lshr_b32 s78,s10,3\n"
"s_lshl_b32 s78,s78,2\n"
"s_add_u32 s78,s78,s11\n"
"s_mul_i32 s78,s78,1280\n"
"v_min_u32 v1,9,v0\n"
"v_lshl_add_u32 v1,v1,7,s78\n"
"v_min_u32 v1,L_fin_%=-L_pc_%=,v1\n"
"global_load_dword v86,v1,s[76:77]\n"
"s_waitcnt lgkmcnt(0)\n"
"s_and_b32 s53,s10,7\n"
"s_lshr_b32 s54,s10,3\n"
"s_and_b32 s72,s54,7\n"
"s_lshr_b32 s73,s54,4\n"
"s_bfe_u32 s74,s54,0x10003\n"
"s_lshl_b32 s75,s53,1\n"
"s_add_u32 s74,s74,s75\n"
"s_add_u32 s92,s75,s73\n"
"s_bfe_u32 s93,s54,0x30001\n"
"s_and_b32 s94,s54,1\n"
"s_lshl_b32 s76,s72,19\n"
"s_add_u32 s36,s14,s76\n"
"s_addc_u32 s37,s15,0\n"
"s_and_b32 s37,s37,0xffff\n"
"s_mov_b32 s38,0x80000\n"
"s_mov_b32 s39,0x20000\n"
"s_cmp_eq_u32 s73,0\n"
"s_cselect_b32 s78,s18,s20\n"
"s_cselect_b32 s79,s19,s21\n"
"s_lshl_b32 s76,s74,9\n"
"s_sub_u32 s77,0x800000,s76\n"
"s_add_u32 s40,s78,s76\n"
"s_addc_u32 s41,s79,0\n"
"s_and_b32 s41,s41,0xffff\n"
"s_mov_b32 s42,s77\n"
"s_mov_b32 s43,0x20000\n"
"v_lshrrev_b32 v53,5,v0\n"
"v_bfe_u32 v54,v0,1,4\n"
"v_and_b32 v55,1,v0\n"
"s_lshl_b32 s76,s11,4\n"
"v_add_u32 v56,s76,v53\n"
"v_lshlrev_b32 v56,13,v56\n"
"v_lshl_add_u32 v56,v55,4,v56\n"
"v_xor_b32 v54,v54,v53\n"
"v_xor_b32 v57,0,v54\n"
"v_lshl_add_u32 v57,v57,5,v56\n"
"v_add_u32 v32,0x0,v57\n"
"v_xor_b32 v57,2,v54\n"
"v_lshl_add_u32 v57,v57,5,v56\n"
"v_add_u32 v33,0x3c00,v57\n"
"v_xor_b32 v57,0,v54\n"
"v_lshl_add_u32 v57,v57,5,v56\n"
"v_add_u32 v34,0x7800,v57\n"
"v_xor_b32 v57,2,v54\n"
"v_lshl_add_u32 v57,v57,5,v56\n"
"v_add_u32 v35,0xb400,v57\n"
"v_xor_b32 v57,4,v54\n"
"v_lshl_add_u32 v57,v57,5,v56\n"
"v_add_u32 v36,0x10000,v57\n"
"v_xor_b32 v57,6,v54\n"
"v_lshl_add_u32 v57,v57,5,v56\n"
"v_add_u32 v37,0x13c00,v57\n"
"v_xor_b32 v57,4,v54\n"
"v_lshl_add_u32 v57,v57,5,v56\n"
"v_add_u32 v38,0x17800,v57\n"
"v_xor_b32 v57,6,v54\n"
"v_lshl_add_u32 v57,v57,5,v56\n"
"v_add_u32 v39,0x1b400,v57\n"
"s_lshl_b32 s57,s11,13\n"
"s_mov_b32 s48,0\n"
"s_mov_b32 s49,0\n"
"s_mov_b32 s50,0\n"
"s_add_u32 m0,s57,0x0\n"
"s_nop 0\n"
"buffer_load_dwordx4 v32,s[40:43],s49 offen offset:0 nt lds\n"
"buffer_load_dwordx4 v33,s[40:43],s49 offen offset:1024 nt lds\n"
"buffer_load_dwordx4 v34,s[40:43],s49 offen offset:2048 nt lds\n"
"buffer_load_dwordx4 v35,s[40:43],s49 offen offset:3072 nt lds\n"
"s_add_u32 m0,s57,0x1000\n"
"s_nop 0\n"
"buffer_load_dwordx4 v36,s[40:43],s49 offen offset:0 nt lds\n"
"buffer_load_dwordx4 v37,s[40:43],s49 offen offset:1024 nt lds\n"
"buffer_load_dwordx4 v38,s[40:43],s49 offen offset:2048 nt lds\n"
"buffer_load_dwordx4 v39,s[40:43],s49 offen offset:3072 nt lds\n"
"s_add_u32 s49,s49,0x80000\n"
"v_lshrrev_b32 v1,3,v0\n"
"v_lshrrev_b32 v2,4,v0\n"
"v_and_b32 v3,7,v0\n"
"v_xor_b32 v4,v3,v2\n"
"v_xor_b32 v5,4,v4\n"
"s_lshl_b32 s76,s11,6\n"
"v_add_u32 v1,s76,v1\n"
"v_lshlrev_b32 v6,11,v1\n"
"v_lshl_add_u32 v7,v5,4,v6\n"
"v_lshl_add_u32 v6,v4,4,v6\n"
"v_add_u32 v24,0x0,v6\n"
"v_add_u32 v25,0x3c00,v7\n"
"v_add_u32 v26,0x7800,v6\n"
"v_add_u32 v27,0xb400,v7\n"
"v_add_u32 v28,0x10000,v6\n"
"v_add_u32 v29,0x13c00,v7\n"
"v_add_u32 v30,0x17800,v6\n"
"v_add_u32 v31,0x1b400,v7\n"
"s_add_u32 s55,s57,0x10000\n"
"s_add_u32 s56,s55,4096\n"
"s_add_u32 m0,s50,s55\n"
"s_nop 0\n"
"buffer_load_dwordx4 v24,s[36:39],s48 offen offset:0 lds\n"
"buffer_load_dwordx4 v25,s[36:39],s48 offen offset:1024 lds\n"
"buffer_load_dwordx4 v26,s[36:39],s48 offen offset:2048 lds\n"
"buffer_load_dwordx4 v27,s[36:39],s48 offen offset:3072 lds\n"
"s_add_u32 m0,s50,s56\n"
"s_nop 0\n"
"buffer_load_dwordx4 v28,s[36:39],s48 offen offset:0 lds\n"
"buffer_load_dwordx4 v29,s[36:39],s48 offen offset:1024 lds\n"
"buffer_load_dwordx4 v30,s[36:39],s48 offen offset:2048 lds\n"
"buffer_load_dwordx4 v31,s[36:39],s48 offen offset:3072 lds\n"
"s_add_u32 s48,s48,128\n"
"s_mov_b32 s51,s50\n"
"s_add_u32 s50,s50,0x8000\n"
"s_cmp_eq_u32 s50,0x18000\n"
"s_cselect_b32 s50,0,s50\n"
"v_lshlrev_b32 v52,4,v0\n"
"v_and_b32 v1,15,v0\n"
"v_lshrrev_b32 v2,4,v0\n"
"v_bfe_u32 v3,v0,1,3\n"
"v_xor_b32 v3,v3,v2\n"
"s_lshr_b32 s76,s11,1\n"
"s_lshl_b32 s76,s76,14\n"
"s_add_u32 s76,s76,0x10000\n"
"v_lshl_add_u32 v4,v1,7,s76\n"
"v_lshl_add_u32 v8,v3,4,v4\n"
"s_add_u32 m0,s57,0x8000\n"
"s_nop 0\n"
"buffer_load_dwordx4 v32,s[40:43],s49 offen offset:0 nt lds\n"
"v_xor_b32 v3,4,v3\n"
"v_lshl_add_u32 v9,v3,4,v4\n"
"v_mov_b32 v10,v8\n"
"v_mov_b32 v11,v9\n"
"v_bfe_u32 v1,v0,2,2\n"
"v_and_b32 v3,3,v0\n"
"v_lshl_add_u32 v4,v2,3,v1\n"
"v_and_b32 v5,1,v2\n"
"v_lshl_or_b32 v5,v5,2,v1\n"
"s_and_b32 s76,s11,1\n"
"buffer_load_dwordx4 v33,s[40:43],s49 offen offset:1024 nt lds\n"
"s_lshl_b32 s80,s76,8\n"
"v_lshl_add_u32 v6,v4,9,s80\n"
"v_lshl_add_u32 v6,v3,3,v6\n"
"v_xor_b32 v7,0,v5\n"
"v_lshl_add_u32 v12,v7,5,v6\n"
"v_xor_b32 v7,1,v5\n"
"v_lshl_add_u32 v13,v7,5,v6\n"
"v_xor_b32 v7,2,v5\n"
"v_lshl_add_u32 v14,v7,5,v6\n"
"v_xor_b32 v7,3,v5\n"
"buffer_load_dwordx4 v34,s[40:43],s49 offen offset:2048 nt lds\n"
"v_lshl_add_u32 v15,v7,5,v6\n"
"v_xor_b32 v7,4,v5\n"
"v_lshl_add_u32 v16,v7,5,v6\n"
"v_xor_b32 v7,5,v5\n"
"v_lshl_add_u32 v17,v7,5,v6\n"
"v_xor_b32 v7,6,v5\n"
"v_lshl_add_u32 v18,v7,5,v6\n"
"v_xor_b32 v7,7,v5\n"
"v_lshl_add_u32 v19,v7,5,v6\n"
"s_lshl_b32 s76,s92,9\n"
"buffer_load_dwordx4 v35,s[40:43],s49 offen offset:3072 nt lds\n"
"s_sub_u32 s77,0x380000,s76\n"
"s_add_u32 s64,s16,s76\n"
"s_addc_u32 s65,s17,0\n"
"s_and_b32 s65,s65,0xffff\n"
"s_mov_b32 s66,s77\n"
"s_mov_b32 s67,0x20000\n"
"s_lshl_b32 s76,s74,21\n"
"s_lshl_b32 s77,s72,18\n"
"s_add_u32 s76,s76,s77\n"
"s_lshr_b32 s77,s11,1\n"
"s_add_u32 m0,s57,0x9000\n"
"s_nop 0\n"
"buffer_load_dwordx4 v36,s[40:43],s49 offen offset:0 nt lds\n"
"s_lshl_b32 s77,s77,17\n"
"s_add_u32 s76,s76,s77\n"
"s_lshl_b32 s77,s73,15\n"
"s_add_u32 s76,s76,s77\n"
"s_and_b32 s77,s11,1\n"
"s_lshl_b32 s77,s77,14\n"
"s_add_u32 s76,s76,s77\n"
"s_add_u32 s44,s32,s76\n"
"s_addc_u32 s45,s33,0\n"
"s_add_u32 s44,s44,0x1400000\n"
"s_addc_u32 s45,s45,0\n"
"buffer_load_dwordx4 v37,s[40:43],s49 offen offset:1024 nt lds\n"
"s_and_b32 s45,s45,0xffff\n"
"s_mov_b32 s46,0x14000\n"
"s_mov_b32 s47,0x20000\n"
"s_sub_u32 s76,15,s93\n"
"s_cmp_eq_u32 s94,0\n"
"s_cselect_b32 s54,s76,s93\n"
"s_lshl_b32 s76,s54,3\n"
"s_lshl_b32 s77,s11,1\n"
"s_add_u32 s76,s76,s77\n"
"s_mul_i32 s76,s76,14336\n"
"s_add_u32 s60,s32,s76\n"
"s_addc_u32 s61,s33,0\n"
"s_add_u32 s60,s60,0x100000\n"
"s_addc_u32 s61,s61,0\n"
"buffer_load_dwordx4 v38,s[40:43],s49 offen offset:2048 nt lds\n"
"s_and_b32 s61,s61,0xffff\n"
"s_mov_b32 s62,0x7000\n"
"s_mov_b32 s63,0x20000\n"
"s_mov_b32 s86,0x1000\n"
"s_mov_b32 s87,0x2000\n"
"s_mov_b32 s88,0x3000\n"
"v_add_u32 v51,14336,v52\n"
"s_lshl_b32 s76,s74,4\n"
"s_lshl_b32 s77,s73,3\n"
"s_add_u32 s76,s76,s77\n"
"buffer_load_dwordx4 v39,s[40:43],s49 offen offset:3072 nt lds\n"
"s_add_u32 s49,s49,0x80000\n"
"s_add_u32 m0,s50,s55\n"
"s_nop 0\n"
"buffer_load_dwordx4 v24,s[36:39],s48 offen offset:0 lds\n"
"s_add_u32 s76,s76,s72\n"
"s_lshl_b32 s76,s76,3\n"
"s_add_u32 s84,s76,0x2000\n"
"s_add_u32 s84,s84,s32\n"
"s_addc_u32 s85,s33,0\n"
"v_and_b32 v1,15,v0\n"
"v_lshlrev_b32 v53,3,v1\n"
"s_lshl_b32 s76,s10,2\n"
"s_add_u32 s76,s76,s11\n"
"s_mul_i32 s77,s76,3584\n"
"buffer_load_dwordx4 v25,s[36:39],s48 offen offset:1024 lds\n"
"s_min_u32 s77,s77,3666176\n"
"s_add_u32 s78,s16,s77\n"
"s_addc_u32 s79,s17,0\n"
"v_lshlrev_b32 v5,7,v0\n"
"v_min_u32 v5,380,v5\n"
"buffer_load_dwordx4 v26,s[36:39],s48 offen offset:2048 lds\n"
"buffer_load_dwordx4 v27,s[36:39],s48 offen offset:3072 lds\n"
"s_add_u32 m0,s50,s56\n"
"s_nop 0\n"
"buffer_load_dwordx4 v28,s[36:39],s48 offen offset:0 lds\n"
"buffer_load_dwordx4 v29,s[36:39],s48 offen offset:1024 lds\n"
"buffer_load_dwordx4 v30,s[36:39],s48 offen offset:2048 lds\n"
"buffer_load_dwordx4 v31,s[36:39],s48 offen offset:3072 lds\n"
"s_add_u32 s48,s48,128\n"
"s_mov_b32 s51,s50\n"
"s_add_u32 s50,s50,0x8000\n"
"s_cmp_eq_u32 s50,0x18000\n"
"s_cselect_b32 s50,0,s50\n"
"s_mov_b32 s81,s51\n"
"v_mov_b32 v1,0\n"
"global_load_dwordx2 v[84:85],v1,s[32:33] sc1\n"
"s_mul_i32 s76,s10,7168\n"
"s_add_u32 s68,s12,s76\n"
"s_addc_u32 s69,s13,0\n"
"s_and_b32 s69,s69,0xffff\n"
"s_mov_b32 s70,0x1c00\n"
"s_mov_b32 s71,0x20000\n"
"v_and_b32 v1,7,v0\n"
"v_lshrrev_b32 v2,3,v0\n"
"v_mul_u32_u24 v3,896,v1\n"
"v_lshl_add_u32 v3,v2,4,v3\n"
"s_lshl_b32 s75,s11,8\n"
"buffer_load_dwordx4 v[56:59],v3,s[68:71],s75 offen\n"
"s_lshl_b32 s96,s75,4\n"
"s_add_u32 s75,s75,128\n"
"s_cmp_eq_u32 s75,896\n"
"s_cselect_b32 s75,0,s75\n"
"buffer_load_dwordx4 v[60:63],v3,s[68:71],s75 offen\n"
"s_lshl_b32 s97,s75,4\n"
"s_add_u32 s75,s75,128\n"
"s_cmp_eq_u32 s75,896\n"
"s_cselect_b32 s75,0,s75\n"
"buffer_load_dwordx4 v[64:67],v3,s[68:71],s75 offen\n"
"s_add_u32 s75,s75,128\n"
"s_cmp_eq_u32 s75,896\n"
"s_cselect_b32 s75,0,s75\n"
"buffer_load_dwordx4 v[68:71],v3,s[68:71],s75 offen\n"
"s_add_u32 s75,s75,128\n"
"s_cmp_eq_u32 s75,896\n"
"s_cselect_b32 s75,0,s75\n"
"buffer_load_dwordx4 v[72:75],v3,s[68:71],s75 offen\n"
"s_add_u32 s75,s75,128\n"
"s_cmp_eq_u32 s75,896\n"
"s_cselect_b32 s75,0,s75\n"
"buffer_load_dwordx4 v[76:79],v3,s[68:71],s75 offen\n"
"s_add_u32 s75,s75,128\n"
"s_cmp_eq_u32 s75,896\n"
"s_cselect_b32 s75,0,s75\n"
"buffer_load_dwordx4 v[80:83],v3,s[68:71],s75 offen\n"
"s_lshr_b32 s76,s10,1\n"
"s_mul_i32 s76,s76,14336\n"
"s_and_b32 s75,s10,1\n"
"s_lshl_b32 s75,s75,7\n"
"s_add_u32 s76,s76,s75\n"
"s_add_u32 s68,s32,s76\n"
"s_addc_u32 s69,s33,0\n"
"s_add_u32 s68,s68,0x100000\n"
"s_addc_u32 s69,s69,0\n"
"s_and_b32 s69,s69,0xffff\n"
"s_mov_b32 s70,0x3800\n"
"s_mov_b32 s71,0x20000\n"
"v_lshrrev_b32 v3,5,v0\n"
"v_and_b32 v2,3,v2\n"
"v_lshlrev_b32 v87,10,v3\n"
"v_lshl_add_u32 v87,v2,8,v87\n"
"v_lshl_add_u32 v87,v1,4,v87\n"
"s_waitcnt vmcnt(24)\n"
"s_barrier\n"
"ds_read_b128 v[96:99],v10 offset:0\n"
"ds_read_b128 v[100:103],v11 offset:0\n"
"ds_read_b128 v[104:107],v10 offset:2048\n"
"ds_read_b128 v[108:111],v11 offset:2048\n"
"ds_read_b64_tr_b16 a[0:1],v12 offset:0\n"
"ds_read_b64_tr_b16 a[2:3],v12 offset:2048\n"
"ds_read_b64_tr_b16 a[4:5],v12 offset:16384\n"
"ds_read_b64_tr_b16 a[6:7],v12 offset:18432\n"
"ds_read_b64_tr_b16 a[8:9],v13 offset:0\n"
"ds_read_b64_tr_b16 a[10:11],v13 offset:2048\n"
"ds_read_b64_tr_b16 a[12:13],v13 offset:16384\n"
"ds_read_b64_tr_b16 a[14:15],v13 offset:18432\n"
"ds_read_b64_tr_b16 a[16:17],v14 offset:0\n"
"ds_read_b64_tr_b16 a[18:19],v14 offset:2048\n"
"ds_read_b64_tr_b16 a[20:21],v14 offset:16384\n"
"ds_read_b64_tr_b16 a[22:23],v14 offset:18432\n"
"ds_read_b64_tr_b16 a[24:25],v15 offset:0\n"
"ds_read_b64_tr_b16 a[26:27],v15 offset:2048\n"
"ds_read_b64_tr_b16 a[28:29],v15 offset:16384\n"
"ds_read_b64_tr_b16 a[30:31],v15 offset:18432\n"
"ds_read_b64_tr_b16 a[32:33],v16 offset:0\n"
"ds_read_b64_tr_b16 a[34:35],v16 offset:2048\n"
"ds_read_b64_tr_b16 a[36:37],v16 offset:16384\n"
"ds_read_b64_tr_b16 a[38:39],v16 offset:18432\n"
"ds_read_b64_tr_b16 a[40:41],v17 offset:0\n"
"ds_read_b64_tr_b16 a[42:43],v17 offset:2048\n"
"ds_read_b64_tr_b16 a[44:45],v17 offset:16384\n"
"ds_read_b64_tr_b16 a[46:47],v17 offset:18432\n"
"ds_read_b64_tr_b16 a[48:49],v18 offset:0\n"
"ds_read_b64_tr_b16 a[50:51],v18 offset:2048\n"
"ds_read_b64_tr_b16 a[52:53],v18 offset:16384\n"
"ds_read_b64_tr_b16 a[54:55],v18 offset:18432\n"
"ds_read_b64_tr_b16 a[56:57],v19 offset:0\n"
"ds_read_b64_tr_b16 a[58:59],v19 offset:2048\n"
"ds_read_b64_tr_b16 a[60:61],v19 offset:16384\n"
"ds_read_b64_tr_b16 a[62:63],v19 offset:18432\n"
"s_waitcnt lgkmcnt(0)\n"
"s_barrier\n"
"s_mov_b32 s52,5\n"
"s_cmp_eq_u32 s73,0\n"
"s_cbranch_scc0 L_kv0v_%=\n"
"L_kv0k_%=:\n"
"v_mfma_f32_16x16x32_bf16 v[128:131],a[0:3],v[96:99],0\n"
"s_add_u32 m0,s57,0x0\n"
"ds_read_b128 v[112:115],v10 offset:4096\n"
"v_mfma_f32_16x16x32_bf16 v[128:131],a[4:7],v[100:103],v[128:131]\n"
"buffer_load_dwordx4 v32,s[40:43],s49 offen offset:0 nt lds\n"
"v_mfma_f32_16x16x32_bf16 v[132:135],a[8:11],v[96:99],0\n"
"ds_read_b128 v[116:119],v11 offset:4096\n"
"v_mfma_f32_16x16x32_bf16 v[132:135],a[12:15],v[100:103],v[132:135]\n"
"v_mfma_f32_16x16x32_bf16 v[136:139],a[16:19],v[96:99],0\n"
"v_mfma_f32_16x16x32_bf16 v[136:139],a[20:23],v[100:103],v[136:139]\n"
"buffer_load_dwordx4 v33,s[40:43],s49 offen offset:1024 nt lds\n"
"v_mfma_f32_16x16x32_bf16 v[140:143],a[24:27],v[96:99],0\n"
"v_mfma_f32_16x16x32_bf16 v[140:143],a[28:31],v[100:103],v[140:143]\n"
"s_waitcnt vmcnt(18)\n"
"s_barrier\n"
"v_mfma_f32_16x16x32_bf16 v[144:147],a[32:35],v[96:99],0\n"
"ds_read_b64_tr_b16 a[64:65],v12 offset:32768\n"
"v_mfma_f32_16x16x32_bf16 v[144:147],a[36:39],v[100:103],v[144:147]\n"
"buffer_load_dwordx4 v34,s[40:43],s49 offen offset:2048 nt lds\n"
"v_mfma_f32_16x16x32_bf16 v[148:151],a[40:43],v[96:99],0\n"
"ds_read_b64_tr_b16 a[66:67],v12 offset:34816\n"
"v_mfma_f32_16x16x32_bf16 v[148:151],a[44:47],v[100:103],v[148:151]\n"
"ds_read_b64_tr_b16 a[68:69],v12 offset:49152\n"
"v_mfma_f32_16x16x32_bf16 v[152:155],a[48:51],v[96:99],0\n"
"ds_read_b64_tr_b16 a[70:71],v12 offset:51200\n"
"v_mfma_f32_16x16x32_bf16 v[152:155],a[52:55],v[100:103],v[152:155]\n"
"buffer_load_dwordx4 v35,s[40:43],s49 offen offset:3072 nt lds\n"
"v_mfma_f32_16x16x32_bf16 v[156:159],a[56:59],v[96:99],0\n"
"ds_read_b64_tr_b16 a[72:73],v13 offset:32768\n"
"v_mfma_f32_16x16x32_bf16 v[156:159],a[60:63],v[100:103],v[156:159]\n"
"ds_read_b64_tr_b16 a[74:75],v13 offset:34816\n"
"v_mfma_f32_16x16x32_bf16 v[160:163],a[0:3],v[104:107],0\n"
"s_add_u32 m0,s57,0x1000\n"
"ds_read_b128 v[120:123],v10 offset:6144\n"
"v_mfma_f32_16x16x32_bf16 v[160:163],a[4:7],v[108:111],v[160:163]\n"
"buffer_load_dwordx4 v36,s[40:43],s49 offen offset:0 nt lds\n"
"v_mfma_f32_16x16x32_bf16 v[164:167],a[8:11],v[104:107],0\n"
"ds_read_b128 v[124:127],v11 offset:6144\n"
"v_mfma_f32_16x16x32_bf16 v[164:167],a[12:15],v[108:111],v[164:167]\n"
"ds_read_b64_tr_b16 a[76:77],v13 offset:49152\n"
"v_mfma_f32_16x16x32_bf16 v[168:171],a[16:19],v[104:107],0\n"
"ds_read_b64_tr_b16 a[78:79],v13 offset:51200\n"
"v_mfma_f32_16x16x32_bf16 v[168:171],a[20:23],v[108:111],v[168:171]\n"
"buffer_load_dwordx4 v37,s[40:43],s49 offen offset:1024 nt lds\n"
"v_mfma_f32_16x16x32_bf16 v[172:175],a[24:27],v[104:107],0\n"
"ds_read_b64_tr_b16 a[80:81],v14 offset:32768\n"
"v_mfma_f32_16x16x32_bf16 v[172:175],a[28:31],v[108:111],v[172:175]\n"
"ds_read_b64_tr_b16 a[82:83],v14 offset:34816\n"
"v_mfma_f32_16x16x32_bf16 v[176:179],a[32:35],v[104:107],0\n"
"s_waitcnt vmcnt(13)\n"
"v_readfirstlane_b32 s76,v84\n"
"v_readfirstlane_b32 s77,v85\n"
"v_mfma_f32_16x16x32_bf16 v[176:179],a[36:39],v[108:111],v[176:179]\n"
"buffer_load_dwordx4 v38,s[40:43],s49 offen offset:2048 nt lds\n"
"v_mfma_f32_16x16x32_bf16 v[180:183],a[40:43],v[104:107],0\n"
"ds_read_b64_tr_b16 a[84:85],v14 offset:49152\n"
"s_waitcnt vmcnt(13)\n"
"v_lshlrev_b32 v88,16,v56\n"
"v_mfma_f32_16x16x32_bf16 v[180:183],a[44:47],v[108:111],v[180:183]\n"
"v_and_b32 v89,0xffff0000,v56\n"
"v_add_f32 v92,v88,v89\n"
"v_mfma_f32_16x16x32_bf16 v[184:187],a[48:51],v[104:107],0\n"
"ds_read_b64_tr_b16 a[86:87],v14 offset:51200\n"
"v_mul_f32 v93,v88,v88\n"
"v_mfma_f32_16x16x32_bf16 v[184:187],a[52:55],v[108:111],v[184:187]\n"
"s_add_u32 s34,s76,0x9e3779b9\n"
"buffer_load_dwordx4 v39,s[40:43],s49 offen offset:3072 nt lds\n"
"v_mfma_f32_16x16x32_bf16 v[188:191],a[56:59],v[104:107],0\n"
"s_add_u32 s49,s49,0x80000\n"
"v_mfma_f32_16x16x32_bf16 v[188:191],a[60:63],v[108:111],v[188:191]\n"
"s_waitcnt lgkmcnt(14)\n"
"ds_read_b64_tr_b16 a[88:89],v15 offset:32768\n"
"v_mfma_f32_16x16x32_bf16 v[192:195],a[0:3],v[112:115],0\n"
"s_add_u32 m0,s50,s55\n"
"ds_read_b128 v[96:99],v10 offset:8192\n"
"v_mfma_f32_16x16x32_bf16 v[192:195],a[4:7],v[116:119],v[192:195]\n"
"s_mul_i32 s77,s77,0x85ebca6b\n"
"buffer_load_dwordx4 v24,s[36:39],s48 offen offset:0 lds\n"
"v_mfma_f32_16x16x32_bf16 v[196:199],a[8:11],v[112:115],0\n"
"s_add_u32 s35,s77,0x27d4eb2f\n"
"ds_read_b128 v[100:103],v11 offset:8192\n"
"v_fmac_f32 v93,v89,v89\n"
"v_mfma_f32_16x16x32_bf16 v[196:199],a[12:15],v[116:119],v[196:199]\n"
"ds_read_b64_tr_b16 a[90:91],v15 offset:34816\n"
"v_lshlrev_b32 v90,16,v57\n"
"v_mfma_f32_16x16x32_bf16 v[200:203],a[16:19],v[112:115],0\n"
"ds_read_b64_tr_b16 a[92:93],v15 offset:49152\n"
"v_and_b32 v91,0xffff0000,v57\n"
"v_mfma_f32_16x16x32_bf16 v[200:203],a[20:23],v[116:119],v[200:203]\n"
"buffer_load_dwordx4 v25,s[36:39],s48 offen offset:1024 lds\n"
"v_mfma_f32_16x16x32_bf16 v[204:207],a[24:27],v[112:115],0\n"
"v_add_f32 v92,v92,v90\n"
"v_fmac_f32 v93,v90,v90\n"
"v_mfma_f32_16x16x32_bf16 v[204:207],a[28:31],v[116:119],v[204:207]\n"
"ds_read_b64_tr_b16 a[94:95],v15 offset:51200\n"
"v_add_f32 v92,v92,v91\n"
"v_mfma_f32_16x16x32_bf16 v[208:211],a[32:35],v[112:115],0\n"
"v_fmac_f32 v93,v91,v91\n"
"v_lshlrev_b32 v88,16,v58\n"
"v_mfma_f32_16x16x32_bf16 v[208:211],a[36:39],v[116:119],v[208:211]\n"
"buffer_load_dwordx4 v26,s[36:39],s48 offen offset:2048 lds\n"
"v_mfma_f32_16x16x32_bf16 v[212:215],a[40:43],v[112:115],0\n"
"ds_read_b64_tr_b16 a[96:97],v16 offset:32768\n"
"v_and_b32 v89,0xffff0000,v58\n"
"v_mfma_f32_16x16x32_bf16 v[212:215],a[44:47],v[116:119],v[212:215]\n"
"v_add_f32 v92,v92,v88\n"
"v_fmac_f32 v93,v88,v88\n"
"v_mfma_f32_16x16x32_bf16 v[216:219],a[48:51],v[112:115],0\n"
"ds_read_b64_tr_b16 a[98:99],v16 offset:34816\n"
"v_add_f32 v92,v92,v89\n"
"v_mfma_f32_16x16x32_bf16 v[216:219],a[52:55],v[116:119],v[216:219]\n"
"buffer_load_dwordx4 v27,s[36:39],s48 offen offset:3072 lds\n"
"v_mfma_f32_16x16x32_bf16 v[220:223],a[56:59],v[112:115],0\n"
"v_fmac_f32 v93,v89,v89\n"
"v_lshlrev_b32 v90,16,v59\n"
"v_mfma_f32_16x16x32_bf16 v[220:223],a[60:63],v[116:119],v[220:223]\n"
"s_waitcnt lgkmcnt(14)\n"
"ds_read_b64_tr_b16 a[100:101],v16 offset:49152\n"
"v_and_b32 v91,0xffff0000,v59\n"
"v_mfma_f32_16x16x32_bf16 v[224:227],a[0:3],v[120:123],0\n"
"s_add_u32 m0,s50,s56\n"
"ds_read_b128 v[104:107],v10 offset:10240\n"
"v_add_f32 v92,v92,v90\n"
"v_mfma_f32_16x16x32_bf16 v[224:227],a[4:7],v[124:127],v[224:227]\n"
"buffer_load_dwordx4 v28,s[36:39],s48 offen offset:0 lds\n"
"v_mfma_f32_16x16x32_bf16 v[228:231],a[8:11],v[120:123],0\n"
"ds_read_b128 v[108:111],v11 offset:10240\n"
"v_fmac_f32 v93,v90,v90\n"
"v_mfma_f32_16x16x32_bf16 v[228:231],a[12:15],v[124:127],v[228:231]\n"
"ds_read_b64_tr_b16 a[102:103],v16 offset:51200\n"
"v_add_f32 v92,v92,v91\n"
"v_mfma_f32_16x16x32_bf16 v[232:235],a[16:19],v[120:123],0\n"
"ds_read_b64_tr_b16 a[104:105],v17 offset:32768\n"
"v_fmac_f32 v93,v91,v91\n"
"v_mfma_f32_16x16x32_bf16 v[232:235],a[20:23],v[124:127],v[232:235]\n"
"buffer_load_dwordx4 v29,s[36:39],s48 offen offset:1024 lds\n"
"v_mfma_f32_16x16x32_bf16 v[236:239],a[24:27],v[120:123],0\n"
"s_waitcnt vmcnt(19)\n"
"v_lshlrev_b32 v88,16,v60\n"
"v_and_b32 v89,0xffff0000,v60\n"
"v_mfma_f32_16x16x32_bf16 v[236:239],a[28:31],v[124:127],v[236:239]\n"
"ds_read_b64_tr_b16 a[106:107],v17 offset:34816\n"
"v_add_f32 v92,v92,v88\n"
"v_mfma_f32_16x16x32_bf16 v[240:243],a[32:35],v[120:123],0\n"
"v_fmac_f32 v93,v88,v88\n"
"v_add_f32 v92,v92,v89\n"
"v_mfma_f32_16x16x32_bf16 v[240:243],a[36:39],v[124:127],v[240:243]\n"
"buffer_load_dwordx4 v30,s[36:39],s48 offen offset:2048 lds\n"
"v_mfma_f32_16x16x32_bf16 v[244:247],a[40:43],v[120:123],0\n"
"ds_read_b64_tr_b16 a[108:109],v17 offset:49152\n"
"v_fmac_f32 v93,v89,v89\n"
"v_mfma_f32_16x16x32_bf16 v[244:247],a[44:47],v[124:127],v[244:247]\n"
"v_lshlrev_b32 v90,16,v61\n"
"v_and_b32 v91,0xffff0000,v61\n"
"v_mfma_f32_16x16x32_bf16 v[248:251],a[48:51],v[120:123],0\n"
"ds_read_b64_tr_b16 a[110:111],v17 offset:51200\n"
"v_add_f32 v92,v92,v90\n"
"v_mfma_f32_16x16x32_bf16 v[248:251],a[52:55],v[124:127],v[248:251]\n"
"buffer_load_dwordx4 v31,s[36:39],s48 offen offset:3072 lds\n"
"v_mfma_f32_16x16x32_bf16 v[252:255],a[56:59],v[120:123],0\n"
"s_add_u32 s48,s48,128\n"
"v_fmac_f32 v93,v90,v90\n"
"v_add_f32 v92,v92,v91\n"
"v_mfma_f32_16x16x32_bf16 v[252:255],a[60:63],v[124:127],v[252:255]\n"
"s_waitcnt lgkmcnt(13)\n"
"ds_read_b64_tr_b16 a[112:113],v18 offset:32768\n"
"v_fmac_f32 v93,v91,v91\n"
"v_mfma_f32_16x16x32_bf16 a[128:131],a[0:3],v[96:99],0\n"
"s_mov_b32 s51,s50\n"
"ds_read_b128 v[112:115],v10 offset:12288\n"
"v_lshlrev_b32 v88,16,v62\n"
"v_mfma_f32_16x16x32_bf16 a[128:131],a[4:7],v[100:103],a[128:131]\n"
"s_add_u32 s50,s50,0x8000\n"
"ds_read_b64_tr_b16 a[114:115],v18 offset:34816\n"
"v_and_b32 v89,0xffff0000,v62\n"
"v_mfma_f32_16x16x32_bf16 a[132:135],a[8:11],v[96:99],0\n"
"s_cmp_eq_u32 s50,0x18000\n"
"ds_read_b128 v[116:119],v11 offset:12288\n"
"v_add_f32 v92,v92,v88\n"
"v_mfma_f32_16x16x32_bf16 a[132:135],a[12:15],v[100:103],a[132:135]\n"
"s_cselect_b32 s50,0,s50\n"
"v_fmac_f32 v93,v88,v88\n"
"v_add_f32 v92,v92,v89\n"
"v_mfma_f32_16x16x32_bf16 a[136:139],a[16:19],v[96:99],0\n"
"ds_read_b64_tr_b16 a[116:117],v18 offset:49152\n"
"v_fmac_f32 v93,v89,v89\n"
"v_mfma_f32_16x16x32_bf16 a[136:139],a[20:23],v[100:103],a[136:139]\n"
"v_lshlrev_b32 v90,16,v63\n"
"v_and_b32 v91,0xffff0000,v63\n"
"v_mfma_f32_16x16x32_bf16 a[140:143],a[24:27],v[96:99],0\n"
"ds_read_b64_tr_b16 a[118:119],v18 offset:51200\n"
"v_add_f32 v92,v92,v90\n"
"v_mfma_f32_16x16x32_bf16 a[140:143],a[28:31],v[100:103],a[140:143]\n"
"v_fmac_f32 v93,v90,v90\n"
"v_add_f32 v92,v92,v91\n"
"v_mfma_f32_16x16x32_bf16 a[144:147],a[32:35],v[96:99],0\n"
"v_fmac_f32 v93,v91,v91\n"
"s_waitcnt vmcnt(20)\n"
"v_lshlrev_b32 v88,16,v64\n"
"v_mfma_f32_16x16x32_bf16 a[144:147],a[36:39],v[100:103],a[144:147]\n"
"ds_read_b64_tr_b16 a[120:121],v19 offset:32768\n"
"v_and_b32 v89,0xffff0000,v64\n"
"v_mfma_f32_16x16x32_bf16 a[148:151],a[40:43],v[96:99],0\n"
"v_add_f32 v92,v92,v88\n"
"v_fmac_f32 v93,v88,v88\n"
"v_mfma_f32_16x16x32_bf16 a[148:151],a[44:47],v[100:103],a[148:151]\n"
"v_add_f32 v92,v92,v89\n"
"v_fmac_f32 v93,v89,v89\n"
"v_mfma_f32_16x16x32_bf16 a[152:155],a[48:51],v[96:99],0\n"
"ds_read_b64_tr_b16 a[122:123],v19 offset:34816\n"
"v_lshlrev_b32 v90,16,v65\n"
"v_mfma_f32_16x16x32_bf16 a[152:155],a[52:55],v[100:103],a[152:155]\n"
"v_and_b32 v91,0xffff0000,v65\n"
"v_add_f32 v92,v92,v90\n"
"v_mfma_f32_16x16x32_bf16 a[156:159],a[56:59],v[96:99],0\n"
"ds_read_b64_tr_b16 a[124:125],v19 offset:49152\n"
"v_fmac_f32 v93,v90,v90\n"
"v_mfma_f32_16x16x32_bf16 a[156:159],a[60:63],v[100:103],a[156:159]\n"
"s_waitcnt lgkmcnt(14)\n"
"v_add_f32 v92,v92,v91\n"
"v_fmac_f32 v93,v91,v91\n"
"v_mfma_f32_16x16x32_bf16 a[160:163],a[0:3],v[104:107],0\n"
"ds_read_b128 v[120:123],v10 offset:14336\n"
"v_lshlrev_b32 v88,16,v66\n"
"v_mfma_f32_16x16x32_bf16 a[160:163],a[4:7],v[108:111],a[160:163]\n"
"ds_read_b64_tr_b16 a[126:127],v19 offset:51200\n"
"v_and_b32 v89,0xffff0000,v66\n"
"v_mfma_f32_16x16x32_bf16 a[164:167],a[8:11],v[104:107],0\n"
"ds_read_b128 v[124:127],v11 offset:14336\n"
"v_add_f32 v92,v92,v88\n"
"v_mfma_f32_16x16x32_bf16 a[164:167],a[12:15],v[108:111],a[164:167]\n"
"v_fmac_f32 v93,v88,v88\n"
"v_add_f32 v92,v92,v89\n"
"v_mfma_f32_16x16x32_bf16 a[168:171],a[16:19],v[104:107],0\n"
"v_fmac_f32 v93,v89,v89\n"
"v_lshlrev_b32 v90,16,v67\n"
"v_mfma_f32_16x16x32_bf16 a[168:171],a[20:23],v[108:111],a[168:171]\n"
"v_and_b32 v91,0xffff0000,v67\n"
"v_add_f32 v92,v92,v90\n"
"v_mfma_f32_16x16x32_bf16 a[172:175],a[24:27],v[104:107],0\n"
"v_fmac_f32 v93,v90,v90\n"
"v_add_f32 v92,v92,v91\n"
"v_mfma_f32_16x16x32_bf16 a[172:175],a[28:31],v[108:111],a[172:175]\n"
"v_fmac_f32 v93,v91,v91\n"
"s_waitcnt vmcnt(19)\n"
"v_lshlrev_b32 v88,16,v68\n"
"v_mfma_f32_16x16x32_bf16 a[176:179],a[32:35],v[104:107],0\n"
"v_and_b32 v89,0xffff0000,v68\n"
"v_add_f32 v92,v92,v88\n"
"v_mfma_f32_16x16x32_bf16 a[176:179],a[36:39],v[108:111],a[176:179]\n"
"v_fmac_f32 v93,v88,v88\n"
"v_add_f32 v92,v92,v89\n"
"v_mfma_f32_16x16x32_bf16 a[180:183],a[40:43],v[104:107],0\n"
"v_fmac_f32 v93,v89,v89\n"
"v_lshlrev_b32 v90,16,v69\n"
"v_mfma_f32_16x16x32_bf16 a[180:183],a[44:47],v[108:111],a[180:183]\n"
"v_and_b32 v91,0xffff0000,v69\n"
"v_add_f32 v92,v92,v90\n"
"v_mfma_f32_16x16x32_bf16 a[184:187],a[48:51],v[104:107],0\n"
"v_fmac_f32 v93,v90,v90\n"
"v_add_f32 v92,v92,v91\n"
"v_mfma_f32_16x16x32_bf16 a[184:187],a[52:55],v[108:111],a[184:187]\n"
"v_fmac_f32 v93,v91,v91\n"
"v_lshlrev_b32 v88,16,v70\n"
"v_mfma_f32_16x16x32_bf16 a[188:191],a[56:59],v[104:107],0\n"
"s_waitcnt lgkmcnt(0)\n"
"v_and_b32 v89,0xffff0000,v70\n"
"v_add_f32 v92,v92,v88\n"
"v_mfma_f32_16x16x32_bf16 a[188:191],a[60:63],v[108:111],a[188:191]\n"
"s_waitcnt vmcnt(8)\n"
"s_barrier\n"
"v_fmac_f32 v93,v88,v88\n"
"v_add_f32 v92,v92,v89\n"
"v_mfma_f32_16x16x32_bf16 a[192:195],a[0:3],v[112:115],0\n"
"s_add_u32 m0,s57,0x8000\n"
"v_add_u32 v10,s81,v8\n"
"v_add_u32 v11,s81,v9\n"
"v_mfma_f32_16x16x32_bf16 a[192:195],a[4:7],v[116:119],a[192:195]\n"
"s_mov_b32 s81,s51\n"
"buffer_load_dwordx4 v32,s[40:43],s49 offen offset:0 nt lds\n"
"v_mfma_f32_16x16x32_bf16 a[196:199],a[8:11],v[112:115],0\n"
"ds_read_b128 v[96:99],v10 offset:0\n"
"v_fmac_f32 v93,v89,v89\n"
"v_mfma_f32_16x16x32_bf16 a[196:199],a[12:15],v[116:119],a[196:199]\n"
"v_lshlrev_b32 v90,16,v71\n"
"v_and_b32 v91,0xffff0000,v71\n"
"v_mfma_f32_16x16x32_bf16 a[200:203],a[16:19],v[112:115],0\n"
"ds_read_b128 v[100:103],v11 offset:0\n"
"v_add_f32 v92,v92,v90\n"
"v_mfma_f32_16x16x32_bf16 a[200:203],a[20:23],v[116:119],a[200:203]\n"
"buffer_load_dwordx4 v33,s[40:43],s49 offen offset:1024 nt lds\n"
"v_mfma_f32_16x16x32_bf16 a[204:207],a[24:27],v[112:115],0\n"
"v_fmac_f32 v93,v90,v90\n"
"v_add_f32 v92,v92,v91\n"
"v_mfma_f32_16x16x32_bf16 a[204:207],a[28:31],v[116:119],a[204:207]\n"
"v_fmac_f32 v93,v91,v91\n"
"v_lshlrev_b32 v88,16,v72\n"
"v_mfma_f32_16x16x32_bf16 a[208:211],a[32:35],v[112:115],0\n"
"v_and_b32 v89,0xffff0000,v72\n"
"v_add_f32 v92,v92,v88\n"
"v_mfma_f32_16x16x32_bf16 a[208:211],a[36:39],v[116:119],a[208:211]\n"
"buffer_load_dwordx4 v34,s[40:43],s49 offen offset:2048 nt lds\n"
"v_mfma_f32_16x16x32_bf16 a[212:215],a[40:43],v[112:115],0\n"
"v_fmac_f32 v93,v88,v88\n"
"v_add_f32 v92,v92,v89\n"
"v_mfma_f32_16x16x32_bf16 a[212:215],a[44:47],v[116:119],a[212:215]\n"
"v_fmac_f32 v93,v89,v89\n"
"v_lshlrev_b32 v90,16,v73\n"
"v_mfma_f32_16x16x32_bf16 a[216:219],a[48:51],v[112:115],0\n"
"v_and_b32 v91,0xffff0000,v73\n"
"v_add_f32 v92,v92,v90\n"
"v_mfma_f32_16x16x32_bf16 a[216:219],a[52:55],v[116:119],a[216:219]\n"
"buffer_load_dwordx4 v35,s[40:43],s49 offen offset:3072 nt lds\n"
"v_mfma_f32_16x16x32_bf16 a[220:223],a[56:59],v[112:115],0\n"
"v_fmac_f32 v93,v90,v90\n"
"v_add_f32 v92,v92,v91\n"
"v_mfma_f32_16x16x32_bf16 a[220:223],a[60:63],v[116:119],a[220:223]\n"
"v_fmac_f32 v93,v91,v91\n"
"v_lshlrev_b32 v88,16,v74\n"
"v_mfma_f32_16x16x32_bf16 a[224:227],a[0:3],v[120:123],0\n"
"s_add_u32 m0,s57,0x9000\n"
"ds_read_b128 v[104:107],v10 offset:2048\n"
"v_and_b32 v89,0xffff0000,v74\n"
"v_mfma_f32_16x16x32_bf16 a[224:227],a[4:7],v[124:127],a[224:227]\n"
"buffer_load_dwordx4 v36,s[40:43],s49 offen offset:0 nt lds\n"
"v_mfma_f32_16x16x32_bf16 a[228:231],a[8:11],v[120:123],0\n"
"ds_read_b128 v[108:111],v11 offset:2048\n"
"v_add_f32 v92,v92,v88\n"
"v_mfma_f32_16x16x32_bf16 a[228:231],a[12:15],v[124:127],a[228:231]\n"
"v_fmac_f32 v93,v88,v88\n"
"v_add_f32 v92,v92,v89\n"
"v_mfma_f32_16x16x32_bf16 a[232:235],a[16:19],v[120:123],0\n"
"v_fmac_f32 v93,v89,v89\n"
"v_lshlrev_b32 v90,16,v75\n"
"v_mfma_f32_16x16x32_bf16 a[232:235],a[20:23],v[124:127],a[232:235]\n"
"buffer_load_dwordx4 v37,s[40:43],s49 offen offset:1024 nt lds\n"
"v_mfma_f32_16x16x32_bf16 a[236:239],a[24:27],v[120:123],0\n"
"v_and_b32 v91,0xffff0000,v75\n"
"v_add_f32 v92,v92,v90\n"
"v_mfma_f32_16x16x32_bf16 a[236:239],a[28:31],v[124:127],a[236:239]\n"
"v_fmac_f32 v93,v90,v90\n"
"v_add_f32 v92,v92,v91\n"
"v_mfma_f32_16x16x32_bf16 a[240:243],a[32:35],v[120:123],0\n"
"v_fmac_f32 v93,v91,v91\n"
"v_lshlrev_b32 v88,16,v76\n"
"v_mfma_f32_16x16x32_bf16 a[240:243],a[36:39],v[124:127],a[240:243]\n"
"buffer_load_dwordx4 v38,s[40:43],s49 offen offset:2048 nt lds\n"
"v_mfma_f32_16x16x32_bf16 a[244:247],a[40:43],v[120:123],0\n"
"v_and_b32 v89,0xffff0000,v76\n"
"v_add_f32 v92,v92,v88\n"
"v_mfma_f32_16x16x32_bf16 a[244:247],a[44:47],v[124:127],a[244:247]\n"
"v_fmac_f32 v93,v88,v88\n"
"v_add_f32 v92,v92,v89\n"
"v_mfma_f32_16x16x32_bf16 a[248:251],a[48:51],v[120:123],0\n"
"v_fmac_f32 v93,v89,v89\n"
"v_lshlrev_b32 v90,16,v77\n"
"v_mfma_f32_16x16x32_bf16 a[248:251],a[52:55],v[124:127],a[248:251]\n"
"buffer_load_dwordx4 v39,s[40:43],s49 offen offset:3072 nt lds\n"
"v_mfma_f32_16x16x32_bf16 a[252:255],a[56:59],v[120:123],0\n"
"s_add_u32 s49,s49,0x80000\n"
"v_and_b32 v91,0xffff0000,v77\n"
"v_add_f32 v92,v92,v90\n"
"v_mfma_f32_16x16x32_bf16 a[252:255],a[60:63],v[124:127],a[252:255]\n"
"s_waitcnt lgkmcnt(2)\n"
"v_fmac_f32 v93,v90,v90\n"
"v_add_f32 v92,v92,v91\n"
"v_mfma_f32_16x16x32_bf16 v[128:131],a[64:67],v[96:99],v[128:131]\n"
"ds_read_b128 v[112:115],v10 offset:4096\n"
"v_fmac_f32 v93,v91,v91\n"
"v_mfma_f32_16x16x32_bf16 v[128:131],a[68:71],v[100:103],v[128:131]\n"
"ds_read_b64_tr_b16 a[0:1],v12 offset:0\n"
"v_lshlrev_b32 v88,16,v78\n"
"v_mfma_f32_16x16x32_bf16 v[132:135],a[72:75],v[96:99],v[132:135]\n"
"ds_read_b128 v[116:119],v11 offset:4096\n"
"v_and_b32 v89,0xffff0000,v78\n"
"v_mfma_f32_16x16x32_bf16 v[132:135],a[76:79],v[100:103],v[132:135]\n"
"ds_read_b64_tr_b16 a[2:3],v12 offset:2048\n"
"v_add_f32 v92,v92,v88\n"
"v_mfma_f32_16x16x32_bf16 v[136:139],a[80:83],v[96:99],v[136:139]\n"
"v_fmac_f32 v93,v88,v88\n"
"v_add_f32 v92,v92,v89\n"
"v_mfma_f32_16x16x32_bf16 v[136:139],a[84:87],v[100:103],v[136:139]\n"
"ds_read_b64_tr_b16 a[4:5],v12 offset:16384\n"
"v_fmac_f32 v93,v89,v89\n"
"v_mfma_f32_16x16x32_bf16 v[140:143],a[88:91],v[96:99],v[140:143]\n"
"s_add_u32 m0,s50,s55\n"
"v_lshlrev_b32 v90,16,v79\n"
"v_and_b32 v91,0xffff0000,v79\n"
"v_mfma_f32_16x16x32_bf16 v[140:143],a[92:95],v[100:103],v[140:143]\n"
"buffer_load_dwordx4 v24,s[36:39],s48 offen offset:0 lds\n"
"v_mfma_f32_16x16x32_bf16 v[144:147],a[96:99],v[96:99],v[144:147]\n"
"ds_read_b64_tr_b16 a[6:7],v12 offset:18432\n"
"v_add_f32 v92,v92,v90\n"
"v_mfma_f32_16x16x32_bf16 v[144:147],a[100:103],v[100:103],v[144:147]\n"
"v_fmac_f32 v93,v90,v90\n"
"v_add_f32 v92,v92,v91\n"
"v_mfma_f32_16x16x32_bf16 v[148:151],a[104:107],v[96:99],v[148:151]\n"
"ds_read_b64_tr_b16 a[8:9],v13 offset:0\n"
"v_fmac_f32 v93,v91,v91\n"
"v_mfma_f32_16x16x32_bf16 v[148:151],a[108:111],v[100:103],v[148:151]\n"
"v_lshlrev_b32 v88,16,v80\n"
"v_and_b32 v89,0xffff0000,v80\n"
"v_mfma_f32_16x16x32_bf16 v[152:155],a[112:115],v[96:99],v[152:155]\n"
"v_add_f32 v92,v92,v88\n"
"v_fmac_f32 v93,v88,v88\n"
"v_mfma_f32_16x16x32_bf16 v[152:155],a[116:119],v[100:103],v[152:155]\n"
"buffer_load_dwordx4 v25,s[36:39],s48 offen offset:1024 lds\n"
"v_mfma_f32_16x16x32_bf16 v[156:159],a[120:123],v[96:99],v[156:159]\n"
"ds_read_b64_tr_b16 a[10:11],v13 offset:2048\n"
"v_add_f32 v92,v92,v89\n"
"v_mfma_f32_16x16x32_bf16 v[156:159],a[124:127],v[100:103],v[156:159]\n"
"s_waitcnt lgkmcnt(8)\n"
"ds_read_b64_tr_b16 a[12:13],v13 offset:16384\n"
"v_fmac_f32 v93,v89,v89\n"
"v_mfma_f32_16x16x32_bf16 v[160:163],a[64:67],v[104:107],v[160:163]\n"
"ds_read_b128 v[120:123],v10 offset:6144\n"
"v_lshlrev_b32 v90,16,v81\n"
"v_mfma_f32_16x16x32_bf16 v[160:163],a[68:71],v[108:111],v[160:163]\n"
"v_and_b32 v91,0xffff0000,v81\n"
"v_add_f32 v92,v92,v90\n"
"v_mfma_f32_16x16x32_bf16 v[164:167],a[72:75],v[104:107],v[164:167]\n"
"ds_read_b128 v[124:127],v11 offset:6144\n"
"v_fmac_f32 v93,v90,v90\n"
"v_mfma_f32_16x16x32_bf16 v[164:167],a[76:79],v[108:111],v[164:167]\n"
"buffer_load_dwordx4 v26,s[36:39],s48 offen offset:2048 lds\n"
"v_mfma_f32_16x16x32_bf16 v[168:171],a[80:83],v[104:107],v[168:171]\n"
"ds_read_b64_tr_b16 a[14:15],v13 offset:18432\n"
"v_add_f32 v92,v92,v91\n"
"v_mfma_f32_16x16x32_bf16 v[168:171],a[84:87],v[108:111],v[168:171]\n"
"ds_read_b64_tr_b16 a[16:17],v14 offset:0\n"
"v_fmac_f32 v93,v91,v91\n"
"v_mfma_f32_16x16x32_bf16 v[172:175],a[88:91],v[104:107],v[172:175]\n"
"v_lshlrev_b32 v88,16,v82\n"
"v_and_b32 v89,0xffff0000,v82\n"
"v_mfma_f32_16x16x32_bf16 v[172:175],a[92:95],v[108:111],v[172:175]\n"
"ds_read_b64_tr_b16 a[18:19],v14 offset:2048\n"
"v_add_f32 v92,v92,v88\n"
"v_mfma_f32_16x16x32_bf16 v[176:179],a[96:99],v[104:107],v[176:179]\n"
"v_fmac_f32 v93,v88,v88\n"
"v_add_f32 v92,v92,v89\n"
"v_mfma_f32_16x16x32_bf16 v[176:179],a[100:103],v[108:111],v[176:179]\n"
"buffer_load_dwordx4 v27,s[36:39],s48 offen offset:3072 lds\n"
"v_mfma_f32_16x16x32_bf16 v[180:183],a[104:107],v[104:107],v[180:183]\n"
"ds_read_b64_tr_b16 a[20:21],v14 offset:16384\n"
"v_fmac_f32 v93,v89,v89\n"
"v_mfma_f32_16x16x32_bf16 v[180:183],a[108:111],v[108:111],v[180:183]\n"
"v_lshlrev_b32 v90,16,v83\n"
"v_and_b32 v91,0xffff0000,v83\n"
"v_mfma_f32_16x16x32_bf16 v[184:187],a[112:115],v[104:107],v[184:187]\n"
"ds_read_b64_tr_b16 a[22:23],v14 offset:18432\n"
"v_add_f32 v92,v92,v90\n"
"v_mfma_f32_16x16x32_bf16 v[184:187],a[116:119],v[108:111],v[184:187]\n"
"v_fmac_f32 v93,v90,v90\n"
"v_add_f32 v92,v92,v91\n"
"v_mfma_f32_16x16x32_bf16 v[188:191],a[120:123],v[104:107],v[188:191]\n"
"s_add_u32 m0,s50,s56\n"
"v_fmac_f32 v93,v91,v91\n"
"v_add_f32_dpp v92,v92,v92 row_ror:8 row_mask:0xf bank_mask:0xf\n"
"v_mfma_f32_16x16x32_bf16 v[188:191],a[124:127],v[108:111],v[188:191]\n"
"s_waitcnt lgkmcnt(13)\n"
"buffer_load_dwordx4 v28,s[36:39],s48 offen offset:0 lds\n"
"v_mfma_f32_16x16x32_bf16 v[192:195],a[64:67],v[112:115],v[192:195]\n"
"ds_read_b128 v[96:99],v10 offset:8192\n"
"v_add_f32_dpp v93,v93,v93 row_ror:8 row_mask:0xf bank_mask:0xf\n"
"v_mfma_f32_16x16x32_bf16 v[192:195],a[68:71],v[116:119],v[192:195]\n"
"ds_read_b64_tr_b16 a[24:25],v15 offset:0\n"
"v_mov_b32 v94,v92\n"
"v_mfma_f32_16x16x32_bf16 v[196:199],a[72:75],v[112:115],v[196:199]\n"
"ds_read_b128 v[100:103],v11 offset:8192\n"
"v_mov_b32 v95,v93\n"
"v_mfma_f32_16x16x32_bf16 v[196:199],a[76:79],v[116:119],v[196:199]\n"
"ds_read_b64_tr_b16 a[26:27],v15 offset:2048\n"
"v_permlane32_swap_b32 v92,v94\n"
"v_mfma_f32_16x16x32_bf16 v[200:203],a[80:83],v[112:115],v[200:203]\n"
"ds_read_b64_tr_b16 a[28:29],v15 offset:16384\n"
"v_permlane32_swap_b32 v93,v95\n"
"v_mfma_f32_16x16x32_bf16 v[200:203],a[84:87],v[116:119],v[200:203]\n"
"buffer_load_dwordx4 v29,s[36:39],s48 offen offset:1024 lds\n"
"v_mfma_f32_16x16x32_bf16 v[204:207],a[88:91],v[112:115],v[204:207]\n"
"v_add_f32 v92,v92,v94\n"
"v_add_f32 v93,v93,v95\n"
"v_mfma_f32_16x16x32_bf16 v[204:207],a[92:95],v[116:119],v[204:207]\n"
"ds_read_b64_tr_b16 a[30:31],v15 offset:18432\n"
"v_mov_b32 v94,v92\n"
"v_mfma_f32_16x16x32_bf16 v[208:211],a[96:99],v[112:115],v[208:211]\n"
"v_mov_b32 v95,v93\n"
"v_permlane16_swap_b32 v92,v94\n"
"v_mfma_f32_16x16x32_bf16 v[208:211],a[100:103],v[116:119],v[208:211]\n"
"v_permlane16_swap_b32 v93,v95\n"
"v_add_f32 v92,v92,v94\n"
"v_mfma_f32_16x16x32_bf16 v[212:215],a[104:107],v[112:115],v[212:215]\n"
"ds_read_b64_tr_b16 a[32:33],v16 offset:0\n"
"v_add_f32 v93,v93,v95\n"
"v_mfma_f32_16x16x32_bf16 v[212:215],a[108:111],v[116:119],v[212:215]\n"
"buffer_load_dwordx4 v30,s[36:39],s48 offen offset:2048 lds\n"
"v_mfma_f32_16x16x32_bf16 v[216:219],a[112:115],v[112:115],v[216:219]\n"
"ds_read_b64_tr_b16 a[34:35],v16 offset:2048\n"
"v_mul_f32 v92,0x3b124925,v92\n"
"v_mfma_f32_16x16x32_bf16 v[216:219],a[116:119],v[116:119],v[216:219]\n"
"v_mul_f32 v93,0x3b124925,v93\n"
"v_fma_f32 v93,-v92,v92,v93\n"
"v_mfma_f32_16x16x32_bf16 v[220:223],a[120:123],v[112:115],v[220:223]\n"
"v_max_f32 v93,0,v93\n"
"v_add_f32 v93,0x3727c5ac,v93\n"
"v_mfma_f32_16x16x32_bf16 v[220:223],a[124:127],v[116:119],v[220:223]\n"
"s_waitcnt lgkmcnt(13)\n"
"ds_read_b64_tr_b16 a[36:37],v16 offset:16384\n"
"v_mfma_f32_16x16x32_bf16 v[224:227],a[64:67],v[120:123],v[224:227]\n"
"ds_read_b128 v[104:107],v10 offset:10240\n"
"v_mfma_f32_16x16x32_bf16 v[224:227],a[68:71],v[124:127],v[224:227]\n"
"buffer_load_dwordx4 v31,s[36:39],s48 offen offset:3072 lds\n"
"v_mfma_f32_16x16x32_bf16 v[228:231],a[72:75],v[120:123],v[228:231]\n"
"ds_read_b128 v[108:111],v11 offset:10240\n"
"v_mfma_f32_16x16x32_bf16 v[228:231],a[76:79],v[124:127],v[228:231]\n"
"ds_read_b64_tr_b16 a[38:39],v16 offset:18432\n"
"v_mfma_f32_16x16x32_bf16 v[232:235],a[80:83],v[120:123],v[232:235]\n"
"ds_read_b64_tr_b16 a[40:41],v17 offset:0\n"
"v_mfma_f32_16x16x32_bf16 v[232:235],a[84:87],v[124:127],v[232:235]\n"
"v_rsq_f32 v93,v93\n"
"v_mfma_f32_16x16x32_bf16 v[236:239],a[88:91],v[120:123],v[236:239]\n"
"s_add_u32 s48,s48,128\n"
"v_mul_f32 v93,0x3db8aa3b,v93\n"
"v_lshlrev_b32 v88,16,v56\n"
"v_mfma_f32_16x16x32_bf16 v[236:239],a[92:95],v[124:127],v[236:239]\n"
"s_mov_b32 s51,s50\n"
"ds_read_b64_tr_b16 a[42:43],v17 offset:2048\n"
"v_and_b32 v89,0xffff0000,v56\n"
"v_mfma_f32_16x16x32_bf16 v[240:243],a[96:99],v[120:123],v[240:243]\n"
"s_add_u32 s50,s50,0x8000\n"
"v_sub_f32 v88,v88,v92\n"
"v_sub_f32 v89,v89,v92\n"
"v_mfma_f32_16x16x32_bf16 v[240:243],a[100:103],v[124:127],v[240:243]\n"
"s_cmp_eq_u32 s50,0x18000\n"
"ds_read_b64_tr_b16 a[44:45],v17 offset:16384\n"
"v_mul_f32 v88,v88,v93\n"
"v_mfma_f32_16x16x32_bf16 v[244:247],a[104:107],v[120:123],v[244:247]\n"
"s_cselect_b32 s50,0,s50\n"
"v_mul_f32 v89,v89,v93\n"
"v_cvt_pk_bf16_f32 v56,v88,v89\n"
"v_mfma_f32_16x16x32_bf16 v[244:247],a[108:111],v[124:127],v[244:247]\n"
"v_lshlrev_b32 v90,16,v57\n"
"v_and_b32 v91,0xffff0000,v57\n"
"v_mfma_f32_16x16x32_bf16 v[248:251],a[112:115],v[120:123],v[248:251]\n"
"ds_read_b64_tr_b16 a[46:47],v17 offset:18432\n"
"v_sub_f32 v90,v90,v92\n"
"v_mfma_f32_16x16x32_bf16 v[248:251],a[116:119],v[124:127],v[248:251]\n"
"v_sub_f32 v91,v91,v92\n"
"v_mul_f32 v90,v90,v93\n"
"v_mfma_f32_16x16x32_bf16 v[252:255],a[120:123],v[120:123],v[252:255]\n"
"v_mul_f32 v91,v91,v93\n"
"v_cvt_pk_bf16_f32 v57,v90,v91\n"
"v_mfma_f32_16x16x32_bf16 v[252:255],a[124:127],v[124:127],v[252:255]\n"
"s_waitcnt lgkmcnt(13)\n"
"ds_read_b64_tr_b16 a[48:49],v18 offset:0\n"
"v_lshlrev_b32 v88,16,v58\n"
"v_mfma_f32_16x16x32_bf16 a[128:131],a[64:67],v[96:99],a[128:131]\n"
"ds_read_b128 v[112:115],v10 offset:12288\n"
"v_and_b32 v89,0xffff0000,v58\n"
"v_mfma_f32_16x16x32_bf16 a[128:131],a[68:71],v[100:103],a[128:131]\n"
"ds_read_b64_tr_b16 a[50:51],v18 offset:2048\n"
"v_sub_f32 v88,v88,v92\n"
"v_mfma_f32_16x16x32_bf16 a[132:135],a[72:75],v[96:99],a[132:135]\n"
"ds_read_b128 v[116:119],v11 offset:12288\n"
"v_sub_f32 v89,v89,v92\n"
"v_mfma_f32_16x16x32_bf16 a[132:135],a[76:79],v[100:103],a[132:135]\n"
"v_mul_f32 v88,v88,v93\n"
"v_mul_f32 v89,v89,v93\n"
"v_mfma_f32_16x16x32_bf16 a[136:139],a[80:83],v[96:99],a[136:139]\n"
"ds_read_b64_tr_b16 a[52:53],v18 offset:16384\n"
"v_cvt_pk_bf16_f32 v58,v88,v89\n"
"v_mfma_f32_16x16x32_bf16 a[136:139],a[84:87],v[100:103],a[136:139]\n"
"v_lshlrev_b32 v90,16,v59\n"
"v_and_b32 v91,0xffff0000,v59\n"
"v_mfma_f32_16x16x32_bf16 a[140:143],a[88:91],v[96:99],a[140:143]\n"
"ds_read_b64_tr_b16 a[54:55],v18 offset:18432\n"
"v_sub_f32 v90,v90,v92\n"
"v_mfma_f32_16x16x32_bf16 a[140:143],a[92:95],v[100:103],a[140:143]\n"
"v_sub_f32 v91,v91,v92\n"
"v_mul_f32 v90,v90,v93\n"
"v_mfma_f32_16x16x32_bf16 a[144:147],a[96:99],v[96:99],a[144:147]\n"
"v_mul_f32 v91,v91,v93\n"
"v_cvt_pk_bf16_f32 v59,v90,v91\n"
"v_mfma_f32_16x16x32_bf16 a[144:147],a[100:103],v[100:103],a[144:147]\n"
"ds_read_b64_tr_b16 a[56:57],v19 offset:0\n"
"buffer_store_dwordx4 v[56:59],v87,s[68:71],s96 offen sc1\n"
"v_mfma_f32_16x16x32_bf16 a[148:151],a[104:107],v[96:99],a[148:151]\n"
"v_lshlrev_b32 v88,16,v60\n"
"v_and_b32 v89,0xffff0000,v60\n"
"v_mfma_f32_16x16x32_bf16 a[148:151],a[108:111],v[100:103],a[148:151]\n"
"v_sub_f32 v88,v88,v92\n"
"v_sub_f32 v89,v89,v92\n"
"v_mfma_f32_16x16x32_bf16 a[152:155],a[112:115],v[96:99],a[152:155]\n"
"ds_read_b64_tr_b16 a[58:59],v19 offset:2048\n"
"v_mul_f32 v88,v88,v93\n"
"v_mfma_f32_16x16x32_bf16 a[152:155],a[116:119],v[100:103],a[152:155]\n"
"v_mul_f32 v89,v89,v93\n"
"v_cvt_pk_bf16_f32 v60,v88,v89\n"
"v_mfma_f32_16x16x32_bf16 a[156:159],a[120:123],v[96:99],a[156:159]\n"
"ds_read_b64_tr_b16 a[60:61],v19 offset:16384\n"
"v_lshlrev_b32 v90,16,v61\n"
"v_mfma_f32_16x16x32_bf16 a[156:159],a[124:127],v[100:103],a[156:159]\n"
"s_waitcnt lgkmcnt(14)\n"
"v_and_b32 v91,0xffff0000,v61\n"
"v_sub_f32 v90,v90,v92\n"
"v_mfma_f32_16x16x32_bf16 a[160:163],a[64:67],v[104:107],a[160:163]\n"
"ds_read_b128 v[120:123],v10 offset:14336\n"
"v_sub_f32 v91,v91,v92\n"
"v_mfma_f32_16x16x32_bf16 a[160:163],a[68:71],v[108:111],a[160:163]\n"
"ds_read_b64_tr_b16 a[62:63],v19 offset:18432\n"
"v_mul_f32 v90,v90,v93\n"
"v_mfma_f32_16x16x32_bf16 a[164:167],a[72:75],v[104:107],a[164:167]\n"
"ds_read_b128 v[124:127],v11 offset:14336\n"
"v_mul_f32 v91,v91,v93\n"
"v_mfma_f32_16x16x32_bf16 a[164:167],a[76:79],v[108:111],a[164:167]\n"
"v_cvt_pk_bf16_f32 v61,v90,v91\n"
"v_lshlrev_b32 v88,16,v62\n"
"v_mfma_f32_16x16x32_bf16 a[168:171],a[80:83],v[104:107],a[168:171]\n"
"v_and_b32 v89,0xffff0000,v62\n"
"v_sub_f32 v88,v88,v92\n"
"v_mfma_f32_16x16x32_bf16 a[168:171],a[84:87],v[108:111],a[168:171]\n"
"v_sub_f32 v89,v89,v92\n"
"v_mul_f32 v88,v88,v93\n"
"v_mfma_f32_16x16x32_bf16 a[172:175],a[88:91],v[104:107],a[172:175]\n"
"v_mul_f32 v89,v89,v93\n"
"v_cvt_pk_bf16_f32 v62,v88,v89\n"
"v_mfma_f32_16x16x32_bf16 a[172:175],a[92:95],v[108:111],a[172:175]\n"
"v_lshlrev_b32 v90,16,v63\n"
"v_and_b32 v91,0xffff0000,v63\n"
"v_mfma_f32_16x16x32_bf16 a[176:179],a[96:99],v[104:107],a[176:179]\n"
"v_sub_f32 v90,v90,v92\n"
"v_sub_f32 v91,v91,v92\n"
"v_mfma_f32_16x16x32_bf16 a[176:179],a[100:103],v[108:111],a[176:179]\n"
"v_mul_f32 v90,v90,v93\n"
"v_mul_f32 v91,v91,v93\n"
"v_mfma_f32_16x16x32_bf16 a[180:183],a[104:107],v[104:107],a[180:183]\n"
"v_cvt_pk_bf16_f32 v63,v90,v91\n"
"v_mfma_f32_16x16x32_bf16 a[180:183],a[108:111],v[108:111],a[180:183]\n"
"buffer_store_dwordx4 v[60:63],v87,s[68:71],s97 offen sc1\n"
"v_mfma_f32_16x16x32_bf16 a[184:187],a[112:115],v[104:107],a[184:187]\n"
"v_mfma_f32_16x16x32_bf16 a[184:187],a[116:119],v[108:111],a[184:187]\n"
"v_mfma_f32_16x16x32_bf16 a[188:191],a[120:123],v[104:107],a[188:191]\n"
"s_waitcnt lgkmcnt(0)\n"
"v_mfma_f32_16x16x32_bf16 a[188:191],a[124:127],v[108:111],a[188:191]\n"
"s_waitcnt vmcnt(10)\n"
"s_barrier\n"
"v_mfma_f32_16x16x32_bf16 a[192:195],a[64:67],v[112:115],a[192:195]\n"
"s_add_u32 m0,s57,0x0\n"
"v_add_u32 v10,s81,v8\n"
"v_add_u32 v11,s81,v9\n"
"v_mfma_f32_16x16x32_bf16 a[192:195],a[68:71],v[116:119],a[192:195]\n"
"s_mov_b32 s81,s51\n"
"buffer_load_dwordx4 v32,s[40:43],s49 offen offset:0 nt lds\n"
"v_mfma_f32_16x16x32_bf16 a[196:199],a[72:75],v[112:115],a[196:199]\n"
"ds_read_b128 v[96:99],v10 offset:0\n"
"v_mfma_f32_16x16x32_bf16 a[196:199],a[76:79],v[116:119],a[196:199]\n"
"v_mfma_f32_16x16x32_bf16 a[200:203],a[80:83],v[112:115],a[200:203]\n"
"ds_read_b128 v[100:103],v11 offset:0\n"
"v_mfma_f32_16x16x32_bf16 a[200:203],a[84:87],v[116:119],a[200:203]\n"
"buffer_load_dwordx4 v33,s[40:43],s49 offen offset:1024 nt lds\n"
"v_mfma_f32_16x16x32_bf16 a[204:207],a[88:91],v[112:115],a[204:207]\n"
"v_mfma_f32_16x16x32_bf16 a[204:207],a[92:95],v[116:119],a[204:207]\n"
"v_mfma_f32_16x16x32_bf16 a[208:211],a[96:99],v[112:115],a[208:211]\n"
"v_mfma_f32_16x16x32_bf16 a[208:211],a[100:103],v[116:119],a[208:211]\n"
"buffer_load_dwordx4 v34,s[40:43],s49 offen offset:2048 nt lds\n"
"v_mfma_f32_16x16x32_bf16 a[212:215],a[104:107],v[112:115],a[212:215]\n"
"v_mfma_f32_16x16x32_bf16 a[212:215],a[108:111],v[116:119],a[212:215]\n"
"v_mfma_f32_16x16x32_bf16 a[216:219],a[112:115],v[112:115],a[216:219]\n"
"v_mfma_f32_16x16x32_bf16 a[216:219],a[116:119],v[116:119],a[216:219]\n"
"buffer_load_dwordx4 v35,s[40:43],s49 offen offset:3072 nt lds\n"
"v_mfma_f32_16x16x32_bf16 a[220:223],a[120:123],v[112:115],a[220:223]\n"
"v_mfma_f32_16x16x32_bf16 a[220:223],a[124:127],v[116:119],a[220:223]\n"
"v_mfma_f32_16x16x32_bf16 a[224:227],a[64:67],v[120:123],a[224:227]\n"
"s_add_u32 m0,s57,0x1000\n"
"ds_read_b128 v[104:107],v10 offset:2048\n"
"v_mfma_f32_16x16x32_bf16 a[224:227],a[68:71],v[124:127],a[224:227]\n"
"buffer_load_dwordx4 v36,s[40:43],s49 offen offset:0 nt lds\n"
"v_mfma_f32_16x16x32_bf16 a[228:231],a[72:75],v[120:123],a[228:231]\n"
"ds_read_b128 v[108:111],v11 offset:2048\n"
"v_mfma_f32_16x16x32_bf16 a[228:231],a[76:79],v[124:127],a[228:231]\n"
"v_mfma_f32_16x16x32_bf16 a[232:235],a[80:83],v[120:123],a[232:235]\n"
"v_mfma_f32_16x16x32_bf16 a[232:235],a[84:87],v[124:127],a[232:235]\n"
"buffer_load_dwordx4 v37,s[40:43],s49 offen offset:1024 nt lds\n"
"v_mfma_f32_16x16x32_bf16 a[236:239],a[88:91],v[120:123],a[236:239]\n"
"v_mfma_f32_16x16x32_bf16 a[236:239],a[92:95],v[124:127],a[236:239]\n"
"v_mfma_f32_16x16x32_bf16 a[240:243],a[96:99],v[120:123],a[240:243]\n"
"v_mfma_f32_16x16x32_bf16 a[240:243],a[100:103],v[124:127],a[240:243]\n"
"buffer_load_dwordx4 v38,s[40:43],s49 offen offset:2048 nt lds\n"
"v_mfma_f32_16x16x32_bf16 a[244:247],a[104:107],v[120:123],a[244:247]\n"
"v_mfma_f32_16x16x32_bf16 a[244:247],a[108:111],v[124:127],a[244:247]\n"
"v_mfma_f32_16x16x32_bf16 a[248:251],a[112:115],v[120:123],a[248:251]\n"
"v_mfma_f32_16x16x32_bf16 a[248:251],a[116:119],v[124:127],a[248:251]\n"
"buffer_load_dwordx4 v39,s[40:43],s49 offen offset:3072 nt lds\n"
"v_mfma_f32_16x16x32_bf16 a[252:255],a[120:123],v[120:123],a[252:255]\n"
"s_add_u32 s49,s49,0x80000\n"
"v_mfma_f32_16x16x32_bf16 a[252:255],a[124:127],v[124:127],a[252:255]\n"
"s_waitcnt lgkmcnt(2)\n"
"L_kvk_%=:\n"
"v_mfma_f32_16x16x32_bf16 v[128:131],a[0:3],v[96:99],v[128:131]\n"
"ds_read_b128 v[112:115],v10 offset:4096\n"
"v_mfma_f32_16x16x32_bf16 v[128:131],a[4:7],v[100:103],v[128:131]\n"
"ds_read_b64_tr_b16 a[64:65],v12 offset:32768\n"
"v_mfma_f32_16x16x32_bf16 v[132:135],a[8:11],v[96:99],v[132:135]\n"
"ds_read_b128 v[116:119],v11 offset:4096\n"
"v_mfma_f32_16x16x32_bf16 v[132:135],a[12:15],v[100:103],v[132:135]\n"
"ds_read_b64_tr_b16 a[66:67],v12 offset:34816\n"
"v_mfma_f32_16x16x32_bf16 v[136:139],a[16:19],v[96:99],v[136:139]\n"
"v_mfma_f32_16x16x32_bf16 v[136:139],a[20:23],v[100:103],v[136:139]\n"
"global_load_dword v86,v5,s[78:79]\n"
"v_mfma_f32_16x16x32_bf16 v[140:143],a[24:27],v[96:99],v[140:143]\n"
"s_add_u32 m0,s50,s55\n"
"ds_read_b64_tr_b16 a[68:69],v12 offset:49152\n"
"v_mfma_f32_16x16x32_bf16 v[140:143],a[28:31],v[100:103],v[140:143]\n"
"buffer_load_dwordx4 v24,s[36:39],s48 offen offset:0 lds\n"
"v_mfma_f32_16x16x32_bf16 v[144:147],a[32:35],v[96:99],v[144:147]\n"
"ds_read_b64_tr_b16 a[70:71],v12 offset:51200\n"
"v_mfma_f32_16x16x32_bf16 v[144:147],a[36:39],v[100:103],v[144:147]\n"
"v_mfma_f32_16x16x32_bf16 v[148:151],a[40:43],v[96:99],v[148:151]\n"
"ds_read_b64_tr_b16 a[72:73],v13 offset:32768\n"
"v_mfma_f32_16x16x32_bf16 v[148:151],a[44:47],v[100:103],v[148:151]\n"
"v_mfma_f32_16x16x32_bf16 v[152:155],a[48:51],v[96:99],v[152:155]\n"
"v_mfma_f32_16x16x32_bf16 v[152:155],a[52:55],v[100:103],v[152:155]\n"
"buffer_load_dwordx4 v25,s[36:39],s48 offen offset:1024 lds\n"
"v_mfma_f32_16x16x32_bf16 v[156:159],a[56:59],v[96:99],v[156:159]\n"
"ds_read_b64_tr_b16 a[74:75],v13 offset:34816\n"
"v_mfma_f32_16x16x32_bf16 v[156:159],a[60:63],v[100:103],v[156:159]\n"
"s_waitcnt lgkmcnt(8)\n"
"ds_read_b64_tr_b16 a[76:77],v13 offset:49152\n"
"v_mfma_f32_16x16x32_bf16 v[160:163],a[0:3],v[104:107],v[160:163]\n"
"ds_read_b128 v[120:123],v10 offset:6144\n"
"v_mfma_f32_16x16x32_bf16 v[160:163],a[4:7],v[108:111],v[160:163]\n"
"v_mfma_f32_16x16x32_bf16 v[164:167],a[8:11],v[104:107],v[164:167]\n"
"ds_read_b128 v[124:127],v11 offset:6144\n"
"v_mfma_f32_16x16x32_bf16 v[164:167],a[12:15],v[108:111],v[164:167]\n"
"buffer_load_dwordx4 v26,s[36:39],s48 offen offset:2048 lds\n"
"v_mfma_f32_16x16x32_bf16 v[168:171],a[16:19],v[104:107],v[168:171]\n"
"ds_read_b64_tr_b16 a[78:79],v13 offset:51200\n"
"v_mfma_f32_16x16x32_bf16 v[168:171],a[20:23],v[108:111],v[168:171]\n"
"ds_read_b64_tr_b16 a[80:81],v14 offset:32768\n"
"v_mfma_f32_16x16x32_bf16 v[172:175],a[24:27],v[104:107],v[172:175]\n"
"v_mfma_f32_16x16x32_bf16 v[172:175],a[28:31],v[108:111],v[172:175]\n"
"ds_read_b64_tr_b16 a[82:83],v14 offset:34816\n"
"v_mfma_f32_16x16x32_bf16 v[176:179],a[32:35],v[104:107],v[176:179]\n"
"v_mfma_f32_16x16x32_bf16 v[176:179],a[36:39],v[108:111],v[176:179]\n"
"buffer_load_dwordx4 v27,s[36:39],s48 offen offset:3072 lds\n"
"v_mfma_f32_16x16x32_bf16 v[180:183],a[40:43],v[104:107],v[180:183]\n"
"ds_read_b64_tr_b16 a[84:85],v14 offset:49152\n"
"v_mfma_f32_16x16x32_bf16 v[180:183],a[44:47],v[108:111],v[180:183]\n"
"v_mfma_f32_16x16x32_bf16 v[184:187],a[48:51],v[104:107],v[184:187]\n"
"ds_read_b64_tr_b16 a[86:87],v14 offset:51200\n"
"v_mfma_f32_16x16x32_bf16 v[184:187],a[52:55],v[108:111],v[184:187]\n"
"v_mfma_f32_16x16x32_bf16 v[188:191],a[56:59],v[104:107],v[188:191]\n"
"s_add_u32 m0,s50,s56\n"
"v_mfma_f32_16x16x32_bf16 v[188:191],a[60:63],v[108:111],v[188:191]\n"
"s_waitcnt lgkmcnt(13)\n"
"buffer_load_dwordx4 v28,s[36:39],s48 offen offset:0 lds\n"
"v_mfma_f32_16x16x32_bf16 v[192:195],a[0:3],v[112:115],v[192:195]\n"
"ds_read_b128 v[96:99],v10 offset:8192\n"
"v_mfma_f32_16x16x32_bf16 v[192:195],a[4:7],v[116:119],v[192:195]\n"
"ds_read_b64_tr_b16 a[88:89],v15 offset:32768\n"
"v_mfma_f32_16x16x32_bf16 v[196:199],a[8:11],v[112:115],v[196:199]\n"
"ds_read_b128 v[100:103],v11 offset:8192\n"
"v_mfma_f32_16x16x32_bf16 v[196:199],a[12:15],v[116:119],v[196:199]\n"
"ds_read_b64_tr_b16 a[90:91],v15 offset:34816\n"
"v_mfma_f32_16x16x32_bf16 v[200:203],a[16:19],v[112:115],v[200:203]\n"
"ds_read_b64_tr_b16 a[92:93],v15 offset:49152\n"
"v_mfma_f32_16x16x32_bf16 v[200:203],a[20:23],v[116:119],v[200:203]\n"
"buffer_load_dwordx4 v29,s[36:39],s48 offen offset:1024 lds\n"
"v_mfma_f32_16x16x32_bf16 v[204:207],a[24:27],v[112:115],v[204:207]\n"
"v_mfma_f32_16x16x32_bf16 v[204:207],a[28:31],v[116:119],v[204:207]\n"
"ds_read_b64_tr_b16 a[94:95],v15 offset:51200\n"
"v_mfma_f32_16x16x32_bf16 v[208:211],a[32:35],v[112:115],v[208:211]\n"
"v_mfma_f32_16x16x32_bf16 v[208:211],a[36:39],v[116:119],v[208:211]\n"
"v_mfma_f32_16x16x32_bf16 v[212:215],a[40:43],v[112:115],v[212:215]\n"
"ds_read_b64_tr_b16 a[96:97],v16 offset:32768\n"
"v_mfma_f32_16x16x32_bf16 v[212:215],a[44:47],v[116:119],v[212:215]\n"
"buffer_load_dwordx4 v30,s[36:39],s48 offen offset:2048 lds\n"
"v_mfma_f32_16x16x32_bf16 v[216:219],a[48:51],v[112:115],v[216:219]\n"
"ds_read_b64_tr_b16 a[98:99],v16 offset:34816\n"
"v_mfma_f32_16x16x32_bf16 v[216:219],a[52:55],v[116:119],v[216:219]\n"
"v_mfma_f32_16x16x32_bf16 v[220:223],a[56:59],v[112:115],v[220:223]\n"
"v_mfma_f32_16x16x32_bf16 v[220:223],a[60:63],v[116:119],v[220:223]\n"
"s_waitcnt lgkmcnt(13)\n"
"ds_read_b64_tr_b16 a[100:101],v16 offset:49152\n"
"v_mfma_f32_16x16x32_bf16 v[224:227],a[0:3],v[120:123],v[224:227]\n"
"ds_read_b128 v[104:107],v10 offset:10240\n"
"v_mfma_f32_16x16x32_bf16 v[224:227],a[4:7],v[124:127],v[224:227]\n"
"buffer_load_dwordx4 v31,s[36:39],s48 offen offset:3072 lds\n"
"v_mfma_f32_16x16x32_bf16 v[228:231],a[8:11],v[120:123],v[228:231]\n"
"ds_read_b128 v[108:111],v11 offset:10240\n"
"v_mfma_f32_16x16x32_bf16 v[228:231],a[12:15],v[124:127],v[228:231]\n"
"ds_read_b64_tr_b16 a[102:103],v16 offset:51200\n"
"v_mfma_f32_16x16x32_bf16 v[232:235],a[16:19],v[120:123],v[232:235]\n"
"ds_read_b64_tr_b16 a[104:105],v17 offset:32768\n"
"v_mfma_f32_16x16x32_bf16 v[232:235],a[20:23],v[124:127],v[232:235]\n"
"v_mfma_f32_16x16x32_bf16 v[236:239],a[24:27],v[120:123],v[236:239]\n"
"s_add_u32 s48,s48,128\n"
"v_mfma_f32_16x16x32_bf16 v[236:239],a[28:31],v[124:127],v[236:239]\n"
"s_mov_b32 s51,s50\n"
"ds_read_b64_tr_b16 a[106:107],v17 offset:34816\n"
"v_mfma_f32_16x16x32_bf16 v[240:243],a[32:35],v[120:123],v[240:243]\n"
"s_add_u32 s50,s50,0x8000\n"
"v_mfma_f32_16x16x32_bf16 v[240:243],a[36:39],v[124:127],v[240:243]\n"
"s_cmp_eq_u32 s50,0x18000\n"
"ds_read_b64_tr_b16 a[108:109],v17 offset:49152\n"
"v_mfma_f32_16x16x32_bf16 v[244:247],a[40:43],v[120:123],v[244:247]\n"
"s_cselect_b32 s50,0,s50\n"
"v_mfma_f32_16x16x32_bf16 v[244:247],a[44:47],v[124:127],v[244:247]\n"
"v_mfma_f32_16x16x32_bf16 v[248:251],a[48:51],v[120:123],v[248:251]\n"
"ds_read_b64_tr_b16 a[110:111],v17 offset:51200\n"
"v_mfma_f32_16x16x32_bf16 v[248:251],a[52:55],v[124:127],v[248:251]\n"
"v_mfma_f32_16x16x32_bf16 v[252:255],a[56:59],v[120:123],v[252:255]\n"
"v_mfma_f32_16x16x32_bf16 v[252:255],a[60:63],v[124:127],v[252:255]\n"
"s_waitcnt lgkmcnt(13)\n"
"ds_read_b64_tr_b16 a[112:113],v18 offset:32768\n"
"v_mfma_f32_16x16x32_bf16 a[128:131],a[0:3],v[96:99],a[128:131]\n"
"ds_read_b128 v[112:115],v10 offset:12288\n"
"v_mfma_f32_16x16x32_bf16 a[128:131],a[4:7],v[100:103],a[128:131]\n"
"ds_read_b64_tr_b16 a[114:115],v18 offset:34816\n"
"v_mfma_f32_16x16x32_bf16 a[132:135],a[8:11],v[96:99],a[132:135]\n"
"ds_read_b128 v[116:119],v11 offset:12288\n"
"v_mfma_f32_16x16x32_bf16 a[132:135],a[12:15],v[100:103],a[132:135]\n"
"v_mfma_f32_16x16x32_bf16 a[136:139],a[16:19],v[96:99],a[136:139]\n"
"ds_read_b64_tr_b16 a[116:117],v18 offset:49152\n"
"v_mfma_f32_16x16x32_bf16 a[136:139],a[20:23],v[100:103],a[136:139]\n"
"v_mfma_f32_16x16x32_bf16 a[140:143],a[24:27],v[96:99],a[140:143]\n"
"ds_read_b64_tr_b16 a[118:119],v18 offset:51200\n"
"v_mfma_f32_16x16x32_bf16 a[140:143],a[28:31],v[100:103],a[140:143]\n"
"v_mfma_f32_16x16x32_bf16 a[144:147],a[32:35],v[96:99],a[144:147]\n"
"v_mfma_f32_16x16x32_bf16 a[144:147],a[36:39],v[100:103],a[144:147]\n"
"ds_read_b64_tr_b16 a[120:121],v19 offset:32768\n"
"v_mfma_f32_16x16x32_bf16 a[148:151],a[40:43],v[96:99],a[148:151]\n"
"v_mfma_f32_16x16x32_bf16 a[148:151],a[44:47],v[100:103],a[148:151]\n"
"v_mfma_f32_16x16x32_bf16 a[152:155],a[48:51],v[96:99],a[152:155]\n"
"ds_read_b64_tr_b16 a[122:123],v19 offset:34816\n"
"v_mfma_f32_16x16x32_bf16 a[152:155],a[52:55],v[100:103],a[152:155]\n"
"v_mfma_f32_16x16x32_bf16 a[156:159],a[56:59],v[96:99],a[156:159]\n"
"ds_read_b64_tr_b16 a[124:125],v19 offset:49152\n"
"v_mfma_f32_16x16x32_bf16 a[156:159],a[60:63],v[100:103],a[156:159]\n"
"s_waitcnt lgkmcnt(14)\n"
"v_mfma_f32_16x16x32_bf16 a[160:163],a[0:3],v[104:107],a[160:163]\n"
"ds_read_b128 v[120:123],v10 offset:14336\n"
"v_mfma_f32_16x16x32_bf16 a[160:163],a[4:7],v[108:111],a[160:163]\n"
"ds_read_b64_tr_b16 a[126:127],v19 offset:51200\n"
"v_mfma_f32_16x16x32_bf16 a[164:167],a[8:11],v[104:107],a[164:167]\n"
"ds_read_b128 v[124:127],v11 offset:14336\n"
"v_mfma_f32_16x16x32_bf16 a[164:167],a[12:15],v[108:111],a[164:167]\n"
"v_mfma_f32_16x16x32_bf16 a[168:171],a[16:19],v[104:107],a[168:171]\n"
"v_mfma_f32_16x16x32_bf16 a[168:171],a[20:23],v[108:111],a[168:171]\n"
"v_mfma_f32_16x16x32_bf16 a[172:175],a[24:27],v[104:107],a[172:175]\n"
"v_mfma_f32_16x16x32_bf16 a[172:175],a[28:31],v[108:111],a[172:175]\n"
"v_mfma_f32_16x16x32_bf16 a[176:179],a[32:35],v[104:107],a[176:179]\n"
"v_mfma_f32_16x16x32_bf16 a[176:179],a[36:39],v[108:111],a[176:179]\n"
"s_add_u32 s78,s78,384\n"
"v_mfma_f32_16x16x32_bf16 a[180:183],a[40:43],v[104:107],a[180:183]\n"
"s_addc_u32 s79,s79,0\n"
"v_mfma_f32_16x16x32_bf16 a[180:183],a[44:47],v[108:111],a[180:183]\n"
"v_mfma_f32_16x16x32_bf16 a[184:187],a[48:51],v[104:107],a[184:187]\n"
"v_mfma_f32_16x16x32_bf16 a[184:187],a[52:55],v[108:111],a[184:187]\n"
"v_mfma_f32_16x16x32_bf16 a[188:191],a[56:59],v[104:107],a[188:191]\n"
"s_waitcnt lgkmcnt(0)\n"
"v_mfma_f32_16x16x32_bf16 a[188:191],a[60:63],v[108:111],a[188:191]\n"
"s_waitcnt vmcnt(9)\n"
"s_barrier\n"
"v_mfma_f32_16x16x32_bf16 a[192:195],a[0:3],v[112:115],a[192:195]\n"
"s_add_u32 m0,s57,0x8000\n"
"v_add_u32 v10,s81,v8\n"
"v_add_u32 v11,s81,v9\n"
"v_mfma_f32_16x16x32_bf16 a[192:195],a[4:7],v[116:119],a[192:195]\n"
"s_mov_b32 s81,s51\n"
"buffer_load_dwordx4 v32,s[40:43],s49 offen offset:0 nt lds\n"
"v_mfma_f32_16x16x32_bf16 a[196:199],a[8:11],v[112:115],a[196:199]\n"
"ds_read_b128 v[96:99],v10 offset:0\n"
"v_mfma_f32_16x16x32_bf16 a[196:199],a[12:15],v[116:119],a[196:199]\n"
"v_mfma_f32_16x16x32_bf16 a[200:203],a[16:19],v[112:115],a[200:203]\n"
"ds_read_b128 v[100:103],v11 offset:0\n"
"v_mfma_f32_16x16x32_bf16 a[200:203],a[20:23],v[116:119],a[200:203]\n"
"buffer_load_dwordx4 v33,s[40:43],s49 offen offset:1024 nt lds\n"
"v_mfma_f32_16x16x32_bf16 a[204:207],a[24:27],v[112:115],a[204:207]\n"
"v_mfma_f32_16x16x32_bf16 a[204:207],a[28:31],v[116:119],a[204:207]\n"
"v_mfma_f32_16x16x32_bf16 a[208:211],a[32:35],v[112:115],a[208:211]\n"
"v_mfma_f32_16x16x32_bf16 a[208:211],a[36:39],v[116:119],a[208:211]\n"
"buffer_load_dwordx4 v34,s[40:43],s49 offen offset:2048 nt lds\n"
"v_mfma_f32_16x16x32_bf16 a[212:215],a[40:43],v[112:115],a[212:215]\n"
"v_mfma_f32_16x16x32_bf16 a[212:215],a[44:47],v[116:119],a[212:215]\n"
"v_mfma_f32_16x16x32_bf16 a[216:219],a[48:51],v[112:115],a[216:219]\n"
"v_mfma_f32_16x16x32_bf16 a[216:219],a[52:55],v[116:119],a[216:219]\n"
"buffer_load_dwordx4 v35,s[40:43],s49 offen offset:3072 nt lds\n"
"v_mfma_f32_16x16x32_bf16 a[220:223],a[56:59],v[112:115],a[220:223]\n"
"v_mfma_f32_16x16x32_bf16 a[220:223],a[60:63],v[116:119],a[220:223]\n"
"v_mfma_f32_16x16x32_bf16 a[224:227],a[0:3],v[120:123],a[224:227]\n"
"s_add_u32 m0,s57,0x9000\n"
"ds_read_b128 v[104:107],v10 offset:2048\n"
"v_mfma_f32_16x16x32_bf16 a[224:227],a[4:7],v[124:127],a[224:227]\n"
"buffer_load_dwordx4 v36,s[40:43],s49 offen offset:0 nt lds\n"
"v_mfma_f32_16x16x32_bf16 a[228:231],a[8:11],v[120:123],a[228:231]\n"
"ds_read_b128 v[108:111],v11 offset:2048\n"
"v_mfma_f32_16x16x32_bf16 a[228:231],a[12:15],v[124:127],a[228:231]\n"
"v_mfma_f32_16x16x32_bf16 a[232:235],a[16:19],v[120:123],a[232:235]\n"
"v_mfma_f32_16x16x32_bf16 a[232:235],a[20:23],v[124:127],a[232:235]\n"
"buffer_load_dwordx4 v37,s[40:43],s49 offen offset:1024 nt lds\n"
"v_mfma_f32_16x16x32_bf16 a[236:239],a[24:27],v[120:123],a[236:239]\n"
"v_mfma_f32_16x16x32_bf16 a[236:239],a[28:31],v[124:127],a[236:239]\n"
"v_mfma_f32_16x16x32_bf16 a[240:243],a[32:35],v[120:123],a[240:243]\n"
"v_mfma_f32_16x16x32_bf16 a[240:243],a[36:39],v[124:127],a[240:243]\n"
"buffer_load_dwordx4 v38,s[40:43],s49 offen offset:2048 nt lds\n"
"v_mfma_f32_16x16x32_bf16 a[244:247],a[40:43],v[120:123],a[244:247]\n"
"v_mfma_f32_16x16x32_bf16 a[244:247],a[44:47],v[124:127],a[244:247]\n"
"v_mfma_f32_16x16x32_bf16 a[248:251],a[48:51],v[120:123],a[248:251]\n"
"v_mfma_f32_16x16x32_bf16 a[248:251],a[52:55],v[124:127],a[248:251]\n"
"buffer_load_dwordx4 v39,s[40:43],s49 offen offset:3072 nt lds\n"
"v_mfma_f32_16x16x32_bf16 a[252:255],a[56:59],v[120:123],a[252:255]\n"
"s_add_u32 s49,s49,0x80000\n"
"v_mfma_f32_16x16x32_bf16 a[252:255],a[60:63],v[124:127],a[252:255]\n"
"s_waitcnt lgkmcnt(2)\n"
"v_mfma_f32_16x16x32_bf16 v[128:131],a[64:67],v[96:99],v[128:131]\n"
"ds_read_b128 v[112:115],v10 offset:4096\n"
"v_mfma_f32_16x16x32_bf16 v[128:131],a[68:71],v[100:103],v[128:131]\n"
"ds_read_b64_tr_b16 a[0:1],v12 offset:0\n"
"v_mfma_f32_16x16x32_bf16 v[132:135],a[72:75],v[96:99],v[132:135]\n"
"ds_read_b128 v[116:119],v11 offset:4096\n"
"v_mfma_f32_16x16x32_bf16 v[132:135],a[76:79],v[100:103],v[132:135]\n"
"ds_read_b64_tr_b16 a[2:3],v12 offset:2048\n"
"v_mfma_f32_16x16x32_bf16 v[136:139],a[80:83],v[96:99],v[136:139]\n"
"v_mfma_f32_16x16x32_bf16 v[136:139],a[84:87],v[100:103],v[136:139]\n"
"global_load_dword v86,v5,s[78:79]\n"
"v_mfma_f32_16x16x32_bf16 v[140:143],a[88:91],v[96:99],v[140:143]\n"
"s_add_u32 m0,s50,s55\n"
"ds_read_b64_tr_b16 a[4:5],v12 offset:16384\n"
"v_mfma_f32_16x16x32_bf16 v[140:143],a[92:95],v[100:103],v[140:143]\n"
"buffer_load_dwordx4 v24,s[36:39],s48 offen offset:0 lds\n"
"v_mfma_f32_16x16x32_bf16 v[144:147],a[96:99],v[96:99],v[144:147]\n"
"ds_read_b64_tr_b16 a[6:7],v12 offset:18432\n"
"v_mfma_f32_16x16x32_bf16 v[144:147],a[100:103],v[100:103],v[144:147]\n"
"v_mfma_f32_16x16x32_bf16 v[148:151],a[104:107],v[96:99],v[148:151]\n"
"ds_read_b64_tr_b16 a[8:9],v13 offset:0\n"
"v_mfma_f32_16x16x32_bf16 v[148:151],a[108:111],v[100:103],v[148:151]\n"
"v_mfma_f32_16x16x32_bf16 v[152:155],a[112:115],v[96:99],v[152:155]\n"
"v_mfma_f32_16x16x32_bf16 v[152:155],a[116:119],v[100:103],v[152:155]\n"
"buffer_load_dwordx4 v25,s[36:39],s48 offen offset:1024 lds\n"
"v_mfma_f32_16x16x32_bf16 v[156:159],a[120:123],v[96:99],v[156:159]\n"
"ds_read_b64_tr_b16 a[10:11],v13 offset:2048\n"
"v_mfma_f32_16x16x32_bf16 v[156:159],a[124:127],v[100:103],v[156:159]\n"
"s_waitcnt lgkmcnt(8)\n"
"ds_read_b64_tr_b16 a[12:13],v13 offset:16384\n"
"v_mfma_f32_16x16x32_bf16 v[160:163],a[64:67],v[104:107],v[160:163]\n"
"ds_read_b128 v[120:123],v10 offset:6144\n"
"v_mfma_f32_16x16x32_bf16 v[160:163],a[68:71],v[108:111],v[160:163]\n"
"v_mfma_f32_16x16x32_bf16 v[164:167],a[72:75],v[104:107],v[164:167]\n"
"ds_read_b128 v[124:127],v11 offset:6144\n"
"v_mfma_f32_16x16x32_bf16 v[164:167],a[76:79],v[108:111],v[164:167]\n"
"buffer_load_dwordx4 v26,s[36:39],s48 offen offset:2048 lds\n"
"v_mfma_f32_16x16x32_bf16 v[168:171],a[80:83],v[104:107],v[168:171]\n"
"ds_read_b64_tr_b16 a[14:15],v13 offset:18432\n"
"v_mfma_f32_16x16x32_bf16 v[168:171],a[84:87],v[108:111],v[168:171]\n"
"ds_read_b64_tr_b16 a[16:17],v14 offset:0\n"
"v_mfma_f32_16x16x32_bf16 v[172:175],a[88:91],v[104:107],v[172:175]\n"
"v_mfma_f32_16x16x32_bf16 v[172:175],a[92:95],v[108:111],v[172:175]\n"
"ds_read_b64_tr_b16 a[18:19],v14 offset:2048\n"
"v_mfma_f32_16x16x32_bf16 v[176:179],a[96:99],v[104:107],v[176:179]\n"
"v_mfma_f32_16x16x32_bf16 v[176:179],a[100:103],v[108:111],v[176:179]\n"
"buffer_load_dwordx4 v27,s[36:39],s48 offen offset:3072 lds\n"
"v_mfma_f32_16x16x32_bf16 v[180:183],a[104:107],v[104:107],v[180:183]\n"
"ds_read_b64_tr_b16 a[20:21],v14 offset:16384\n"
"v_mfma_f32_16x16x32_bf16 v[180:183],a[108:111],v[108:111],v[180:183]\n"
"v_mfma_f32_16x16x32_bf16 v[184:187],a[112:115],v[104:107],v[184:187]\n"
"ds_read_b64_tr_b16 a[22:23],v14 offset:18432\n"
"v_mfma_f32_16x16x32_bf16 v[184:187],a[116:119],v[108:111],v[184:187]\n"
"v_mfma_f32_16x16x32_bf16 v[188:191],a[120:123],v[104:107],v[188:191]\n"
"s_add_u32 m0,s50,s56\n"
"v_mfma_f32_16x16x32_bf16 v[188:191],a[124:127],v[108:111],v[188:191]\n"
"s_waitcnt lgkmcnt(13)\n"
"buffer_load_dwordx4 v28,s[36:39],s48 offen offset:0 lds\n"
"v_mfma_f32_16x16x32_bf16 v[192:195],a[64:67],v[112:115],v[192:195]\n"
"ds_read_b128 v[96:99],v10 offset:8192\n"
"v_mfma_f32_16x16x32_bf16 v[192:195],a[68:71],v[116:119],v[192:195]\n"
"ds_read_b64_tr_b16 a[24:25],v15 offset:0\n"
"v_mfma_f32_16x16x32_bf16 v[196:199],a[72:75],v[112:115],v[196:199]\n"
"ds_read_b128 v[100:103],v11 offset:8192\n"
"v_mfma_f32_16x16x32_bf16 v[196:199],a[76:79],v[116:119],v[196:199]\n"
"ds_read_b64_tr_b16 a[26:27],v15 offset:2048\n"
"v_mfma_f32_16x16x32_bf16 v[200:203],a[80:83],v[112:115],v[200:203]\n"
"ds_read_b64_tr_b16 a[28:29],v15 offset:16384\n"
"v_mfma_f32_16x16x32_bf16 v[200:203],a[84:87],v[116:119],v[200:203]\n"
"buffer_load_dwordx4 v29,s[36:39],s48 offen offset:1024 lds\n"
"v_mfma_f32_16x16x32_bf16 v[204:207],a[88:91],v[112:115],v[204:207]\n"
"v_mfma_f32_16x16x32_bf16 v[204:207],a[92:95],v[116:119],v[204:207]\n"
"ds_read_b64_tr_b16 a[30:31],v15 offset:18432\n"
"v_mfma_f32_16x16x32_bf16 v[208:211],a[96:99],v[112:115],v[208:211]\n"
"v_mfma_f32_16x16x32_bf16 v[208:211],a[100:103],v[116:119],v[208:211]\n"
"v_mfma_f32_16x16x32_bf16 v[212:215],a[104:107],v[112:115],v[212:215]\n"
"ds_read_b64_tr_b16 a[32:33],v16 offset:0\n"
"v_mfma_f32_16x16x32_bf16 v[212:215],a[108:111],v[116:119],v[212:215]\n"
"buffer_load_dwordx4 v30,s[36:39],s48 offen offset:2048 lds\n"
"v_mfma_f32_16x16x32_bf16 v[216:219],a[112:115],v[112:115],v[216:219]\n"
"ds_read_b64_tr_b16 a[34:35],v16 offset:2048\n"
"v_mfma_f32_16x16x32_bf16 v[216:219],a[116:119],v[116:119],v[216:219]\n"
"v_mfma_f32_16x16x32_bf16 v[220:223],a[120:123],v[112:115],v[220:223]\n"
"v_mfma_f32_16x16x32_bf16 v[220:223],a[124:127],v[116:119],v[220:223]\n"
"s_waitcnt lgkmcnt(13)\n"
"ds_read_b64_tr_b16 a[36:37],v16 offset:16384\n"
"v_mfma_f32_16x16x32_bf16 v[224:227],a[64:67],v[120:123],v[224:227]\n"
"ds_read_b128 v[104:107],v10 offset:10240\n"
"v_mfma_f32_16x16x32_bf16 v[224:227],a[68:71],v[124:127],v[224:227]\n"
"buffer_load_dwordx4 v31,s[36:39],s48 offen offset:3072 lds\n"
"v_mfma_f32_16x16x32_bf16 v[228:231],a[72:75],v[120:123],v[228:231]\n"
"ds_read_b128 v[108:111],v11 offset:10240\n"
"v_mfma_f32_16x16x32_bf16 v[228:231],a[76:79],v[124:127],v[228:231]\n"
"ds_read_b64_tr_b16 a[38:39],v16 offset:18432\n"
"v_mfma_f32_16x16x32_bf16 v[232:235],a[80:83],v[120:123],v[232:235]\n"
"ds_read_b64_tr_b16 a[40:41],v17 offset:0\n"
"v_mfma_f32_16x16x32_bf16 v[232:235],a[84:87],v[124:127],v[232:235]\n"
"v_mfma_f32_16x16x32_bf16 v[236:239],a[88:91],v[120:123],v[236:239]\n"
"s_add_u32 s48,s48,128\n"
"v_mfma_f32_16x16x32_bf16 v[236:239],a[92:95],v[124:127],v[236:239]\n"
"s_mov_b32 s51,s50\n"
"ds_read_b64_tr_b16 a[42:43],v17 offset:2048\n"
"v_mfma_f32_16x16x32_bf16 v[240:243],a[96:99],v[120:123],v[240:243]\n"
"s_add_u32 s50,s50,0x8000\n"
"v_mfma_f32_16x16x32_bf16 v[240:243],a[100:103],v[124:127],v[240:243]\n"
"s_cmp_eq_u32 s50,0x18000\n"
"ds_read_b64_tr_b16 a[44:45],v17 offset:16384\n"
"v_mfma_f32_16x16x32_bf16 v[244:247],a[104:107],v[120:123],v[244:247]\n"
"s_cselect_b32 s50,0,s50\n"
"v_mfma_f32_16x16x32_bf16 v[244:247],a[108:111],v[124:127],v[244:247]\n"
"v_mfma_f32_16x16x32_bf16 v[248:251],a[112:115],v[120:123],v[248:251]\n"
"ds_read_b64_tr_b16 a[46:47],v17 offset:18432\n"
"v_mfma_f32_16x16x32_bf16 v[248:251],a[116:119],v[124:127],v[248:251]\n"
"v_mfma_f32_16x16x32_bf16 v[252:255],a[120:123],v[120:123],v[252:255]\n"
"v_mfma_f32_16x16x32_bf16 v[252:255],a[124:127],v[124:127],v[252:255]\n"
"s_waitcnt lgkmcnt(13)\n"
"ds_read_b64_tr_b16 a[48:49],v18 offset:0\n"
"v_mfma_f32_16x16x32_bf16 a[128:131],a[64:67],v[96:99],a[128:131]\n"
"ds_read_b128 v[112:115],v10 offset:12288\n"
"v_mfma_f32_16x16x32_bf16 a[128:131],a[68:71],v[100:103],a[128:131]\n"
"ds_read_b64_tr_b16 a[50:51],v18 offset:2048\n"
"v_mfma_f32_16x16x32_bf16 a[132:135],a[72:75],v[96:99],a[132:135]\n"
"ds_read_b128 v[116:119],v11 offset:12288\n"
"v_mfma_f32_16x16x32_bf16 a[132:135],a[76:79],v[100:103],a[132:135]\n"
"v_mfma_f32_16x16x32_bf16 a[136:139],a[80:83],v[96:99],a[136:139]\n"
"ds_read_b64_tr_b16 a[52:53],v18 offset:16384\n"
"v_mfma_f32_16x16x32_bf16 a[136:139],a[84:87],v[100:103],a[136:139]\n"
"v_mfma_f32_16x16x32_bf16 a[140:143],a[88:91],v[96:99],a[140:143]\n"
"ds_read_b64_tr_b16 a[54:55],v18 offset:18432\n"
"v_mfma_f32_16x16x32_bf16 a[140:143],a[92:95],v[100:103],a[140:143]\n"
"v_mfma_f32_16x16x32_bf16 a[144:147],a[96:99],v[96:99],a[144:147]\n"
"v_mfma_f32_16x16x32_bf16 a[144:147],a[100:103],v[100:103],a[144:147]\n"
"ds_read_b64_tr_b16 a[56:57],v19 offset:0\n"
"v_mfma_f32_16x16x32_bf16 a[148:151],a[104:107],v[96:99],a[148:151]\n"
"v_mfma_f32_16x16x32_bf16 a[148:151],a[108:111],v[100:103],a[148:151]\n"
"v_mfma_f32_16x16x32_bf16 a[152:155],a[112:115],v[96:99],a[152:155]\n"
"ds_read_b64_tr_b16 a[58:59],v19 offset:2048\n"
"v_mfma_f32_16x16x32_bf16 a[152:155],a[116:119],v[100:103],a[152:155]\n"
"v_mfma_f32_16x16x32_bf16 a[156:159],a[120:123],v[96:99],a[156:159]\n"
"ds_read_b64_tr_b16 a[60:61],v19 offset:16384\n"
"v_mfma_f32_16x16x32_bf16 a[156:159],a[124:127],v[100:103],a[156:159]\n"
"s_waitcnt lgkmcnt(14)\n"
"v_mfma_f32_16x16x32_bf16 a[160:163],a[64:67],v[104:107],a[160:163]\n"
"ds_read_b128 v[120:123],v10 offset:14336\n"
"v_mfma_f32_16x16x32_bf16 a[160:163],a[68:71],v[108:111],a[160:163]\n"
"ds_read_b64_tr_b16 a[62:63],v19 offset:18432\n"
"v_mfma_f32_16x16x32_bf16 a[164:167],a[72:75],v[104:107],a[164:167]\n"
"ds_read_b128 v[124:127],v11 offset:14336\n"
"v_mfma_f32_16x16x32_bf16 a[164:167],a[76:79],v[108:111],a[164:167]\n"
"v_mfma_f32_16x16x32_bf16 a[168:171],a[80:83],v[104:107],a[168:171]\n"
"v_mfma_f32_16x16x32_bf16 a[168:171],a[84:87],v[108:111],a[168:171]\n"
"v_mfma_f32_16x16x32_bf16 a[172:175],a[88:91],v[104:107],a[172:175]\n"
"v_mfma_f32_16x16x32_bf16 a[172:175],a[92:95],v[108:111],a[172:175]\n"
"v_mfma_f32_16x16x32_bf16 a[176:179],a[96:99],v[104:107],a[176:179]\n"
"v_mfma_f32_16x16x32_bf16 a[176:179],a[100:103],v[108:111],a[176:179]\n"
"s_add_u32 s78,s78,384\n"
"v_mfma_f32_16x16x32_bf16 a[180:183],a[104:107],v[104:107],a[180:183]\n"
"s_addc_u32 s79,s79,0\n"
"v_mfma_f32_16x16x32_bf16 a[180:183],a[108:111],v[108:111],a[180:183]\n"
"v_mfma_f32_16x16x32_bf16 a[184:187],a[112:115],v[104:107],a[184:187]\n"
"v_mfma_f32_16x16x32_bf16 a[184:187],a[116:119],v[108:111],a[184:187]\n"
"v_mfma_f32_16x16x32_bf16 a[188:191],a[120:123],v[104:107],a[188:191]\n"
"s_waitcnt lgkmcnt(0)\n"
"v_mfma_f32_16x16x32_bf16 a[188:191],a[124:127],v[108:111],a[188:191]\n"
"s_waitcnt vmcnt(9)\n"
"s_barrier\n"
"v_mfma_f32_16x16x32_bf16 a[192:195],a[64:67],v[112:115],a[192:195]\n"
"s_add_u32 m0,s57,0x0\n"
"v_add_u32 v10,s81,v8\n"
"v_add_u32 v11,s81,v9\n"
"v_mfma_f32_16x16x32_bf16 a[192:195],a[68:71],v[116:119],a[192:195]\n"
"s_mov_b32 s81,s51\n"
"buffer_load_dwordx4 v32,s[40:43],s49 offen offset:0 nt lds\n"
"v_mfma_f32_16x16x32_bf16 a[196:199],a[72:75],v[112:115],a[196:199]\n"
"s_cmp_eq_u32 s52,5\n"
"ds_read_b128 v[96:99],v10 offset:0\n"
"v_mfma_f32_16x16x32_bf16 a[196:199],a[76:79],v[116:119],a[196:199]\n"
"v_mfma_f32_16x16x32_bf16 a[200:203],a[80:83],v[112:115],a[200:203]\n"
"s_cbranch_scc1 L_oncek_%=\n"
"L_oncek_ret_%=:\n"
"ds_read_b128 v[100:103],v11 offset:0\n"
"v_mfma_f32_16x16x32_bf16 a[200:203],a[84:87],v[116:119],a[200:203]\n"
"buffer_load_dwordx4 v33,s[40:43],s49 offen offset:1024 nt lds\n"
"v_mfma_f32_16x16x32_bf16 a[204:207],a[88:91],v[112:115],a[204:207]\n"
"v_mfma_f32_16x16x32_bf16 a[204:207],a[92:95],v[116:119],a[204:207]\n"
"v_mfma_f32_16x16x32_bf16 a[208:211],a[96:99],v[112:115],a[208:211]\n"
"v_mfma_f32_16x16x32_bf16 a[208:211],a[100:103],v[116:119],a[208:211]\n"
"buffer_load_dwordx4 v34,s[40:43],s49 offen offset:2048 nt lds\n"
"v_mfma_f32_16x16x32_bf16 a[212:215],a[104:107],v[112:115],a[212:215]\n"
"v_mfma_f32_16x16x32_bf16 a[212:215],a[108:111],v[116:119],a[212:215]\n"
"v_mfma_f32_16x16x32_bf16 a[216:219],a[112:115],v[112:115],a[216:219]\n"
"v_mfma_f32_16x16x32_bf16 a[216:219],a[116:119],v[116:119],a[216:219]\n"
"buffer_load_dwordx4 v35,s[40:43],s49 offen offset:3072 nt lds\n"
"v_mfma_f32_16x16x32_bf16 a[220:223],a[120:123],v[112:115],a[220:223]\n"
"v_mfma_f32_16x16x32_bf16 a[220:223],a[124:127],v[116:119],a[220:223]\n"
"v_mfma_f32_16x16x32_bf16 a[224:227],a[64:67],v[120:123],a[224:227]\n"
"s_add_u32 m0,s57,0x1000\n"
"ds_read_b128 v[104:107],v10 offset:2048\n"
"v_mfma_f32_16x16x32_bf16 a[224:227],a[68:71],v[124:127],a[224:227]\n"
"buffer_load_dwordx4 v36,s[40:43],s49 offen offset:0 nt lds\n"
"v_mfma_f32_16x16x32_bf16 a[228:231],a[72:75],v[120:123],a[228:231]\n"
"ds_read_b128 v[108:111],v11 offset:2048\n"
"v_mfma_f32_16x16x32_bf16 a[228:231],a[76:79],v[124:127],a[228:231]\n"
"v_mfma_f32_16x16x32_bf16 a[232:235],a[80:83],v[120:123],a[232:235]\n"
"v_mfma_f32_16x16x32_bf16 a[232:235],a[84:87],v[124:127],a[232:235]\n"
"buffer_load_dwordx4 v37,s[40:43],s49 offen offset:1024 nt lds\n"
"v_mfma_f32_16x16x32_bf16 a[236:239],a[88:91],v[120:123],a[236:239]\n"
"v_mfma_f32_16x16x32_bf16 a[236:239],a[92:95],v[124:127],a[236:239]\n"
"v_mfma_f32_16x16x32_bf16 a[240:243],a[96:99],v[120:123],a[240:243]\n"
"v_mfma_f32_16x16x32_bf16 a[240:243],a[100:103],v[124:127],a[240:243]\n"
"buffer_load_dwordx4 v38,s[40:43],s49 offen offset:2048 nt lds\n"
"v_mfma_f32_16x16x32_bf16 a[244:247],a[104:107],v[120:123],a[244:247]\n"
"s_sub_u32 s52,s52,1\n"
"v_mfma_f32_16x16x32_bf16 a[244:247],a[108:111],v[124:127],a[244:247]\n"
"v_mfma_f32_16x16x32_bf16 a[248:251],a[112:115],v[120:123],a[248:251]\n"
"v_mfma_f32_16x16x32_bf16 a[248:251],a[116:119],v[124:127],a[248:251]\n"
"buffer_load_dwordx4 v39,s[40:43],s49 offen offset:3072 nt lds\n"
"v_mfma_f32_16x16x32_bf16 a[252:255],a[120:123],v[120:123],a[252:255]\n"
"s_add_u32 s49,s49,0x80000\n"
"s_cmp_lg_u32 s52,0\n"
"v_mfma_f32_16x16x32_bf16 a[252:255],a[124:127],v[124:127],a[252:255]\n"
"s_waitcnt lgkmcnt(2)\n"
"s_cbranch_scc1 L_kvk_%=\n"
"v_mfma_f32_16x16x32_bf16 v[128:131],a[0:3],v[96:99],v[128:131]\n"
"ds_read_b128 v[112:115],v10 offset:4096\n"
"v_mfma_f32_16x16x32_bf16 v[128:131],a[4:7],v[100:103],v[128:131]\n"
"s_lshl_b32 s76,s54,7\n"
"ds_read_b64_tr_b16 a[64:65],v12 offset:32768\n"
"v_mfma_f32_16x16x32_bf16 v[132:135],a[8:11],v[96:99],v[132:135]\n"
"s_add_u32 s76,s76,0x1000\n"
"ds_read_b128 v[116:119],v11 offset:4096\n"
"v_mfma_f32_16x16x32_bf16 v[132:135],a[12:15],v[100:103],v[132:135]\n"
"s_add_u32 s82,s76,s32\n"
"s_addc_u32 s83,s33,0\n"
"ds_read_b64_tr_b16 a[66:67],v12 offset:34816\n"
"v_mfma_f32_16x16x32_bf16 v[136:139],a[16:19],v[96:99],v[136:139]\n"
"v_mfma_f32_16x16x32_bf16 v[136:139],a[20:23],v[100:103],v[136:139]\n"
"ds_read_b64_tr_b16 a[68:69],v12 offset:49152\n"
"global_load_dwordx2 v[54:55],v53,s[82:83] sc1\n"
"v_mfma_f32_16x16x32_bf16 v[140:143],a[24:27],v[96:99],v[140:143]\n"
"s_add_u32 m0,s50,s55\n"
"v_mfma_f32_16x16x32_bf16 v[140:143],a[28:31],v[100:103],v[140:143]\n"
"buffer_load_dwordx4 v24,s[36:39],s48 offen offset:0 lds\n"
"v_mfma_f32_16x16x32_bf16 v[144:147],a[32:35],v[96:99],v[144:147]\n"
"ds_read_b64_tr_b16 a[70:71],v12 offset:51200\n"
"v_mfma_f32_16x16x32_bf16 v[144:147],a[36:39],v[100:103],v[144:147]\n"
"v_mfma_f32_16x16x32_bf16 v[148:151],a[40:43],v[96:99],v[148:151]\n"
"ds_read_b64_tr_b16 a[72:73],v13 offset:32768\n"
"v_mfma_f32_16x16x32_bf16 v[148:151],a[44:47],v[100:103],v[148:151]\n"
"v_mfma_f32_16x16x32_bf16 v[152:155],a[48:51],v[96:99],v[152:155]\n"
"v_mfma_f32_16x16x32_bf16 v[152:155],a[52:55],v[100:103],v[152:155]\n"
"buffer_load_dwordx4 v25,s[36:39],s48 offen offset:1024 lds\n"
"v_mfma_f32_16x16x32_bf16 v[156:159],a[56:59],v[96:99],v[156:159]\n"
"ds_read_b64_tr_b16 a[74:75],v13 offset:34816\n"
"v_mfma_f32_16x16x32_bf16 v[156:159],a[60:63],v[100:103],v[156:159]\n"
"s_waitcnt lgkmcnt(8)\n"
"ds_read_b64_tr_b16 a[76:77],v13 offset:49152\n"
"v_mfma_f32_16x16x32_bf16 v[160:163],a[0:3],v[104:107],v[160:163]\n"
"ds_read_b128 v[120:123],v10 offset:6144\n"
"v_mfma_f32_16x16x32_bf16 v[160:163],a[4:7],v[108:111],v[160:163]\n"
"v_mfma_f32_16x16x32_bf16 v[164:167],a[8:11],v[104:107],v[164:167]\n"
"ds_read_b128 v[124:127],v11 offset:6144\n"
"v_mfma_f32_16x16x32_bf16 v[164:167],a[12:15],v[108:111],v[164:167]\n"
"buffer_load_dwordx4 v26,s[36:39],s48 offen offset:2048 lds\n"
"v_mfma_f32_16x16x32_bf16 v[168:171],a[16:19],v[104:107],v[168:171]\n"
"ds_read_b64_tr_b16 a[78:79],v13 offset:51200\n"
"v_mfma_f32_16x16x32_bf16 v[168:171],a[20:23],v[108:111],v[168:171]\n"
"ds_read_b64_tr_b16 a[80:81],v14 offset:32768\n"
"v_mfma_f32_16x16x32_bf16 v[172:175],a[24:27],v[104:107],v[172:175]\n"
"v_mfma_f32_16x16x32_bf16 v[172:175],a[28:31],v[108:111],v[172:175]\n"
"ds_read_b64_tr_b16 a[82:83],v14 offset:34816\n"
"v_mfma_f32_16x16x32_bf16 v[176:179],a[32:35],v[104:107],v[176:179]\n"
"v_mfma_f32_16x16x32_bf16 v[176:179],a[36:39],v[108:111],v[176:179]\n"
"buffer_load_dwordx4 v27,s[36:39],s48 offen offset:3072 lds\n"
"v_mfma_f32_16x16x32_bf16 v[180:183],a[40:43],v[104:107],v[180:183]\n"
"ds_read_b64_tr_b16 a[84:85],v14 offset:49152\n"
"v_mfma_f32_16x16x32_bf16 v[180:183],a[44:47],v[108:111],v[180:183]\n"
"v_mfma_f32_16x16x32_bf16 v[184:187],a[48:51],v[104:107],v[184:187]\n"
"ds_read_b64_tr_b16 a[86:87],v14 offset:51200\n"
"v_mfma_f32_16x16x32_bf16 v[184:187],a[52:55],v[108:111],v[184:187]\n"
"v_mfma_f32_16x16x32_bf16 v[188:191],a[56:59],v[104:107],v[188:191]\n"
"s_add_u32 m0,s50,s56\n"
"v_mfma_f32_16x16x32_bf16 v[188:191],a[60:63],v[108:111],v[188:191]\n"
"s_waitcnt lgkmcnt(13)\n"
"buffer_load_dwordx4 v28,s[36:39],s48 offen offset:0 lds\n"
"v_mfma_f32_16x16x32_bf16 v[192:195],a[0:3],v[112:115],v[192:195]\n"
"ds_read_b128 v[96:99],v10 offset:8192\n"
"v_mfma_f32_16x16x32_bf16 v[192:195],a[4:7],v[116:119],v[192:195]\n"
"ds_read_b64_tr_b16 a[88:89],v15 offset:32768\n"
"v_mfma_f32_16x16x32_bf16 v[196:199],a[8:11],v[112:115],v[196:199]\n"
"ds_read_b128 v[100:103],v11 offset:8192\n"
"v_mfma_f32_16x16x32_bf16 v[196:199],a[12:15],v[116:119],v[196:199]\n"
"ds_read_b64_tr_b16 a[90:91],v15 offset:34816\n"
"v_mfma_f32_16x16x32_bf16 v[200:203],a[16:19],v[112:115],v[200:203]\n"
"ds_read_b64_tr_b16 a[92:93],v15 offset:49152\n"
"v_mfma_f32_16x16x32_bf16 v[200:203],a[20:23],v[116:119],v[200:203]\n"
"buffer_load_dwordx4 v29,s[36:39],s48 offen offset:1024 lds\n"
"v_mfma_f32_16x16x32_bf16 v[204:207],a[24:27],v[112:115],v[204:207]\n"
"v_mfma_f32_16x16x32_bf16 v[204:207],a[28:31],v[116:119],v[204:207]\n"
"ds_read_b64_tr_b16 a[94:95],v15 offset:51200\n"
"v_mfma_f32_16x16x32_bf16 v[208:211],a[32:35],v[112:115],v[208:211]\n"
"v_mfma_f32_16x16x32_bf16 v[208:211],a[36:39],v[116:119],v[208:211]\n"
"v_mfma_f32_16x16x32_bf16 v[212:215],a[40:43],v[112:115],v[212:215]\n"
"ds_read_b64_tr_b16 a[96:97],v16 offset:32768\n"
"v_mfma_f32_16x16x32_bf16 v[212:215],a[44:47],v[116:119],v[212:215]\n"
"buffer_load_dwordx4 v30,s[36:39],s48 offen offset:2048 lds\n"
"v_mfma_f32_16x16x32_bf16 v[216:219],a[48:51],v[112:115],v[216:219]\n"
"ds_read_b64_tr_b16 a[98:99],v16 offset:34816\n"
"v_mfma_f32_16x16x32_bf16 v[216:219],a[52:55],v[116:119],v[216:219]\n"
"v_mfma_f32_16x16x32_bf16 v[220:223],a[56:59],v[112:115],v[220:223]\n"
"v_mfma_f32_16x16x32_bf16 v[220:223],a[60:63],v[116:119],v[220:223]\n"
"s_waitcnt lgkmcnt(13)\n"
"ds_read_b64_tr_b16 a[100:101],v16 offset:49152\n"
"v_mfma_f32_16x16x32_bf16 v[224:227],a[0:3],v[120:123],v[224:227]\n"
"ds_read_b128 v[104:107],v10 offset:10240\n"
"v_mfma_f32_16x16x32_bf16 v[224:227],a[4:7],v[124:127],v[224:227]\n"
"buffer_load_dwordx4 v31,s[36:39],s48 offen offset:3072 lds\n"
"v_mfma_f32_16x16x32_bf16 v[228:231],a[8:11],v[120:123],v[228:231]\n"
"ds_read_b128 v[108:111],v11 offset:10240\n"
"v_mfma_f32_16x16x32_bf16 v[228:231],a[12:15],v[124:127],v[228:231]\n"
"ds_read_b64_tr_b16 a[102:103],v16 offset:51200\n"
"v_mfma_f32_16x16x32_bf16 v[232:235],a[16:19],v[120:123],v[232:235]\n"
"ds_read_b64_tr_b16 a[104:105],v17 offset:32768\n"
"v_mfma_f32_16x16x32_bf16 v[232:235],a[20:23],v[124:127],v[232:235]\n"
"v_mfma_f32_16x16x32_bf16 v[236:239],a[24:27],v[120:123],v[236:239]\n"
"s_add_u32 s48,s48,128\n"
"v_mfma_f32_16x16x32_bf16 v[236:239],a[28:31],v[124:127],v[236:239]\n"
"s_mov_b32 s51,s50\n"
"ds_read_b64_tr_b16 a[106:107],v17 offset:34816\n"
"v_mfma_f32_16x16x32_bf16 v[240:243],a[32:35],v[120:123],v[240:243]\n"
"s_add_u32 s50,s50,0x8000\n"
"v_mfma_f32_16x16x32_bf16 v[240:243],a[36:39],v[124:127],v[240:243]\n"
"s_cmp_eq_u32 s50,0x18000\n"
"ds_read_b64_tr_b16 a[108:109],v17 offset:49152\n"
"v_mfma_f32_16x16x32_bf16 v[244:247],a[40:43],v[120:123],v[244:247]\n"
"s_cselect_b32 s50,0,s50\n"
"v_mfma_f32_16x16x32_bf16 v[244:247],a[44:47],v[124:127],v[244:247]\n"
"v_mfma_f32_16x16x32_bf16 v[248:251],a[48:51],v[120:123],v[248:251]\n"
"ds_read_b64_tr_b16 a[110:111],v17 offset:51200\n"
"v_mfma_f32_16x16x32_bf16 v[248:251],a[52:55],v[124:127],v[248:251]\n"
"v_mfma_f32_16x16x32_bf16 v[252:255],a[56:59],v[120:123],v[252:255]\n"
"v_mfma_f32_16x16x32_bf16 v[252:255],a[60:63],v[124:127],v[252:255]\n"
"s_waitcnt lgkmcnt(13)\n"
"ds_read_b64_tr_b16 a[112:113],v18 offset:32768\n"
"v_mfma_f32_16x16x32_bf16 a[128:131],a[0:3],v[96:99],a[128:131]\n"
"ds_read_b128 v[112:115],v10 offset:12288\n"
"v_mfma_f32_16x16x32_bf16 a[128:131],a[4:7],v[100:103],a[128:131]\n"
"ds_read_b64_tr_b16 a[114:115],v18 offset:34816\n"
"v_mfma_f32_16x16x32_bf16 a[132:135],a[8:11],v[96:99],a[132:135]\n"
"ds_read_b128 v[116:119],v11 offset:12288\n"
"v_mfma_f32_16x16x32_bf16 a[132:135],a[12:15],v[100:103],a[132:135]\n"
"v_mfma_f32_16x16x32_bf16 a[136:139],a[16:19],v[96:99],a[136:139]\n"
"ds_read_b64_tr_b16 a[116:117],v18 offset:49152\n"
"v_mfma_f32_16x16x32_bf16 a[136:139],a[20:23],v[100:103],a[136:139]\n"
"v_mfma_f32_16x16x32_bf16 a[140:143],a[24:27],v[96:99],a[140:143]\n"
"ds_read_b64_tr_b16 a[118:119],v18 offset:51200\n"
"v_mfma_f32_16x16x32_bf16 a[140:143],a[28:31],v[100:103],a[140:143]\n"
"v_mfma_f32_16x16x32_bf16 a[144:147],a[32:35],v[96:99],a[144:147]\n"
"v_mfma_f32_16x16x32_bf16 a[144:147],a[36:39],v[100:103],a[144:147]\n"
"ds_read_b64_tr_b16 a[120:121],v19 offset:32768\n"
"v_mfma_f32_16x16x32_bf16 a[148:151],a[40:43],v[96:99],a[148:151]\n"
"v_mfma_f32_16x16x32_bf16 a[148:151],a[44:47],v[100:103],a[148:151]\n"
"v_mfma_f32_16x16x32_bf16 a[152:155],a[48:51],v[96:99],a[152:155]\n"
"ds_read_b64_tr_b16 a[122:123],v19 offset:34816\n"
"v_mfma_f32_16x16x32_bf16 a[152:155],a[52:55],v[100:103],a[152:155]\n"
"v_mfma_f32_16x16x32_bf16 a[156:159],a[56:59],v[96:99],a[156:159]\n"
"ds_read_b64_tr_b16 a[124:125],v19 offset:49152\n"
"v_mfma_f32_16x16x32_bf16 a[156:159],a[60:63],v[100:103],a[156:159]\n"
"s_waitcnt lgkmcnt(14)\n"
"v_mfma_f32_16x16x32_bf16 a[160:163],a[0:3],v[104:107],a[160:163]\n"
"ds_read_b128 v[120:123],v10 offset:14336\n"
"v_mfma_f32_16x16x32_bf16 a[160:163],a[4:7],v[108:111],a[160:163]\n"
"ds_read_b64_tr_b16 a[126:127],v19 offset:51200\n"
"v_mfma_f32_16x16x32_bf16 a[164:167],a[8:11],v[104:107],a[164:167]\n"
"ds_read_b128 v[124:127],v11 offset:14336\n"
"v_mfma_f32_16x16x32_bf16 a[164:167],a[12:15],v[108:111],a[164:167]\n"
"v_mfma_f32_16x16x32_bf16 a[168:171],a[16:19],v[104:107],a[168:171]\n"
"v_mfma_f32_16x16x32_bf16 a[168:171],a[20:23],v[108:111],a[168:171]\n"
"v_mfma_f32_16x16x32_bf16 a[172:175],a[24:27],v[104:107],a[172:175]\n"
"v_mfma_f32_16x16x32_bf16 a[172:175],a[28:31],v[108:111],a[172:175]\n"
"v_mfma_f32_16x16x32_bf16 a[176:179],a[32:35],v[104:107],a[176:179]\n"
"v_mfma_f32_16x16x32_bf16 a[176:179],a[36:39],v[108:111],a[176:179]\n"
"v_mfma_f32_16x16x32_bf16 a[180:183],a[40:43],v[104:107],a[180:183]\n"
"v_mfma_f32_16x16x32_bf16 a[180:183],a[44:47],v[108:111],a[180:183]\n"
"v_mfma_f32_16x16x32_bf16 a[184:187],a[48:51],v[104:107],a[184:187]\n"
"v_mfma_f32_16x16x32_bf16 a[184:187],a[52:55],v[108:111],a[184:187]\n"
"v_mfma_f32_16x16x32_bf16 a[188:191],a[56:59],v[104:107],a[188:191]\n"
"s_waitcnt lgkmcnt(0)\n"
"v_mfma_f32_16x16x32_bf16 a[188:191],a[60:63],v[108:111],a[188:191]\n"
"s_waitcnt vmcnt(9)\n"
"s_barrier\n"
"v_mfma_f32_16x16x32_bf16 a[192:195],a[0:3],v[112:115],a[192:195]\n"
"s_add_u32 m0,s57,0x8000\n"
"v_add_u32 v10,s81,v8\n"
"v_add_u32 v11,s81,v9\n"
"v_mfma_f32_16x16x32_bf16 a[192:195],a[4:7],v[116:119],a[192:195]\n"
"s_mov_b32 s81,s51\n"
"buffer_load_dwordx4 v32,s[40:43],s49 offen offset:0 nt lds\n"
"v_mfma_f32_16x16x32_bf16 a[196:199],a[8:11],v[112:115],a[196:199]\n"
"ds_read_b128 v[96:99],v10 offset:0\n"
"v_mfma_f32_16x16x32_bf16 a[196:199],a[12:15],v[116:119],a[196:199]\n"
"v_mfma_f32_16x16x32_bf16 a[200:203],a[16:19],v[112:115],a[200:203]\n"
"ds_read_b128 v[100:103],v11 offset:0\n"
"v_mfma_f32_16x16x32_bf16 a[200:203],a[20:23],v[116:119],a[200:203]\n"
"buffer_load_dwordx4 v33,s[40:43],s49 offen offset:1024 nt lds\n"
"v_mfma_f32_16x16x32_bf16 a[204:207],a[24:27],v[112:115],a[204:207]\n"
"v_mfma_f32_16x16x32_bf16 a[204:207],a[28:31],v[116:119],a[204:207]\n"
"v_mfma_f32_16x16x32_bf16 a[208:211],a[32:35],v[112:115],a[208:211]\n"
"v_mfma_f32_16x16x32_bf16 a[208:211],a[36:39],v[116:119],a[208:211]\n"
"buffer_load_dwordx4 v34,s[40:43],s49 offen offset:2048 nt lds\n"
"v_mfma_f32_16x16x32_bf16 a[212:215],a[40:43],v[112:115],a[212:215]\n"
"v_mfma_f32_16x16x32_bf16 a[212:215],a[44:47],v[116:119],a[212:215]\n"
"v_mfma_f32_16x16x32_bf16 a[216:219],a[48:51],v[112:115],a[216:219]\n"
"v_mfma_f32_16x16x32_bf16 a[216:219],a[52:55],v[116:119],a[216:219]\n"
"buffer_load_dwordx4 v35,s[40:43],s49 offen offset:3072 nt lds\n"
"v_mfma_f32_16x16x32_bf16 a[220:223],a[56:59],v[112:115],a[220:223]\n"
"v_mfma_f32_16x16x32_bf16 a[220:223],a[60:63],v[116:119],a[220:223]\n"
"v_mfma_f32_16x16x32_bf16 a[224:227],a[0:3],v[120:123],a[224:227]\n"
"s_add_u32 m0,s57,0x9000\n"
"ds_read_b128 v[104:107],v10 offset:2048\n"
"v_mfma_f32_16x16x32_bf16 a[224:227],a[4:7],v[124:127],a[224:227]\n"
"buffer_load_dwordx4 v36,s[40:43],s49 offen offset:0 nt lds\n"
"v_mfma_f32_16x16x32_bf16 a[228:231],a[8:11],v[120:123],a[228:231]\n"
"ds_read_b128 v[108:111],v11 offset:2048\n"
"v_mfma_f32_16x16x32_bf16 a[228:231],a[12:15],v[124:127],a[228:231]\n"
"v_mfma_f32_16x16x32_bf16 a[232:235],a[16:19],v[120:123],a[232:235]\n"
"v_mfma_f32_16x16x32_bf16 a[232:235],a[20:23],v[124:127],a[232:235]\n"
"buffer_load_dwordx4 v37,s[40:43],s49 offen offset:1024 nt lds\n"
"v_mfma_f32_16x16x32_bf16 a[236:239],a[24:27],v[120:123],a[236:239]\n"
"v_mfma_f32_16x16x32_bf16 a[236:239],a[28:31],v[124:127],a[236:239]\n"
"v_mfma_f32_16x16x32_bf16 a[240:243],a[32:35],v[120:123],a[240:243]\n"
"v_mfma_f32_16x16x32_bf16 a[240:243],a[36:39],v[124:127],a[240:243]\n"
"buffer_load_dwordx4 v38,s[40:43],s49 offen offset:2048 nt lds\n"
"v_mfma_f32_16x16x32_bf16 a[244:247],a[40:43],v[120:123],a[244:247]\n"
"v_mfma_f32_16x16x32_bf16 a[244:247],a[44:47],v[124:127],a[244:247]\n"
"v_mfma_f32_16x16x32_bf16 a[248:251],a[48:51],v[120:123],a[248:251]\n"
"v_mfma_f32_16x16x32_bf16 a[248:251],a[52:55],v[124:127],a[248:251]\n"
"buffer_load_dwordx4 v39,s[40:43],s49 offen offset:3072 nt lds\n"
"v_mfma_f32_16x16x32_bf16 a[252:255],a[56:59],v[120:123],a[252:255]\n"
"s_add_u32 s49,s49,0x80000\n"
"v_mfma_f32_16x16x32_bf16 a[252:255],a[60:63],v[124:127],a[252:255]\n"
"s_waitcnt lgkmcnt(2)\n"
"v_mfma_f32_16x16x32_bf16 v[128:131],a[64:67],v[96:99],v[128:131]\n"
"s_add_u32 m0,s50,s55\n"
"ds_read_b128 v[112:115],v10 offset:4096\n"
"v_mfma_f32_16x16x32_bf16 v[128:131],a[68:71],v[100:103],v[128:131]\n"
"s_waitcnt vmcnt(16)\n"
"ds_read_b64_tr_b16 a[0:1],v12 offset:0\n"
"buffer_load_dwordx4 v24,s[36:39],s48 offen offset:0 lds\n"
"v_mfma_f32_16x16x32_bf16 v[132:135],a[72:75],v[96:99],v[132:135]\n"
"ds_read_b128 v[116:119],v11 offset:4096\n"
"v_cmp_ne_u64 vcc,s[34:35],v[54:55]\n"
"v_mfma_f32_16x16x32_bf16 v[132:135],a[76:79],v[100:103],v[132:135]\n"
"ds_read_b64_tr_b16 a[2:3],v12 offset:2048\n"
"v_mfma_f32_16x16x32_bf16 v[136:139],a[80:83],v[96:99],v[136:139]\n"
"s_cbranch_vccnz L_lnsk_%=\n"
"L_lnsk_ret_%=:\n"
"v_mfma_f32_16x16x32_bf16 v[136:139],a[84:87],v[100:103],v[136:139]\n"
"ds_read_b64_tr_b16 a[4:5],v12 offset:16384\n"
"buffer_load_dwordx4 v25,s[36:39],s48 offen offset:1024 lds\n"
"v_mfma_f32_16x16x32_bf16 v[140:143],a[88:91],v[96:99],v[140:143]\n"
"v_mfma_f32_16x16x32_bf16 v[140:143],a[92:95],v[100:103],v[140:143]\n"
"ds_read_b64_tr_b16 a[6:7],v12 offset:18432\n"
"v_mfma_f32_16x16x32_bf16 v[144:147],a[96:99],v[96:99],v[144:147]\n"
"v_mfma_f32_16x16x32_bf16 v[144:147],a[100:103],v[100:103],v[144:147]\n"
"buffer_load_dwordx4 v26,s[36:39],s48 offen offset:2048 lds\n"
"v_mfma_f32_16x16x32_bf16 v[148:151],a[104:107],v[96:99],v[148:151]\n"
"ds_read_b64_tr_b16 a[8:9],v13 offset:0\n"
"v_mfma_f32_16x16x32_bf16 v[148:151],a[108:111],v[100:103],v[148:151]\n"
"v_mfma_f32_16x16x32_bf16 v[152:155],a[112:115],v[96:99],v[152:155]\n"
"v_mfma_f32_16x16x32_bf16 v[152:155],a[116:119],v[100:103],v[152:155]\n"
"ds_read_b64_tr_b16 a[10:11],v13 offset:2048\n"
"buffer_load_dwordx4 v27,s[36:39],s48 offen offset:3072 lds\n"
"v_mfma_f32_16x16x32_bf16 v[156:159],a[120:123],v[96:99],v[156:159]\n"
"v_mfma_f32_16x16x32_bf16 v[156:159],a[124:127],v[100:103],v[156:159]\n"
"s_waitcnt lgkmcnt(8)\n"
"ds_read_b64_tr_b16 a[12:13],v13 offset:16384\n"
"v_mfma_f32_16x16x32_bf16 v[160:163],a[64:67],v[104:107],v[160:163]\n"
"s_add_u32 m0,s50,s56\n"
"ds_read_b128 v[120:123],v10 offset:6144\n"
"v_mfma_f32_16x16x32_bf16 v[160:163],a[68:71],v[108:111],v[160:163]\n"
"buffer_load_dwordx4 v28,s[36:39],s48 offen offset:0 lds\n"
"v_mfma_f32_16x16x32_bf16 v[164:167],a[72:75],v[104:107],v[164:167]\n"
"ds_read_b128 v[124:127],v11 offset:6144\n"
"v_mfma_f32_16x16x32_bf16 v[164:167],a[76:79],v[108:111],v[164:167]\n"
"ds_read_b64_tr_b16 a[14:15],v13 offset:18432\n"
"v_mfma_f32_16x16x32_bf16 v[168:171],a[80:83],v[104:107],v[168:171]\n"
"v_mfma_f32_16x16x32_bf16 v[168:171],a[84:87],v[108:111],v[168:171]\n"
"ds_read_b64_tr_b16 a[16:17],v14 offset:0\n"
"buffer_load_dwordx4 v29,s[36:39],s48 offen offset:1024 lds\n"
"v_mfma_f32_16x16x32_bf16 v[172:175],a[88:91],v[104:107],v[172:175]\n"
"v_mfma_f32_16x16x32_bf16 v[172:175],a[92:95],v[108:111],v[172:175]\n"
"ds_read_b64_tr_b16 a[18:19],v14 offset:2048\n"
"v_mfma_f32_16x16x32_bf16 v[176:179],a[96:99],v[104:107],v[176:179]\n"
"v_mfma_f32_16x16x32_bf16 v[176:179],a[100:103],v[108:111],v[176:179]\n"
"buffer_load_dwordx4 v30,s[36:39],s48 offen offset:2048 lds\n"
"v_mfma_f32_16x16x32_bf16 v[180:183],a[104:107],v[104:107],v[180:183]\n"
"ds_read_b64_tr_b16 a[20:21],v14 offset:16384\n"
"v_mfma_f32_16x16x32_bf16 v[180:183],a[108:111],v[108:111],v[180:183]\n"
"v_mfma_f32_16x16x32_bf16 v[184:187],a[112:115],v[104:107],v[184:187]\n"
"ds_read_b64_tr_b16 a[22:23],v14 offset:18432\n"
"v_mfma_f32_16x16x32_bf16 v[184:187],a[116:119],v[108:111],v[184:187]\n"
"buffer_load_dwordx4 v31,s[36:39],s48 offen offset:3072 lds\n"
"v_mfma_f32_16x16x32_bf16 v[188:191],a[120:123],v[104:107],v[188:191]\n"
"v_mfma_f32_16x16x32_bf16 v[188:191],a[124:127],v[108:111],v[188:191]\n"
"s_waitcnt lgkmcnt(13)\n"
"ds_read_b64_tr_b16 a[24:25],v15 offset:0\n"
"v_mfma_f32_16x16x32_bf16 v[192:195],a[64:67],v[112:115],v[192:195]\n"
"ds_read_b128 v[96:99],v10 offset:8192\n"
"v_mfma_f32_16x16x32_bf16 v[192:195],a[68:71],v[116:119],v[192:195]\n"
"v_mfma_f32_16x16x32_bf16 v[196:199],a[72:75],v[112:115],v[196:199]\n"
"ds_read_b128 v[100:103],v11 offset:8192\n"
"buffer_load_dwordx4 v[72:75],v52,s[60:63],0 offen offset:0 sc1\n"
"v_mfma_f32_16x16x32_bf16 v[196:199],a[76:79],v[116:119],v[196:199]\n"
"ds_read_b64_tr_b16 a[26:27],v15 offset:2048\n"
"v_mfma_f32_16x16x32_bf16 v[200:203],a[80:83],v[112:115],v[200:203]\n"
"ds_read_b64_tr_b16 a[28:29],v15 offset:16384\n"
"v_mfma_f32_16x16x32_bf16 v[200:203],a[84:87],v[116:119],v[200:203]\n"
"v_mfma_f32_16x16x32_bf16 v[204:207],a[88:91],v[112:115],v[204:207]\n"
"v_mfma_f32_16x16x32_bf16 v[204:207],a[92:95],v[116:119],v[204:207]\n"
"ds_read_b64_tr_b16 a[30:31],v15 offset:18432\n"
"buffer_load_dwordx4 v[76:79],v51,s[60:63],0 offen offset:0 sc1\n"
"v_mfma_f32_16x16x32_bf16 v[208:211],a[96:99],v[112:115],v[208:211]\n"
"v_mfma_f32_16x16x32_bf16 v[208:211],a[100:103],v[116:119],v[208:211]\n"
"v_mfma_f32_16x16x32_bf16 v[212:215],a[104:107],v[112:115],v[212:215]\n"
"ds_read_b64_tr_b16 a[32:33],v16 offset:0\n"
"v_mfma_f32_16x16x32_bf16 v[212:215],a[108:111],v[116:119],v[212:215]\n"
"v_mfma_f32_16x16x32_bf16 v[216:219],a[112:115],v[112:115],v[216:219]\n"
"ds_read_b64_tr_b16 a[34:35],v16 offset:2048\n"
"buffer_load_dwordx4 v[80:83],v52,s[60:63],0 offen offset:1024 sc1\n"
"v_mfma_f32_16x16x32_bf16 v[216:219],a[116:119],v[116:119],v[216:219]\n"
"v_mfma_f32_16x16x32_bf16 v[220:223],a[120:123],v[112:115],v[220:223]\n"
"v_mfma_f32_16x16x32_bf16 v[220:223],a[124:127],v[116:119],v[220:223]\n"
"s_waitcnt lgkmcnt(13)\n"
"ds_read_b64_tr_b16 a[36:37],v16 offset:16384\n"
"v_mfma_f32_16x16x32_bf16 v[224:227],a[64:67],v[120:123],v[224:227]\n"
"ds_read_b128 v[104:107],v10 offset:10240\n"
"v_mfma_f32_16x16x32_bf16 v[224:227],a[68:71],v[124:127],v[224:227]\n"
"ds_read_b64_tr_b16 a[38:39],v16 offset:18432\n"
"buffer_load_dwordx4 v[84:87],v51,s[60:63],0 offen offset:1024 sc1\n"
"v_mfma_f32_16x16x32_bf16 v[228:231],a[72:75],v[120:123],v[228:231]\n"
"ds_read_b128 v[108:111],v11 offset:10240\n"
"v_mfma_f32_16x16x32_bf16 v[228:231],a[76:79],v[124:127],v[228:231]\n"
"v_mfma_f32_16x16x32_bf16 v[232:235],a[80:83],v[120:123],v[232:235]\n"
"ds_read_b64_tr_b16 a[40:41],v17 offset:0\n"
"v_mfma_f32_16x16x32_bf16 v[232:235],a[84:87],v[124:127],v[232:235]\n"
"v_mfma_f32_16x16x32_bf16 v[236:239],a[88:91],v[120:123],v[236:239]\n"
"buffer_load_dwordx4 v[88:91],v52,s[60:63],0 offen offset:2048 sc1\n"
"v_mfma_f32_16x16x32_bf16 v[236:239],a[92:95],v[124:127],v[236:239]\n"
"ds_read_b64_tr_b16 a[42:43],v17 offset:2048\n"
"v_mfma_f32_16x16x32_bf16 v[240:243],a[96:99],v[120:123],v[240:243]\n"
"v_mfma_f32_16x16x32_bf16 v[240:243],a[100:103],v[124:127],v[240:243]\n"
"ds_read_b64_tr_b16 a[44:45],v17 offset:16384\n"
"v_mfma_f32_16x16x32_bf16 v[244:247],a[104:107],v[120:123],v[244:247]\n"
"v_mfma_f32_16x16x32_bf16 v[244:247],a[108:111],v[124:127],v[244:247]\n"
"buffer_load_dwordx4 v[92:95],v51,s[60:63],0 offen offset:2048 sc1\n"
"v_mfma_f32_16x16x32_bf16 v[248:251],a[112:115],v[120:123],v[248:251]\n"
"ds_read_b64_tr_b16 a[46:47],v17 offset:18432\n"
"v_mfma_f32_16x16x32_bf16 v[248:251],a[116:119],v[124:127],v[248:251]\n"
"v_mfma_f32_16x16x32_bf16 v[252:255],a[120:123],v[120:123],v[252:255]\n"
"v_mfma_f32_16x16x32_bf16 v[252:255],a[124:127],v[124:127],v[252:255]\n"
"s_waitcnt lgkmcnt(13)\n"
"ds_read_b64_tr_b16 a[48:49],v18 offset:0\n"
"v_mfma_f32_16x16x32_bf16 a[128:131],a[64:67],v[96:99],a[128:131]\n"
"ds_read_b128 v[112:115],v10 offset:12288\n"
"buffer_load_dwordx4 v[24:27],v52,s[60:63],0 offen offset:3072 sc1\n"
"v_mfma_f32_16x16x32_bf16 a[128:131],a[68:71],v[100:103],a[128:131]\n"
"ds_read_b64_tr_b16 a[50:51],v18 offset:2048\n"
"v_mfma_f32_16x16x32_bf16 a[132:135],a[72:75],v[96:99],a[132:135]\n"
"ds_read_b128 v[116:119],v11 offset:12288\n"
"v_mfma_f32_16x16x32_bf16 a[132:135],a[76:79],v[100:103],a[132:135]\n"
"v_mfma_f32_16x16x32_bf16 a[136:139],a[80:83],v[96:99],a[136:139]\n"
"ds_read_b64_tr_b16 a[52:53],v18 offset:16384\n"
"v_mfma_f32_16x16x32_bf16 a[136:139],a[84:87],v[100:103],a[136:139]\n"
"buffer_load_dwordx4 v[28:31],v51,s[60:63],0 offen offset:3072 sc1\n"
"v_mfma_f32_16x16x32_bf16 a[140:143],a[88:91],v[96:99],a[140:143]\n"
"ds_read_b64_tr_b16 a[54:55],v18 offset:18432\n"
"v_mfma_f32_16x16x32_bf16 a[140:143],a[92:95],v[100:103],a[140:143]\n"
"v_mfma_f32_16x16x32_bf16 a[144:147],a[96:99],v[96:99],a[144:147]\n"
"v_mfma_f32_16x16x32_bf16 a[144:147],a[100:103],v[100:103],a[144:147]\n"
"ds_read_b64_tr_b16 a[56:57],v19 offset:0\n"
"v_mfma_f32_16x16x32_bf16 a[148:151],a[104:107],v[96:99],a[148:151]\n"
"buffer_load_dwordx4 v[40:43],v52,s[60:63],s86 offen offset:0 sc1\n"
"v_mfma_f32_16x16x32_bf16 a[148:151],a[108:111],v[100:103],a[148:151]\n"
"v_mfma_f32_16x16x32_bf16 a[152:155],a[112:115],v[96:99],a[152:155]\n"
"ds_read_b64_tr_b16 a[58:59],v19 offset:2048\n"
"v_mfma_f32_16x16x32_bf16 a[152:155],a[116:119],v[100:103],a[152:155]\n"
"v_mfma_f32_16x16x32_bf16 a[156:159],a[120:123],v[96:99],a[156:159]\n"
"ds_read_b64_tr_b16 a[60:61],v19 offset:16384\n"
"v_mfma_f32_16x16x32_bf16 a[156:159],a[124:127],v[100:103],a[156:159]\n"
"s_waitcnt lgkmcnt(13)\n"
"buffer_load_dwordx4 v[44:47],v51,s[60:63],s86 offen offset:0 sc1\n"
"v_mfma_f32_16x16x32_bf16 a[160:163],a[64:67],v[104:107],a[160:163]\n"
"ds_read_b128 v[120:123],v10 offset:14336\n"
"v_mfma_f32_16x16x32_bf16 a[160:163],a[68:71],v[108:111],a[160:163]\n"
"ds_read_b64_tr_b16 a[62:63],v19 offset:18432\n"
"v_mfma_f32_16x16x32_bf16 a[164:167],a[72:75],v[104:107],a[164:167]\n"
"ds_read_b128 v[124:127],v11 offset:14336\n"
"v_mfma_f32_16x16x32_bf16 a[164:167],a[76:79],v[108:111],a[164:167]\n"
"v_mfma_f32_16x16x32_bf16 a[168:171],a[80:83],v[104:107],a[168:171]\n"
"v_mfma_f32_16x16x32_bf16 a[168:171],a[84:87],v[108:111],a[168:171]\n"
"v_mfma_f32_16x16x32_bf16 a[172:175],a[88:91],v[104:107],a[172:175]\n"
"v_mfma_f32_16x16x32_bf16 a[172:175],a[92:95],v[108:111],a[172:175]\n"
"v_mfma_f32_16x16x32_bf16 a[176:179],a[96:99],v[104:107],a[176:179]\n"
"v_mfma_f32_16x16x32_bf16 a[176:179],a[100:103],v[108:111],a[176:179]\n"
"v_mfma_f32_16x16x32_bf16 a[180:183],a[104:107],v[104:107],a[180:183]\n"
"v_mfma_f32_16x16x32_bf16 a[180:183],a[108:111],v[108:111],a[180:183]\n"
"v_mfma_f32_16x16x32_bf16 a[184:187],a[112:115],v[104:107],a[184:187]\n"
"v_mfma_f32_16x16x32_bf16 a[184:187],a[116:119],v[108:111],a[184:187]\n"
"v_mfma_f32_16x16x32_bf16 a[188:191],a[120:123],v[104:107],a[188:191]\n"
"s_waitcnt lgkmcnt(0)\n"
"v_mfma_f32_16x16x32_bf16 a[188:191],a[124:127],v[108:111],a[188:191]\n"
"s_waitcnt vmcnt(26)\n"
"s_barrier\n"
"v_mfma_f32_16x16x32_bf16 a[192:195],a[64:67],v[112:115],a[192:195]\n"
"s_mov_b32 s53,0x0\n"
"v_add_u32 v10,s81,v8\n"
"v_add_u32 v11,s81,v9\n"
"v_mfma_f32_16x16x32_bf16 a[192:195],a[68:71],v[116:119],a[192:195]\n"
"s_add_u32 m0,s57,0x0\n"
"v_mfma_f32_16x16x32_bf16 a[196:199],a[72:75],v[112:115],a[196:199]\n"
"ds_read_b128 v[96:99],v10 offset:0\n"
"buffer_load_dwordx4 v32,s[64:67],s53 offen offset:0 lds\n"
"v_mfma_f32_16x16x32_bf16 a[196:199],a[76:79],v[116:119],a[196:199]\n"
"v_mfma_f32_16x16x32_bf16 a[200:203],a[80:83],v[112:115],a[200:203]\n"
"ds_read_b128 v[100:103],v11 offset:0\n"
"v_mfma_f32_16x16x32_bf16 a[200:203],a[84:87],v[116:119],a[200:203]\n"
"v_mfma_f32_16x16x32_bf16 a[204:207],a[88:91],v[112:115],a[204:207]\n"
"buffer_load_dwordx4 v33,s[64:67],s53 offen offset:1024 lds\n"
"v_mfma_f32_16x16x32_bf16 a[204:207],a[92:95],v[116:119],a[204:207]\n"
"v_mfma_f32_16x16x32_bf16 a[208:211],a[96:99],v[112:115],a[208:211]\n"
"v_mfma_f32_16x16x32_bf16 a[208:211],a[100:103],v[116:119],a[208:211]\n"
"v_mfma_f32_16x16x32_bf16 a[212:215],a[104:107],v[112:115],a[212:215]\n"
"buffer_load_dwordx4 v34,s[64:67],s53 offen offset:2048 lds\n"
"v_mfma_f32_16x16x32_bf16 a[212:215],a[108:111],v[116:119],a[212:215]\n"
"v_mfma_f32_16x16x32_bf16 a[216:219],a[112:115],v[112:115],a[216:219]\n"
"v_mfma_f32_16x16x32_bf16 a[216:219],a[116:119],v[116:119],a[216:219]\n"
"v_mfma_f32_16x16x32_bf16 a[220:223],a[120:123],v[112:115],a[220:223]\n"
"buffer_load_dwordx4 v35,s[64:67],s53 offen offset:3072 lds\n"
"v_mfma_f32_16x16x32_bf16 a[220:223],a[124:127],v[116:119],a[220:223]\n"
"s_waitcnt vmcnt(14)\n"
"s_barrier\n"
"v_mfma_f32_16x16x32_bf16 a[224:227],a[64:67],v[120:123],a[224:227]\n"
"ds_read_b128 v[104:107],v8 offset:0\n"
"v_mfma_f32_16x16x32_bf16 a[224:227],a[68:71],v[124:127],a[224:227]\n"
"s_add_u32 m0,s57,0x1000\n"
"ds_read_b128 v[108:111],v9 offset:0\n"
"v_mfma_f32_16x16x32_bf16 a[228:231],a[72:75],v[120:123],a[228:231]\n"
"ds_read_b64_tr_b16 a[64:65],v12 offset:32768\n"
"buffer_load_dwordx4 v36,s[64:67],s53 offen offset:0 lds\n"
"v_mfma_f32_16x16x32_bf16 a[228:231],a[76:79],v[124:127],a[228:231]\n"
"ds_read_b64_tr_b16 a[66:67],v12 offset:34816\n"
"v_mfma_f32_16x16x32_bf16 a[232:235],a[80:83],v[120:123],a[232:235]\n"
"ds_read_b64_tr_b16 a[68:69],v12 offset:49152\n"
"v_mfma_f32_16x16x32_bf16 a[232:235],a[84:87],v[124:127],a[232:235]\n"
"ds_read_b64_tr_b16 a[70:71],v12 offset:51200\n"
"v_mfma_f32_16x16x32_bf16 a[236:239],a[88:91],v[120:123],a[236:239]\n"
"ds_read_b64_tr_b16 a[72:73],v13 offset:32768\n"
"buffer_load_dwordx4 v37,s[64:67],s53 offen offset:1024 lds\n"
"v_mfma_f32_16x16x32_bf16 a[236:239],a[92:95],v[124:127],a[236:239]\n"
"ds_read_b64_tr_b16 a[74:75],v13 offset:34816\n"
"v_mfma_f32_16x16x32_bf16 a[240:243],a[96:99],v[120:123],a[240:243]\n"
"ds_read_b64_tr_b16 a[76:77],v13 offset:49152\n"
"v_mfma_f32_16x16x32_bf16 a[240:243],a[100:103],v[124:127],a[240:243]\n"
"ds_read_b64_tr_b16 a[78:79],v13 offset:51200\n"
"v_mfma_f32_16x16x32_bf16 a[244:247],a[104:107],v[120:123],a[244:247]\n"
"ds_read_b64_tr_b16 a[80:81],v14 offset:32768\n"
"buffer_load_dwordx4 v38,s[64:67],s53 offen offset:2048 lds\n"
"v_mfma_f32_16x16x32_bf16 a[244:247],a[108:111],v[124:127],a[244:247]\n"
"ds_read_b64_tr_b16 a[82:83],v14 offset:34816\n"
"v_mfma_f32_16x16x32_bf16 a[248:251],a[112:115],v[120:123],a[248:251]\n"
"ds_read_b64_tr_b16 a[84:85],v14 offset:49152\n"
"v_mfma_f32_16x16x32_bf16 a[248:251],a[116:119],v[124:127],a[248:251]\n"
"ds_read_b64_tr_b16 a[86:87],v14 offset:51200\n"
"v_mfma_f32_16x16x32_bf16 a[252:255],a[120:123],v[120:123],a[252:255]\n"
"ds_read_b64_tr_b16 a[88:89],v15 offset:32768\n"
"buffer_load_dwordx4 v39,s[64:67],s53 offen offset:3072 lds\n"
"v_mfma_f32_16x16x32_bf16 a[252:255],a[124:127],v[124:127],a[252:255]\n"
"s_waitcnt lgkmcnt(15)\n"
"ds_read_b64_tr_b16 a[90:91],v15 offset:34816\n"
"v_mfma_f32_16x16x32_bf16 v[128:131],a[0:3],v[96:99],v[128:131]\n"
"ds_read_b64_tr_b16 a[92:93],v15 offset:49152\n"
"v_mfma_f32_16x16x32_bf16 v[128:131],a[4:7],v[100:103],v[128:131]\n"
"s_mov_b32 s53,0x100000\n"
"ds_read_b64_tr_b16 a[94:95],v15 offset:51200\n"
"v_mfma_f32_16x16x32_bf16 v[132:135],a[8:11],v[96:99],v[132:135]\n"
"s_add_u32 m0,s57,0x18000\n"
"ds_read_b64_tr_b16 a[96:97],v16 offset:32768\n"
"v_mfma_f32_16x16x32_bf16 v[132:135],a[12:15],v[100:103],v[132:135]\n"
"ds_read_b64_tr_b16 a[98:99],v16 offset:34816\n"
"buffer_load_dwordx4 v32,s[64:67],s53 offen offset:0 lds\n"
"v_mfma_f32_16x16x32_bf16 v[136:139],a[16:19],v[96:99],v[136:139]\n"
"ds_read_b64_tr_b16 a[100:101],v16 offset:49152\n"
"v_mfma_f32_16x16x32_bf16 v[136:139],a[20:23],v[100:103],v[136:139]\n"
"ds_read_b64_tr_b16 a[102:103],v16 offset:51200\n"
"v_mfma_f32_16x16x32_bf16 v[140:143],a[24:27],v[96:99],v[140:143]\n"
"ds_read_b64_tr_b16 a[104:105],v17 offset:32768\n"
"v_mfma_f32_16x16x32_bf16 v[140:143],a[28:31],v[100:103],v[140:143]\n"
"ds_read_b64_tr_b16 a[106:107],v17 offset:34816\n"
"buffer_load_dwordx4 v33,s[64:67],s53 offen offset:1024 lds\n"
"v_mfma_f32_16x16x32_bf16 v[144:147],a[32:35],v[96:99],v[144:147]\n"
"ds_read_b64_tr_b16 a[108:109],v17 offset:49152\n"
"v_mfma_f32_16x16x32_bf16 v[144:147],a[36:39],v[100:103],v[144:147]\n"
"ds_read_b64_tr_b16 a[110:111],v17 offset:51200\n"
"v_mfma_f32_16x16x32_bf16 v[148:151],a[40:43],v[96:99],v[148:151]\n"
"ds_read_b64_tr_b16 a[112:113],v18 offset:32768\n"
"v_mfma_f32_16x16x32_bf16 v[148:151],a[44:47],v[100:103],v[148:151]\n"
"ds_read_b64_tr_b16 a[114:115],v18 offset:34816\n"
"buffer_load_dwordx4 v34,s[64:67],s53 offen offset:2048 lds\n"
"v_mfma_f32_16x16x32_bf16 v[152:155],a[48:51],v[96:99],v[152:155]\n"
"ds_read_b64_tr_b16 a[116:117],v18 offset:49152\n"
"v_mfma_f32_16x16x32_bf16 v[152:155],a[52:55],v[100:103],v[152:155]\n"
"ds_read_b64_tr_b16 a[118:119],v18 offset:51200\n"
"v_mfma_f32_16x16x32_bf16 v[156:159],a[56:59],v[96:99],v[156:159]\n"
"ds_read_b64_tr_b16 a[120:121],v19 offset:32768\n"
"v_mfma_f32_16x16x32_bf16 v[156:159],a[60:63],v[100:103],v[156:159]\n"
"s_waitcnt lgkmcnt(13)\n"
"ds_read_b64_tr_b16 a[122:123],v19 offset:34816\n"
"buffer_load_dwordx4 v35,s[64:67],s53 offen offset:3072 lds\n"
"v_mfma_f32_16x16x32_bf16 v[128:131],a[64:67],v[104:107],v[128:131]\n"
"ds_read_b64_tr_b16 a[124:125],v19 offset:49152\n"
"v_mfma_f32_16x16x32_bf16 v[128:131],a[68:71],v[108:111],v[128:131]\n"
"ds_read_b64_tr_b16 a[126:127],v19 offset:51200\n"
"v_mfma_f32_16x16x32_bf16 v[132:135],a[72:75],v[104:107],v[132:135]\n"
"s_add_u32 m0,s57,0x19000\n"
"ds_read_b128 v[112:115],v10 offset:2048\n"
"v_mfma_f32_16x16x32_bf16 v[132:135],a[76:79],v[108:111],v[132:135]\n"
"ds_read_b128 v[116:119],v11 offset:2048\n"
"buffer_load_dwordx4 v36,s[64:67],s53 offen offset:0 lds\n"
"v_mfma_f32_16x16x32_bf16 v[136:139],a[80:83],v[104:107],v[136:139]\n"
"ds_read_b128 v[120:123],v8 offset:2048\n"
"v_mfma_f32_16x16x32_bf16 v[136:139],a[84:87],v[108:111],v[136:139]\n"
"ds_read_b128 v[124:127],v9 offset:2048\n"
"v_mfma_f32_16x16x32_bf16 v[140:143],a[88:91],v[104:107],v[140:143]\n"
"v_subrev_u32 v12,s80,v12\n"
"v_mfma_f32_16x16x32_bf16 v[140:143],a[92:95],v[108:111],v[140:143]\n"
"s_waitcnt lgkmcnt(8)\n"
"buffer_load_dwordx4 v37,s[64:67],s53 offen offset:1024 lds\n"
"v_mfma_f32_16x16x32_bf16 v[144:147],a[96:99],v[104:107],v[144:147]\n"
"v_subrev_u32 v13,s80,v13\n"
"v_mfma_f32_16x16x32_bf16 v[144:147],a[100:103],v[108:111],v[144:147]\n"
"v_mfma_f32_16x16x32_bf16 v[148:151],a[104:107],v[104:107],v[148:151]\n"
"v_subrev_u32 v14,s80,v14\n"
"v_mfma_f32_16x16x32_bf16 v[148:151],a[108:111],v[108:111],v[148:151]\n"
"buffer_load_dwordx4 v38,s[64:67],s53 offen offset:2048 lds\n"
"v_mfma_f32_16x16x32_bf16 v[152:155],a[112:115],v[104:107],v[152:155]\n"
"v_subrev_u32 v15,s80,v15\n"
"v_mfma_f32_16x16x32_bf16 v[152:155],a[116:119],v[108:111],v[152:155]\n"
"s_waitcnt lgkmcnt(4)\n"
"v_mfma_f32_16x16x32_bf16 v[156:159],a[120:123],v[104:107],v[156:159]\n"
"v_subrev_u32 v16,s80,v16\n"
"v_mfma_f32_16x16x32_bf16 v[156:159],a[124:127],v[108:111],v[156:159]\n"
"s_waitcnt lgkmcnt(0)\n"
"buffer_load_dwordx4 v39,s[64:67],s53 offen offset:3072 lds\n"
"v_mfma_f32_16x16x32_bf16 v[160:163],a[0:3],v[112:115],v[160:163]\n"
"ds_read_b128 v[96:99],v10 offset:4096\n"
"v_subrev_u32 v17,s80,v17\n"
"v_mfma_f32_16x16x32_bf16 v[160:163],a[4:7],v[116:119],v[160:163]\n"
"v_mfma_f32_16x16x32_bf16 v[160:163],a[64:67],v[120:123],v[160:163]\n"
"ds_read_b128 v[100:103],v11 offset:4096\n"
"v_subrev_u32 v18,s80,v18\n"
"v_mfma_f32_16x16x32_bf16 v[160:163],a[68:71],v[124:127],v[160:163]\n"
"v_cvt_pk_bf16_f32 v128,v128,v129\n"
"v_cvt_pk_bf16_f32 v129,v130,v131\n"
"v_mfma_f32_16x16x32_bf16 v[164:167],a[8:11],v[112:115],v[164:167]\n"
"ds_read_b128 v[104:107],v8 offset:4096\n"
"v_subrev_u32 v19,s80,v19\n"
"v_mfma_f32_16x16x32_bf16 v[164:167],a[12:15],v[116:119],v[164:167]\n"
"v_cvt_pk_bf16_f32 v130,v132,v133\n"
"v_cvt_pk_bf16_f32 v131,v134,v135\n"
"v_mfma_f32_16x16x32_bf16 v[164:167],a[72:75],v[120:123],v[164:167]\n"
"ds_read_b128 v[108:111],v9 offset:4096\n"
"v_cvt_pk_bf16_f32 v136,v136,v137\n"
"v_mfma_f32_16x16x32_bf16 v[164:167],a[76:79],v[124:127],v[164:167]\n"
"s_mov_b32 s59,0x0\n"
"v_cvt_pk_bf16_f32 v137,v138,v139\n"
"v_cvt_pk_bf16_f32 v138,v140,v141\n"
"v_mfma_f32_16x16x32_bf16 v[168:171],a[16:19],v[112:115],v[168:171]\n"
"s_barrier\n"
"buffer_store_dwordx4 v[128:131],v52,s[44:47],s59 offen offset:0 sc1\n"
"v_mfma_f32_16x16x32_bf16 v[168:171],a[20:23],v[116:119],v[168:171]\n"
"v_cvt_pk_bf16_f32 v139,v142,v143\n"
"v_cvt_pk_bf16_f32 v144,v144,v145\n"
"v_mfma_f32_16x16x32_bf16 v[168:171],a[80:83],v[120:123],v[168:171]\n"
"s_mov_b32 s53,0x80000\n"
"v_cvt_pk_bf16_f32 v145,v146,v147\n"
"v_cvt_pk_bf16_f32 v146,v148,v149\n"
"v_mfma_f32_16x16x32_bf16 v[168:171],a[84:87],v[124:127],v[168:171]\n"
"s_add_u32 m0,s57,0x8000\n"
"v_cvt_pk_bf16_f32 v147,v150,v151\n"
"v_mfma_f32_16x16x32_bf16 v[172:175],a[24:27],v[112:115],v[172:175]\n"
"buffer_load_dwordx4 v32,s[64:67],s53 offen offset:0 lds\n"
"v_mfma_f32_16x16x32_bf16 v[172:175],a[28:31],v[116:119],v[172:175]\n"
"v_mfma_f32_16x16x32_bf16 v[172:175],a[88:91],v[120:123],v[172:175]\n"
"v_mfma_f32_16x16x32_bf16 v[172:175],a[92:95],v[124:127],v[172:175]\n"
"v_mfma_f32_16x16x32_bf16 v[176:179],a[32:35],v[112:115],v[176:179]\n"
"v_mfma_f32_16x16x32_bf16 v[176:179],a[36:39],v[116:119],v[176:179]\n"
"v_mfma_f32_16x16x32_bf16 v[176:179],a[96:99],v[120:123],v[176:179]\n"
"s_mov_b32 s59,0x0\n"
"v_mfma_f32_16x16x32_bf16 v[176:179],a[100:103],v[124:127],v[176:179]\n"
"buffer_store_dwordx4 v[136:139],v52,s[44:47],s59 offen offset:1024 sc1\n"
"v_mfma_f32_16x16x32_bf16 v[180:183],a[40:43],v[112:115],v[180:183]\n"
"v_cvt_pk_bf16_f32 v152,v152,v153\n"
"v_cvt_pk_bf16_f32 v153,v154,v155\n"
"v_mfma_f32_16x16x32_bf16 v[180:183],a[44:47],v[116:119],v[180:183]\n"
"v_cvt_pk_bf16_f32 v154,v156,v157\n"
"v_cvt_pk_bf16_f32 v155,v158,v159\n"
"v_mfma_f32_16x16x32_bf16 v[180:183],a[104:107],v[120:123],v[180:183]\n"
"v_mfma_f32_16x16x32_bf16 v[180:183],a[108:111],v[124:127],v[180:183]\n"
"buffer_load_dwordx4 v33,s[64:67],s53 offen offset:1024 lds\n"
"v_mfma_f32_16x16x32_bf16 v[184:187],a[48:51],v[112:115],v[184:187]\n"
"v_mfma_f32_16x16x32_bf16 v[184:187],a[52:55],v[116:119],v[184:187]\n"
"v_mfma_f32_16x16x32_bf16 v[184:187],a[112:115],v[120:123],v[184:187]\n"
"v_mfma_f32_16x16x32_bf16 v[184:187],a[116:119],v[124:127],v[184:187]\n"
"v_mfma_f32_16x16x32_bf16 v[188:191],a[56:59],v[112:115],v[188:191]\n"
"v_mfma_f32_16x16x32_bf16 v[188:191],a[60:63],v[116:119],v[188:191]\n"
"s_mov_b32 s59,0x0\n"
"v_mfma_f32_16x16x32_bf16 v[188:191],a[120:123],v[120:123],v[188:191]\n"
"buffer_store_dwordx4 v[144:147],v52,s[44:47],s59 offen offset:2048 sc1\n"
"v_mfma_f32_16x16x32_bf16 v[188:191],a[124:127],v[124:127],v[188:191]\n"
"s_waitcnt lgkmcnt(0)\n"
"v_cvt_pk_bf16_f32 v160,v160,v161\n"
"v_cvt_pk_bf16_f32 v161,v162,v163\n"
"v_mfma_f32_16x16x32_bf16 v[192:195],a[0:3],v[96:99],v[192:195]\n"
"ds_read_b128 v[112:115],v10 offset:6144\n"
"v_cvt_pk_bf16_f32 v162,v164,v165\n"
"v_mfma_f32_16x16x32_bf16 v[192:195],a[4:7],v[100:103],v[192:195]\n"
"v_cvt_pk_bf16_f32 v163,v166,v167\n"
"v_mfma_f32_16x16x32_bf16 v[192:195],a[64:67],v[104:107],v[192:195]\n"
"ds_read_b128 v[116:119],v11 offset:6144\n"
"buffer_load_dwordx4 v34,s[64:67],s53 offen offset:2048 lds\n"
"v_mfma_f32_16x16x32_bf16 v[192:195],a[68:71],v[108:111],v[192:195]\n"
"v_mfma_f32_16x16x32_bf16 v[196:199],a[8:11],v[96:99],v[196:199]\n"
"ds_read_b128 v[120:123],v8 offset:6144\n"
"v_mfma_f32_16x16x32_bf16 v[196:199],a[12:15],v[100:103],v[196:199]\n"
"v_mfma_f32_16x16x32_bf16 v[196:199],a[72:75],v[104:107],v[196:199]\n"
"ds_read_b128 v[124:127],v9 offset:6144\n"
"v_mfma_f32_16x16x32_bf16 v[196:199],a[76:79],v[108:111],v[196:199]\n"
"v_mfma_f32_16x16x32_bf16 v[200:203],a[16:19],v[96:99],v[200:203]\n"
"s_mov_b32 s59,0x0\n"
"v_mfma_f32_16x16x32_bf16 v[200:203],a[20:23],v[100:103],v[200:203]\n"
"buffer_store_dwordx4 v[152:155],v52,s[44:47],s59 offen offset:3072 sc1\n"
"v_mfma_f32_16x16x32_bf16 v[200:203],a[80:83],v[104:107],v[200:203]\n"
"v_cvt_pk_bf16_f32 v168,v168,v169\n"
"v_cvt_pk_bf16_f32 v169,v170,v171\n"
"v_mfma_f32_16x16x32_bf16 v[200:203],a[84:87],v[108:111],v[200:203]\n"
"v_cvt_pk_bf16_f32 v170,v172,v173\n"
"v_cvt_pk_bf16_f32 v171,v174,v175\n"
"v_mfma_f32_16x16x32_bf16 v[204:207],a[24:27],v[96:99],v[204:207]\n"
"v_mfma_f32_16x16x32_bf16 v[204:207],a[28:31],v[100:103],v[204:207]\n"
"buffer_load_dwordx4 v35,s[64:67],s53 offen offset:3072 lds\n"
"v_mfma_f32_16x16x32_bf16 v[204:207],a[88:91],v[104:107],v[204:207]\n"
"v_mfma_f32_16x16x32_bf16 v[204:207],a[92:95],v[108:111],v[204:207]\n"
"v_mfma_f32_16x16x32_bf16 v[208:211],a[32:35],v[96:99],v[208:211]\n"
"v_mfma_f32_16x16x32_bf16 v[208:211],a[36:39],v[100:103],v[208:211]\n"
"v_mfma_f32_16x16x32_bf16 v[208:211],a[96:99],v[104:107],v[208:211]\n"
"v_mfma_f32_16x16x32_bf16 v[208:211],a[100:103],v[108:111],v[208:211]\n"
"s_mov_b32 s59,0x1000\n"
"v_mfma_f32_16x16x32_bf16 v[212:215],a[40:43],v[96:99],v[212:215]\n"
"buffer_store_dwordx4 v[160:163],v52,s[44:47],s59 offen offset:0 sc1\n"
"v_mfma_f32_16x16x32_bf16 v[212:215],a[44:47],v[100:103],v[212:215]\n"
"v_cvt_pk_bf16_f32 v176,v176,v177\n"
"v_cvt_pk_bf16_f32 v177,v178,v179\n"
"v_mfma_f32_16x16x32_bf16 v[212:215],a[104:107],v[104:107],v[212:215]\n"
"v_cvt_pk_bf16_f32 v178,v180,v181\n"
"v_cvt_pk_bf16_f32 v179,v182,v183\n"
"v_mfma_f32_16x16x32_bf16 v[212:215],a[108:111],v[108:111],v[212:215]\n"
"s_add_u32 m0,s57,0x9000\n"
"v_mfma_f32_16x16x32_bf16 v[216:219],a[48:51],v[96:99],v[216:219]\n"
"buffer_load_dwordx4 v36,s[64:67],s53 offen offset:0 lds\n"
"v_mfma_f32_16x16x32_bf16 v[216:219],a[52:55],v[100:103],v[216:219]\n"
"v_mfma_f32_16x16x32_bf16 v[216:219],a[112:115],v[104:107],v[216:219]\n"
"v_mfma_f32_16x16x32_bf16 v[216:219],a[116:119],v[108:111],v[216:219]\n"
"v_mfma_f32_16x16x32_bf16 v[220:223],a[56:59],v[96:99],v[220:223]\n"
"v_mfma_f32_16x16x32_bf16 v[220:223],a[60:63],v[100:103],v[220:223]\n"
"v_mfma_f32_16x16x32_bf16 v[220:223],a[120:123],v[104:107],v[220:223]\n"
"s_mov_b32 s59,0x1000\n"
"v_mfma_f32_16x16x32_bf16 v[220:223],a[124:127],v[108:111],v[220:223]\n"
"s_waitcnt lgkmcnt(0)\n"
"buffer_store_dwordx4 v[168:171],v52,s[44:47],s59 offen offset:1024 sc1\n"
"v_mfma_f32_16x16x32_bf16 v[224:227],a[0:3],v[112:115],v[224:227]\n"
"ds_read_b128 v[96:99],v10 offset:8192\n"
"v_cvt_pk_bf16_f32 v184,v184,v185\n"
"v_mfma_f32_16x16x32_bf16 v[224:227],a[4:7],v[116:119],v[224:227]\n"
"v_cvt_pk_bf16_f32 v185,v186,v187\n"
"v_cvt_pk_bf16_f32 v186,v188,v189\n"
"v_mfma_f32_16x16x32_bf16 v[224:227],a[64:67],v[120:123],v[224:227]\n"
"ds_read_b128 v[100:103],v11 offset:8192\n"
"v_cvt_pk_bf16_f32 v187,v190,v191\n"
"v_mfma_f32_16x16x32_bf16 v[224:227],a[68:71],v[124:127],v[224:227]\n"
"buffer_load_dwordx4 v37,s[64:67],s53 offen offset:1024 lds\n"
"v_mfma_f32_16x16x32_bf16 v[228:231],a[8:11],v[112:115],v[228:231]\n"
"ds_read_b128 v[104:107],v8 offset:8192\n"
"v_mfma_f32_16x16x32_bf16 v[228:231],a[12:15],v[116:119],v[228:231]\n"
"v_mfma_f32_16x16x32_bf16 v[228:231],a[72:75],v[120:123],v[228:231]\n"
"ds_read_b128 v[108:111],v9 offset:8192\n"
"v_mfma_f32_16x16x32_bf16 v[228:231],a[76:79],v[124:127],v[228:231]\n"
"v_mfma_f32_16x16x32_bf16 v[232:235],a[16:19],v[112:115],v[232:235]\n"
"v_mfma_f32_16x16x32_bf16 v[232:235],a[20:23],v[116:119],v[232:235]\n"
"s_mov_b32 s59,0x1000\n"
"v_mfma_f32_16x16x32_bf16 v[232:235],a[80:83],v[120:123],v[232:235]\n"
"buffer_store_dwordx4 v[176:179],v52,s[44:47],s59 offen offset:2048 sc1\n"
"v_mfma_f32_16x16x32_bf16 v[232:235],a[84:87],v[124:127],v[232:235]\n"
"v_cvt_pk_bf16_f32 v192,v192,v193\n"
"v_cvt_pk_bf16_f32 v193,v194,v195\n"
"v_mfma_f32_16x16x32_bf16 v[236:239],a[24:27],v[112:115],v[236:239]\n"
"v_cvt_pk_bf16_f32 v194,v196,v197\n"
"v_cvt_pk_bf16_f32 v195,v198,v199\n"
"v_mfma_f32_16x16x32_bf16 v[236:239],a[28:31],v[116:119],v[236:239]\n"
"v_mfma_f32_16x16x32_bf16 v[236:239],a[88:91],v[120:123],v[236:239]\n"
"buffer_load_dwordx4 v38,s[64:67],s53 offen offset:2048 lds\n"
"v_mfma_f32_16x16x32_bf16 v[236:239],a[92:95],v[124:127],v[236:239]\n"
"v_mfma_f32_16x16x32_bf16 v[240:243],a[32:35],v[112:115],v[240:243]\n"
"v_mfma_f32_16x16x32_bf16 v[240:243],a[36:39],v[116:119],v[240:243]\n"
"v_mfma_f32_16x16x32_bf16 v[240:243],a[96:99],v[120:123],v[240:243]\n"
"v_mfma_f32_16x16x32_bf16 v[240:243],a[100:103],v[124:127],v[240:243]\n"
"v_mfma_f32_16x16x32_bf16 v[244:247],a[40:43],v[112:115],v[244:247]\n"
"s_mov_b32 s59,0x1000\n"
"v_mfma_f32_16x16x32_bf16 v[244:247],a[44:47],v[116:119],v[244:247]\n"
"buffer_store_dwordx4 v[184:187],v52,s[44:47],s59 offen offset:3072 sc1\n"
"v_mfma_f32_16x16x32_bf16 v[244:247],a[104:107],v[120:123],v[244:247]\n"
"v_cvt_pk_bf16_f32 v200,v200,v201\n"
"v_cvt_pk_bf16_f32 v201,v202,v203\n"
"v_mfma_f32_16x16x32_bf16 v[244:247],a[108:111],v[124:127],v[244:247]\n"
"v_cvt_pk_bf16_f32 v202,v204,v205\n"
"v_cvt_pk_bf16_f32 v203,v206,v207\n"
"v_mfma_f32_16x16x32_bf16 v[248:251],a[48:51],v[112:115],v[248:251]\n"
"v_mfma_f32_16x16x32_bf16 v[248:251],a[52:55],v[116:119],v[248:251]\n"
"buffer_load_dwordx4 v39,s[64:67],s53 offen offset:3072 lds\n"
"v_mfma_f32_16x16x32_bf16 v[248:251],a[112:115],v[120:123],v[248:251]\n"
"v_mfma_f32_16x16x32_bf16 v[248:251],a[116:119],v[124:127],v[248:251]\n"
"v_mfma_f32_16x16x32_bf16 v[252:255],a[56:59],v[112:115],v[252:255]\n"
"v_mfma_f32_16x16x32_bf16 v[252:255],a[60:63],v[116:119],v[252:255]\n"
"v_mfma_f32_16x16x32_bf16 v[252:255],a[120:123],v[120:123],v[252:255]\n"
"s_mov_b32 s59,0x2000\n"
"v_mfma_f32_16x16x32_bf16 v[252:255],a[124:127],v[124:127],v[252:255]\n"
"s_waitcnt lgkmcnt(0)\n"
"v_mfma_f32_16x16x32_bf16 a[128:131],a[0:3],v[96:99],a[128:131]\n"
"ds_read_b128 v[112:115],v10 offset:10240\n"
"buffer_store_dwordx4 v[192:195],v52,s[44:47],s59 offen offset:0 sc1\n"
"v_mfma_f32_16x16x32_bf16 a[128:131],a[4:7],v[100:103],a[128:131]\n"
"v_cvt_pk_bf16_f32 v208,v208,v209\n"
"v_cvt_pk_bf16_f32 v209,v210,v211\n"
"v_mfma_f32_16x16x32_bf16 a[128:131],a[64:67],v[104:107],a[128:131]\n"
"ds_read_b128 v[116:119],v11 offset:10240\n"
"v_cvt_pk_bf16_f32 v210,v212,v213\n"
"v_mfma_f32_16x16x32_bf16 a[128:131],a[68:71],v[108:111],a[128:131]\n"
"v_cvt_pk_bf16_f32 v211,v214,v215\n"
"v_mfma_f32_16x16x32_bf16 a[132:135],a[8:11],v[96:99],a[132:135]\n"
"ds_read_b128 v[120:123],v8 offset:10240\n"
"v_mfma_f32_16x16x32_bf16 a[132:135],a[12:15],v[100:103],a[132:135]\n"
"v_mfma_f32_16x16x32_bf16 a[132:135],a[72:75],v[104:107],a[132:135]\n"
"ds_read_b128 v[124:127],v9 offset:10240\n"
"v_mfma_f32_16x16x32_bf16 a[132:135],a[76:79],v[108:111],a[132:135]\n"
"v_mfma_f32_16x16x32_bf16 a[136:139],a[16:19],v[96:99],a[136:139]\n"
"v_mfma_f32_16x16x32_bf16 a[136:139],a[20:23],v[100:103],a[136:139]\n"
"v_mfma_f32_16x16x32_bf16 a[136:139],a[80:83],v[104:107],a[136:139]\n"
"s_mov_b32 s59,0x2000\n"
"v_mfma_f32_16x16x32_bf16 a[136:139],a[84:87],v[108:111],a[136:139]\n"
"buffer_store_dwordx4 v[200:203],v52,s[44:47],s59 offen offset:1024 sc1\n"
"v_mfma_f32_16x16x32_bf16 a[140:143],a[24:27],v[96:99],a[140:143]\n"
"v_cvt_pk_bf16_f32 v216,v216,v217\n"
"v_cvt_pk_bf16_f32 v217,v218,v219\n"
"v_mfma_f32_16x16x32_bf16 a[140:143],a[28:31],v[100:103],a[140:143]\n"
"v_cvt_pk_bf16_f32 v218,v220,v221\n"
"v_cvt_pk_bf16_f32 v219,v222,v223\n"
"v_mfma_f32_16x16x32_bf16 a[140:143],a[88:91],v[104:107],a[140:143]\n"
"v_mfma_f32_16x16x32_bf16 a[140:143],a[92:95],v[108:111],a[140:143]\n"
"v_mfma_f32_16x16x32_bf16 a[144:147],a[32:35],v[96:99],a[144:147]\n"
"v_mfma_f32_16x16x32_bf16 a[144:147],a[36:39],v[100:103],a[144:147]\n"
"v_mfma_f32_16x16x32_bf16 a[144:147],a[96:99],v[104:107],a[144:147]\n"
"v_mfma_f32_16x16x32_bf16 a[144:147],a[100:103],v[108:111],a[144:147]\n"
"v_mfma_f32_16x16x32_bf16 a[148:151],a[40:43],v[96:99],a[148:151]\n"
"v_mfma_f32_16x16x32_bf16 a[148:151],a[44:47],v[100:103],a[148:151]\n"
"s_mov_b32 s59,0x2000\n"
"v_mfma_f32_16x16x32_bf16 a[148:151],a[104:107],v[104:107],a[148:151]\n"
"buffer_store_dwordx4 v[208:211],v52,s[44:47],s59 offen offset:2048 sc1\n"
"v_mfma_f32_16x16x32_bf16 a[148:151],a[108:111],v[108:111],a[148:151]\n"
"v_cvt_pk_bf16_f32 v224,v224,v225\n"
"v_cvt_pk_bf16_f32 v225,v226,v227\n"
"v_mfma_f32_16x16x32_bf16 a[152:155],a[48:51],v[96:99],a[152:155]\n"
"v_cvt_pk_bf16_f32 v226,v228,v229\n"
"v_cvt_pk_bf16_f32 v227,v230,v231\n"
"v_mfma_f32_16x16x32_bf16 a[152:155],a[52:55],v[100:103],a[152:155]\n"
"v_mfma_f32_16x16x32_bf16 a[152:155],a[112:115],v[104:107],a[152:155]\n"
"v_mfma_f32_16x16x32_bf16 a[152:155],a[116:119],v[108:111],a[152:155]\n"
"v_mfma_f32_16x16x32_bf16 a[156:159],a[56:59],v[96:99],a[156:159]\n"
"v_mfma_f32_16x16x32_bf16 a[156:159],a[60:63],v[100:103],a[156:159]\n"
"v_mfma_f32_16x16x32_bf16 a[156:159],a[120:123],v[104:107],a[156:159]\n"
"v_mfma_f32_16x16x32_bf16 a[156:159],a[124:127],v[108:111],a[156:159]\n"
"s_waitcnt lgkmcnt(0)\n"
"v_mfma_f32_16x16x32_bf16 a[160:163],a[0:3],v[112:115],a[160:163]\n"
"s_mov_b32 s59,0x2000\n"
"ds_read_b128 v[96:99],v10 offset:12288\n"
"v_mfma_f32_16x16x32_bf16 a[160:163],a[4:7],v[116:119],a[160:163]\n"
"buffer_store_dwordx4 v[216:219],v52,s[44:47],s59 offen offset:3072 sc1\n"
"v_mfma_f32_16x16x32_bf16 a[160:163],a[64:67],v[120:123],a[160:163]\n"
"ds_read_b128 v[100:103],v11 offset:12288\n"
"v_cvt_pk_bf16_f32 v232,v232,v233\n"
"v_mfma_f32_16x16x32_bf16 a[160:163],a[68:71],v[124:127],a[160:163]\n"
"v_cvt_pk_bf16_f32 v233,v234,v235\n"
"v_cvt_pk_bf16_f32 v234,v236,v237\n"
"v_mfma_f32_16x16x32_bf16 a[164:167],a[8:11],v[112:115],a[164:167]\n"
"ds_read_b128 v[104:107],v8 offset:12288\n"
"v_cvt_pk_bf16_f32 v235,v238,v239\n"
"v_mfma_f32_16x16x32_bf16 a[164:167],a[12:15],v[116:119],a[164:167]\n"
"v_mfma_f32_16x16x32_bf16 a[164:167],a[72:75],v[120:123],a[164:167]\n"
"ds_read_b128 v[108:111],v9 offset:12288\n"
"v_mfma_f32_16x16x32_bf16 a[164:167],a[76:79],v[124:127],a[164:167]\n"
"v_mfma_f32_16x16x32_bf16 a[168:171],a[16:19],v[112:115],a[168:171]\n"
"v_mfma_f32_16x16x32_bf16 a[168:171],a[20:23],v[116:119],a[168:171]\n"
"v_mfma_f32_16x16x32_bf16 a[168:171],a[80:83],v[120:123],a[168:171]\n"
"v_mfma_f32_16x16x32_bf16 a[168:171],a[84:87],v[124:127],a[168:171]\n"
"s_mov_b32 s59,0x3000\n"
"v_mfma_f32_16x16x32_bf16 a[172:175],a[24:27],v[112:115],a[172:175]\n"
"buffer_store_dwordx4 v[224:227],v52,s[44:47],s59 offen offset:0 sc1\n"
"v_mfma_f32_16x16x32_bf16 a[172:175],a[28:31],v[116:119],a[172:175]\n"
"v_cvt_pk_bf16_f32 v240,v240,v241\n"
"v_cvt_pk_bf16_f32 v241,v242,v243\n"
"v_mfma_f32_16x16x32_bf16 a[172:175],a[88:91],v[120:123],a[172:175]\n"
"v_cvt_pk_bf16_f32 v242,v244,v245\n"
"v_cvt_pk_bf16_f32 v243,v246,v247\n"
"v_mfma_f32_16x16x32_bf16 a[172:175],a[92:95],v[124:127],a[172:175]\n"
"v_mfma_f32_16x16x32_bf16 a[176:179],a[32:35],v[112:115],a[176:179]\n"
"v_mfma_f32_16x16x32_bf16 a[176:179],a[36:39],v[116:119],a[176:179]\n"
"v_mfma_f32_16x16x32_bf16 a[176:179],a[96:99],v[120:123],a[176:179]\n"
"v_mfma_f32_16x16x32_bf16 a[176:179],a[100:103],v[124:127],a[176:179]\n"
"v_mfma_f32_16x16x32_bf16 a[180:183],a[40:43],v[112:115],a[180:183]\n"
"v_mfma_f32_16x16x32_bf16 a[180:183],a[44:47],v[116:119],a[180:183]\n"
"v_mfma_f32_16x16x32_bf16 a[180:183],a[104:107],v[120:123],a[180:183]\n"
"s_mov_b32 s59,0x3000\n"
"v_mfma_f32_16x16x32_bf16 a[180:183],a[108:111],v[124:127],a[180:183]\n"
"buffer_store_dwordx4 v[232:235],v52,s[44:47],s59 offen offset:1024 sc1\n"
"v_mfma_f32_16x16x32_bf16 a[184:187],a[48:51],v[112:115],a[184:187]\n"
"v_cvt_pk_bf16_f32 v248,v248,v249\n"
"v_cvt_pk_bf16_f32 v249,v250,v251\n"
"v_mfma_f32_16x16x32_bf16 a[184:187],a[52:55],v[116:119],a[184:187]\n"
"v_cvt_pk_bf16_f32 v250,v252,v253\n"
"v_cvt_pk_bf16_f32 v251,v254,v255\n"
"v_mfma_f32_16x16x32_bf16 a[184:187],a[112:115],v[120:123],a[184:187]\n"
"v_mfma_f32_16x16x32_bf16 a[184:187],a[116:119],v[124:127],a[184:187]\n"
"v_mfma_f32_16x16x32_bf16 a[188:191],a[56:59],v[112:115],a[188:191]\n"
"v_mfma_f32_16x16x32_bf16 a[188:191],a[60:63],v[116:119],a[188:191]\n"
"v_mfma_f32_16x16x32_bf16 a[188:191],a[120:123],v[120:123],a[188:191]\n"
"v_mfma_f32_16x16x32_bf16 a[188:191],a[124:127],v[124:127],a[188:191]\n"
"s_waitcnt lgkmcnt(0)\n"
"v_mfma_f32_16x16x32_bf16 a[192:195],a[0:3],v[96:99],a[192:195]\n"
"ds_read_b128 v[112:115],v10 offset:14336\n"
"v_mfma_f32_16x16x32_bf16 a[192:195],a[4:7],v[100:103],a[192:195]\n"
"s_mov_b32 s59,0x3000\n"
"v_mfma_f32_16x16x32_bf16 a[192:195],a[64:67],v[104:107],a[192:195]\n"
"ds_read_b128 v[116:119],v11 offset:14336\n"
"buffer_store_dwordx4 v[240:243],v52,s[44:47],s59 offen offset:2048 sc1\n"
"v_mfma_f32_16x16x32_bf16 a[192:195],a[68:71],v[108:111],a[192:195]\n"
"v_accvgpr_read_b32 v56,a128\n"
"v_accvgpr_read_b32 v57,a129\n"
"v_mfma_f32_16x16x32_bf16 a[196:199],a[8:11],v[96:99],a[196:199]\n"
"ds_read_b128 v[120:123],v8 offset:14336\n"
"v_accvgpr_read_b32 v58,a130\n"
"v_mfma_f32_16x16x32_bf16 a[196:199],a[12:15],v[100:103],a[196:199]\n"
"v_accvgpr_read_b32 v59,a131\n"
"v_accvgpr_read_b32 v60,a132\n"
"v_mfma_f32_16x16x32_bf16 a[196:199],a[72:75],v[104:107],a[196:199]\n"
"ds_read_b128 v[124:127],v9 offset:14336\n"
"v_accvgpr_read_b32 v61,a133\n"
"v_mfma_f32_16x16x32_bf16 a[196:199],a[76:79],v[108:111],a[196:199]\n"
"v_accvgpr_read_b32 v62,a134\n"
"v_accvgpr_read_b32 v63,a135\n"
"v_mfma_f32_16x16x32_bf16 a[200:203],a[16:19],v[96:99],a[200:203]\n"
"v_cvt_pk_bf16_f32 v56,v56,v57\n"
"v_cvt_pk_bf16_f32 v57,v58,v59\n"
"v_mfma_f32_16x16x32_bf16 a[200:203],a[20:23],v[100:103],a[200:203]\n"
"v_cvt_pk_bf16_f32 v58,v60,v61\n"
"v_cvt_pk_bf16_f32 v59,v62,v63\n"
"v_mfma_f32_16x16x32_bf16 a[200:203],a[80:83],v[104:107],a[200:203]\n"
"v_mfma_f32_16x16x32_bf16 a[200:203],a[84:87],v[108:111],a[200:203]\n"
"v_mfma_f32_16x16x32_bf16 a[204:207],a[24:27],v[96:99],a[204:207]\n"
"s_mov_b32 s59,0x3000\n"
"v_mfma_f32_16x16x32_bf16 a[204:207],a[28:31],v[100:103],a[204:207]\n"
"buffer_store_dwordx4 v[248:251],v52,s[44:47],s59 offen offset:3072 sc1\n"
"v_mfma_f32_16x16x32_bf16 a[204:207],a[88:91],v[104:107],a[204:207]\n"
"v_accvgpr_read_b32 v64,a136\n"
"v_accvgpr_read_b32 v65,a137\n"
"v_mfma_f32_16x16x32_bf16 a[204:207],a[92:95],v[108:111],a[204:207]\n"
"v_accvgpr_read_b32 v66,a138\n"
"v_accvgpr_read_b32 v67,a139\n"
"v_mfma_f32_16x16x32_bf16 a[208:211],a[32:35],v[96:99],a[208:211]\n"
"v_accvgpr_read_b32 v68,a140\n"
"v_accvgpr_read_b32 v69,a141\n"
"v_mfma_f32_16x16x32_bf16 a[208:211],a[36:39],v[100:103],a[208:211]\n"
"s_waitcnt lgkmcnt(0)\n"
"v_accvgpr_read_b32 v70,a142\n"
"v_accvgpr_read_b32 v71,a143\n"
"v_mfma_f32_16x16x32_bf16 a[208:211],a[96:99],v[104:107],a[208:211]\n"
"s_waitcnt vmcnt(32)\n"
"s_barrier\n"
"v_cvt_pk_bf16_f32 v64,v64,v65\n"
"v_cvt_pk_bf16_f32 v65,v66,v67\n"
"v_mfma_f32_16x16x32_bf16 a[208:211],a[100:103],v[108:111],a[208:211]\n"
"v_cvt_pk_bf16_f32 v66,v68,v69\n"
"v_cvt_pk_bf16_f32 v67,v70,v71\n"
"v_mfma_f32_16x16x32_bf16 a[212:215],a[40:43],v[96:99],a[212:215]\n"
"v_mfma_f32_16x16x32_bf16 a[212:215],a[44:47],v[100:103],a[212:215]\n"
"v_mfma_f32_16x16x32_bf16 a[212:215],a[104:107],v[104:107],a[212:215]\n"
"v_mfma_f32_16x16x32_bf16 a[212:215],a[108:111],v[108:111],a[212:215]\n"
"s_mov_b32 s59,0x10000\n"
"v_mfma_f32_16x16x32_bf16 a[216:219],a[48:51],v[96:99],a[216:219]\n"
"buffer_store_dwordx4 v[56:59],v52,s[44:47],s59 offen offset:0 sc1\n"
"v_mfma_f32_16x16x32_bf16 a[216:219],a[52:55],v[100:103],a[216:219]\n"
"s_mov_b32 s53,0x180000\n"
"v_accvgpr_read_b32 v56,a144\n"
"v_accvgpr_read_b32 v57,a145\n"
"v_mfma_f32_16x16x32_bf16 a[216:219],a[112:115],v[104:107],a[216:219]\n"
"s_add_u32 m0,s57,0x20000\n"
"v_accvgpr_read_b32 v58,a146\n"
"v_accvgpr_read_b32 v59,a147\n"
"v_mfma_f32_16x16x32_bf16 a[216:219],a[116:119],v[108:111],a[216:219]\n"
"buffer_load_dwordx4 v32,s[64:67],s53 offen offset:0 lds\n"
"v_mfma_f32_16x16x32_bf16 a[220:223],a[56:59],v[96:99],a[220:223]\n"
"v_accvgpr_read_b32 v60,a148\n"
"v_accvgpr_read_b32 v61,a149\n"
"v_mfma_f32_16x16x32_bf16 a[220:223],a[60:63],v[100:103],a[220:223]\n"
"v_accvgpr_read_b32 v62,a150\n"
"v_accvgpr_read_b32 v63,a151\n"
"v_mfma_f32_16x16x32_bf16 a[220:223],a[120:123],v[104:107],a[220:223]\n"
"buffer_load_dwordx4 v33,s[64:67],s53 offen offset:1024 lds\n"
"v_mfma_f32_16x16x32_bf16 a[220:223],a[124:127],v[108:111],a[220:223]\n"
"v_cvt_pk_bf16_f32 v56,v56,v57\n"
"v_cvt_pk_bf16_f32 v57,v58,v59\n"
"v_mfma_f32_16x16x32_bf16 a[224:227],a[0:3],v[112:115],a[224:227]\n"
"v_cvt_pk_bf16_f32 v58,v60,v61\n"
"v_cvt_pk_bf16_f32 v59,v62,v63\n"
"v_mfma_f32_16x16x32_bf16 a[224:227],a[4:7],v[116:119],a[224:227]\n"
"v_mfma_f32_16x16x32_bf16 a[224:227],a[64:67],v[120:123],a[224:227]\n"
"s_mov_b32 s59,0x10000\n"
"v_mfma_f32_16x16x32_bf16 a[224:227],a[68:71],v[124:127],a[224:227]\n"
"buffer_store_dwordx4 v[64:67],v52,s[44:47],s59 offen offset:1024 sc1\n"
"v_mfma_f32_16x16x32_bf16 a[228:231],a[8:11],v[112:115],a[228:231]\n"
"s_nop 0\n"
"v_accvgpr_read_b32 v64,a152\n"
"v_accvgpr_read_b32 v65,a153\n"
"v_mfma_f32_16x16x32_bf16 a[228:231],a[12:15],v[116:119],a[228:231]\n"
"v_accvgpr_read_b32 v66,a154\n"
"v_accvgpr_read_b32 v67,a155\n"
"v_mfma_f32_16x16x32_bf16 a[228:231],a[72:75],v[120:123],a[228:231]\n"
"buffer_load_dwordx4 v34,s[64:67],s53 offen offset:2048 lds\n"
"v_mfma_f32_16x16x32_bf16 a[228:231],a[76:79],v[124:127],a[228:231]\n"
"v_accvgpr_read_b32 v68,a156\n"
"v_accvgpr_read_b32 v69,a157\n"
"v_mfma_f32_16x16x32_bf16 a[232:235],a[16:19],v[112:115],a[232:235]\n"
"v_accvgpr_read_b32 v70,a158\n"
"v_accvgpr_read_b32 v71,a159\n"
"v_mfma_f32_16x16x32_bf16 a[232:235],a[20:23],v[116:119],a[232:235]\n"
"buffer_load_dwordx4 v35,s[64:67],s53 offen offset:3072 lds\n"
"v_mfma_f32_16x16x32_bf16 a[232:235],a[80:83],v[120:123],a[232:235]\n"
"v_cvt_pk_bf16_f32 v64,v64,v65\n"
"v_cvt_pk_bf16_f32 v65,v66,v67\n"
"v_mfma_f32_16x16x32_bf16 a[232:235],a[84:87],v[124:127],a[232:235]\n"
"v_cvt_pk_bf16_f32 v66,v68,v69\n"
"v_cvt_pk_bf16_f32 v67,v70,v71\n"
"v_mfma_f32_16x16x32_bf16 a[236:239],a[24:27],v[112:115],a[236:239]\n"
"v_mfma_f32_16x16x32_bf16 a[236:239],a[28:31],v[116:119],a[236:239]\n"
"s_mov_b32 s59,0x10000\n"
"v_mfma_f32_16x16x32_bf16 a[236:239],a[88:91],v[120:123],a[236:239]\n"
"buffer_store_dwordx4 v[56:59],v52,s[44:47],s59 offen offset:2048 sc1\n"
"v_mfma_f32_16x16x32_bf16 a[236:239],a[92:95],v[124:127],a[236:239]\n"
"v_mfma_f32_16x16x32_bf16 a[240:243],a[32:35],v[112:115],a[240:243]\n"
"s_add_u32 m0,s57,0x21000\n"
"ds_read_b64_tr_b16 a[0:1],v12 offset:0\n"
"v_mfma_f32_16x16x32_bf16 a[240:243],a[36:39],v[116:119],a[240:243]\n"
"ds_read_b64_tr_b16 a[2:3],v12 offset:2048\n"
"buffer_load_dwordx4 v36,s[64:67],s53 offen offset:0 lds\n"
"v_mfma_f32_16x16x32_bf16 a[240:243],a[96:99],v[120:123],a[240:243]\n"
"ds_read_b64_tr_b16 a[4:5],v12 offset:16384\n"
"v_mfma_f32_16x16x32_bf16 a[240:243],a[100:103],v[124:127],a[240:243]\n"
"ds_read_b64_tr_b16 a[6:7],v12 offset:18432\n"
"v_mfma_f32_16x16x32_bf16 a[244:247],a[40:43],v[112:115],a[244:247]\n"
"ds_read_b64_tr_b16 a[8:9],v13 offset:0\n"
"buffer_load_dwordx4 v37,s[64:67],s53 offen offset:1024 lds\n"
"v_mfma_f32_16x16x32_bf16 a[244:247],a[44:47],v[116:119],a[244:247]\n"
"ds_read_b64_tr_b16 a[10:11],v13 offset:2048\n"
"v_mfma_f32_16x16x32_bf16 a[244:247],a[104:107],v[120:123],a[244:247]\n"
"ds_read_b64_tr_b16 a[12:13],v13 offset:16384\n"
"v_mfma_f32_16x16x32_bf16 a[244:247],a[108:111],v[124:127],a[244:247]\n"
"ds_read_b64_tr_b16 a[14:15],v13 offset:18432\n"
"buffer_load_dwordx4 v38,s[64:67],s53 offen offset:2048 lds\n"
"v_mfma_f32_16x16x32_bf16 a[248:251],a[48:51],v[112:115],a[248:251]\n"
"ds_read_b64_tr_b16 a[16:17],v14 offset:0\n"
"v_mfma_f32_16x16x32_bf16 a[248:251],a[52:55],v[116:119],a[248:251]\n"
"s_mov_b32 s59,0x10000\n"
"ds_read_b64_tr_b16 a[18:19],v14 offset:2048\n"
"v_mfma_f32_16x16x32_bf16 a[248:251],a[112:115],v[120:123],a[248:251]\n"
"ds_read_b64_tr_b16 a[20:21],v14 offset:16384\n"
"buffer_store_dwordx4 v[64:67],v52,s[44:47],s59 offen offset:3072 sc1\n"
"v_mfma_f32_16x16x32_bf16 a[248:251],a[116:119],v[124:127],a[248:251]\n"
"ds_read_b64_tr_b16 a[22:23],v14 offset:18432\n"
"v_mfma_f32_16x16x32_bf16 a[252:255],a[56:59],v[112:115],a[252:255]\n"
"ds_read_b64_tr_b16 a[24:25],v15 offset:0\n"
"v_mfma_f32_16x16x32_bf16 a[252:255],a[60:63],v[116:119],a[252:255]\n"
"ds_read_b64_tr_b16 a[26:27],v15 offset:2048\n"
"buffer_load_dwordx4 v39,s[64:67],s53 offen offset:3072 lds\n"
"v_mfma_f32_16x16x32_bf16 a[252:255],a[120:123],v[120:123],a[252:255]\n"
"ds_read_b64_tr_b16 a[28:29],v15 offset:16384\n"
"v_mfma_f32_16x16x32_bf16 a[252:255],a[124:127],v[124:127],a[252:255]\n"
"s_waitcnt lgkmcnt(11)\n"
"ds_read_b64_tr_b16 a[30:31],v15 offset:18432\n"
"v_mfma_f32_16x16x32_bf16 v[128:131],a[0:3],v[72:75],0\n"
"ds_read_b64_tr_b16 a[32:33],v16 offset:0\n"
"v_accvgpr_read_b32 v56,a160\n"
"v_accvgpr_read_b32 v57,a161\n"
"v_mfma_f32_16x16x32_bf16 v[128:131],a[4:7],v[80:83],v[128:131]\n"
"ds_read_b64_tr_b16 a[34:35],v16 offset:2048\n"
"buffer_load_dwordx4 a[40:43],v52,s[60:63],s86 offen offset:1024 sc1\n"
"v_mfma_f32_16x16x32_bf16 v[136:139],a[0:3],v[76:79],0\n"
"ds_read_b64_tr_b16 a[36:37],v16 offset:16384\n"
"v_accvgpr_read_b32 v58,a162\n"
"v_accvgpr_read_b32 v59,a163\n"
"v_mfma_f32_16x16x32_bf16 v[136:139],a[4:7],v[84:87],v[136:139]\n"
"s_waitcnt lgkmcnt(11)\n"
"ds_read_b64_tr_b16 a[38:39],v16 offset:18432\n"
"v_accvgpr_read_b32 v60,a164\n"
"v_accvgpr_read_b32 v61,a165\n"
"v_mfma_f32_16x16x32_bf16 v[132:135],a[8:11],v[72:75],0\n"
"ds_read_b64_tr_b16 a[0:1],v17 offset:0\n"
"v_accvgpr_read_b32 v62,a166\n"
"v_accvgpr_read_b32 v63,a167\n"
"v_mfma_f32_16x16x32_bf16 v[132:135],a[12:15],v[80:83],v[132:135]\n"
"ds_read_b64_tr_b16 a[2:3],v17 offset:2048\n"
"buffer_load_dwordx4 a[44:47],v51,s[60:63],s86 offen offset:1024 sc1\n"
"v_mfma_f32_16x16x32_bf16 v[140:143],a[8:11],v[76:79],0\n"
"ds_read_b64_tr_b16 a[4:5],v17 offset:16384\n"
"v_cvt_pk_bf16_f32 v56,v56,v57\n"
"v_cvt_pk_bf16_f32 v57,v58,v59\n"
"v_mfma_f32_16x16x32_bf16 v[140:143],a[12:15],v[84:87],v[140:143]\n"
"s_waitcnt lgkmcnt(11)\n"
"ds_read_b64_tr_b16 a[6:7],v17 offset:18432\n"
"v_cvt_pk_bf16_f32 v58,v60,v61\n"
"v_mfma_f32_16x16x32_bf16 v[144:147],a[16:19],v[72:75],0\n"
"s_mov_b32 s59,0x11000\n"
"ds_read_b64_tr_b16 a[8:9],v18 offset:0\n"
"v_cvt_pk_bf16_f32 v59,v62,v63\n"
"v_accvgpr_read_b32 v64,a168\n"
"v_mfma_f32_16x16x32_bf16 v[144:147],a[20:23],v[80:83],v[144:147]\n"
"ds_read_b64_tr_b16 a[10:11],v18 offset:2048\n"
"buffer_store_dwordx4 v[56:59],v52,s[44:47],s59 offen offset:0 sc1\n"
"v_mfma_f32_16x16x32_bf16 v[152:155],a[16:19],v[76:79],0\n"
"ds_read_b64_tr_b16 a[12:13],v18 offset:16384\n"
"v_accvgpr_read_b32 v65,a169\n"
"v_accvgpr_read_b32 v66,a170\n"
"v_mfma_f32_16x16x32_bf16 v[152:155],a[20:23],v[84:87],v[152:155]\n"
"s_waitcnt lgkmcnt(11)\n"
"ds_read_b64_tr_b16 a[14:15],v18 offset:18432\n"
"v_accvgpr_read_b32 v67,a171\n"
"v_accvgpr_read_b32 v68,a172\n"
"v_mfma_f32_16x16x32_bf16 v[148:151],a[24:27],v[72:75],0\n"
"ds_read_b64_tr_b16 a[16:17],v19 offset:0\n"
"v_accvgpr_read_b32 v69,a173\n"
"v_mfma_f32_16x16x32_bf16 v[148:151],a[28:31],v[80:83],v[148:151]\n"
"ds_read_b64_tr_b16 a[18:19],v19 offset:2048\n"
"buffer_load_dwordx4 a[48:51],v52,s[60:63],s86 offen offset:2048 sc1\n"
"v_mfma_f32_16x16x32_bf16 v[156:159],a[24:27],v[76:79],0\n"
"ds_read_b64_tr_b16 a[20:21],v19 offset:16384\n"
"v_accvgpr_read_b32 v70,a174\n"
"v_mfma_f32_16x16x32_bf16 v[156:159],a[28:31],v[84:87],v[156:159]\n"
"s_waitcnt lgkmcnt(11)\n"
"ds_read_b64_tr_b16 a[22:23],v19 offset:18432\n"
"v_accvgpr_read_b32 v71,a175\n"
"v_mfma_f32_16x16x32_bf16 v[160:163],a[32:35],v[72:75],0\n"
"ds_read_b64_tr_b16 a[24:25],v12 offset:256\n"
"v_cvt_pk_bf16_f32 v64,v64,v65\n"
"v_mfma_f32_16x16x32_bf16 v[160:163],a[36:39],v[80:83],v[160:163]\n"
"ds_read_b64_tr_b16 a[26:27],v12 offset:2304\n"
"v_cvt_pk_bf16_f32 v65,v66,v67\n"
"v_mfma_f32_16x16x32_bf16 v[168:171],a[32:35],v[76:79],0\n"
"s_mov_b32 s59,0x11000\n"
"ds_read_b64_tr_b16 a[28:29],v12 offset:16640\n"
"v_cvt_pk_bf16_f32 v66,v68,v69\n"
"v_mfma_f32_16x16x32_bf16 v[168:171],a[36:39],v[84:87],v[168:171]\n"
"s_waitcnt lgkmcnt(11)\n"
"ds_read_b64_tr_b16 a[30:31],v12 offset:18688\n"
"v_cvt_pk_bf16_f32 v67,v70,v71\n"
"v_accvgpr_read_b32 v56,a176\n"
"v_mfma_f32_16x16x32_bf16 v[164:167],a[0:3],v[72:75],0\n"
"ds_read_b64_tr_b16 a[32:33],v13 offset:256\n"
"buffer_store_dwordx4 v[64:67],v52,s[44:47],s59 offen offset:1024 sc1\n"
"v_mfma_f32_16x16x32_bf16 v[164:167],a[4:7],v[80:83],v[164:167]\n"
"ds_read_b64_tr_b16 a[34:35],v13 offset:2304\n"
"v_accvgpr_read_b32 v57,a177\n"
"v_accvgpr_read_b32 v58,a178\n"
"v_mfma_f32_16x16x32_bf16 v[172:175],a[0:3],v[76:79],0\n"
"ds_read_b64_tr_b16 a[36:37],v13 offset:16640\n"
"v_accvgpr_read_b32 v59,a179\n"
"v_accvgpr_read_b32 v60,a180\n"
"v_mfma_f32_16x16x32_bf16 v[172:175],a[4:7],v[84:87],v[172:175]\n"
"s_waitcnt lgkmcnt(11)\n"
"ds_read_b64_tr_b16 a[38:39],v13 offset:18688\n"
"v_accvgpr_read_b32 v61,a181\n"
"v_mfma_f32_16x16x32_bf16 v[176:179],a[8:11],v[72:75],0\n"
"ds_read_b64_tr_b16 a[0:1],v14 offset:256\n"
"buffer_load_dwordx4 a[52:55],v51,s[60:63],s86 offen offset:2048 sc1\n"
"v_mfma_f32_16x16x32_bf16 v[176:179],a[12:15],v[80:83],v[176:179]\n"
"ds_read_b64_tr_b16 a[2:3],v14 offset:2304\n"
"v_accvgpr_read_b32 v62,a182\n"
"v_mfma_f32_16x16x32_bf16 v[184:187],a[8:11],v[76:79],0\n"
"ds_read_b64_tr_b16 a[4:5],v14 offset:16640\n"
"v_accvgpr_read_b32 v63,a183\n"
"v_mfma_f32_16x16x32_bf16 v[184:187],a[12:15],v[84:87],v[184:187]\n"
"s_waitcnt lgkmcnt(11)\n"
"ds_read_b64_tr_b16 a[6:7],v14 offset:18688\n"
"v_cvt_pk_bf16_f32 v56,v56,v57\n"
"v_mfma_f32_16x16x32_bf16 v[180:183],a[16:19],v[72:75],0\n"
"ds_read_b64_tr_b16 a[8:9],v15 offset:256\n"
"v_cvt_pk_bf16_f32 v57,v58,v59\n"
"v_mfma_f32_16x16x32_bf16 v[180:183],a[20:23],v[80:83],v[180:183]\n"
"ds_read_b64_tr_b16 a[10:11],v15 offset:2304\n"
"v_cvt_pk_bf16_f32 v58,v60,v61\n"
"v_mfma_f32_16x16x32_bf16 v[188:191],a[16:19],v[76:79],0\n"
"s_mov_b32 s59,0x11000\n"
"ds_read_b64_tr_b16 a[12:13],v15 offset:16640\n"
"v_cvt_pk_bf16_f32 v59,v62,v63\n"
"v_accvgpr_read_b32 v64,a184\n"
"v_mfma_f32_16x16x32_bf16 v[188:191],a[20:23],v[84:87],v[188:191]\n"
"s_waitcnt lgkmcnt(11)\n"
"ds_read_b64_tr_b16 a[14:15],v15 offset:18688\n"
"buffer_store_dwordx4 v[56:59],v52,s[44:47],s59 offen offset:2048 sc1\n"
"v_mfma_f32_16x16x32_bf16 v[192:195],a[24:27],v[72:75],0\n"
"ds_read_b64_tr_b16 a[16:17],v16 offset:256\n"
"v_accvgpr_read_b32 v65,a185\n"
"v_accvgpr_read_b32 v66,a186\n"
"v_mfma_f32_16x16x32_bf16 v[192:195],a[28:31],v[80:83],v[192:195]\n"
"ds_read_b64_tr_b16 a[18:19],v16 offset:2304\n"
"v_accvgpr_read_b32 v67,a187\n"
"v_accvgpr_read_b32 v68,a188\n"
"v_mfma_f32_16x16x32_bf16 v[200:203],a[24:27],v[76:79],0\n"
"ds_read_b64_tr_b16 a[20:21],v16 offset:16640\n"
"v_accvgpr_read_b32 v69,a189\n"
"v_mfma_f32_16x16x32_bf16 v[200:203],a[28:31],v[84:87],v[200:203]\n"
"s_waitcnt lgkmcnt(11)\n"
"ds_read_b64_tr_b16 a[22:23],v16 offset:18688\n"
"buffer_load_dwordx4 a[56:59],v52,s[60:63],s86 offen offset:3072 sc1\n"
"v_mfma_f32_16x16x32_bf16 v[196:199],a[32:35],v[72:75],0\n"
"ds_read_b64_tr_b16 a[24:25],v17 offset:256\n"
"v_accvgpr_read_b32 v70,a190\n"
"v_mfma_f32_16x16x32_bf16 v[196:199],a[36:39],v[80:83],v[196:199]\n"
"ds_read_b64_tr_b16 a[26:27],v17 offset:2304\n"
"v_accvgpr_read_b32 v71,a191\n"
"v_mfma_f32_16x16x32_bf16 v[204:207],a[32:35],v[76:79],0\n"
"ds_read_b64_tr_b16 a[28:29],v17 offset:16640\n"
"v_cvt_pk_bf16_f32 v64,v64,v65\n"
"v_mfma_f32_16x16x32_bf16 v[204:207],a[36:39],v[84:87],v[204:207]\n"
"s_waitcnt lgkmcnt(11)\n"
"ds_read_b64_tr_b16 a[30:31],v17 offset:18688\n"
"v_cvt_pk_bf16_f32 v65,v66,v67\n"
"v_mfma_f32_16x16x32_bf16 v[208:211],a[0:3],v[72:75],0\n"
"ds_read_b64_tr_b16 a[32:33],v18 offset:256\n"
"v_cvt_pk_bf16_f32 v66,v68,v69\n"
"v_mfma_f32_16x16x32_bf16 v[208:211],a[4:7],v[80:83],v[208:211]\n"
"s_mov_b32 s59,0x11000\n"
"ds_read_b64_tr_b16 a[34:35],v18 offset:2304\n"
"v_cvt_pk_bf16_f32 v67,v70,v71\n"
"v_accvgpr_read_b32 v56,a192\n"
"v_mfma_f32_16x16x32_bf16 v[216:219],a[0:3],v[76:79],0\n"
"ds_read_b64_tr_b16 a[36:37],v18 offset:16640\n"
"buffer_store_dwordx4 v[64:67],v52,s[44:47],s59 offen offset:3072 sc1\n"
"v_mfma_f32_16x16x32_bf16 v[216:219],a[4:7],v[84:87],v[216:219]\n"
"s_waitcnt lgkmcnt(11)\n"
"ds_read_b64_tr_b16 a[38:39],v18 offset:18688\n"
"v_accvgpr_read_b32 v57,a193\n"
"v_accvgpr_read_b32 v58,a194\n"
"v_mfma_f32_16x16x32_bf16 v[212:215],a[8:11],v[72:75],0\n"
"ds_read_b64_tr_b16 a[0:1],v19 offset:256\n"
"v_accvgpr_read_b32 v59,a195\n"
"v_accvgpr_read_b32 v60,a196\n"
"v_mfma_f32_16x16x32_bf16 v[212:215],a[12:15],v[80:83],v[212:215]\n"
"ds_read_b64_tr_b16 a[2:3],v19 offset:2304\n"
"v_accvgpr_read_b32 v61,a197\n"
"v_mfma_f32_16x16x32_bf16 v[220:223],a[8:11],v[76:79],0\n"
"s_waitcnt vmcnt(29)\n"
"s_barrier\n"
"ds_read_b64_tr_b16 a[4:5],v19 offset:16640\n"
"buffer_load_dwordx4 a[60:63],v51,s[60:63],s86 offen offset:3072 sc1\n"
"v_mfma_f32_16x16x32_bf16 v[220:223],a[12:15],v[84:87],v[220:223]\n"
"s_waitcnt lgkmcnt(11)\n"
"ds_read_b64_tr_b16 a[6:7],v19 offset:18688\n"
"v_accvgpr_read_b32 v62,a198\n"
"v_mfma_f32_16x16x32_bf16 v[224:227],a[16:19],v[72:75],0\n"
"ds_read_b64_tr_b16 a[8:9],v12 offset:32768\n"
"v_accvgpr_read_b32 v63,a199\n"
"v_mfma_f32_16x16x32_bf16 v[224:227],a[20:23],v[80:83],v[224:227]\n"
"ds_read_b64_tr_b16 a[10:11],v12 offset:34816\n"
"v_cvt_pk_bf16_f32 v56,v56,v57\n"
"v_mfma_f32_16x16x32_bf16 v[232:235],a[16:19],v[76:79],0\n"
"ds_read_b64_tr_b16 a[12:13],v12 offset:49152\n"
"v_cvt_pk_bf16_f32 v57,v58,v59\n"
"v_mfma_f32_16x16x32_bf16 v[232:235],a[20:23],v[84:87],v[232:235]\n"
"s_waitcnt lgkmcnt(11)\n"
"ds_read_b64_tr_b16 a[14:15],v12 offset:51200\n"
"v_cvt_pk_bf16_f32 v58,v60,v61\n"
"v_mfma_f32_16x16x32_bf16 v[228:231],a[24:27],v[72:75],0\n"
"s_mov_b32 s59,0x12000\n"
"ds_read_b64_tr_b16 a[16:17],v13 offset:32768\n"
"v_cvt_pk_bf16_f32 v59,v62,v63\n"
"v_accvgpr_read_b32 v64,a200\n"
"v_mfma_f32_16x16x32_bf16 v[228:231],a[28:31],v[80:83],v[228:231]\n"
"ds_read_b64_tr_b16 a[18:19],v13 offset:34816\n"
"buffer_store_dwordx4 v[56:59],v52,s[44:47],s59 offen offset:0 sc1\n"
"v_mfma_f32_16x16x32_bf16 v[236:239],a[24:27],v[76:79],0\n"
"s_mov_b32 s53,0x200000\n"
"ds_read_b64_tr_b16 a[20:21],v13 offset:49152\n"
"v_accvgpr_read_b32 v65,a201\n"
"v_accvgpr_read_b32 v66,a202\n"
"v_mfma_f32_16x16x32_bf16 v[236:239],a[28:31],v[84:87],v[236:239]\n"
"s_waitcnt lgkmcnt(11)\n"
"ds_read_b64_tr_b16 a[22:23],v13 offset:51200\n"
"v_accvgpr_read_b32 v67,a203\n"
"v_accvgpr_read_b32 v68,a204\n"
"v_mfma_f32_16x16x32_bf16 v[240:243],a[32:35],v[72:75],0\n"
"s_add_u32 m0,s57,0x10000\n"
"ds_read_b64_tr_b16 a[24:25],v14 offset:32768\n"
"v_accvgpr_read_b32 v69,a205\n"
"v_mfma_f32_16x16x32_bf16 v[240:243],a[36:39],v[80:83],v[240:243]\n"
"ds_read_b64_tr_b16 a[26:27],v14 offset:34816\n"
"buffer_load_dwordx4 v32,s[64:67],s53 offen offset:0 lds\n"
"v_mfma_f32_16x16x32_bf16 v[248:251],a[32:35],v[76:79],0\n"
"ds_read_b64_tr_b16 a[28:29],v14 offset:49152\n"
"v_accvgpr_read_b32 v70,a206\n"
"v_mfma_f32_16x16x32_bf16 v[248:251],a[36:39],v[84:87],v[248:251]\n"
"s_waitcnt lgkmcnt(11)\n"
"ds_read_b64_tr_b16 a[30:31],v14 offset:51200\n"
"v_accvgpr_read_b32 v71,a207\n"
"v_mfma_f32_16x16x32_bf16 v[244:247],a[0:3],v[72:75],0\n"
"ds_read_b64_tr_b16 a[32:33],v15 offset:32768\n"
"v_cvt_pk_bf16_f32 v64,v64,v65\n"
"v_mfma_f32_16x16x32_bf16 v[244:247],a[4:7],v[80:83],v[244:247]\n"
"s_mov_b32 s59,0x12000\n"
"ds_read_b64_tr_b16 a[34:35],v15 offset:34816\n"
"v_cvt_pk_bf16_f32 v65,v66,v67\n"
"v_mfma_f32_16x16x32_bf16 v[252:255],a[0:3],v[76:79],0\n"
"ds_read_b64_tr_b16 a[36:37],v15 offset:49152\n"
"v_cvt_pk_bf16_f32 v66,v68,v69\n"
"v_mfma_f32_16x16x32_bf16 v[252:255],a[4:7],v[84:87],v[252:255]\n"
"s_waitcnt lgkmcnt(11)\n"
"ds_read_b64_tr_b16 a[38:39],v15 offset:51200\n"
"v_cvt_pk_bf16_f32 v67,v70,v71\n"
"v_accvgpr_read_b32 v56,a208\n"
"v_mfma_f32_16x16x32_bf16 v[128:131],a[8:11],v[88:91],v[128:131]\n"
"ds_read_b64_tr_b16 a[0:1],v16 offset:32768\n"
"buffer_store_dwordx4 v[64:67],v52,s[44:47],s59 offen offset:1024 sc1\n"
"v_mfma_f32_16x16x32_bf16 v[128:131],a[12:15],v[24:27],v[128:131]\n"
"ds_read_b64_tr_b16 a[2:3],v16 offset:34816\n"
"v_accvgpr_read_b32 v57,a209\n"
"v_accvgpr_read_b32 v58,a210\n"
"v_mfma_f32_16x16x32_bf16 v[136:139],a[8:11],v[92:95],v[136:139]\n"
"ds_read_b64_tr_b16 a[4:5],v16 offset:49152\n"
"v_accvgpr_read_b32 v59,a211\n"
"v_accvgpr_read_b32 v60,a212\n"
"v_mfma_f32_16x16x32_bf16 v[136:139],a[12:15],v[28:31],v[136:139]\n"
"s_waitcnt lgkmcnt(11)\n"
"ds_read_b64_tr_b16 a[6:7],v16 offset:51200\n"
"v_accvgpr_read_b32 v61,a213\n"
"v_mfma_f32_16x16x32_bf16 v[132:135],a[16:19],v[88:91],v[132:135]\n"
"ds_read_b64_tr_b16 a[8:9],v17 offset:32768\n"
"buffer_load_dwordx4 v33,s[64:67],s53 offen offset:1024 lds\n"
"v_mfma_f32_16x16x32_bf16 v[132:135],a[20:23],v[24:27],v[132:135]\n"
"ds_read_b64_tr_b16 a[10:11],v17 offset:34816\n"
"v_accvgpr_read_b32 v62,a214\n"
"v_mfma_f32_16x16x32_bf16 v[140:143],a[16:19],v[92:95],v[140:143]\n"
"ds_read_b64_tr_b16 a[12:13],v17 offset:49152\n"
"v_accvgpr_read_b32 v63,a215\n"
"v_mfma_f32_16x16x32_bf16 v[140:143],a[20:23],v[28:31],v[140:143]\n"
"s_waitcnt lgkmcnt(11)\n"
"ds_read_b64_tr_b16 a[14:15],v17 offset:51200\n"
"v_cvt_pk_bf16_f32 v56,v56,v57\n"
"v_mfma_f32_16x16x32_bf16 v[144:147],a[24:27],v[88:91],v[144:147]\n"
"ds_read_b64_tr_b16 a[16:17],v18 offset:32768\n"
"v_cvt_pk_bf16_f32 v57,v58,v59\n"
"v_mfma_f32_16x16x32_bf16 v[144:147],a[28:31],v[24:27],v[144:147]\n"
"ds_read_b64_tr_b16 a[18:19],v18 offset:34816\n"
"v_cvt_pk_bf16_f32 v58,v60,v61\n"
"v_mfma_f32_16x16x32_bf16 v[152:155],a[24:27],v[92:95],v[152:155]\n"
"s_mov_b32 s59,0x12000\n"
"ds_read_b64_tr_b16 a[20:21],v18 offset:49152\n"
"v_cvt_pk_bf16_f32 v59,v62,v63\n"
"v_accvgpr_read_b32 v64,a216\n"
"v_mfma_f32_16x16x32_bf16 v[152:155],a[28:31],v[28:31],v[152:155]\n"
"s_waitcnt lgkmcnt(11)\n"
"ds_read_b64_tr_b16 a[22:23],v18 offset:51200\n"
"buffer_store_dwordx4 v[56:59],v52,s[44:47],s59 offen offset:2048 sc1\n"
"v_mfma_f32_16x16x32_bf16 v[148:151],a[32:35],v[88:91],v[148:151]\n"
"ds_read_b64_tr_b16 a[24:25],v19 offset:32768\n"
"v_accvgpr_read_b32 v65,a217\n"
"v_accvgpr_read_b32 v66,a218\n"
"v_mfma_f32_16x16x32_bf16 v[148:151],a[36:39],v[24:27],v[148:151]\n"
"ds_read_b64_tr_b16 a[26:27],v19 offset:34816\n"
"v_accvgpr_read_b32 v67,a219\n"
"v_accvgpr_read_b32 v68,a220\n"
"v_mfma_f32_16x16x32_bf16 v[156:159],a[32:35],v[92:95],v[156:159]\n"
"ds_read_b64_tr_b16 a[28:29],v19 offset:49152\n"
"v_accvgpr_read_b32 v69,a221\n"
"v_accvgpr_read_b32 v70,a222\n"
"v_mfma_f32_16x16x32_bf16 v[156:159],a[36:39],v[28:31],v[156:159]\n"
"s_waitcnt lgkmcnt(11)\n"
"ds_read_b64_tr_b16 a[30:31],v19 offset:51200\n"
"buffer_load_dwordx4 v34,s[64:67],s53 offen offset:2048 lds\n"
"v_mfma_f32_16x16x32_bf16 v[160:163],a[0:3],v[88:91],v[160:163]\n"
"ds_read_b64_tr_b16 a[32:33],v12 offset:33024\n"
"v_accvgpr_read_b32 v71,a223\n"
"v_mfma_f32_16x16x32_bf16 v[160:163],a[4:7],v[24:27],v[160:163]\n"
"ds_read_b64_tr_b16 a[34:35],v12 offset:35072\n"
"v_cvt_pk_bf16_f32 v64,v64,v65\n"
"v_mfma_f32_16x16x32_bf16 v[168:171],a[0:3],v[92:95],v[168:171]\n"
"ds_read_b64_tr_b16 a[36:37],v12 offset:49408\n"
"v_cvt_pk_bf16_f32 v65,v66,v67\n"
"v_mfma_f32_16x16x32_bf16 v[168:171],a[4:7],v[28:31],v[168:171]\n"
"s_waitcnt lgkmcnt(11)\n"
"ds_read_b64_tr_b16 a[38:39],v12 offset:51456\n"
"buffer_load_dwordx4 v35,s[64:67],s53 offen offset:3072 lds\n"
"v_mfma_f32_16x16x32_bf16 v[164:167],a[8:11],v[88:91],v[164:167]\n"
"ds_read_b64_tr_b16 a[0:1],v13 offset:33024\n"
"v_cvt_pk_bf16_f32 v66,v68,v69\n"
"v_mfma_f32_16x16x32_bf16 v[164:167],a[12:15],v[24:27],v[164:167]\n"
"ds_read_b64_tr_b16 a[2:3],v13 offset:35072\n"
"v_cvt_pk_bf16_f32 v67,v70,v71\n"
"v_accvgpr_read_b32 v56,a224\n"
"v_mfma_f32_16x16x32_bf16 v[172:175],a[8:11],v[92:95],v[172:175]\n"
"s_mov_b32 s59,0x12000\n"
"ds_read_b64_tr_b16 a[4:5],v13 offset:49408\n"
"v_add_u32 v12,0x10000,v12\n"
"v_accvgpr_read_b32 v57,a225\n"
"v_mfma_f32_16x16x32_bf16 v[172:175],a[12:15],v[28:31],v[172:175]\n"
"s_waitcnt lgkmcnt(11)\n"
"ds_read_b64_tr_b16 a[6:7],v13 offset:51456\n"
"buffer_store_dwordx4 v[64:67],v52,s[44:47],s59 offen offset:3072 sc1\n"
"v_mfma_f32_16x16x32_bf16 v[176:179],a[16:19],v[88:91],v[176:179]\n"
"ds_read_b64_tr_b16 a[8:9],v14 offset:33024\n"
"v_accvgpr_read_b32 v58,a226\n"
"v_accvgpr_read_b32 v59,a227\n"
"v_mfma_f32_16x16x32_bf16 v[176:179],a[20:23],v[24:27],v[176:179]\n"
"ds_read_b64_tr_b16 a[10:11],v14 offset:35072\n"
"v_accvgpr_read_b32 v60,a228\n"
"v_accvgpr_read_b32 v61,a229\n"
"v_mfma_f32_16x16x32_bf16 v[184:187],a[16:19],v[92:95],v[184:187]\n"
"s_add_u32 m0,s57,0x11000\n"
"ds_read_b64_tr_b16 a[12:13],v14 offset:49408\n"
"v_add_u32 v13,0x10000,v13\n"
"v_accvgpr_read_b32 v62,a230\n"
"v_mfma_f32_16x16x32_bf16 v[184:187],a[20:23],v[28:31],v[184:187]\n"
"s_waitcnt lgkmcnt(11)\n"
"ds_read_b64_tr_b16 a[14:15],v14 offset:51456\n"
"buffer_load_dwordx4 v36,s[64:67],s53 offen offset:0 lds\n"
"v_mfma_f32_16x16x32_bf16 v[180:183],a[24:27],v[88:91],v[180:183]\n"
"ds_read_b64_tr_b16 a[16:17],v15 offset:33024\n"
"v_accvgpr_read_b32 v63,a231\n"
"v_cvt_pk_bf16_f32 v56,v56,v57\n"
"v_mfma_f32_16x16x32_bf16 v[180:183],a[28:31],v[24:27],v[180:183]\n"
"ds_read_b64_tr_b16 a[18:19],v15 offset:35072\n"
"v_cvt_pk_bf16_f32 v57,v58,v59\n"
"v_mfma_f32_16x16x32_bf16 v[188:191],a[24:27],v[92:95],v[188:191]\n"
"ds_read_b64_tr_b16 a[20:21],v15 offset:49408\n"
"v_add_u32 v14,0x10000,v14\n"
"v_mfma_f32_16x16x32_bf16 v[188:191],a[28:31],v[28:31],v[188:191]\n"
"s_waitcnt lgkmcnt(11)\n"
"ds_read_b64_tr_b16 a[22:23],v15 offset:51456\n"
"v_cvt_pk_bf16_f32 v58,v60,v61\n"
"v_mfma_f32_16x16x32_bf16 v[192:195],a[32:35],v[88:91],v[192:195]\n"
"s_mov_b32 s59,0x13000\n"
"ds_read_b64_tr_b16 a[24:25],v16 offset:33024\n"
"v_cvt_pk_bf16_f32 v59,v62,v63\n"
"v_accvgpr_read_b32 v64,a232\n"
"v_mfma_f32_16x16x32_bf16 v[192:195],a[36:39],v[24:27],v[192:195]\n"
"ds_read_b64_tr_b16 a[26:27],v16 offset:35072\n"
"buffer_store_dwordx4 v[56:59],v52,s[44:47],s59 offen offset:0 sc1\n"
"v_mfma_f32_16x16x32_bf16 v[200:203],a[32:35],v[92:95],v[200:203]\n"
"ds_read_b64_tr_b16 a[28:29],v16 offset:49408\n"
"v_add_u32 v15,0x10000,v15\n"
"v_accvgpr_read_b32 v65,a233\n"
"v_mfma_f32_16x16x32_bf16 v[200:203],a[36:39],v[28:31],v[200:203]\n"
"s_waitcnt lgkmcnt(11)\n"
"ds_read_b64_tr_b16 a[30:31],v16 offset:51456\n"
"v_accvgpr_read_b32 v66,a234\n"
"v_accvgpr_read_b32 v67,a235\n"
"v_mfma_f32_16x16x32_bf16 v[196:199],a[0:3],v[88:91],v[196:199]\n"
"ds_read_b64_tr_b16 a[32:33],v17 offset:33024\n"
"v_accvgpr_read_b32 v68,a236\n"
"v_accvgpr_read_b32 v69,a237\n"
"v_mfma_f32_16x16x32_bf16 v[196:199],a[4:7],v[24:27],v[196:199]\n"
"ds_read_b64_tr_b16 a[34:35],v17 offset:35072\n"
"buffer_load_dwordx4 v37,s[64:67],s53 offen offset:1024 lds\n"
"v_mfma_f32_16x16x32_bf16 v[204:207],a[0:3],v[92:95],v[204:207]\n"
"ds_read_b64_tr_b16 a[36:37],v17 offset:49408\n"
"v_add_u32 v16,0x10000,v16\n"
"v_accvgpr_read_b32 v70,a238\n"
"v_mfma_f32_16x16x32_bf16 v[204:207],a[4:7],v[28:31],v[204:207]\n"
"s_waitcnt lgkmcnt(11)\n"
"ds_read_b64_tr_b16 a[38:39],v17 offset:51456\n"
"v_accvgpr_read_b32 v71,a239\n"
"v_cvt_pk_bf16_f32 v64,v64,v65\n"
"v_mfma_f32_16x16x32_bf16 v[208:211],a[8:11],v[88:91],v[208:211]\n"
"ds_read_b64_tr_b16 a[0:1],v18 offset:33024\n"
"v_cvt_pk_bf16_f32 v65,v66,v67\n"
"v_mfma_f32_16x16x32_bf16 v[208:211],a[12:15],v[24:27],v[208:211]\n"
"ds_read_b64_tr_b16 a[2:3],v18 offset:35072\n"
"v_cvt_pk_bf16_f32 v66,v68,v69\n"
"v_mfma_f32_16x16x32_bf16 v[216:219],a[8:11],v[92:95],v[216:219]\n"
"s_mov_b32 s59,0x13000\n"
"ds_read_b64_tr_b16 a[4:5],v18 offset:49408\n"
"v_add_u32 v17,0x10000,v17\n"
"v_mfma_f32_16x16x32_bf16 v[216:219],a[12:15],v[28:31],v[216:219]\n"
"s_waitcnt lgkmcnt(11)\n"
"ds_read_b64_tr_b16 a[6:7],v18 offset:51456\n"
"v_cvt_pk_bf16_f32 v67,v70,v71\n"
"v_accvgpr_read_b32 v56,a240\n"
"v_mfma_f32_16x16x32_bf16 v[212:215],a[16:19],v[88:91],v[212:215]\n"
"ds_read_b64_tr_b16 a[8:9],v19 offset:33024\n"
"buffer_store_dwordx4 v[64:67],v52,s[44:47],s59 offen offset:1024 sc1\n"
"v_mfma_f32_16x16x32_bf16 v[212:215],a[20:23],v[24:27],v[212:215]\n"
"ds_read_b64_tr_b16 a[10:11],v19 offset:35072\n"
"v_accvgpr_read_b32 v57,a241\n"
"v_accvgpr_read_b32 v58,a242\n"
"v_mfma_f32_16x16x32_bf16 v[220:223],a[16:19],v[92:95],v[220:223]\n"
"s_barrier\n"
"ds_read_b64_tr_b16 a[12:13],v19 offset:49408\n"
"v_add_u32 v18,0x10000,v18\n"
"v_accvgpr_read_b32 v59,a243\n"
"v_mfma_f32_16x16x32_bf16 v[220:223],a[20:23],v[28:31],v[220:223]\n"
"s_waitcnt lgkmcnt(11)\n"
"ds_read_b64_tr_b16 a[14:15],v19 offset:51456\n"
"v_accvgpr_read_b32 v60,a244\n"
"v_accvgpr_read_b32 v61,a245\n"
"v_mfma_f32_16x16x32_bf16 v[224:227],a[24:27],v[88:91],v[224:227]\n"
"ds_read_b64_tr_b16 a[16:17],v12 offset:32768\n"
"buffer_load_dwordx4 v38,s[64:67],s53 offen offset:2048 lds\n"
"v_mfma_f32_16x16x32_bf16 v[224:227],a[28:31],v[24:27],v[224:227]\n"
"ds_read_b64_tr_b16 a[18:19],v12 offset:34816\n"
"v_accvgpr_read_b32 v62,a246\n"
"v_accvgpr_read_b32 v63,a247\n"
"v_mfma_f32_16x16x32_bf16 v[232:235],a[24:27],v[92:95],v[232:235]\n"
"ds_read_b64_tr_b16 a[20:21],v12 offset:49152\n"
"v_add_u32 v19,0x10000,v19\n"
"v_mfma_f32_16x16x32_bf16 v[232:235],a[28:31],v[28:31],v[232:235]\n"
"s_waitcnt lgkmcnt(11)\n"
"ds_read_b64_tr_b16 a[22:23],v12 offset:51200\n"
"v_cvt_pk_bf16_f32 v56,v56,v57\n"
"v_mfma_f32_16x16x32_bf16 v[228:231],a[32:35],v[88:91],v[228:231]\n"
"ds_read_b64_tr_b16 a[24:25],v13 offset:32768\n"
"v_cvt_pk_bf16_f32 v57,v58,v59\n"
"v_mfma_f32_16x16x32_bf16 v[228:231],a[36:39],v[24:27],v[228:231]\n"
"ds_read_b64_tr_b16 a[26:27],v13 offset:34816\n"
"v_cvt_pk_bf16_f32 v58,v60,v61\n"
"v_mfma_f32_16x16x32_bf16 v[236:239],a[32:35],v[92:95],v[236:239]\n"
"s_mov_b32 s59,0x13000\n"
"ds_read_b64_tr_b16 a[28:29],v13 offset:49152\n"
"v_cvt_pk_bf16_f32 v59,v62,v63\n"
"v_accvgpr_read_b32 v64,a248\n"
"v_mfma_f32_16x16x32_bf16 v[236:239],a[36:39],v[28:31],v[236:239]\n"
"s_waitcnt lgkmcnt(11)\n"
"ds_read_b64_tr_b16 a[30:31],v13 offset:51200\n"
"buffer_store_dwordx4 v[56:59],v52,s[44:47],s59 offen offset:2048 sc1\n"
"v_mfma_f32_16x16x32_bf16 v[240:243],a[0:3],v[88:91],v[240:243]\n"
"ds_read_b64_tr_b16 a[32:33],v14 offset:32768\n"
"v_accvgpr_read_b32 v65,a249\n"
"v_accvgpr_read_b32 v66,a250\n"
"v_mfma_f32_16x16x32_bf16 v[240:243],a[4:7],v[24:27],v[240:243]\n"
"ds_read_b64_tr_b16 a[34:35],v14 offset:34816\n"
"v_accvgpr_read_b32 v67,a251\n"
"v_accvgpr_read_b32 v68,a252\n"
"v_mfma_f32_16x16x32_bf16 v[248:251],a[0:3],v[92:95],v[248:251]\n"
"ds_read_b64_tr_b16 a[36:37],v14 offset:49152\n"
"v_accvgpr_read_b32 v69,a253\n"
"v_mfma_f32_16x16x32_bf16 v[248:251],a[4:7],v[28:31],v[248:251]\n"
"s_waitcnt lgkmcnt(11)\n"
"ds_read_b64_tr_b16 a[38:39],v14 offset:51200\n"
"buffer_load_dwordx4 v39,s[64:67],s53 offen offset:3072 lds\n"
"v_mfma_f32_16x16x32_bf16 v[244:247],a[8:11],v[88:91],v[244:247]\n"
"ds_read_b64_tr_b16 a[0:1],v15 offset:32768\n"
"v_accvgpr_read_b32 v70,a254\n"
"v_mfma_f32_16x16x32_bf16 v[244:247],a[12:15],v[24:27],v[244:247]\n"
"ds_read_b64_tr_b16 a[2:3],v15 offset:34816\n"
"v_accvgpr_read_b32 v71,a255\n"
"v_mfma_f32_16x16x32_bf16 v[252:255],a[8:11],v[92:95],v[252:255]\n"
"s_waitcnt vmcnt(23)\n"
"ds_read_b64_tr_b16 a[4:5],v15 offset:49152\n"
"v_cvt_pk_bf16_f32 v64,v64,v65\n"
"v_mfma_f32_16x16x32_bf16 v[252:255],a[12:15],v[28:31],v[252:255]\n"
"s_waitcnt lgkmcnt(11)\n"
"ds_read_b64_tr_b16 a[6:7],v15 offset:51200\n"
"v_cvt_pk_bf16_f32 v65,v66,v67\n"
"v_mfma_f32_16x16x32_bf16 v[128:131],a[16:19],v[40:43],v[128:131]\n"
"ds_read_b64_tr_b16 a[8:9],v16 offset:32768\n"
"v_cvt_pk_bf16_f32 v66,v68,v69\n"
"v_mfma_f32_16x16x32_bf16 v[128:131],a[20:23],a[40:43],v[128:131]\n"
"s_mov_b32 s59,0x13000\n"
"ds_read_b64_tr_b16 a[10:11],v16 offset:34816\n"
"v_cvt_pk_bf16_f32 v67,v70,v71\n"
"v_mfma_f32_16x16x32_bf16 v[136:139],a[16:19],v[44:47],v[136:139]\n"
"ds_read_b64_tr_b16 a[12:13],v16 offset:49152\n"
"buffer_store_dwordx4 v[64:67],v52,s[44:47],s59 offen offset:3072 sc1\n"
"v_mfma_f32_16x16x32_bf16 v[136:139],a[20:23],a[44:47],v[136:139]\n"
"s_waitcnt lgkmcnt(11)\n"
"ds_read_b64_tr_b16 a[14:15],v16 offset:51200\n"
"v_mfma_f32_16x16x32_bf16 v[132:135],a[24:27],v[40:43],v[132:135]\n"
"ds_read_b64_tr_b16 a[16:17],v17 offset:32768\n"
"v_mfma_f32_16x16x32_bf16 v[132:135],a[28:31],a[40:43],v[132:135]\n"
"ds_read_b64_tr_b16 a[18:19],v17 offset:34816\n"
"v_mfma_f32_16x16x32_bf16 v[140:143],a[24:27],v[44:47],v[140:143]\n"
"ds_read_b64_tr_b16 a[20:21],v17 offset:49152\n"
"buffer_load_dwordx4 a[64:67],v52,s[60:63],s87 offen offset:0 sc1\n"
"v_mfma_f32_16x16x32_bf16 v[140:143],a[28:31],a[44:47],v[140:143]\n"
"s_waitcnt lgkmcnt(11)\n"
"ds_read_b64_tr_b16 a[22:23],v17 offset:51200\n"
"v_mfma_f32_16x16x32_bf16 v[144:147],a[32:35],v[40:43],v[144:147]\n"
"ds_read_b64_tr_b16 a[24:25],v18 offset:32768\n"
"v_mfma_f32_16x16x32_bf16 v[144:147],a[36:39],a[40:43],v[144:147]\n"
"ds_read_b64_tr_b16 a[26:27],v18 offset:34816\n"
"v_mfma_f32_16x16x32_bf16 v[152:155],a[32:35],v[44:47],v[152:155]\n"
"ds_read_b64_tr_b16 a[28:29],v18 offset:49152\n"
"buffer_load_dwordx4 a[68:71],v51,s[60:63],s87 offen offset:0 sc1\n"
"v_mfma_f32_16x16x32_bf16 v[152:155],a[36:39],a[44:47],v[152:155]\n"
"s_waitcnt lgkmcnt(11)\n"
"ds_read_b64_tr_b16 a[30:31],v18 offset:51200\n"
"v_mfma_f32_16x16x32_bf16 v[148:151],a[0:3],v[40:43],v[148:151]\n"
"ds_read_b64_tr_b16 a[32:33],v19 offset:32768\n"
"v_mfma_f32_16x16x32_bf16 v[148:151],a[4:7],a[40:43],v[148:151]\n"
"ds_read_b64_tr_b16 a[34:35],v19 offset:34816\n"
"v_mfma_f32_16x16x32_bf16 v[156:159],a[0:3],v[44:47],v[156:159]\n"
"ds_read_b64_tr_b16 a[36:37],v19 offset:49152\n"
"buffer_load_dwordx4 a[72:75],v52,s[60:63],s87 offen offset:1024 sc1\n"
"v_mfma_f32_16x16x32_bf16 v[156:159],a[4:7],a[44:47],v[156:159]\n"
"s_waitcnt lgkmcnt(11)\n"
"ds_read_b64_tr_b16 a[38:39],v19 offset:51200\n"
"v_mfma_f32_16x16x32_bf16 v[160:163],a[8:11],v[40:43],v[160:163]\n"
"ds_read_b64_tr_b16 a[0:1],v12 offset:33024\n"
"v_mfma_f32_16x16x32_bf16 v[160:163],a[12:15],a[40:43],v[160:163]\n"
"ds_read_b64_tr_b16 a[2:3],v12 offset:35072\n"
"v_mfma_f32_16x16x32_bf16 v[168:171],a[8:11],v[44:47],v[168:171]\n"
"ds_read_b64_tr_b16 a[4:5],v12 offset:49408\n"
"buffer_load_dwordx4 a[76:79],v51,s[60:63],s87 offen offset:1024 sc1\n"
"v_mfma_f32_16x16x32_bf16 v[168:171],a[12:15],a[44:47],v[168:171]\n"
"s_waitcnt lgkmcnt(11)\n"
"ds_read_b64_tr_b16 a[6:7],v12 offset:51456\n"
"v_mfma_f32_16x16x32_bf16 v[164:167],a[16:19],v[40:43],v[164:167]\n"
"ds_read_b64_tr_b16 a[8:9],v13 offset:33024\n"
"v_mfma_f32_16x16x32_bf16 v[164:167],a[20:23],a[40:43],v[164:167]\n"
"ds_read_b64_tr_b16 a[10:11],v13 offset:35072\n"
"v_mfma_f32_16x16x32_bf16 v[172:175],a[16:19],v[44:47],v[172:175]\n"
"ds_read_b64_tr_b16 a[12:13],v13 offset:49408\n"
"v_add_u32 v12,0x10000,v12\n"
"v_mfma_f32_16x16x32_bf16 v[172:175],a[20:23],a[44:47],v[172:175]\n"
"s_waitcnt lgkmcnt(11)\n"
"ds_read_b64_tr_b16 a[14:15],v13 offset:51456\n"
"buffer_load_dwordx4 a[80:83],v52,s[60:63],s87 offen offset:2048 sc1\n"
"v_mfma_f32_16x16x32_bf16 v[176:179],a[24:27],v[40:43],v[176:179]\n"
"ds_read_b64_tr_b16 a[16:17],v14 offset:33024\n"
"v_mfma_f32_16x16x32_bf16 v[176:179],a[28:31],a[40:43],v[176:179]\n"
"ds_read_b64_tr_b16 a[18:19],v14 offset:35072\n"
"v_mfma_f32_16x16x32_bf16 v[184:187],a[24:27],v[44:47],v[184:187]\n"
"ds_read_b64_tr_b16 a[20:21],v14 offset:49408\n"
"v_add_u32 v13,0x10000,v13\n"
"v_mfma_f32_16x16x32_bf16 v[184:187],a[28:31],a[44:47],v[184:187]\n"
"s_waitcnt lgkmcnt(11)\n"
"ds_read_b64_tr_b16 a[22:23],v14 offset:51456\n"
"buffer_load_dwordx4 a[84:87],v51,s[60:63],s87 offen offset:2048 sc1\n"
"v_mfma_f32_16x16x32_bf16 v[180:183],a[32:35],v[40:43],v[180:183]\n"
"ds_read_b64_tr_b16 a[24:25],v15 offset:33024\n"
"v_mfma_f32_16x16x32_bf16 v[180:183],a[36:39],a[40:43],v[180:183]\n"
"ds_read_b64_tr_b16 a[26:27],v15 offset:35072\n"
"v_mfma_f32_16x16x32_bf16 v[188:191],a[32:35],v[44:47],v[188:191]\n"
"ds_read_b64_tr_b16 a[28:29],v15 offset:49408\n"
"v_add_u32 v14,0x10000,v14\n"
"v_mfma_f32_16x16x32_bf16 v[188:191],a[36:39],a[44:47],v[188:191]\n"
"s_waitcnt lgkmcnt(11)\n"
"ds_read_b64_tr_b16 a[30:31],v15 offset:51456\n"
"buffer_load_dwordx4 a[88:91],v52,s[60:63],s87 offen offset:3072 sc1\n"
"v_mfma_f32_16x16x32_bf16 v[192:195],a[0:3],v[40:43],v[192:195]\n"
"ds_read_b64_tr_b16 a[32:33],v16 offset:33024\n"
"v_mfma_f32_16x16x32_bf16 v[192:195],a[4:7],a[40:43],v[192:195]\n"
"ds_read_b64_tr_b16 a[34:35],v16 offset:35072\n"
"v_mfma_f32_16x16x32_bf16 v[200:203],a[0:3],v[44:47],v[200:203]\n"
"ds_read_b64_tr_b16 a[36:37],v16 offset:49408\n"
"v_add_u32 v15,0x10000,v15\n"
"v_mfma_f32_16x16x32_bf16 v[200:203],a[4:7],a[44:47],v[200:203]\n"
"s_waitcnt lgkmcnt(11)\n"
"ds_read_b64_tr_b16 a[38:39],v16 offset:51456\n"
"buffer_load_dwordx4 a[92:95],v51,s[60:63],s87 offen offset:3072 sc1\n"
"v_mfma_f32_16x16x32_bf16 v[196:199],a[8:11],v[40:43],v[196:199]\n"
"ds_read_b64_tr_b16 a[0:1],v17 offset:33024\n"
"v_mfma_f32_16x16x32_bf16 v[196:199],a[12:15],a[40:43],v[196:199]\n"
"ds_read_b64_tr_b16 a[2:3],v17 offset:35072\n"
"v_mfma_f32_16x16x32_bf16 v[204:207],a[8:11],v[44:47],v[204:207]\n"
"ds_read_b64_tr_b16 a[4:5],v17 offset:49408\n"
"v_add_u32 v16,0x10000,v16\n"
"v_mfma_f32_16x16x32_bf16 v[204:207],a[12:15],a[44:47],v[204:207]\n"
"s_waitcnt lgkmcnt(11)\n"
"ds_read_b64_tr_b16 a[6:7],v17 offset:51456\n"
"buffer_load_dwordx4 a[96:99],v52,s[60:63],s88 offen offset:0 sc1\n"
"v_mfma_f32_16x16x32_bf16 v[208:211],a[16:19],v[40:43],v[208:211]\n"
"ds_read_b64_tr_b16 a[8:9],v18 offset:33024\n"
"v_mfma_f32_16x16x32_bf16 v[208:211],a[20:23],a[40:43],v[208:211]\n"
"ds_read_b64_tr_b16 a[10:11],v18 offset:35072\n"
"v_mfma_f32_16x16x32_bf16 v[216:219],a[16:19],v[44:47],v[216:219]\n"
"ds_read_b64_tr_b16 a[12:13],v18 offset:49408\n"
"v_add_u32 v17,0x10000,v17\n"
"v_mfma_f32_16x16x32_bf16 v[216:219],a[20:23],a[44:47],v[216:219]\n"
"s_waitcnt lgkmcnt(11)\n"
"ds_read_b64_tr_b16 a[14:15],v18 offset:51456\n"
"buffer_load_dwordx4 a[100:103],v51,s[60:63],s88 offen offset:0 sc1\n"
"v_mfma_f32_16x16x32_bf16 v[212:215],a[24:27],v[40:43],v[212:215]\n"
"ds_read_b64_tr_b16 a[16:17],v19 offset:33024\n"
"v_mfma_f32_16x16x32_bf16 v[212:215],a[28:31],a[40:43],v[212:215]\n"
"ds_read_b64_tr_b16 a[18:19],v19 offset:35072\n"
"v_mfma_f32_16x16x32_bf16 v[220:223],a[24:27],v[44:47],v[220:223]\n"
"s_barrier\n"
"ds_read_b64_tr_b16 a[20:21],v19 offset:49408\n"
"v_add_u32 v18,0x10000,v18\n"
"v_mfma_f32_16x16x32_bf16 v[220:223],a[28:31],a[44:47],v[220:223]\n"
"s_waitcnt lgkmcnt(11)\n"
"ds_read_b64_tr_b16 a[22:23],v19 offset:51456\n"
"buffer_load_dwordx4 a[104:107],v52,s[60:63],s88 offen offset:1024 sc1\n"
"v_mfma_f32_16x16x32_bf16 v[224:227],a[32:35],v[40:43],v[224:227]\n"
"ds_read_b64_tr_b16 a[24:25],v12 offset:0\n"
"v_mfma_f32_16x16x32_bf16 v[224:227],a[36:39],a[40:43],v[224:227]\n"
"ds_read_b64_tr_b16 a[26:27],v12 offset:2048\n"
"v_mfma_f32_16x16x32_bf16 v[232:235],a[32:35],v[44:47],v[232:235]\n"
"ds_read_b64_tr_b16 a[28:29],v12 offset:16384\n"
"v_add_u32 v19,0x10000,v19\n"
"v_mfma_f32_16x16x32_bf16 v[232:235],a[36:39],a[44:47],v[232:235]\n"
"s_waitcnt lgkmcnt(11)\n"
"ds_read_b64_tr_b16 a[30:31],v12 offset:18432\n"
"buffer_load_dwordx4 a[108:111],v51,s[60:63],s88 offen offset:1024 sc1\n"
"v_mfma_f32_16x16x32_bf16 v[228:231],a[0:3],v[40:43],v[228:231]\n"
"ds_read_b64_tr_b16 a[32:33],v13 offset:0\n"
"v_mfma_f32_16x16x32_bf16 v[228:231],a[4:7],a[40:43],v[228:231]\n"
"s_mov_b32 s53,0x280000\n"
"ds_read_b64_tr_b16 a[34:35],v13 offset:2048\n"
"v_mfma_f32_16x16x32_bf16 v[236:239],a[0:3],v[44:47],v[236:239]\n"
"s_add_u32 m0,s57,0x0\n"
"ds_read_b64_tr_b16 a[36:37],v13 offset:16384\n"
"v_mfma_f32_16x16x32_bf16 v[236:239],a[4:7],a[44:47],v[236:239]\n"
"s_waitcnt lgkmcnt(11)\n"
"ds_read_b64_tr_b16 a[38:39],v13 offset:18432\n"
"buffer_load_dwordx4 v32,s[64:67],s53 offen offset:0 lds\n"
"v_mfma_f32_16x16x32_bf16 v[240:243],a[8:11],v[40:43],v[240:243]\n"
"ds_read_b64_tr_b16 a[0:1],v14 offset:0\n"
"v_mfma_f32_16x16x32_bf16 v[240:243],a[12:15],a[40:43],v[240:243]\n"
"ds_read_b64_tr_b16 a[2:3],v14 offset:2048\n"
"v_mfma_f32_16x16x32_bf16 v[248:251],a[8:11],v[44:47],v[248:251]\n"
"ds_read_b64_tr_b16 a[4:5],v14 offset:16384\n"
"v_mfma_f32_16x16x32_bf16 v[248:251],a[12:15],a[44:47],v[248:251]\n"
"s_waitcnt lgkmcnt(11)\n"
"ds_read_b64_tr_b16 a[6:7],v14 offset:18432\n"
"buffer_load_dwordx4 v33,s[64:67],s53 offen offset:1024 lds\n"
"v_mfma_f32_16x16x32_bf16 v[244:247],a[16:19],v[40:43],v[244:247]\n"
"ds_read_b64_tr_b16 a[8:9],v15 offset:0\n"
"v_mfma_f32_16x16x32_bf16 v[244:247],a[20:23],a[40:43],v[244:247]\n"
"ds_read_b64_tr_b16 a[10:11],v15 offset:2048\n"
"v_mfma_f32_16x16x32_bf16 v[252:255],a[16:19],v[44:47],v[252:255]\n"
"s_waitcnt vmcnt(30)\n"
"ds_read_b64_tr_b16 a[12:13],v15 offset:16384\n"
"v_mfma_f32_16x16x32_bf16 v[252:255],a[20:23],a[44:47],v[252:255]\n"
"s_waitcnt lgkmcnt(11)\n"
"ds_read_b64_tr_b16 a[14:15],v15 offset:18432\n"
"buffer_load_dwordx4 v34,s[64:67],s53 offen offset:2048 lds\n"
"v_mfma_f32_16x16x32_bf16 v[128:131],a[24:27],a[48:51],v[128:131]\n"
"ds_read_b64_tr_b16 a[16:17],v16 offset:0\n"
"v_mfma_f32_16x16x32_bf16 v[128:131],a[28:31],a[56:59],v[128:131]\n"
"ds_read_b64_tr_b16 a[18:19],v16 offset:2048\n"
"v_mfma_f32_16x16x32_bf16 v[136:139],a[24:27],a[52:55],v[136:139]\n"
"ds_read_b64_tr_b16 a[20:21],v16 offset:16384\n"
"v_mfma_f32_16x16x32_bf16 v[136:139],a[28:31],a[60:63],v[136:139]\n"
"s_waitcnt lgkmcnt(11)\n"
"ds_read_b64_tr_b16 a[22:23],v16 offset:18432\n"
"buffer_load_dwordx4 v35,s[64:67],s53 offen offset:3072 lds\n"
"v_mfma_f32_16x16x32_bf16 v[132:135],a[32:35],a[48:51],v[132:135]\n"
"ds_read_b64_tr_b16 a[24:25],v17 offset:0\n"
"v_mfma_f32_16x16x32_bf16 v[132:135],a[36:39],a[56:59],v[132:135]\n"
"ds_read_b64_tr_b16 a[26:27],v17 offset:2048\n"
"v_mfma_f32_16x16x32_bf16 v[140:143],a[32:35],a[52:55],v[140:143]\n"
"s_add_u32 m0,s57,0x1000\n"
"ds_read_b64_tr_b16 a[28:29],v17 offset:16384\n"
"v_mfma_f32_16x16x32_bf16 v[140:143],a[36:39],a[60:63],v[140:143]\n"
"s_waitcnt lgkmcnt(11)\n"
"ds_read_b64_tr_b16 a[30:31],v17 offset:18432\n"
"buffer_load_dwordx4 v36,s[64:67],s53 offen offset:0 lds\n"
"v_mfma_f32_16x16x32_bf16 v[144:147],a[0:3],a[48:51],v[144:147]\n"
"ds_read_b64_tr_b16 a[32:33],v18 offset:0\n"
"v_mfma_f32_16x16x32_bf16 v[144:147],a[4:7],a[56:59],v[144:147]\n"
"ds_read_b64_tr_b16 a[34:35],v18 offset:2048\n"
"v_mfma_f32_16x16x32_bf16 v[152:155],a[0:3],a[52:55],v[152:155]\n"
"ds_read_b64_tr_b16 a[36:37],v18 offset:16384\n"
"v_mfma_f32_16x16x32_bf16 v[152:155],a[4:7],a[60:63],v[152:155]\n"
"s_waitcnt lgkmcnt(11)\n"
"ds_read_b64_tr_b16 a[38:39],v18 offset:18432\n"
"buffer_load_dwordx4 v37,s[64:67],s53 offen offset:1024 lds\n"
"v_mfma_f32_16x16x32_bf16 v[148:151],a[8:11],a[48:51],v[148:151]\n"
"ds_read_b64_tr_b16 a[0:1],v19 offset:0\n"
"v_mfma_f32_16x16x32_bf16 v[148:151],a[12:15],a[56:59],v[148:151]\n"
"ds_read_b64_tr_b16 a[2:3],v19 offset:2048\n"
"v_mfma_f32_16x16x32_bf16 v[156:159],a[8:11],a[52:55],v[156:159]\n"
"ds_read_b64_tr_b16 a[4:5],v19 offset:16384\n"
"v_mfma_f32_16x16x32_bf16 v[156:159],a[12:15],a[60:63],v[156:159]\n"
"s_waitcnt lgkmcnt(11)\n"
"ds_read_b64_tr_b16 a[6:7],v19 offset:18432\n"
"buffer_load_dwordx4 v38,s[64:67],s53 offen offset:2048 lds\n"
"v_mfma_f32_16x16x32_bf16 v[160:163],a[16:19],a[48:51],v[160:163]\n"
"ds_read_b64_tr_b16 a[8:9],v12 offset:256\n"
"v_mfma_f32_16x16x32_bf16 v[160:163],a[20:23],a[56:59],v[160:163]\n"
"ds_read_b64_tr_b16 a[10:11],v12 offset:2304\n"
"v_mfma_f32_16x16x32_bf16 v[168:171],a[16:19],a[52:55],v[168:171]\n"
"ds_read_b64_tr_b16 a[12:13],v12 offset:16640\n"
"v_mfma_f32_16x16x32_bf16 v[168:171],a[20:23],a[60:63],v[168:171]\n"
"s_waitcnt lgkmcnt(11)\n"
"ds_read_b64_tr_b16 a[14:15],v12 offset:18688\n"
"buffer_load_dwordx4 v39,s[64:67],s53 offen offset:3072 lds\n"
"v_mfma_f32_16x16x32_bf16 v[164:167],a[24:27],a[48:51],v[164:167]\n"
"ds_read_b64_tr_b16 a[16:17],v13 offset:256\n"
"v_mfma_f32_16x16x32_bf16 v[164:167],a[28:31],a[56:59],v[164:167]\n"
"s_mov_b32 s53,0x300000\n"
"ds_read_b64_tr_b16 a[18:19],v13 offset:2304\n"
"v_mfma_f32_16x16x32_bf16 v[172:175],a[24:27],a[52:55],v[172:175]\n"
"s_add_u32 m0,s57,0x8000\n"
"ds_read_b64_tr_b16 a[20:21],v13 offset:16640\n"
"v_add_u32 v12,0xffff0000,v12\n"
"v_mfma_f32_16x16x32_bf16 v[172:175],a[28:31],a[60:63],v[172:175]\n"
"s_waitcnt lgkmcnt(11)\n"
"ds_read_b64_tr_b16 a[22:23],v13 offset:18688\n"
"buffer_load_dwordx4 v32,s[64:67],s53 offen offset:0 lds\n"
"v_mfma_f32_16x16x32_bf16 v[176:179],a[32:35],a[48:51],v[176:179]\n"
"ds_read_b64_tr_b16 a[24:25],v14 offset:256\n"
"v_mfma_f32_16x16x32_bf16 v[176:179],a[36:39],a[56:59],v[176:179]\n"
"ds_read_b64_tr_b16 a[26:27],v14 offset:2304\n"
"v_mfma_f32_16x16x32_bf16 v[184:187],a[32:35],a[52:55],v[184:187]\n"
"ds_read_b64_tr_b16 a[28:29],v14 offset:16640\n"
"v_add_u32 v13,0xffff0000,v13\n"
"v_mfma_f32_16x16x32_bf16 v[184:187],a[36:39],a[60:63],v[184:187]\n"
"s_waitcnt lgkmcnt(11)\n"
"ds_read_b64_tr_b16 a[30:31],v14 offset:18688\n"
"buffer_load_dwordx4 v33,s[64:67],s53 offen offset:1024 lds\n"
"v_mfma_f32_16x16x32_bf16 v[180:183],a[0:3],a[48:51],v[180:183]\n"
"ds_read_b64_tr_b16 a[32:33],v15 offset:256\n"
"v_mfma_f32_16x16x32_bf16 v[180:183],a[4:7],a[56:59],v[180:183]\n"
"ds_read_b64_tr_b16 a[34:35],v15 offset:2304\n"
"v_mfma_f32_16x16x32_bf16 v[188:191],a[0:3],a[52:55],v[188:191]\n"
"ds_read_b64_tr_b16 a[36:37],v15 offset:16640\n"
"v_add_u32 v14,0xffff0000,v14\n"
"v_mfma_f32_16x16x32_bf16 v[188:191],a[4:7],a[60:63],v[188:191]\n"
"s_waitcnt lgkmcnt(11)\n"
"ds_read_b64_tr_b16 a[38:39],v15 offset:18688\n"
"buffer_load_dwordx4 v34,s[64:67],s53 offen offset:2048 lds\n"
"v_mfma_f32_16x16x32_bf16 v[192:195],a[8:11],a[48:51],v[192:195]\n"
"ds_read_b64_tr_b16 a[0:1],v16 offset:256\n"
"v_mfma_f32_16x16x32_bf16 v[192:195],a[12:15],a[56:59],v[192:195]\n"
"ds_read_b64_tr_b16 a[2:3],v16 offset:2304\n"
"v_mfma_f32_16x16x32_bf16 v[200:203],a[8:11],a[52:55],v[200:203]\n"
"ds_read_b64_tr_b16 a[4:5],v16 offset:16640\n"
"v_add_u32 v15,0xffff0000,v15\n"
"v_mfma_f32_16x16x32_bf16 v[200:203],a[12:15],a[60:63],v[200:203]\n"
"s_waitcnt lgkmcnt(11)\n"
"ds_read_b64_tr_b16 a[6:7],v16 offset:18688\n"
"buffer_load_dwordx4 v35,s[64:67],s53 offen offset:3072 lds\n"
"v_mfma_f32_16x16x32_bf16 v[196:199],a[16:19],a[48:51],v[196:199]\n"
"ds_read_b64_tr_b16 a[8:9],v17 offset:256\n"
"v_mfma_f32_16x16x32_bf16 v[196:199],a[20:23],a[56:59],v[196:199]\n"
"ds_read_b64_tr_b16 a[10:11],v17 offset:2304\n"
"v_mfma_f32_16x16x32_bf16 v[204:207],a[16:19],a[52:55],v[204:207]\n"
"s_add_u32 m0,s57,0x9000\n"
"ds_read_b64_tr_b16 a[12:13],v17 offset:16640\n"
"v_add_u32 v16,0xffff0000,v16\n"
"v_mfma_f32_16x16x32_bf16 v[204:207],a[20:23],a[60:63],v[204:207]\n"
"s_waitcnt lgkmcnt(11)\n"
"ds_read_b64_tr_b16 a[14:15],v17 offset:18688\n"
"buffer_load_dwordx4 v36,s[64:67],s53 offen offset:0 lds\n"
"v_mfma_f32_16x16x32_bf16 v[208:211],a[24:27],a[48:51],v[208:211]\n"
"ds_read_b64_tr_b16 a[16:17],v18 offset:256\n"
"v_mfma_f32_16x16x32_bf16 v[208:211],a[28:31],a[56:59],v[208:211]\n"
"ds_read_b64_tr_b16 a[18:19],v18 offset:2304\n"
"v_mfma_f32_16x16x32_bf16 v[216:219],a[24:27],a[52:55],v[216:219]\n"
"ds_read_b64_tr_b16 a[20:21],v18 offset:16640\n"
"v_add_u32 v17,0xffff0000,v17\n"
"v_mfma_f32_16x16x32_bf16 v[216:219],a[28:31],a[60:63],v[216:219]\n"
"s_waitcnt lgkmcnt(11)\n"
"ds_read_b64_tr_b16 a[22:23],v18 offset:18688\n"
"buffer_load_dwordx4 v37,s[64:67],s53 offen offset:1024 lds\n"
"v_mfma_f32_16x16x32_bf16 v[212:215],a[32:35],a[48:51],v[212:215]\n"
"ds_read_b64_tr_b16 a[24:25],v19 offset:256\n"
"v_mfma_f32_16x16x32_bf16 v[212:215],a[36:39],a[56:59],v[212:215]\n"
"ds_read_b64_tr_b16 a[26:27],v19 offset:2304\n"
"v_mfma_f32_16x16x32_bf16 v[220:223],a[32:35],a[52:55],v[220:223]\n"
"s_waitcnt vmcnt(26)\n"
"s_barrier\n"
"ds_read_b64_tr_b16 a[28:29],v19 offset:16640\n"
"v_add_u32 v18,0xffff0000,v18\n"
"v_mfma_f32_16x16x32_bf16 v[220:223],a[36:39],a[60:63],v[220:223]\n"
"s_waitcnt lgkmcnt(11)\n"
"s_branch L_pubkvk_%=\n"
"L_pubkvk_ret_%=:\n"
"ds_read_b64_tr_b16 a[30:31],v19 offset:18688\n"
"buffer_load_dwordx4 v38,s[64:67],s53 offen offset:2048 lds\n"
"v_mfma_f32_16x16x32_bf16 v[224:227],a[0:3],a[48:51],v[224:227]\n"
"ds_read_b64_tr_b16 a[32:33],v12 offset:0\n"
"v_mfma_f32_16x16x32_bf16 v[224:227],a[4:7],a[56:59],v[224:227]\n"
"ds_read_b64_tr_b16 a[34:35],v12 offset:2048\n"
"v_mfma_f32_16x16x32_bf16 v[232:235],a[0:3],a[52:55],v[232:235]\n"
"ds_read_b64_tr_b16 a[36:37],v12 offset:16384\n"
"v_add_u32 v19,0xffff0000,v19\n"
"v_mfma_f32_16x16x32_bf16 v[232:235],a[4:7],a[60:63],v[232:235]\n"
"s_waitcnt lgkmcnt(11)\n"
"ds_read_b64_tr_b16 a[38:39],v12 offset:18432\n"
"buffer_load_dwordx4 v39,s[64:67],s53 offen offset:3072 lds\n"
"v_mfma_f32_16x16x32_bf16 v[228:231],a[8:11],a[48:51],v[228:231]\n"
"ds_read_b64_tr_b16 a[0:1],v13 offset:0\n"
"v_mfma_f32_16x16x32_bf16 v[228:231],a[12:15],a[56:59],v[228:231]\n"
"s_add_u32 s36,s32,0x1400000\n"
"ds_read_b64_tr_b16 a[2:3],v13 offset:2048\n"
"v_mfma_f32_16x16x32_bf16 v[236:239],a[8:11],a[52:55],v[236:239]\n"
"s_addc_u32 s37,s33,0\n"
"ds_read_b64_tr_b16 a[4:5],v13 offset:16384\n"
"v_mfma_f32_16x16x32_bf16 v[236:239],a[12:15],a[60:63],v[236:239]\n"
"s_waitcnt lgkmcnt(11)\n"
"ds_read_b64_tr_b16 a[6:7],v13 offset:18432\n"
"v_mfma_f32_16x16x32_bf16 v[240:243],a[16:19],a[48:51],v[240:243]\n"
"s_and_b32 s37,s37,0xffff\n"
"ds_read_b64_tr_b16 a[8:9],v14 offset:0\n"
"v_mfma_f32_16x16x32_bf16 v[240:243],a[20:23],a[56:59],v[240:243]\n"
"s_mov_b32 s38,0x2000000\n"
"ds_read_b64_tr_b16 a[10:11],v14 offset:2048\n"
"v_mfma_f32_16x16x32_bf16 v[248:251],a[16:19],a[52:55],v[248:251]\n"
"s_mov_b32 s39,0x20000\n"
"ds_read_b64_tr_b16 a[12:13],v14 offset:16384\n"
"v_lshlrev_b32 v8,4,v0\n"
"v_add_u32 v10,s57,v8\n"
"v_mfma_f32_16x16x32_bf16 v[248:251],a[20:23],a[60:63],v[248:251]\n"
"s_waitcnt lgkmcnt(11)\n"
"ds_read_b64_tr_b16 a[14:15],v14 offset:18432\n"
"v_add_u32 v7,0x1000,v10\n"
"v_add_u32 v9,0x10000,v8\n"
"v_mfma_f32_16x16x32_bf16 v[244:247],a[24:27],a[48:51],v[244:247]\n"
"s_lshl_b32 s78,s92,21\n"
"ds_read_b64_tr_b16 a[16:17],v15 offset:0\n"
"v_mfma_f32_16x16x32_bf16 v[244:247],a[28:31],a[56:59],v[244:247]\n"
"s_cmp_lt_u32 s93,6\n"
"ds_read_b64_tr_b16 a[18:19],v15 offset:2048\n"
"v_mfma_f32_16x16x32_bf16 v[252:255],a[24:27],a[52:55],v[252:255]\n"
"s_waitcnt vmcnt(24)\n"
"ds_read_b64_tr_b16 a[20:21],v15 offset:16384\n"
"v_mfma_f32_16x16x32_bf16 v[252:255],a[28:31],a[60:63],v[252:255]\n"
"s_waitcnt lgkmcnt(11)\n"
"ds_read_b64_tr_b16 a[22:23],v15 offset:18432\n"
"v_mfma_f32_16x16x32_bf16 v[128:131],a[32:35],a[64:67],v[128:131]\n"
"s_cselect_b32 s56,s94,2\n"
"ds_read_b64_tr_b16 a[24:25],v16 offset:0\n"
"v_mfma_f32_16x16x32_bf16 v[128:131],a[36:39],a[72:75],v[128:131]\n"
"s_lshl_b32 s79,s54,1\n"
"ds_read_b64_tr_b16 a[26:27],v16 offset:2048\n"
"v_mfma_f32_16x16x32_bf16 v[136:139],a[32:35],a[68:71],v[136:139]\n"
"s_add_u32 s79,s79,2\n"
"ds_read_b64_tr_b16 a[28:29],v16 offset:16384\n"
"v_mfma_f32_16x16x32_bf16 v[136:139],a[36:39],a[76:79],v[136:139]\n"
"s_waitcnt lgkmcnt(11)\n"
"ds_read_b64_tr_b16 a[30:31],v16 offset:18432\n"
"v_mfma_f32_16x16x32_bf16 v[132:135],a[0:3],a[64:67],v[132:135]\n"
"s_add_u32 s55,s78,0x140000\n"
"ds_read_b64_tr_b16 a[32:33],v17 offset:0\n"
"v_mfma_f32_16x16x32_bf16 v[132:135],a[4:7],a[72:75],v[132:135]\n"
"s_cmp_eq_u32 s56,1\n"
"ds_read_b64_tr_b16 a[34:35],v17 offset:2048\n"
"v_mfma_f32_16x16x32_bf16 v[140:143],a[0:3],a[68:71],v[140:143]\n"
"s_cselect_b32 s55,s55,s78\n"
"ds_read_b64_tr_b16 a[36:37],v17 offset:16384\n"
"v_mfma_f32_16x16x32_bf16 v[140:143],a[4:7],a[76:79],v[140:143]\n"
"s_waitcnt lgkmcnt(11)\n"
"ds_read_b64_tr_b16 a[38:39],v17 offset:18432\n"
"v_mfma_f32_16x16x32_bf16 v[144:147],a[8:11],a[64:67],v[144:147]\n"
"s_cmp_eq_u32 s56,0\n"
"ds_read_b64_tr_b16 a[0:1],v18 offset:0\n"
"v_mfma_f32_16x16x32_bf16 v[144:147],a[12:15],a[72:75],v[144:147]\n"
"s_cselect_b32 s79,19,s79\n"
"ds_read_b64_tr_b16 a[2:3],v18 offset:2048\n"
"v_mfma_f32_16x16x32_bf16 v[152:155],a[8:11],a[68:71],v[152:155]\n"
"s_cselect_b32 s52,-1,1\n"
"ds_read_b64_tr_b16 a[4:5],v18 offset:16384\n"
"v_mfma_f32_16x16x32_bf16 v[152:155],a[12:15],a[76:79],v[152:155]\n"
"s_waitcnt lgkmcnt(11)\n"
"ds_read_b64_tr_b16 a[6:7],v18 offset:18432\n"
"v_mfma_f32_16x16x32_bf16 v[148:151],a[16:19],a[64:67],v[148:151]\n"
"s_cselect_b32 s58,1,0\n"
"ds_read_b64_tr_b16 a[8:9],v19 offset:0\n"
"v_mfma_f32_16x16x32_bf16 v[148:151],a[20:23],a[72:75],v[148:151]\n"
"s_lshl_b32 s90,s58,15\n"
"ds_read_b64_tr_b16 a[10:11],v19 offset:2048\n"
"v_mfma_f32_16x16x32_bf16 v[156:159],a[16:19],a[68:71],v[156:159]\n"
"s_xor_b32 s91,s90,0x18000\n"
"ds_read_b64_tr_b16 a[12:13],v19 offset:16384\n"
"v_mfma_f32_16x16x32_bf16 v[156:159],a[20:23],a[76:79],v[156:159]\n"
"s_waitcnt lgkmcnt(11)\n"
"ds_read_b64_tr_b16 a[14:15],v19 offset:18432\n"
"v_mfma_f32_16x16x32_bf16 v[160:163],a[24:27],a[64:67],v[160:163]\n"
"s_add_u32 s90,s90,0x10000\n"
"ds_read_b64_tr_b16 a[16:17],v12 offset:256\n"
"v_mfma_f32_16x16x32_bf16 v[160:163],a[28:31],a[72:75],v[160:163]\n"
"s_lshl_b32 s96,s92,7\n"
"ds_read_b64_tr_b16 a[18:19],v12 offset:2304\n"
"v_mfma_f32_16x16x32_bf16 v[168:171],a[24:27],a[68:71],v[168:171]\n"
"s_add_u32 s82,s96,0x2000\n"
"ds_read_b64_tr_b16 a[20:21],v12 offset:16640\n"
"v_mfma_f32_16x16x32_bf16 v[168:171],a[28:31],a[76:79],v[168:171]\n"
"s_waitcnt lgkmcnt(11)\n"
"ds_read_b64_tr_b16 a[22:23],v12 offset:18688\n"
"v_mfma_f32_16x16x32_bf16 v[164:167],a[32:35],a[64:67],v[164:167]\n"
"s_add_u32 s82,s82,s32\n"
"ds_read_b64_tr_b16 a[24:25],v13 offset:256\n"
"v_mfma_f32_16x16x32_bf16 v[164:167],a[36:39],a[72:75],v[164:167]\n"
"s_addc_u32 s83,s33,0\n"
"ds_read_b64_tr_b16 a[26:27],v13 offset:2304\n"
"v_and_b32 v5,7,v0\n"
"v_lshlrev_b32 v5,3,v5\n"
"v_mfma_f32_16x16x32_bf16 v[172:175],a[32:35],a[68:71],v[172:175]\n"
"s_add_u32 s49,s78,0x8000\n"
"ds_read_b64_tr_b16 a[28:29],v13 offset:16640\n"
"v_add_u32 v12,0xffff0000,v12\n"
"v_mfma_f32_16x16x32_bf16 v[172:175],a[36:39],a[76:79],v[172:175]\n"
"s_waitcnt lgkmcnt(11)\n"
"ds_read_b64_tr_b16 a[30:31],v13 offset:18688\n"
"v_mfma_f32_16x16x32_bf16 v[176:179],a[0:3],a[64:67],v[176:179]\n"
"s_add_u32 s48,s78,0x10000\n"
"ds_read_b64_tr_b16 a[32:33],v14 offset:256\n"
"v_mfma_f32_16x16x32_bf16 v[176:179],a[4:7],a[72:75],v[176:179]\n"
"ds_read_b64_tr_b16 a[34:35],v14 offset:2304\n"
"v_mfma_f32_16x16x32_bf16 v[184:187],a[0:3],a[68:71],v[184:187]\n"
"ds_read_b64_tr_b16 a[36:37],v14 offset:16640\n"
"v_add_u32 v13,0xffff0000,v13\n"
"v_mfma_f32_16x16x32_bf16 v[184:187],a[4:7],a[76:79],v[184:187]\n"
"s_waitcnt lgkmcnt(11)\n"
"ds_read_b64_tr_b16 a[38:39],v14 offset:18688\n"
"v_mfma_f32_16x16x32_bf16 v[180:183],a[8:11],a[64:67],v[180:183]\n"
"ds_read_b64_tr_b16 a[0:1],v15 offset:256\n"
"v_mfma_f32_16x16x32_bf16 v[180:183],a[12:15],a[72:75],v[180:183]\n"
"ds_read_b64_tr_b16 a[2:3],v15 offset:2304\n"
"v_mfma_f32_16x16x32_bf16 v[188:191],a[8:11],a[68:71],v[188:191]\n"
"ds_read_b64_tr_b16 a[4:5],v15 offset:16640\n"
"v_add_u32 v14,0xffff0000,v14\n"
"v_mfma_f32_16x16x32_bf16 v[188:191],a[12:15],a[76:79],v[188:191]\n"
"s_waitcnt lgkmcnt(11)\n"
"ds_read_b64_tr_b16 a[6:7],v15 offset:18688\n"
"v_mfma_f32_16x16x32_bf16 v[192:195],a[16:19],a[64:67],v[192:195]\n"
"ds_read_b64_tr_b16 a[8:9],v16 offset:256\n"
"v_mfma_f32_16x16x32_bf16 v[192:195],a[20:23],a[72:75],v[192:195]\n"
"ds_read_b64_tr_b16 a[10:11],v16 offset:2304\n"
"v_mfma_f32_16x16x32_bf16 v[200:203],a[16:19],a[68:71],v[200:203]\n"
"ds_read_b64_tr_b16 a[12:13],v16 offset:16640\n"
"v_add_u32 v15,0xffff0000,v15\n"
"v_mfma_f32_16x16x32_bf16 v[200:203],a[20:23],a[76:79],v[200:203]\n"
"s_waitcnt lgkmcnt(11)\n"
"ds_read_b64_tr_b16 a[14:15],v16 offset:18688\n"
"v_mfma_f32_16x16x32_bf16 v[196:199],a[24:27],a[64:67],v[196:199]\n"
"ds_read_b64_tr_b16 a[16:17],v17 offset:256\n"
"v_mfma_f32_16x16x32_bf16 v[196:199],a[28:31],a[72:75],v[196:199]\n"
"ds_read_b64_tr_b16 a[18:19],v17 offset:2304\n"
"v_mfma_f32_16x16x32_bf16 v[204:207],a[24:27],a[68:71],v[204:207]\n"
"ds_read_b64_tr_b16 a[20:21],v17 offset:16640\n"
"v_add_u32 v16,0xffff0000,v16\n"
"v_mfma_f32_16x16x32_bf16 v[204:207],a[28:31],a[76:79],v[204:207]\n"
"s_waitcnt lgkmcnt(11)\n"
"ds_read_b64_tr_b16 a[22:23],v17 offset:18688\n"
"v_mfma_f32_16x16x32_bf16 v[208:211],a[32:35],a[64:67],v[208:211]\n"
"ds_read_b64_tr_b16 a[24:25],v18 offset:256\n"
"v_mfma_f32_16x16x32_bf16 v[208:211],a[36:39],a[72:75],v[208:211]\n"
"ds_read_b64_tr_b16 a[26:27],v18 offset:2304\n"
"v_mfma_f32_16x16x32_bf16 v[216:219],a[32:35],a[68:71],v[216:219]\n"
"ds_read_b64_tr_b16 a[28:29],v18 offset:16640\n"
"v_add_u32 v17,0xffff0000,v17\n"
"v_mfma_f32_16x16x32_bf16 v[216:219],a[36:39],a[76:79],v[216:219]\n"
"s_waitcnt lgkmcnt(11)\n"
"ds_read_b64_tr_b16 a[30:31],v18 offset:18688\n"
"v_mfma_f32_16x16x32_bf16 v[212:215],a[0:3],a[64:67],v[212:215]\n"
"ds_read_b64_tr_b16 a[32:33],v19 offset:256\n"
"v_mfma_f32_16x16x32_bf16 v[212:215],a[4:7],a[72:75],v[212:215]\n"
"ds_read_b64_tr_b16 a[34:35],v19 offset:2304\n"
"v_mfma_f32_16x16x32_bf16 v[220:223],a[0:3],a[68:71],v[220:223]\n"
"s_waitcnt vmcnt(8)\n"
"s_barrier\n"
"ds_read_b64_tr_b16 a[36:37],v19 offset:16640\n"
"v_add_u32 v18,0xffff0000,v18\n"
"v_mfma_f32_16x16x32_bf16 v[220:223],a[4:7],a[76:79],v[220:223]\n"
"s_waitcnt lgkmcnt(11)\n"
"ds_read_b64_tr_b16 a[38:39],v19 offset:18688\n"
"v_mfma_f32_16x16x32_bf16 v[224:227],a[8:11],a[64:67],v[224:227]\n"
"ds_read_b64_tr_b16 a[0:1],v12 offset:0\n"
"v_mfma_f32_16x16x32_bf16 v[224:227],a[12:15],a[72:75],v[224:227]\n"
"ds_read_b64_tr_b16 a[2:3],v12 offset:2048\n"
"v_mfma_f32_16x16x32_bf16 v[232:235],a[8:11],a[68:71],v[232:235]\n"
"ds_read_b64_tr_b16 a[4:5],v12 offset:16384\n"
"v_add_u32 v19,0xffff0000,v19\n"
"v_mfma_f32_16x16x32_bf16 v[232:235],a[12:15],a[76:79],v[232:235]\n"
"s_waitcnt lgkmcnt(11)\n"
"ds_read_b64_tr_b16 a[6:7],v12 offset:18432\n"
"v_mfma_f32_16x16x32_bf16 v[228:231],a[16:19],a[64:67],v[228:231]\n"
"ds_read_b64_tr_b16 a[8:9],v13 offset:0\n"
"v_mfma_f32_16x16x32_bf16 v[228:231],a[20:23],a[72:75],v[228:231]\n"
"ds_read_b64_tr_b16 a[10:11],v13 offset:2048\n"
"v_mfma_f32_16x16x32_bf16 v[236:239],a[16:19],a[68:71],v[236:239]\n"
"ds_read_b64_tr_b16 a[12:13],v13 offset:16384\n"
"v_mfma_f32_16x16x32_bf16 v[236:239],a[20:23],a[76:79],v[236:239]\n"
"s_waitcnt lgkmcnt(11)\n"
"ds_read_b64_tr_b16 a[14:15],v13 offset:18432\n"
"v_mfma_f32_16x16x32_bf16 v[240:243],a[24:27],a[64:67],v[240:243]\n"
"ds_read_b64_tr_b16 a[16:17],v14 offset:0\n"
"v_mfma_f32_16x16x32_bf16 v[240:243],a[28:31],a[72:75],v[240:243]\n"
"ds_read_b64_tr_b16 a[18:19],v14 offset:2048\n"
"v_mfma_f32_16x16x32_bf16 v[248:251],a[24:27],a[68:71],v[248:251]\n"
"ds_read_b64_tr_b16 a[20:21],v14 offset:16384\n"
"v_mfma_f32_16x16x32_bf16 v[248:251],a[28:31],a[76:79],v[248:251]\n"
"s_waitcnt lgkmcnt(11)\n"
"ds_read_b64_tr_b16 a[22:23],v14 offset:18432\n"
"v_mfma_f32_16x16x32_bf16 v[244:247],a[32:35],a[64:67],v[244:247]\n"
"ds_read_b64_tr_b16 a[24:25],v15 offset:0\n"
"v_mfma_f32_16x16x32_bf16 v[244:247],a[36:39],a[72:75],v[244:247]\n"
"ds_read_b64_tr_b16 a[26:27],v15 offset:2048\n"
"v_mfma_f32_16x16x32_bf16 v[252:255],a[32:35],a[68:71],v[252:255]\n"
"ds_read_b64_tr_b16 a[28:29],v15 offset:16384\n"
"v_mfma_f32_16x16x32_bf16 v[252:255],a[36:39],a[76:79],v[252:255]\n"
"s_waitcnt lgkmcnt(11)\n"
"ds_read_b64_tr_b16 a[30:31],v15 offset:18432\n"
"v_mfma_f32_16x16x32_bf16 v[128:131],a[0:3],a[80:83],v[128:131]\n"
"ds_read_b64_tr_b16 a[32:33],v16 offset:0\n"
"v_mfma_f32_16x16x32_bf16 v[128:131],a[4:7],a[88:91],v[128:131]\n"
"ds_read_b64_tr_b16 a[34:35],v16 offset:2048\n"
"v_mfma_f32_16x16x32_bf16 v[136:139],a[0:3],a[84:87],v[136:139]\n"
"ds_read_b64_tr_b16 a[36:37],v16 offset:16384\n"
"v_mfma_f32_16x16x32_bf16 v[136:139],a[4:7],a[92:95],v[136:139]\n"
"s_waitcnt lgkmcnt(11)\n"
"ds_read_b64_tr_b16 a[38:39],v16 offset:18432\n"
"v_mfma_f32_16x16x32_bf16 v[132:135],a[8:11],a[80:83],v[132:135]\n"
"ds_read_b64_tr_b16 a[0:1],v17 offset:0\n"
"v_mfma_f32_16x16x32_bf16 v[132:135],a[12:15],a[88:91],v[132:135]\n"
"ds_read_b64_tr_b16 a[2:3],v17 offset:2048\n"
"v_mfma_f32_16x16x32_bf16 v[140:143],a[8:11],a[84:87],v[140:143]\n"
"ds_read_b64_tr_b16 a[4:5],v17 offset:16384\n"
"v_mfma_f32_16x16x32_bf16 v[140:143],a[12:15],a[92:95],v[140:143]\n"
"s_waitcnt lgkmcnt(11)\n"
"ds_read_b64_tr_b16 a[6:7],v17 offset:18432\n"
"v_mfma_f32_16x16x32_bf16 v[144:147],a[16:19],a[80:83],v[144:147]\n"
"ds_read_b64_tr_b16 a[8:9],v18 offset:0\n"
"v_mfma_f32_16x16x32_bf16 v[144:147],a[20:23],a[88:91],v[144:147]\n"
"ds_read_b64_tr_b16 a[10:11],v18 offset:2048\n"
"v_mfma_f32_16x16x32_bf16 v[152:155],a[16:19],a[84:87],v[152:155]\n"
"ds_read_b64_tr_b16 a[12:13],v18 offset:16384\n"
"v_mfma_f32_16x16x32_bf16 v[152:155],a[20:23],a[92:95],v[152:155]\n"
"s_waitcnt lgkmcnt(11)\n"
"ds_read_b64_tr_b16 a[14:15],v18 offset:18432\n"
"v_mfma_f32_16x16x32_bf16 v[148:151],a[24:27],a[80:83],v[148:151]\n"
"ds_read_b64_tr_b16 a[16:17],v19 offset:0\n"
"v_mfma_f32_16x16x32_bf16 v[148:151],a[28:31],a[88:91],v[148:151]\n"
"ds_read_b64_tr_b16 a[18:19],v19 offset:2048\n"
"v_mfma_f32_16x16x32_bf16 v[156:159],a[24:27],a[84:87],v[156:159]\n"
"ds_read_b64_tr_b16 a[20:21],v19 offset:16384\n"
"v_mfma_f32_16x16x32_bf16 v[156:159],a[28:31],a[92:95],v[156:159]\n"
"s_waitcnt lgkmcnt(11)\n"
"ds_read_b64_tr_b16 a[22:23],v19 offset:18432\n"
"v_mfma_f32_16x16x32_bf16 v[160:163],a[32:35],a[80:83],v[160:163]\n"
"ds_read_b64_tr_b16 a[24:25],v12 offset:256\n"
"v_mfma_f32_16x16x32_bf16 v[160:163],a[36:39],a[88:91],v[160:163]\n"
"ds_read_b64_tr_b16 a[26:27],v12 offset:2304\n"
"v_mfma_f32_16x16x32_bf16 v[168:171],a[32:35],a[84:87],v[168:171]\n"
"ds_read_b64_tr_b16 a[28:29],v12 offset:16640\n"
"v_mfma_f32_16x16x32_bf16 v[168:171],a[36:39],a[92:95],v[168:171]\n"
"s_waitcnt lgkmcnt(11)\n"
"ds_read_b64_tr_b16 a[30:31],v12 offset:18688\n"
"v_mfma_f32_16x16x32_bf16 v[164:167],a[0:3],a[80:83],v[164:167]\n"
"ds_read_b64_tr_b16 a[32:33],v13 offset:256\n"
"v_mfma_f32_16x16x32_bf16 v[164:167],a[4:7],a[88:91],v[164:167]\n"
"ds_read_b64_tr_b16 a[34:35],v13 offset:2304\n"
"global_load_dwordx2 v[2:3],v5,s[82:83] sc1\n"
"v_mfma_f32_16x16x32_bf16 v[172:175],a[0:3],a[84:87],v[172:175]\n"
"ds_read_b64_tr_b16 a[36:37],v13 offset:16640\n"
"v_mfma_f32_16x16x32_bf16 v[172:175],a[4:7],a[92:95],v[172:175]\n"
"s_waitcnt lgkmcnt(11)\n"
"ds_read_b64_tr_b16 a[38:39],v13 offset:18688\n"
"v_mfma_f32_16x16x32_bf16 v[176:179],a[8:11],a[80:83],v[176:179]\n"
"ds_read_b64_tr_b16 a[0:1],v14 offset:256\n"
"v_mfma_f32_16x16x32_bf16 v[176:179],a[12:15],a[88:91],v[176:179]\n"
"ds_read_b64_tr_b16 a[2:3],v14 offset:2304\n"
"v_mfma_f32_16x16x32_bf16 v[184:187],a[8:11],a[84:87],v[184:187]\n"
"ds_read_b64_tr_b16 a[4:5],v14 offset:16640\n"
"v_mfma_f32_16x16x32_bf16 v[184:187],a[12:15],a[92:95],v[184:187]\n"
"s_waitcnt lgkmcnt(11)\n"
"ds_read_b64_tr_b16 a[6:7],v14 offset:18688\n"
"v_mfma_f32_16x16x32_bf16 v[180:183],a[16:19],a[80:83],v[180:183]\n"
"ds_read_b64_tr_b16 a[8:9],v15 offset:256\n"
"v_mfma_f32_16x16x32_bf16 v[180:183],a[20:23],a[88:91],v[180:183]\n"
"ds_read_b64_tr_b16 a[10:11],v15 offset:2304\n"
"v_mfma_f32_16x16x32_bf16 v[188:191],a[16:19],a[84:87],v[188:191]\n"
"ds_read_b64_tr_b16 a[12:13],v15 offset:16640\n"
"v_mfma_f32_16x16x32_bf16 v[188:191],a[20:23],a[92:95],v[188:191]\n"
"s_waitcnt lgkmcnt(11)\n"
"ds_read_b64_tr_b16 a[14:15],v15 offset:18688\n"
"v_mfma_f32_16x16x32_bf16 v[192:195],a[24:27],a[80:83],v[192:195]\n"
"ds_read_b64_tr_b16 a[16:17],v16 offset:256\n"
"v_mfma_f32_16x16x32_bf16 v[192:195],a[28:31],a[88:91],v[192:195]\n"
"ds_read_b64_tr_b16 a[18:19],v16 offset:2304\n"
"v_mfma_f32_16x16x32_bf16 v[200:203],a[24:27],a[84:87],v[200:203]\n"
"ds_read_b64_tr_b16 a[20:21],v16 offset:16640\n"
"v_mfma_f32_16x16x32_bf16 v[200:203],a[28:31],a[92:95],v[200:203]\n"
"s_waitcnt lgkmcnt(11)\n"
"ds_read_b64_tr_b16 a[22:23],v16 offset:18688\n"
"v_mfma_f32_16x16x32_bf16 v[196:199],a[32:35],a[80:83],v[196:199]\n"
"ds_read_b64_tr_b16 a[24:25],v17 offset:256\n"
"v_mfma_f32_16x16x32_bf16 v[196:199],a[36:39],a[88:91],v[196:199]\n"
"ds_read_b64_tr_b16 a[26:27],v17 offset:2304\n"
"v_mfma_f32_16x16x32_bf16 v[204:207],a[32:35],a[84:87],v[204:207]\n"
"ds_read_b64_tr_b16 a[28:29],v17 offset:16640\n"
"v_mfma_f32_16x16x32_bf16 v[204:207],a[36:39],a[92:95],v[204:207]\n"
"s_waitcnt lgkmcnt(11)\n"
"ds_read_b64_tr_b16 a[30:31],v17 offset:18688\n"
"v_mfma_f32_16x16x32_bf16 v[208:211],a[0:3],a[80:83],v[208:211]\n"
"ds_read_b64_tr_b16 a[32:33],v18 offset:256\n"
"v_mfma_f32_16x16x32_bf16 v[208:211],a[4:7],a[88:91],v[208:211]\n"
"ds_read_b64_tr_b16 a[34:35],v18 offset:2304\n"
"v_mfma_f32_16x16x32_bf16 v[216:219],a[0:3],a[84:87],v[216:219]\n"
"ds_read_b64_tr_b16 a[36:37],v18 offset:16640\n"
"v_mfma_f32_16x16x32_bf16 v[216:219],a[4:7],a[92:95],v[216:219]\n"
"s_waitcnt lgkmcnt(11)\n"
"ds_read_b64_tr_b16 a[38:39],v18 offset:18688\n"
"v_mfma_f32_16x16x32_bf16 v[212:215],a[8:11],a[80:83],v[212:215]\n"
"ds_read_b64_tr_b16 a[0:1],v19 offset:256\n"
"v_mfma_f32_16x16x32_bf16 v[212:215],a[12:15],a[88:91],v[212:215]\n"
"ds_read_b64_tr_b16 a[2:3],v19 offset:2304\n"
"v_mfma_f32_16x16x32_bf16 v[220:223],a[8:11],a[84:87],v[220:223]\n"
"s_waitcnt vmcnt(1)\n"
"s_barrier\n"
"ds_read_b64_tr_b16 a[4:5],v19 offset:16640\n"
"v_mfma_f32_16x16x32_bf16 v[220:223],a[12:15],a[92:95],v[220:223]\n"
"s_waitcnt lgkmcnt(11)\n"
"ds_read_b64_tr_b16 a[6:7],v19 offset:18688\n"
"v_mfma_f32_16x16x32_bf16 v[224:227],a[16:19],a[80:83],v[224:227]\n"
"s_waitcnt vmcnt(0)\n"
"ds_read_b64_tr_b16 a[8:9],v12 offset:32768\n"
"v_mfma_f32_16x16x32_bf16 v[224:227],a[20:23],a[88:91],v[224:227]\n"
"ds_read_b64_tr_b16 a[10:11],v12 offset:34816\n"
"v_cmp_ne_u64 vcc,s[34:35],v[2:3]\n"
"v_mfma_f32_16x16x32_bf16 v[232:235],a[16:19],a[84:87],v[232:235]\n"
"ds_read_b64_tr_b16 a[12:13],v12 offset:49152\n"
"v_mfma_f32_16x16x32_bf16 v[232:235],a[20:23],a[92:95],v[232:235]\n"
"s_waitcnt lgkmcnt(11)\n"
"ds_read_b64_tr_b16 a[14:15],v12 offset:51200\n"
"v_mfma_f32_16x16x32_bf16 v[228:231],a[24:27],a[80:83],v[228:231]\n"
"s_cbranch_vccnz L_kfk_%=\n"
"L_kfk_ret_%=:\n"
"ds_read_b64_tr_b16 a[16:17],v13 offset:32768\n"
"v_mfma_f32_16x16x32_bf16 v[228:231],a[28:31],a[88:91],v[228:231]\n"
"ds_read_b64_tr_b16 a[18:19],v13 offset:34816\n"
"v_mfma_f32_16x16x32_bf16 v[236:239],a[24:27],a[84:87],v[236:239]\n"
"s_add_u32 m0,s57,s90\n"
"ds_read_b64_tr_b16 a[20:21],v13 offset:49152\n"
"v_mfma_f32_16x16x32_bf16 v[236:239],a[28:31],a[92:95],v[236:239]\n"
"s_waitcnt lgkmcnt(11)\n"
"ds_read_b64_tr_b16 a[22:23],v13 offset:51200\n"
"buffer_load_dwordx4 v10,s[36:39],s78 offen offset:0 sc1 lds\n"
"v_mfma_f32_16x16x32_bf16 v[240:243],a[32:35],a[80:83],v[240:243]\n"
"ds_read_b64_tr_b16 a[24:25],v14 offset:32768\n"
"v_mfma_f32_16x16x32_bf16 v[240:243],a[36:39],a[88:91],v[240:243]\n"
"ds_read_b64_tr_b16 a[26:27],v14 offset:34816\n"
"v_mfma_f32_16x16x32_bf16 v[248:251],a[32:35],a[84:87],v[248:251]\n"
"ds_read_b64_tr_b16 a[28:29],v14 offset:49152\n"
"v_mfma_f32_16x16x32_bf16 v[248:251],a[36:39],a[92:95],v[248:251]\n"
"s_waitcnt lgkmcnt(11)\n"
"ds_read_b64_tr_b16 a[30:31],v14 offset:51200\n"
"buffer_load_dwordx4 v10,s[36:39],s78 offen offset:1024 sc1 lds\n"
"v_mfma_f32_16x16x32_bf16 v[244:247],a[0:3],a[80:83],v[244:247]\n"
"ds_read_b64_tr_b16 a[32:33],v15 offset:32768\n"
"v_mfma_f32_16x16x32_bf16 v[244:247],a[4:7],a[88:91],v[244:247]\n"
"ds_read_b64_tr_b16 a[34:35],v15 offset:34816\n"
"v_mfma_f32_16x16x32_bf16 v[252:255],a[0:3],a[84:87],v[252:255]\n"
"ds_read_b64_tr_b16 a[36:37],v15 offset:49152\n"
"v_mfma_f32_16x16x32_bf16 v[252:255],a[4:7],a[92:95],v[252:255]\n"
"s_waitcnt lgkmcnt(11)\n"
"ds_read_b64_tr_b16 a[38:39],v15 offset:51200\n"
"buffer_load_dwordx4 v10,s[36:39],s78 offen offset:2048 sc1 lds\n"
"v_mfma_f32_16x16x32_bf16 v[128:131],a[8:11],a[96:99],v[128:131]\n"
"ds_read_b64_tr_b16 a[0:1],v16 offset:32768\n"
"v_mfma_f32_16x16x32_bf16 v[128:131],a[12:15],a[104:107],v[128:131]\n"
"ds_read_b64_tr_b16 a[2:3],v16 offset:34816\n"
"v_mfma_f32_16x16x32_bf16 v[136:139],a[8:11],a[100:103],v[136:139]\n"
"ds_read_b64_tr_b16 a[4:5],v16 offset:49152\n"
"v_mfma_f32_16x16x32_bf16 v[136:139],a[12:15],a[108:111],v[136:139]\n"
"s_waitcnt lgkmcnt(11)\n"
"ds_read_b64_tr_b16 a[6:7],v16 offset:51200\n"
"buffer_load_dwordx4 v10,s[36:39],s78 offen offset:3072 sc1 lds\n"
"v_mfma_f32_16x16x32_bf16 v[132:135],a[16:19],a[96:99],v[132:135]\n"
"ds_read_b64_tr_b16 a[8:9],v17 offset:32768\n"
"v_mfma_f32_16x16x32_bf16 v[132:135],a[20:23],a[104:107],v[132:135]\n"
"ds_read_b64_tr_b16 a[10:11],v17 offset:34816\n"
"v_mfma_f32_16x16x32_bf16 v[140:143],a[16:19],a[100:103],v[140:143]\n"
"s_add_u32 m0,m0,0x1000\n"
"ds_read_b64_tr_b16 a[12:13],v17 offset:49152\n"
"v_mfma_f32_16x16x32_bf16 v[140:143],a[20:23],a[108:111],v[140:143]\n"
"s_waitcnt lgkmcnt(11)\n"
"ds_read_b64_tr_b16 a[14:15],v17 offset:51200\n"
"buffer_load_dwordx4 v7,s[36:39],s78 offen offset:0 sc1 lds\n"
"v_mfma_f32_16x16x32_bf16 v[144:147],a[24:27],a[96:99],v[144:147]\n"
"ds_read_b64_tr_b16 a[16:17],v18 offset:32768\n"
"v_mfma_f32_16x16x32_bf16 v[144:147],a[28:31],a[104:107],v[144:147]\n"
"ds_read_b64_tr_b16 a[18:19],v18 offset:34816\n"
"v_cvt_pk_bf16_f32 v128,v128,v129\n"
"v_mfma_f32_16x16x32_bf16 v[152:155],a[24:27],a[100:103],v[152:155]\n"
"ds_read_b64_tr_b16 a[20:21],v18 offset:49152\n"
"v_cvt_pk_bf16_f32 v129,v130,v131\n"
"v_mfma_f32_16x16x32_bf16 v[152:155],a[28:31],a[108:111],v[152:155]\n"
"s_waitcnt lgkmcnt(11)\n"
"ds_read_b64_tr_b16 a[22:23],v18 offset:51200\n"
"buffer_load_dwordx4 v7,s[36:39],s78 offen offset:1024 sc1 lds\n"
"v_mfma_f32_16x16x32_bf16 v[148:151],a[32:35],a[96:99],v[148:151]\n"
"ds_read_b64_tr_b16 a[24:25],v19 offset:32768\n"
"v_cvt_pk_bf16_f32 v130,v132,v133\n"
"v_mfma_f32_16x16x32_bf16 v[148:151],a[36:39],a[104:107],v[148:151]\n"
"ds_read_b64_tr_b16 a[26:27],v19 offset:34816\n"
"v_cvt_pk_bf16_f32 v131,v134,v135\n"
"v_mfma_f32_16x16x32_bf16 v[156:159],a[32:35],a[100:103],v[156:159]\n"
"ds_read_b64_tr_b16 a[28:29],v19 offset:49152\n"
"v_accvgpr_write_b32 a128,v128\n"
"v_mfma_f32_16x16x32_bf16 v[156:159],a[36:39],a[108:111],v[156:159]\n"
"s_waitcnt lgkmcnt(11)\n"
"ds_read_b64_tr_b16 a[30:31],v19 offset:51200\n"
"buffer_load_dwordx4 v7,s[36:39],s78 offen offset:2048 sc1 lds\n"
"v_mfma_f32_16x16x32_bf16 v[160:163],a[0:3],a[96:99],v[160:163]\n"
"ds_read_b64_tr_b16 a[32:33],v12 offset:33024\n"
"v_accvgpr_write_b32 a129,v129\n"
"v_mfma_f32_16x16x32_bf16 v[160:163],a[4:7],a[104:107],v[160:163]\n"
"ds_read_b64_tr_b16 a[34:35],v12 offset:35072\n"
"v_accvgpr_write_b32 a130,v130\n"
"v_mfma_f32_16x16x32_bf16 v[168:171],a[0:3],a[100:103],v[168:171]\n"
"ds_read_b64_tr_b16 a[36:37],v12 offset:49408\n"
"v_accvgpr_write_b32 a131,v131\n"
"v_mfma_f32_16x16x32_bf16 v[168:171],a[4:7],a[108:111],v[168:171]\n"
"s_waitcnt lgkmcnt(11)\n"
"ds_read_b64_tr_b16 a[38:39],v12 offset:51456\n"
"buffer_load_dwordx4 v7,s[36:39],s78 offen offset:3072 sc1 lds\n"
"v_mfma_f32_16x16x32_bf16 v[164:167],a[8:11],a[96:99],v[164:167]\n"
"ds_read_b64_tr_b16 a[0:1],v13 offset:33024\n"
"v_cvt_pk_bf16_f32 v136,v136,v137\n"
"v_mfma_f32_16x16x32_bf16 v[164:167],a[12:15],a[104:107],v[164:167]\n"
"ds_read_b64_tr_b16 a[2:3],v13 offset:35072\n"
"v_cvt_pk_bf16_f32 v137,v138,v139\n"
"v_mfma_f32_16x16x32_bf16 v[172:175],a[8:11],a[100:103],v[172:175]\n"
"s_add_u32 m0,s57,s91\n"
"ds_read_b64_tr_b16 a[4:5],v13 offset:49408\n"
"v_cvt_pk_bf16_f32 v138,v140,v141\n"
"v_mfma_f32_16x16x32_bf16 v[172:175],a[12:15],a[108:111],v[172:175]\n"
"s_waitcnt lgkmcnt(11)\n"
"ds_read_b64_tr_b16 a[6:7],v13 offset:51456\n"
"buffer_load_dwordx4 v10,s[36:39],s48 offen offset:0 sc1 lds\n"
"v_mfma_f32_16x16x32_bf16 v[176:179],a[16:19],a[96:99],v[176:179]\n"
"ds_read_b64_tr_b16 a[8:9],v14 offset:33024\n"
"v_cvt_pk_bf16_f32 v139,v142,v143\n"
"v_mfma_f32_16x16x32_bf16 v[176:179],a[20:23],a[104:107],v[176:179]\n"
"ds_read_b64_tr_b16 a[10:11],v14 offset:35072\n"
"v_accvgpr_write_b32 a160,v136\n"
"v_mfma_f32_16x16x32_bf16 v[184:187],a[16:19],a[100:103],v[184:187]\n"
"ds_read_b64_tr_b16 a[12:13],v14 offset:49408\n"
"v_accvgpr_write_b32 a161,v137\n"
"v_mfma_f32_16x16x32_bf16 v[184:187],a[20:23],a[108:111],v[184:187]\n"
"s_waitcnt lgkmcnt(11)\n"
"ds_read_b64_tr_b16 a[14:15],v14 offset:51456\n"
"buffer_load_dwordx4 v10,s[36:39],s48 offen offset:1024 sc1 lds\n"
"v_mfma_f32_16x16x32_bf16 v[180:183],a[24:27],a[96:99],v[180:183]\n"
"ds_read_b64_tr_b16 a[16:17],v15 offset:33024\n"
"v_accvgpr_write_b32 a162,v138\n"
"v_mfma_f32_16x16x32_bf16 v[180:183],a[28:31],a[104:107],v[180:183]\n"
"ds_read_b64_tr_b16 a[18:19],v15 offset:35072\n"
"v_accvgpr_write_b32 a163,v139\n"
"v_mfma_f32_16x16x32_bf16 v[188:191],a[24:27],a[100:103],v[188:191]\n"
"ds_read_b64_tr_b16 a[20:21],v15 offset:49408\n"
"v_cvt_pk_bf16_f32 v144,v144,v145\n"
"v_mfma_f32_16x16x32_bf16 v[188:191],a[28:31],a[108:111],v[188:191]\n"
"s_waitcnt lgkmcnt(11)\n"
"ds_read_b64_tr_b16 a[22:23],v15 offset:51456\n"
"buffer_load_dwordx4 v10,s[36:39],s48 offen offset:2048 sc1 lds\n"
"v_mfma_f32_16x16x32_bf16 v[192:195],a[32:35],a[96:99],v[192:195]\n"
"ds_read_b64_tr_b16 a[24:25],v16 offset:33024\n"
"v_cvt_pk_bf16_f32 v145,v146,v147\n"
"v_mfma_f32_16x16x32_bf16 v[192:195],a[36:39],a[104:107],v[192:195]\n"
"ds_read_b64_tr_b16 a[26:27],v16 offset:35072\n"
"v_cvt_pk_bf16_f32 v146,v148,v149\n"
"v_mfma_f32_16x16x32_bf16 v[200:203],a[32:35],a[100:103],v[200:203]\n"
"ds_read_b64_tr_b16 a[28:29],v16 offset:49408\n"
"v_cvt_pk_bf16_f32 v147,v150,v151\n"
"v_mfma_f32_16x16x32_bf16 v[200:203],a[36:39],a[108:111],v[200:203]\n"
"s_waitcnt lgkmcnt(11)\n"
"ds_read_b64_tr_b16 a[30:31],v16 offset:51456\n"
"buffer_load_dwordx4 v10,s[36:39],s48 offen offset:3072 sc1 lds\n"
"v_mfma_f32_16x16x32_bf16 v[196:199],a[0:3],a[96:99],v[196:199]\n"
"ds_read_b64_tr_b16 a[32:33],v17 offset:33024\n"
"v_accvgpr_write_b32 a132,v144\n"
"v_mfma_f32_16x16x32_bf16 v[196:199],a[4:7],a[104:107],v[196:199]\n"
"ds_read_b64_tr_b16 a[34:35],v17 offset:35072\n"
"v_accvgpr_write_b32 a133,v145\n"
"v_mfma_f32_16x16x32_bf16 v[204:207],a[0:3],a[100:103],v[204:207]\n"
"s_add_u32 m0,m0,0x1000\n"
"ds_read_b64_tr_b16 a[36:37],v17 offset:49408\n"
"v_accvgpr_write_b32 a134,v146\n"
"v_mfma_f32_16x16x32_bf16 v[204:207],a[4:7],a[108:111],v[204:207]\n"
"s_waitcnt lgkmcnt(11)\n"
"ds_read_b64_tr_b16 a[38:39],v17 offset:51456\n"
"buffer_load_dwordx4 v7,s[36:39],s48 offen offset:0 sc1 lds\n"
"v_mfma_f32_16x16x32_bf16 v[208:211],a[8:11],a[96:99],v[208:211]\n"
"ds_read_b64_tr_b16 a[0:1],v18 offset:33024\n"
"v_accvgpr_write_b32 a135,v147\n"
"v_mfma_f32_16x16x32_bf16 v[208:211],a[12:15],a[104:107],v[208:211]\n"
"ds_read_b64_tr_b16 a[2:3],v18 offset:35072\n"
"v_cvt_pk_bf16_f32 v152,v152,v153\n"
"v_mfma_f32_16x16x32_bf16 v[216:219],a[8:11],a[100:103],v[216:219]\n"
"ds_read_b64_tr_b16 a[4:5],v18 offset:49408\n"
"v_cvt_pk_bf16_f32 v153,v154,v155\n"
"v_mfma_f32_16x16x32_bf16 v[216:219],a[12:15],a[108:111],v[216:219]\n"
"s_waitcnt lgkmcnt(11)\n"
"ds_read_b64_tr_b16 a[6:7],v18 offset:51456\n"
"buffer_load_dwordx4 v7,s[36:39],s48 offen offset:1024 sc1 lds\n"
"v_mfma_f32_16x16x32_bf16 v[212:215],a[16:19],a[96:99],v[212:215]\n"
"ds_read_b64_tr_b16 a[8:9],v19 offset:33024\n"
"v_cvt_pk_bf16_f32 v154,v156,v157\n"
"v_mfma_f32_16x16x32_bf16 v[212:215],a[20:23],a[104:107],v[212:215]\n"
"ds_read_b64_tr_b16 a[10:11],v19 offset:35072\n"
"v_cvt_pk_bf16_f32 v155,v158,v159\n"
"v_mfma_f32_16x16x32_bf16 v[220:223],a[16:19],a[100:103],v[220:223]\n"
"ds_read_b64_tr_b16 a[12:13],v19 offset:49408\n"
"v_accvgpr_write_b32 a164,v152\n"
"v_mfma_f32_16x16x32_bf16 v[220:223],a[20:23],a[108:111],v[220:223]\n"
"s_waitcnt lgkmcnt(11)\n"
"ds_read_b64_tr_b16 a[14:15],v19 offset:51456\n"
"buffer_load_dwordx4 v7,s[36:39],s48 offen offset:2048 sc1 lds\n"
"v_mfma_f32_16x16x32_bf16 v[224:227],a[24:27],a[96:99],v[224:227]\n"
"v_accvgpr_write_b32 a165,v153\n"
"v_accvgpr_write_b32 a166,v154\n"
"v_mfma_f32_16x16x32_bf16 v[224:227],a[28:31],a[104:107],v[224:227]\n"
"v_accvgpr_write_b32 a167,v155\n"
"v_cvt_pk_bf16_f32 v160,v160,v161\n"
"v_mfma_f32_16x16x32_bf16 v[232:235],a[24:27],a[100:103],v[232:235]\n"
"v_cvt_pk_bf16_f32 v161,v162,v163\n"
"v_cvt_pk_bf16_f32 v162,v164,v165\n"
"v_mfma_f32_16x16x32_bf16 v[232:235],a[28:31],a[108:111],v[232:235]\n"
"s_waitcnt lgkmcnt(8)\n"
"buffer_load_dwordx4 v7,s[36:39],s48 offen offset:3072 sc1 lds\n"
"v_mfma_f32_16x16x32_bf16 v[228:231],a[32:35],a[96:99],v[228:231]\n"
"v_cvt_pk_bf16_f32 v163,v166,v167\n"
"v_accvgpr_write_b32 a136,v160\n"
"v_mfma_f32_16x16x32_bf16 v[228:231],a[36:39],a[104:107],v[228:231]\n"
"v_accvgpr_write_b32 a137,v161\n"
"v_accvgpr_write_b32 a138,v162\n"
"v_mfma_f32_16x16x32_bf16 v[236:239],a[32:35],a[100:103],v[236:239]\n"
"v_accvgpr_write_b32 a139,v163\n"
"v_cvt_pk_bf16_f32 v168,v168,v169\n"
"v_mfma_f32_16x16x32_bf16 v[236:239],a[36:39],a[108:111],v[236:239]\n"
"s_waitcnt lgkmcnt(4)\n"
"v_cvt_pk_bf16_f32 v169,v170,v171\n"
"v_cvt_pk_bf16_f32 v170,v172,v173\n"
"v_mfma_f32_16x16x32_bf16 v[240:243],a[0:3],a[96:99],v[240:243]\n"
"v_cvt_pk_bf16_f32 v171,v174,v175\n"
"v_accvgpr_write_b32 a168,v168\n"
"v_mfma_f32_16x16x32_bf16 v[240:243],a[4:7],a[104:107],v[240:243]\n"
"v_accvgpr_write_b32 a169,v169\n"
"v_accvgpr_write_b32 a170,v170\n"
"v_mfma_f32_16x16x32_bf16 v[248:251],a[0:3],a[100:103],v[248:251]\n"
"v_accvgpr_write_b32 a171,v171\n"
"v_cvt_pk_bf16_f32 v176,v176,v177\n"
"v_mfma_f32_16x16x32_bf16 v[248:251],a[4:7],a[108:111],v[248:251]\n"
"s_waitcnt lgkmcnt(0)\n"
"v_cvt_pk_bf16_f32 v177,v178,v179\n"
"v_cvt_pk_bf16_f32 v178,v180,v181\n"
"v_mfma_f32_16x16x32_bf16 v[244:247],a[8:11],a[96:99],v[244:247]\n"
"v_cvt_pk_bf16_f32 v179,v182,v183\n"
"v_accvgpr_write_b32 a140,v176\n"
"v_mfma_f32_16x16x32_bf16 v[244:247],a[12:15],a[104:107],v[244:247]\n"
"v_accvgpr_write_b32 a141,v177\n"
"v_accvgpr_write_b32 a142,v178\n"
"v_mfma_f32_16x16x32_bf16 v[252:255],a[8:11],a[100:103],v[252:255]\n"
"v_accvgpr_write_b32 a143,v179\n"
"v_cvt_pk_bf16_f32 v184,v184,v185\n"
"v_mfma_f32_16x16x32_bf16 v[252:255],a[12:15],a[108:111],v[252:255]\n"
"v_cvt_pk_bf16_f32 v185,v186,v187\n"
"v_cvt_pk_bf16_f32 v186,v188,v189\n"
"v_cvt_pk_bf16_f32 v187,v190,v191\n"
"v_accvgpr_write_b32 a172,v184\n"
"v_accvgpr_write_b32 a173,v185\n"
"v_accvgpr_write_b32 a174,v186\n"
"v_accvgpr_write_b32 a175,v187\n"
"v_cvt_pk_bf16_f32 v192,v192,v193\n"
"v_cvt_pk_bf16_f32 v193,v194,v195\n"
"v_cvt_pk_bf16_f32 v194,v196,v197\n"
"v_cvt_pk_bf16_f32 v195,v198,v199\n"
"v_accvgpr_write_b32 a144,v192\n"
"v_accvgpr_write_b32 a145,v193\n"
"v_accvgpr_write_b32 a146,v194\n"
"v_accvgpr_write_b32 a147,v195\n"
"v_cvt_pk_bf16_f32 v200,v200,v201\n"
"v_cvt_pk_bf16_f32 v201,v202,v203\n"
"v_cvt_pk_bf16_f32 v202,v204,v205\n"
"v_cvt_pk_bf16_f32 v203,v206,v207\n"
"v_accvgpr_write_b32 a176,v200\n"
"v_accvgpr_write_b32 a177,v201\n"
"v_accvgpr_write_b32 a178,v202\n"
"v_accvgpr_write_b32 a179,v203\n"
"v_cvt_pk_bf16_f32 v208,v208,v209\n"
"v_cvt_pk_bf16_f32 v209,v210,v211\n"
"v_cvt_pk_bf16_f32 v210,v212,v213\n"
"v_cvt_pk_bf16_f32 v211,v214,v215\n"
"v_accvgpr_write_b32 a148,v208\n"
"v_accvgpr_write_b32 a149,v209\n"
"v_accvgpr_write_b32 a150,v210\n"
"v_accvgpr_write_b32 a151,v211\n"
"v_cvt_pk_bf16_f32 v216,v216,v217\n"
"v_cvt_pk_bf16_f32 v217,v218,v219\n"
"v_cvt_pk_bf16_f32 v218,v220,v221\n"
"v_cvt_pk_bf16_f32 v219,v222,v223\n"
"v_accvgpr_write_b32 a180,v216\n"
"v_accvgpr_write_b32 a181,v217\n"
"v_accvgpr_write_b32 a182,v218\n"
"v_accvgpr_write_b32 a183,v219\n"
"v_cvt_pk_bf16_f32 v224,v224,v225\n"
"v_cvt_pk_bf16_f32 v225,v226,v227\n"
"v_cvt_pk_bf16_f32 v226,v228,v229\n"
"v_cvt_pk_bf16_f32 v227,v230,v231\n"
"v_accvgpr_write_b32 a152,v224\n"
"v_accvgpr_write_b32 a153,v225\n"
"v_accvgpr_write_b32 a154,v226\n"
"v_accvgpr_write_b32 a155,v227\n"
"v_cvt_pk_bf16_f32 v232,v232,v233\n"
"v_cvt_pk_bf16_f32 v233,v234,v235\n"
"v_cvt_pk_bf16_f32 v234,v236,v237\n"
"v_cvt_pk_bf16_f32 v235,v238,v239\n"
"v_accvgpr_write_b32 a184,v232\n"
"v_accvgpr_write_b32 a185,v233\n"
"v_accvgpr_write_b32 a186,v234\n"
"v_accvgpr_write_b32 a187,v235\n"
"v_cvt_pk_bf16_f32 v240,v240,v241\n"
"v_cvt_pk_bf16_f32 v241,v242,v243\n"
"v_cvt_pk_bf16_f32 v242,v244,v245\n"
"v_cvt_pk_bf16_f32 v243,v246,v247\n"
"v_accvgpr_write_b32 a156,v240\n"
"v_accvgpr_write_b32 a157,v241\n"
"v_accvgpr_write_b32 a158,v242\n"
"v_accvgpr_write_b32 a159,v243\n"
"v_cvt_pk_bf16_f32 v248,v248,v249\n"
"v_cvt_pk_bf16_f32 v249,v250,v251\n"
"v_cvt_pk_bf16_f32 v250,v252,v253\n"
"v_cvt_pk_bf16_f32 v251,v254,v255\n"
"v_accvgpr_write_b32 a188,v248\n"
"v_accvgpr_write_b32 a189,v249\n"
"v_accvgpr_write_b32 a190,v250\n"
"v_accvgpr_write_b32 a191,v251\n"
"s_branch L_qdone_%=\n"
"L_kv0v_%=:\n"
"v_mfma_f32_16x16x32_bf16 v[128:131],v[96:99],a[0:3],0\n"
"s_add_u32 m0,s57,0x0\n"
"ds_read_b128 v[112:115],v10 offset:4096\n"
"v_mfma_f32_16x16x32_bf16 v[128:131],v[100:103],a[4:7],v[128:131]\n"
"buffer_load_dwordx4 v32,s[40:43],s49 offen offset:0 nt lds\n"
"v_mfma_f32_16x16x32_bf16 v[136:139],v[96:99],a[8:11],0\n"
"ds_read_b128 v[116:119],v11 offset:4096\n"
"v_mfma_f32_16x16x32_bf16 v[136:139],v[100:103],a[12:15],v[136:139]\n"
"v_mfma_f32_16x16x32_bf16 v[144:147],v[96:99],a[16:19],0\n"
"v_mfma_f32_16x16x32_bf16 v[144:147],v[100:103],a[20:23],v[144:147]\n"
"buffer_load_dwordx4 v33,s[40:43],s49 offen offset:1024 nt lds\n"
"v_mfma_f32_16x16x32_bf16 v[152:155],v[96:99],a[24:27],0\n"
"v_mfma_f32_16x16x32_bf16 v[152:155],v[100:103],a[28:31],v[152:155]\n"
"s_waitcnt vmcnt(18)\n"
"s_barrier\n"
"v_mfma_f32_16x16x32_bf16 v[160:163],v[96:99],a[32:35],0\n"
"ds_read_b64_tr_b16 a[64:65],v12 offset:32768\n"
"v_mfma_f32_16x16x32_bf16 v[160:163],v[100:103],a[36:39],v[160:163]\n"
"buffer_load_dwordx4 v34,s[40:43],s49 offen offset:2048 nt lds\n"
"v_mfma_f32_16x16x32_bf16 v[168:171],v[96:99],a[40:43],0\n"
"ds_read_b64_tr_b16 a[66:67],v12 offset:34816\n"
"v_mfma_f32_16x16x32_bf16 v[168:171],v[100:103],a[44:47],v[168:171]\n"
"ds_read_b64_tr_b16 a[68:69],v12 offset:49152\n"
"v_mfma_f32_16x16x32_bf16 v[176:179],v[96:99],a[48:51],0\n"
"ds_read_b64_tr_b16 a[70:71],v12 offset:51200\n"
"v_mfma_f32_16x16x32_bf16 v[176:179],v[100:103],a[52:55],v[176:179]\n"
"buffer_load_dwordx4 v35,s[40:43],s49 offen offset:3072 nt lds\n"
"v_mfma_f32_16x16x32_bf16 v[184:187],v[96:99],a[56:59],0\n"
"ds_read_b64_tr_b16 a[72:73],v13 offset:32768\n"
"v_mfma_f32_16x16x32_bf16 v[184:187],v[100:103],a[60:63],v[184:187]\n"
"ds_read_b64_tr_b16 a[74:75],v13 offset:34816\n"
"v_mfma_f32_16x16x32_bf16 v[132:135],v[104:107],a[0:3],0\n"
"s_add_u32 m0,s57,0x1000\n"
"ds_read_b128 v[120:123],v10 offset:6144\n"
"v_mfma_f32_16x16x32_bf16 v[132:135],v[108:111],a[4:7],v[132:135]\n"
"buffer_load_dwordx4 v36,s[40:43],s49 offen offset:0 nt lds\n"
"v_mfma_f32_16x16x32_bf16 v[140:143],v[104:107],a[8:11],0\n"
"ds_read_b128 v[124:127],v11 offset:6144\n"
"v_mfma_f32_16x16x32_bf16 v[140:143],v[108:111],a[12:15],v[140:143]\n"
"ds_read_b64_tr_b16 a[76:77],v13 offset:49152\n"
"v_mfma_f32_16x16x32_bf16 v[148:151],v[104:107],a[16:19],0\n"
"ds_read_b64_tr_b16 a[78:79],v13 offset:51200\n"
"v_mfma_f32_16x16x32_bf16 v[148:151],v[108:111],a[20:23],v[148:151]\n"
"buffer_load_dwordx4 v37,s[40:43],s49 offen offset:1024 nt lds\n"
"v_mfma_f32_16x16x32_bf16 v[156:159],v[104:107],a[24:27],0\n"
"ds_read_b64_tr_b16 a[80:81],v14 offset:32768\n"
"v_mfma_f32_16x16x32_bf16 v[156:159],v[108:111],a[28:31],v[156:159]\n"
"ds_read_b64_tr_b16 a[82:83],v14 offset:34816\n"
"v_mfma_f32_16x16x32_bf16 v[164:167],v[104:107],a[32:35],0\n"
"s_waitcnt vmcnt(13)\n"
"v_readfirstlane_b32 s76,v84\n"
"v_readfirstlane_b32 s77,v85\n"
"v_mfma_f32_16x16x32_bf16 v[164:167],v[108:111],a[36:39],v[164:167]\n"
"buffer_load_dwordx4 v38,s[40:43],s49 offen offset:2048 nt lds\n"
"v_mfma_f32_16x16x32_bf16 v[172:175],v[104:107],a[40:43],0\n"
"ds_read_b64_tr_b16 a[84:85],v14 offset:49152\n"
"s_waitcnt vmcnt(13)\n"
"v_lshlrev_b32 v88,16,v56\n"
"v_mfma_f32_16x16x32_bf16 v[172:175],v[108:111],a[44:47],v[172:175]\n"
"v_and_b32 v89,0xffff0000,v56\n"
"v_add_f32 v92,v88,v89\n"
"v_mfma_f32_16x16x32_bf16 v[180:183],v[104:107],a[48:51],0\n"
"ds_read_b64_tr_b16 a[86:87],v14 offset:51200\n"
"v_mul_f32 v93,v88,v88\n"
"v_mfma_f32_16x16x32_bf16 v[180:183],v[108:111],a[52:55],v[180:183]\n"
"s_add_u32 s34,s76,0x9e3779b9\n"
"buffer_load_dwordx4 v39,s[40:43],s49 offen offset:3072 nt lds\n"
"v_mfma_f32_16x16x32_bf16 v[188:191],v[104:107],a[56:59],0\n"
"s_add_u32 s49,s49,0x80000\n"
"v_mfma_f32_16x16x32_bf16 v[188:191],v[108:111],a[60:63],v[188:191]\n"
"s_waitcnt lgkmcnt(14)\n"
"ds_read_b64_tr_b16 a[88:89],v15 offset:32768\n"
"v_mfma_f32_16x16x32_bf16 v[192:195],v[112:115],a[0:3],0\n"
"s_add_u32 m0,s50,s55\n"
"ds_read_b128 v[96:99],v10 offset:8192\n"
"v_mfma_f32_16x16x32_bf16 v[192:195],v[116:119],a[4:7],v[192:195]\n"
"s_mul_i32 s77,s77,0x85ebca6b\n"
"buffer_load_dwordx4 v24,s[36:39],s48 offen offset:0 lds\n"
"v_mfma_f32_16x16x32_bf16 v[200:203],v[112:115],a[8:11],0\n"
"s_add_u32 s35,s77,0x27d4eb2f\n"
"ds_read_b128 v[100:103],v11 offset:8192\n"
"v_fmac_f32 v93,v89,v89\n"
"v_mfma_f32_16x16x32_bf16 v[200:203],v[116:119],a[12:15],v[200:203]\n"
"ds_read_b64_tr_b16 a[90:91],v15 offset:34816\n"
"v_lshlrev_b32 v90,16,v57\n"
"v_mfma_f32_16x16x32_bf16 v[208:211],v[112:115],a[16:19],0\n"
"ds_read_b64_tr_b16 a[92:93],v15 offset:49152\n"
"v_and_b32 v91,0xffff0000,v57\n"
"v_mfma_f32_16x16x32_bf16 v[208:211],v[116:119],a[20:23],v[208:211]\n"
"buffer_load_dwordx4 v25,s[36:39],s48 offen offset:1024 lds\n"
"v_mfma_f32_16x16x32_bf16 v[216:219],v[112:115],a[24:27],0\n"
"v_add_f32 v92,v92,v90\n"
"v_fmac_f32 v93,v90,v90\n"
"v_mfma_f32_16x16x32_bf16 v[216:219],v[116:119],a[28:31],v[216:219]\n"
"ds_read_b64_tr_b16 a[94:95],v15 offset:51200\n"
"v_add_f32 v92,v92,v91\n"
"v_mfma_f32_16x16x32_bf16 v[224:227],v[112:115],a[32:35],0\n"
"v_fmac_f32 v93,v91,v91\n"
"v_lshlrev_b32 v88,16,v58\n"
"v_mfma_f32_16x16x32_bf16 v[224:227],v[116:119],a[36:39],v[224:227]\n"
"buffer_load_dwordx4 v26,s[36:39],s48 offen offset:2048 lds\n"
"v_mfma_f32_16x16x32_bf16 v[232:235],v[112:115],a[40:43],0\n"
"ds_read_b64_tr_b16 a[96:97],v16 offset:32768\n"
"v_and_b32 v89,0xffff0000,v58\n"
"v_mfma_f32_16x16x32_bf16 v[232:235],v[116:119],a[44:47],v[232:235]\n"
"v_add_f32 v92,v92,v88\n"
"v_fmac_f32 v93,v88,v88\n"
"v_mfma_f32_16x16x32_bf16 v[240:243],v[112:115],a[48:51],0\n"
"ds_read_b64_tr_b16 a[98:99],v16 offset:34816\n"
"v_add_f32 v92,v92,v89\n"
"v_mfma_f32_16x16x32_bf16 v[240:243],v[116:119],a[52:55],v[240:243]\n"
"buffer_load_dwordx4 v27,s[36:39],s48 offen offset:3072 lds\n"
"v_mfma_f32_16x16x32_bf16 v[248:251],v[112:115],a[56:59],0\n"
"v_fmac_f32 v93,v89,v89\n"
"v_lshlrev_b32 v90,16,v59\n"
"v_mfma_f32_16x16x32_bf16 v[248:251],v[116:119],a[60:63],v[248:251]\n"
"s_waitcnt lgkmcnt(14)\n"
"ds_read_b64_tr_b16 a[100:101],v16 offset:49152\n"
"v_and_b32 v91,0xffff0000,v59\n"
"v_mfma_f32_16x16x32_bf16 v[196:199],v[120:123],a[0:3],0\n"
"s_add_u32 m0,s50,s56\n"
"ds_read_b128 v[104:107],v10 offset:10240\n"
"v_add_f32 v92,v92,v90\n"
"v_mfma_f32_16x16x32_bf16 v[196:199],v[124:127],a[4:7],v[196:199]\n"
"buffer_load_dwordx4 v28,s[36:39],s48 offen offset:0 lds\n"
"v_mfma_f32_16x16x32_bf16 v[204:207],v[120:123],a[8:11],0\n"
"ds_read_b128 v[108:111],v11 offset:10240\n"
"v_fmac_f32 v93,v90,v90\n"
"v_mfma_f32_16x16x32_bf16 v[204:207],v[124:127],a[12:15],v[204:207]\n"
"ds_read_b64_tr_b16 a[102:103],v16 offset:51200\n"
"v_add_f32 v92,v92,v91\n"
"v_mfma_f32_16x16x32_bf16 v[212:215],v[120:123],a[16:19],0\n"
"ds_read_b64_tr_b16 a[104:105],v17 offset:32768\n"
"v_fmac_f32 v93,v91,v91\n"
"v_mfma_f32_16x16x32_bf16 v[212:215],v[124:127],a[20:23],v[212:215]\n"
"buffer_load_dwordx4 v29,s[36:39],s48 offen offset:1024 lds\n"
"v_mfma_f32_16x16x32_bf16 v[220:223],v[120:123],a[24:27],0\n"
"s_waitcnt vmcnt(19)\n"
"v_lshlrev_b32 v88,16,v60\n"
"v_and_b32 v89,0xffff0000,v60\n"
"v_mfma_f32_16x16x32_bf16 v[220:223],v[124:127],a[28:31],v[220:223]\n"
"ds_read_b64_tr_b16 a[106:107],v17 offset:34816\n"
"v_add_f32 v92,v92,v88\n"
"v_mfma_f32_16x16x32_bf16 v[228:231],v[120:123],a[32:35],0\n"
"v_fmac_f32 v93,v88,v88\n"
"v_add_f32 v92,v92,v89\n"
"v_mfma_f32_16x16x32_bf16 v[228:231],v[124:127],a[36:39],v[228:231]\n"
"buffer_load_dwordx4 v30,s[36:39],s48 offen offset:2048 lds\n"
"v_mfma_f32_16x16x32_bf16 v[236:239],v[120:123],a[40:43],0\n"
"ds_read_b64_tr_b16 a[108:109],v17 offset:49152\n"
"v_fmac_f32 v93,v89,v89\n"
"v_mfma_f32_16x16x32_bf16 v[236:239],v[124:127],a[44:47],v[236:239]\n"
"v_lshlrev_b32 v90,16,v61\n"
"v_and_b32 v91,0xffff0000,v61\n"
"v_mfma_f32_16x16x32_bf16 v[244:247],v[120:123],a[48:51],0\n"
"ds_read_b64_tr_b16 a[110:111],v17 offset:51200\n"
"v_add_f32 v92,v92,v90\n"
"v_mfma_f32_16x16x32_bf16 v[244:247],v[124:127],a[52:55],v[244:247]\n"
"buffer_load_dwordx4 v31,s[36:39],s48 offen offset:3072 lds\n"
"v_mfma_f32_16x16x32_bf16 v[252:255],v[120:123],a[56:59],0\n"
"s_add_u32 s48,s48,128\n"
"v_fmac_f32 v93,v90,v90\n"
"v_add_f32 v92,v92,v91\n"
"v_mfma_f32_16x16x32_bf16 v[252:255],v[124:127],a[60:63],v[252:255]\n"
"s_waitcnt lgkmcnt(13)\n"
"ds_read_b64_tr_b16 a[112:113],v18 offset:32768\n"
"v_fmac_f32 v93,v91,v91\n"
"v_mfma_f32_16x16x32_bf16 a[128:131],v[96:99],a[0:3],0\n"
"s_mov_b32 s51,s50\n"
"ds_read_b128 v[112:115],v10 offset:12288\n"
"v_lshlrev_b32 v88,16,v62\n"
"v_mfma_f32_16x16x32_bf16 a[128:131],v[100:103],a[4:7],a[128:131]\n"
"s_add_u32 s50,s50,0x8000\n"
"ds_read_b64_tr_b16 a[114:115],v18 offset:34816\n"
"v_and_b32 v89,0xffff0000,v62\n"
"v_mfma_f32_16x16x32_bf16 a[136:139],v[96:99],a[8:11],0\n"
"s_cmp_eq_u32 s50,0x18000\n"
"ds_read_b128 v[116:119],v11 offset:12288\n"
"v_add_f32 v92,v92,v88\n"
"v_mfma_f32_16x16x32_bf16 a[136:139],v[100:103],a[12:15],a[136:139]\n"
"s_cselect_b32 s50,0,s50\n"
"v_fmac_f32 v93,v88,v88\n"
"v_add_f32 v92,v92,v89\n"
"v_mfma_f32_16x16x32_bf16 a[144:147],v[96:99],a[16:19],0\n"
"ds_read_b64_tr_b16 a[116:117],v18 offset:49152\n"
"v_fmac_f32 v93,v89,v89\n"
"v_mfma_f32_16x16x32_bf16 a[144:147],v[100:103],a[20:23],a[144:147]\n"
"v_lshlrev_b32 v90,16,v63\n"
"v_and_b32 v91,0xffff0000,v63\n"
"v_mfma_f32_16x16x32_bf16 a[152:155],v[96:99],a[24:27],0\n"
"ds_read_b64_tr_b16 a[118:119],v18 offset:51200\n"
"v_add_f32 v92,v92,v90\n"
"v_mfma_f32_16x16x32_bf16 a[152:155],v[100:103],a[28:31],a[152:155]\n"
"v_fmac_f32 v93,v90,v90\n"
"v_add_f32 v92,v92,v91\n"
"v_mfma_f32_16x16x32_bf16 a[160:163],v[96:99],a[32:35],0\n"
"v_fmac_f32 v93,v91,v91\n"
"s_waitcnt vmcnt(20)\n"
"v_lshlrev_b32 v88,16,v64\n"
"v_mfma_f32_16x16x32_bf16 a[160:163],v[100:103],a[36:39],a[160:163]\n"
"ds_read_b64_tr_b16 a[120:121],v19 offset:32768\n"
"v_and_b32 v89,0xffff0000,v64\n"
"v_mfma_f32_16x16x32_bf16 a[168:171],v[96:99],a[40:43],0\n"
"v_add_f32 v92,v92,v88\n"
"v_fmac_f32 v93,v88,v88\n"
"v_mfma_f32_16x16x32_bf16 a[168:171],v[100:103],a[44:47],a[168:171]\n"
"v_add_f32 v92,v92,v89\n"
"v_fmac_f32 v93,v89,v89\n"
"v_mfma_f32_16x16x32_bf16 a[176:179],v[96:99],a[48:51],0\n"
"ds_read_b64_tr_b16 a[122:123],v19 offset:34816\n"
"v_lshlrev_b32 v90,16,v65\n"
"v_mfma_f32_16x16x32_bf16 a[176:179],v[100:103],a[52:55],a[176:179]\n"
"v_and_b32 v91,0xffff0000,v65\n"
"v_add_f32 v92,v92,v90\n"
"v_mfma_f32_16x16x32_bf16 a[184:187],v[96:99],a[56:59],0\n"
"ds_read_b64_tr_b16 a[124:125],v19 offset:49152\n"
"v_fmac_f32 v93,v90,v90\n"
"v_mfma_f32_16x16x32_bf16 a[184:187],v[100:103],a[60:63],a[184:187]\n"
"s_waitcnt lgkmcnt(14)\n"
"v_add_f32 v92,v92,v91\n"
"v_fmac_f32 v93,v91,v91\n"
"v_mfma_f32_16x16x32_bf16 a[132:135],v[104:107],a[0:3],0\n"
"ds_read_b128 v[120:123],v10 offset:14336\n"
"v_lshlrev_b32 v88,16,v66\n"
"v_mfma_f32_16x16x32_bf16 a[132:135],v[108:111],a[4:7],a[132:135]\n"
"ds_read_b64_tr_b16 a[126:127],v19 offset:51200\n"
"v_and_b32 v89,0xffff0000,v66\n"
"v_mfma_f32_16x16x32_bf16 a[140:143],v[104:107],a[8:11],0\n"
"ds_read_b128 v[124:127],v11 offset:14336\n"
"v_add_f32 v92,v92,v88\n"
"v_mfma_f32_16x16x32_bf16 a[140:143],v[108:111],a[12:15],a[140:143]\n"
"v_fmac_f32 v93,v88,v88\n"
"v_add_f32 v92,v92,v89\n"
"v_mfma_f32_16x16x32_bf16 a[148:151],v[104:107],a[16:19],0\n"
"v_fmac_f32 v93,v89,v89\n"
"v_lshlrev_b32 v90,16,v67\n"
"v_mfma_f32_16x16x32_bf16 a[148:151],v[108:111],a[20:23],a[148:151]\n"
"v_and_b32 v91,0xffff0000,v67\n"
"v_add_f32 v92,v92,v90\n"
"v_mfma_f32_16x16x32_bf16 a[156:159],v[104:107],a[24:27],0\n"
"v_fmac_f32 v93,v90,v90\n"
"v_add_f32 v92,v92,v91\n"
"v_mfma_f32_16x16x32_bf16 a[156:159],v[108:111],a[28:31],a[156:159]\n"
"v_fmac_f32 v93,v91,v91\n"
"s_waitcnt vmcnt(19)\n"
"v_lshlrev_b32 v88,16,v68\n"
"v_mfma_f32_16x16x32_bf16 a[164:167],v[104:107],a[32:35],0\n"
"v_and_b32 v89,0xffff0000,v68\n"
"v_add_f32 v92,v92,v88\n"
"v_mfma_f32_16x16x32_bf16 a[164:167],v[108:111],a[36:39],a[164:167]\n"
"v_fmac_f32 v93,v88,v88\n"
"v_add_f32 v92,v92,v89\n"
"v_mfma_f32_16x16x32_bf16 a[172:175],v[104:107],a[40:43],0\n"
"v_fmac_f32 v93,v89,v89\n"
"v_lshlrev_b32 v90,16,v69\n"
"v_mfma_f32_16x16x32_bf16 a[172:175],v[108:111],a[44:47],a[172:175]\n"
"v_and_b32 v91,0xffff0000,v69\n"
"v_add_f32 v92,v92,v90\n"
"v_mfma_f32_16x16x32_bf16 a[180:183],v[104:107],a[48:51],0\n"
"v_fmac_f32 v93,v90,v90\n"
"v_add_f32 v92,v92,v91\n"
"v_mfma_f32_16x16x32_bf16 a[180:183],v[108:111],a[52:55],a[180:183]\n"
"v_fmac_f32 v93,v91,v91\n"
"v_lshlrev_b32 v88,16,v70\n"
"v_mfma_f32_16x16x32_bf16 a[188:191],v[104:107],a[56:59],0\n"
"s_waitcnt lgkmcnt(0)\n"
"v_and_b32 v89,0xffff0000,v70\n"
"v_add_f32 v92,v92,v88\n"
"v_mfma_f32_16x16x32_bf16 a[188:191],v[108:111],a[60:63],a[188:191]\n"
"s_waitcnt vmcnt(8)\n"
"s_barrier\n"
"v_fmac_f32 v93,v88,v88\n"
"v_add_f32 v92,v92,v89\n"
"v_mfma_f32_16x16x32_bf16 a[192:195],v[112:115],a[0:3],0\n"
"s_add_u32 m0,s57,0x8000\n"
"v_add_u32 v10,s81,v8\n"
"v_add_u32 v11,s81,v9\n"
"v_mfma_f32_16x16x32_bf16 a[192:195],v[116:119],a[4:7],a[192:195]\n"
"s_mov_b32 s81,s51\n"
"buffer_load_dwordx4 v32,s[40:43],s49 offen offset:0 nt lds\n"
"v_mfma_f32_16x16x32_bf16 a[200:203],v[112:115],a[8:11],0\n"
"ds_read_b128 v[96:99],v10 offset:0\n"
"v_fmac_f32 v93,v89,v89\n"
"v_mfma_f32_16x16x32_bf16 a[200:203],v[116:119],a[12:15],a[200:203]\n"
"v_lshlrev_b32 v90,16,v71\n"
"v_and_b32 v91,0xffff0000,v71\n"
"v_mfma_f32_16x16x32_bf16 a[208:211],v[112:115],a[16:19],0\n"
"ds_read_b128 v[100:103],v11 offset:0\n"
"v_add_f32 v92,v92,v90\n"
"v_mfma_f32_16x16x32_bf16 a[208:211],v[116:119],a[20:23],a[208:211]\n"
"buffer_load_dwordx4 v33,s[40:43],s49 offen offset:1024 nt lds\n"
"v_mfma_f32_16x16x32_bf16 a[216:219],v[112:115],a[24:27],0\n"
"v_fmac_f32 v93,v90,v90\n"
"v_add_f32 v92,v92,v91\n"
"v_mfma_f32_16x16x32_bf16 a[216:219],v[116:119],a[28:31],a[216:219]\n"
"v_fmac_f32 v93,v91,v91\n"
"v_lshlrev_b32 v88,16,v72\n"
"v_mfma_f32_16x16x32_bf16 a[224:227],v[112:115],a[32:35],0\n"
"v_and_b32 v89,0xffff0000,v72\n"
"v_add_f32 v92,v92,v88\n"
"v_mfma_f32_16x16x32_bf16 a[224:227],v[116:119],a[36:39],a[224:227]\n"
"buffer_load_dwordx4 v34,s[40:43],s49 offen offset:2048 nt lds\n"
"v_mfma_f32_16x16x32_bf16 a[232:235],v[112:115],a[40:43],0\n"
"v_fmac_f32 v93,v88,v88\n"
"v_add_f32 v92,v92,v89\n"
"v_mfma_f32_16x16x32_bf16 a[232:235],v[116:119],a[44:47],a[232:235]\n"
"v_fmac_f32 v93,v89,v89\n"
"v_lshlrev_b32 v90,16,v73\n"
"v_mfma_f32_16x16x32_bf16 a[240:243],v[112:115],a[48:51],0\n"
"v_and_b32 v91,0xffff0000,v73\n"
"v_add_f32 v92,v92,v90\n"
"v_mfma_f32_16x16x32_bf16 a[240:243],v[116:119],a[52:55],a[240:243]\n"
"buffer_load_dwordx4 v35,s[40:43],s49 offen offset:3072 nt lds\n"
"v_mfma_f32_16x16x32_bf16 a[248:251],v[112:115],a[56:59],0\n"
"v_fmac_f32 v93,v90,v90\n"
"v_add_f32 v92,v92,v91\n"
"v_mfma_f32_16x16x32_bf16 a[248:251],v[116:119],a[60:63],a[248:251]\n"
"v_fmac_f32 v93,v91,v91\n"
"v_lshlrev_b32 v88,16,v74\n"
"v_mfma_f32_16x16x32_bf16 a[196:199],v[120:123],a[0:3],0\n"
"s_add_u32 m0,s57,0x9000\n"
"ds_read_b128 v[104:107],v10 offset:2048\n"
"v_and_b32 v89,0xffff0000,v74\n"
"v_mfma_f32_16x16x32_bf16 a[196:199],v[124:127],a[4:7],a[196:199]\n"
"buffer_load_dwordx4 v36,s[40:43],s49 offen offset:0 nt lds\n"
"v_mfma_f32_16x16x32_bf16 a[204:207],v[120:123],a[8:11],0\n"
"ds_read_b128 v[108:111],v11 offset:2048\n"
"v_add_f32 v92,v92,v88\n"
"v_mfma_f32_16x16x32_bf16 a[204:207],v[124:127],a[12:15],a[204:207]\n"
"v_fmac_f32 v93,v88,v88\n"
"v_add_f32 v92,v92,v89\n"
"v_mfma_f32_16x16x32_bf16 a[212:215],v[120:123],a[16:19],0\n"
"v_fmac_f32 v93,v89,v89\n"
"v_lshlrev_b32 v90,16,v75\n"
"v_mfma_f32_16x16x32_bf16 a[212:215],v[124:127],a[20:23],a[212:215]\n"
"buffer_load_dwordx4 v37,s[40:43],s49 offen offset:1024 nt lds\n"
"v_mfma_f32_16x16x32_bf16 a[220:223],v[120:123],a[24:27],0\n"
"v_and_b32 v91,0xffff0000,v75\n"
"v_add_f32 v92,v92,v90\n"
"v_mfma_f32_16x16x32_bf16 a[220:223],v[124:127],a[28:31],a[220:223]\n"
"v_fmac_f32 v93,v90,v90\n"
"v_add_f32 v92,v92,v91\n"
"v_mfma_f32_16x16x32_bf16 a[228:231],v[120:123],a[32:35],0\n"
"v_fmac_f32 v93,v91,v91\n"
"v_lshlrev_b32 v88,16,v76\n"
"v_mfma_f32_16x16x32_bf16 a[228:231],v[124:127],a[36:39],a[228:231]\n"
"buffer_load_dwordx4 v38,s[40:43],s49 offen offset:2048 nt lds\n"
"v_mfma_f32_16x16x32_bf16 a[236:239],v[120:123],a[40:43],0\n"
"v_and_b32 v89,0xffff0000,v76\n"
"v_add_f32 v92,v92,v88\n"
"v_mfma_f32_16x16x32_bf16 a[236:239],v[124:127],a[44:47],a[236:239]\n"
"v_fmac_f32 v93,v88,v88\n"
"v_add_f32 v92,v92,v89\n"
"v_mfma_f32_16x16x32_bf16 a[244:247],v[120:123],a[48:51],0\n"
"v_fmac_f32 v93,v89,v89\n"
"v_lshlrev_b32 v90,16,v77\n"
"v_mfma_f32_16x16x32_bf16 a[244:247],v[124:127],a[52:55],a[244:247]\n"
"buffer_load_dwordx4 v39,s[40:43],s49 offen offset:3072 nt lds\n"
"v_mfma_f32_16x16x32_bf16 a[252:255],v[120:123],a[56:59],0\n"
"s_add_u32 s49,s49,0x80000\n"
"v_and_b32 v91,0xffff0000,v77\n"
"v_add_f32 v92,v92,v90\n"
"v_mfma_f32_16x16x32_bf16 a[252:255],v[124:127],a[60:63],a[252:255]\n"
"s_waitcnt lgkmcnt(2)\n"
"v_fmac_f32 v93,v90,v90\n"
"v_add_f32 v92,v92,v91\n"
"v_mfma_f32_16x16x32_bf16 v[128:131],v[96:99],a[64:67],v[128:131]\n"
"ds_read_b128 v[112:115],v10 offset:4096\n"
"v_fmac_f32 v93,v91,v91\n"
"v_mfma_f32_16x16x32_bf16 v[128:131],v[100:103],a[68:71],v[128:131]\n"
"ds_read_b64_tr_b16 a[0:1],v12 offset:0\n"
"v_lshlrev_b32 v88,16,v78\n"
"v_mfma_f32_16x16x32_bf16 v[136:139],v[96:99],a[72:75],v[136:139]\n"
"ds_read_b128 v[116:119],v11 offset:4096\n"
"v_and_b32 v89,0xffff0000,v78\n"
"v_mfma_f32_16x16x32_bf16 v[136:139],v[100:103],a[76:79],v[136:139]\n"
"ds_read_b64_tr_b16 a[2:3],v12 offset:2048\n"
"v_add_f32 v92,v92,v88\n"
"v_mfma_f32_16x16x32_bf16 v[144:147],v[96:99],a[80:83],v[144:147]\n"
"v_fmac_f32 v93,v88,v88\n"
"v_add_f32 v92,v92,v89\n"
"v_mfma_f32_16x16x32_bf16 v[144:147],v[100:103],a[84:87],v[144:147]\n"
"ds_read_b64_tr_b16 a[4:5],v12 offset:16384\n"
"v_fmac_f32 v93,v89,v89\n"
"v_mfma_f32_16x16x32_bf16 v[152:155],v[96:99],a[88:91],v[152:155]\n"
"s_add_u32 m0,s50,s55\n"
"v_lshlrev_b32 v90,16,v79\n"
"v_and_b32 v91,0xffff0000,v79\n"
"v_mfma_f32_16x16x32_bf16 v[152:155],v[100:103],a[92:95],v[152:155]\n"
"buffer_load_dwordx4 v24,s[36:39],s48 offen offset:0 lds\n"
"v_mfma_f32_16x16x32_bf16 v[160:163],v[96:99],a[96:99],v[160:163]\n"
"ds_read_b64_tr_b16 a[6:7],v12 offset:18432\n"
"v_add_f32 v92,v92,v90\n"
"v_mfma_f32_16x16x32_bf16 v[160:163],v[100:103],a[100:103],v[160:163]\n"
"v_fmac_f32 v93,v90,v90\n"
"v_add_f32 v92,v92,v91\n"
"v_mfma_f32_16x16x32_bf16 v[168:171],v[96:99],a[104:107],v[168:171]\n"
"ds_read_b64_tr_b16 a[8:9],v13 offset:0\n"
"v_fmac_f32 v93,v91,v91\n"
"v_mfma_f32_16x16x32_bf16 v[168:171],v[100:103],a[108:111],v[168:171]\n"
"v_lshlrev_b32 v88,16,v80\n"
"v_and_b32 v89,0xffff0000,v80\n"
"v_mfma_f32_16x16x32_bf16 v[176:179],v[96:99],a[112:115],v[176:179]\n"
"v_add_f32 v92,v92,v88\n"
"v_fmac_f32 v93,v88,v88\n"
"v_mfma_f32_16x16x32_bf16 v[176:179],v[100:103],a[116:119],v[176:179]\n"
"buffer_load_dwordx4 v25,s[36:39],s48 offen offset:1024 lds\n"
"v_mfma_f32_16x16x32_bf16 v[184:187],v[96:99],a[120:123],v[184:187]\n"
"ds_read_b64_tr_b16 a[10:11],v13 offset:2048\n"
"v_add_f32 v92,v92,v89\n"
"v_mfma_f32_16x16x32_bf16 v[184:187],v[100:103],a[124:127],v[184:187]\n"
"s_waitcnt lgkmcnt(8)\n"
"ds_read_b64_tr_b16 a[12:13],v13 offset:16384\n"
"v_fmac_f32 v93,v89,v89\n"
"v_mfma_f32_16x16x32_bf16 v[132:135],v[104:107],a[64:67],v[132:135]\n"
"ds_read_b128 v[120:123],v10 offset:6144\n"
"v_lshlrev_b32 v90,16,v81\n"
"v_mfma_f32_16x16x32_bf16 v[132:135],v[108:111],a[68:71],v[132:135]\n"
"v_and_b32 v91,0xffff0000,v81\n"
"v_add_f32 v92,v92,v90\n"
"v_mfma_f32_16x16x32_bf16 v[140:143],v[104:107],a[72:75],v[140:143]\n"
"ds_read_b128 v[124:127],v11 offset:6144\n"
"v_fmac_f32 v93,v90,v90\n"
"v_mfma_f32_16x16x32_bf16 v[140:143],v[108:111],a[76:79],v[140:143]\n"
"buffer_load_dwordx4 v26,s[36:39],s48 offen offset:2048 lds\n"
"v_mfma_f32_16x16x32_bf16 v[148:151],v[104:107],a[80:83],v[148:151]\n"
"ds_read_b64_tr_b16 a[14:15],v13 offset:18432\n"
"v_add_f32 v92,v92,v91\n"
"v_mfma_f32_16x16x32_bf16 v[148:151],v[108:111],a[84:87],v[148:151]\n"
"ds_read_b64_tr_b16 a[16:17],v14 offset:0\n"
"v_fmac_f32 v93,v91,v91\n"
"v_mfma_f32_16x16x32_bf16 v[156:159],v[104:107],a[88:91],v[156:159]\n"
"v_lshlrev_b32 v88,16,v82\n"
"v_and_b32 v89,0xffff0000,v82\n"
"v_mfma_f32_16x16x32_bf16 v[156:159],v[108:111],a[92:95],v[156:159]\n"
"ds_read_b64_tr_b16 a[18:19],v14 offset:2048\n"
"v_add_f32 v92,v92,v88\n"
"v_mfma_f32_16x16x32_bf16 v[164:167],v[104:107],a[96:99],v[164:167]\n"
"v_fmac_f32 v93,v88,v88\n"
"v_add_f32 v92,v92,v89\n"
"v_mfma_f32_16x16x32_bf16 v[164:167],v[108:111],a[100:103],v[164:167]\n"
"buffer_load_dwordx4 v27,s[36:39],s48 offen offset:3072 lds\n"
"v_mfma_f32_16x16x32_bf16 v[172:175],v[104:107],a[104:107],v[172:175]\n"
"ds_read_b64_tr_b16 a[20:21],v14 offset:16384\n"
"v_fmac_f32 v93,v89,v89\n"
"v_mfma_f32_16x16x32_bf16 v[172:175],v[108:111],a[108:111],v[172:175]\n"
"v_lshlrev_b32 v90,16,v83\n"
"v_and_b32 v91,0xffff0000,v83\n"
"v_mfma_f32_16x16x32_bf16 v[180:183],v[104:107],a[112:115],v[180:183]\n"
"ds_read_b64_tr_b16 a[22:23],v14 offset:18432\n"
"v_add_f32 v92,v92,v90\n"
"v_mfma_f32_16x16x32_bf16 v[180:183],v[108:111],a[116:119],v[180:183]\n"
"v_fmac_f32 v93,v90,v90\n"
"v_add_f32 v92,v92,v91\n"
"v_mfma_f32_16x16x32_bf16 v[188:191],v[104:107],a[120:123],v[188:191]\n"
"s_add_u32 m0,s50,s56\n"
"v_fmac_f32 v93,v91,v91\n"
"v_add_f32_dpp v92,v92,v92 row_ror:8 row_mask:0xf bank_mask:0xf\n"
"v_mfma_f32_16x16x32_bf16 v[188:191],v[108:111],a[124:127],v[188:191]\n"
"s_waitcnt lgkmcnt(13)\n"
"buffer_load_dwordx4 v28,s[36:39],s48 offen offset:0 lds\n"
"v_mfma_f32_16x16x32_bf16 v[192:195],v[112:115],a[64:67],v[192:195]\n"
"ds_read_b128 v[96:99],v10 offset:8192\n"
"v_add_f32_dpp v93,v93,v93 row_ror:8 row_mask:0xf bank_mask:0xf\n"
"v_mfma_f32_16x16x32_bf16 v[192:195],v[116:119],a[68:71],v[192:195]\n"
"ds_read_b64_tr_b16 a[24:25],v15 offset:0\n"
"v_mov_b32 v94,v92\n"
"v_mfma_f32_16x16x32_bf16 v[200:203],v[112:115],a[72:75],v[200:203]\n"
"ds_read_b128 v[100:103],v11 offset:8192\n"
"v_mov_b32 v95,v93\n"
"v_mfma_f32_16x16x32_bf16 v[200:203],v[116:119],a[76:79],v[200:203]\n"
"ds_read_b64_tr_b16 a[26:27],v15 offset:2048\n"
"v_permlane32_swap_b32 v92,v94\n"
"v_mfma_f32_16x16x32_bf16 v[208:211],v[112:115],a[80:83],v[208:211]\n"
"ds_read_b64_tr_b16 a[28:29],v15 offset:16384\n"
"v_permlane32_swap_b32 v93,v95\n"
"v_mfma_f32_16x16x32_bf16 v[208:211],v[116:119],a[84:87],v[208:211]\n"
"buffer_load_dwordx4 v29,s[36:39],s48 offen offset:1024 lds\n"
"v_mfma_f32_16x16x32_bf16 v[216:219],v[112:115],a[88:91],v[216:219]\n"
"v_add_f32 v92,v92,v94\n"
"v_add_f32 v93,v93,v95\n"
"v_mfma_f32_16x16x32_bf16 v[216:219],v[116:119],a[92:95],v[216:219]\n"
"ds_read_b64_tr_b16 a[30:31],v15 offset:18432\n"
"v_mov_b32 v94,v92\n"
"v_mfma_f32_16x16x32_bf16 v[224:227],v[112:115],a[96:99],v[224:227]\n"
"v_mov_b32 v95,v93\n"
"v_permlane16_swap_b32 v92,v94\n"
"v_mfma_f32_16x16x32_bf16 v[224:227],v[116:119],a[100:103],v[224:227]\n"
"v_permlane16_swap_b32 v93,v95\n"
"v_add_f32 v92,v92,v94\n"
"v_mfma_f32_16x16x32_bf16 v[232:235],v[112:115],a[104:107],v[232:235]\n"
"ds_read_b64_tr_b16 a[32:33],v16 offset:0\n"
"v_add_f32 v93,v93,v95\n"
"v_mfma_f32_16x16x32_bf16 v[232:235],v[116:119],a[108:111],v[232:235]\n"
"buffer_load_dwordx4 v30,s[36:39],s48 offen offset:2048 lds\n"
"v_mfma_f32_16x16x32_bf16 v[240:243],v[112:115],a[112:115],v[240:243]\n"
"ds_read_b64_tr_b16 a[34:35],v16 offset:2048\n"
"v_mul_f32 v92,0x3b124925,v92\n"
"v_mfma_f32_16x16x32_bf16 v[240:243],v[116:119],a[116:119],v[240:243]\n"
"v_mul_f32 v93,0x3b124925,v93\n"
"v_fma_f32 v93,-v92,v92,v93\n"
"v_mfma_f32_16x16x32_bf16 v[248:251],v[112:115],a[120:123],v[248:251]\n"
"v_max_f32 v93,0,v93\n"
"v_add_f32 v93,0x3727c5ac,v93\n"
"v_mfma_f32_16x16x32_bf16 v[248:251],v[116:119],a[124:127],v[248:251]\n"
"s_waitcnt lgkmcnt(13)\n"
"ds_read_b64_tr_b16 a[36:37],v16 offset:16384\n"
"v_mfma_f32_16x16x32_bf16 v[196:199],v[120:123],a[64:67],v[196:199]\n"
"ds_read_b128 v[104:107],v10 offset:10240\n"
"v_mfma_f32_16x16x32_bf16 v[196:199],v[124:127],a[68:71],v[196:199]\n"
"buffer_load_dwordx4 v31,s[36:39],s48 offen offset:3072 lds\n"
"v_mfma_f32_16x16x32_bf16 v[204:207],v[120:123],a[72:75],v[204:207]\n"
"ds_read_b128 v[108:111],v11 offset:10240\n"
"v_mfma_f32_16x16x32_bf16 v[204:207],v[124:127],a[76:79],v[204:207]\n"
"ds_read_b64_tr_b16 a[38:39],v16 offset:18432\n"
"v_mfma_f32_16x16x32_bf16 v[212:215],v[120:123],a[80:83],v[212:215]\n"
"ds_read_b64_tr_b16 a[40:41],v17 offset:0\n"
"v_mfma_f32_16x16x32_bf16 v[212:215],v[124:127],a[84:87],v[212:215]\n"
"v_rsq_f32 v93,v93\n"
"v_mfma_f32_16x16x32_bf16 v[220:223],v[120:123],a[88:91],v[220:223]\n"
"s_add_u32 s48,s48,128\n"
"v_mul_f32 v93,0x3db8aa3b,v93\n"
"v_lshlrev_b32 v88,16,v56\n"
"v_mfma_f32_16x16x32_bf16 v[220:223],v[124:127],a[92:95],v[220:223]\n"
"s_mov_b32 s51,s50\n"
"ds_read_b64_tr_b16 a[42:43],v17 offset:2048\n"
"v_and_b32 v89,0xffff0000,v56\n"
"v_mfma_f32_16x16x32_bf16 v[228:231],v[120:123],a[96:99],v[228:231]\n"
"s_add_u32 s50,s50,0x8000\n"
"v_sub_f32 v88,v88,v92\n"
"v_sub_f32 v89,v89,v92\n"
"v_mfma_f32_16x16x32_bf16 v[228:231],v[124:127],a[100:103],v[228:231]\n"
"s_cmp_eq_u32 s50,0x18000\n"
"ds_read_b64_tr_b16 a[44:45],v17 offset:16384\n"
"v_mul_f32 v88,v88,v93\n"
"v_mfma_f32_16x16x32_bf16 v[236:239],v[120:123],a[104:107],v[236:239]\n"
"s_cselect_b32 s50,0,s50\n"
"v_mul_f32 v89,v89,v93\n"
"v_cvt_pk_bf16_f32 v56,v88,v89\n"
"v_mfma_f32_16x16x32_bf16 v[236:239],v[124:127],a[108:111],v[236:239]\n"
"v_lshlrev_b32 v90,16,v57\n"
"v_and_b32 v91,0xffff0000,v57\n"
"v_mfma_f32_16x16x32_bf16 v[244:247],v[120:123],a[112:115],v[244:247]\n"
"ds_read_b64_tr_b16 a[46:47],v17 offset:18432\n"
"v_sub_f32 v90,v90,v92\n"
"v_mfma_f32_16x16x32_bf16 v[244:247],v[124:127],a[116:119],v[244:247]\n"
"v_sub_f32 v91,v91,v92\n"
"v_mul_f32 v90,v90,v93\n"
"v_mfma_f32_16x16x32_bf16 v[252:255],v[120:123],a[120:123],v[252:255]\n"
"v_mul_f32 v91,v91,v93\n"
"v_cvt_pk_bf16_f32 v57,v90,v91\n"
"v_mfma_f32_16x16x32_bf16 v[252:255],v[124:127],a[124:127],v[252:255]\n"
"s_waitcnt lgkmcnt(13)\n"
"ds_read_b64_tr_b16 a[48:49],v18 offset:0\n"
"v_lshlrev_b32 v88,16,v58\n"
"v_mfma_f32_16x16x32_bf16 a[128:131],v[96:99],a[64:67],a[128:131]\n"
"ds_read_b128 v[112:115],v10 offset:12288\n"
"v_and_b32 v89,0xffff0000,v58\n"
"v_mfma_f32_16x16x32_bf16 a[128:131],v[100:103],a[68:71],a[128:131]\n"
"ds_read_b64_tr_b16 a[50:51],v18 offset:2048\n"
"v_sub_f32 v88,v88,v92\n"
"v_mfma_f32_16x16x32_bf16 a[136:139],v[96:99],a[72:75],a[136:139]\n"
"ds_read_b128 v[116:119],v11 offset:12288\n"
"v_sub_f32 v89,v89,v92\n"
"v_mfma_f32_16x16x32_bf16 a[136:139],v[100:103],a[76:79],a[136:139]\n"
"v_mul_f32 v88,v88,v93\n"
"v_mul_f32 v89,v89,v93\n"
"v_mfma_f32_16x16x32_bf16 a[144:147],v[96:99],a[80:83],a[144:147]\n"
"ds_read_b64_tr_b16 a[52:53],v18 offset:16384\n"
"v_cvt_pk_bf16_f32 v58,v88,v89\n"
"v_mfma_f32_16x16x32_bf16 a[144:147],v[100:103],a[84:87],a[144:147]\n"
"v_lshlrev_b32 v90,16,v59\n"
"v_and_b32 v91,0xffff0000,v59\n"
"v_mfma_f32_16x16x32_bf16 a[152:155],v[96:99],a[88:91],a[152:155]\n"
"ds_read_b64_tr_b16 a[54:55],v18 offset:18432\n"
"v_sub_f32 v90,v90,v92\n"
"v_mfma_f32_16x16x32_bf16 a[152:155],v[100:103],a[92:95],a[152:155]\n"
"v_sub_f32 v91,v91,v92\n"
"v_mul_f32 v90,v90,v93\n"
"v_mfma_f32_16x16x32_bf16 a[160:163],v[96:99],a[96:99],a[160:163]\n"
"v_mul_f32 v91,v91,v93\n"
"v_cvt_pk_bf16_f32 v59,v90,v91\n"
"v_mfma_f32_16x16x32_bf16 a[160:163],v[100:103],a[100:103],a[160:163]\n"
"ds_read_b64_tr_b16 a[56:57],v19 offset:0\n"
"buffer_store_dwordx4 v[56:59],v87,s[68:71],s96 offen sc1\n"
"v_mfma_f32_16x16x32_bf16 a[168:171],v[96:99],a[104:107],a[168:171]\n"
"v_lshlrev_b32 v88,16,v60\n"
"v_and_b32 v89,0xffff0000,v60\n"
"v_mfma_f32_16x16x32_bf16 a[168:171],v[100:103],a[108:111],a[168:171]\n"
"v_sub_f32 v88,v88,v92\n"
"v_sub_f32 v89,v89,v92\n"
"v_mfma_f32_16x16x32_bf16 a[176:179],v[96:99],a[112:115],a[176:179]\n"
"ds_read_b64_tr_b16 a[58:59],v19 offset:2048\n"
"v_mul_f32 v88,v88,v93\n"
"v_mfma_f32_16x16x32_bf16 a[176:179],v[100:103],a[116:119],a[176:179]\n"
"v_mul_f32 v89,v89,v93\n"
"v_cvt_pk_bf16_f32 v60,v88,v89\n"
"v_mfma_f32_16x16x32_bf16 a[184:187],v[96:99],a[120:123],a[184:187]\n"
"ds_read_b64_tr_b16 a[60:61],v19 offset:16384\n"
"v_lshlrev_b32 v90,16,v61\n"
"v_mfma_f32_16x16x32_bf16 a[184:187],v[100:103],a[124:127],a[184:187]\n"
"s_waitcnt lgkmcnt(14)\n"
"v_and_b32 v91,0xffff0000,v61\n"
"v_sub_f32 v90,v90,v92\n"
"v_mfma_f32_16x16x32_bf16 a[132:135],v[104:107],a[64:67],a[132:135]\n"
"ds_read_b128 v[120:123],v10 offset:14336\n"
"v_sub_f32 v91,v91,v92\n"
"v_mfma_f32_16x16x32_bf16 a[132:135],v[108:111],a[68:71],a[132:135]\n"
"ds_read_b64_tr_b16 a[62:63],v19 offset:18432\n"
"v_mul_f32 v90,v90,v93\n"
"v_mfma_f32_16x16x32_bf16 a[140:143],v[104:107],a[72:75],a[140:143]\n"
"ds_read_b128 v[124:127],v11 offset:14336\n"
"v_mul_f32 v91,v91,v93\n"
"v_mfma_f32_16x16x32_bf16 a[140:143],v[108:111],a[76:79],a[140:143]\n"
"v_cvt_pk_bf16_f32 v61,v90,v91\n"
"v_lshlrev_b32 v88,16,v62\n"
"v_mfma_f32_16x16x32_bf16 a[148:151],v[104:107],a[80:83],a[148:151]\n"
"v_and_b32 v89,0xffff0000,v62\n"
"v_sub_f32 v88,v88,v92\n"
"v_mfma_f32_16x16x32_bf16 a[148:151],v[108:111],a[84:87],a[148:151]\n"
"v_sub_f32 v89,v89,v92\n"
"v_mul_f32 v88,v88,v93\n"
"v_mfma_f32_16x16x32_bf16 a[156:159],v[104:107],a[88:91],a[156:159]\n"
"v_mul_f32 v89,v89,v93\n"
"v_cvt_pk_bf16_f32 v62,v88,v89\n"
"v_mfma_f32_16x16x32_bf16 a[156:159],v[108:111],a[92:95],a[156:159]\n"
"v_lshlrev_b32 v90,16,v63\n"
"v_and_b32 v91,0xffff0000,v63\n"
"v_mfma_f32_16x16x32_bf16 a[164:167],v[104:107],a[96:99],a[164:167]\n"
"v_sub_f32 v90,v90,v92\n"
"v_sub_f32 v91,v91,v92\n"
"v_mfma_f32_16x16x32_bf16 a[164:167],v[108:111],a[100:103],a[164:167]\n"
"v_mul_f32 v90,v90,v93\n"
"v_mul_f32 v91,v91,v93\n"
"v_mfma_f32_16x16x32_bf16 a[172:175],v[104:107],a[104:107],a[172:175]\n"
"v_cvt_pk_bf16_f32 v63,v90,v91\n"
"v_mfma_f32_16x16x32_bf16 a[172:175],v[108:111],a[108:111],a[172:175]\n"
"buffer_store_dwordx4 v[60:63],v87,s[68:71],s97 offen sc1\n"
"v_mfma_f32_16x16x32_bf16 a[180:183],v[104:107],a[112:115],a[180:183]\n"
"v_mfma_f32_16x16x32_bf16 a[180:183],v[108:111],a[116:119],a[180:183]\n"
"v_mfma_f32_16x16x32_bf16 a[188:191],v[104:107],a[120:123],a[188:191]\n"
"s_waitcnt lgkmcnt(0)\n"
"v_mfma_f32_16x16x32_bf16 a[188:191],v[108:111],a[124:127],a[188:191]\n"
"s_waitcnt vmcnt(10)\n"
"s_barrier\n"
"v_mfma_f32_16x16x32_bf16 a[192:195],v[112:115],a[64:67],a[192:195]\n"
"s_add_u32 m0,s57,0x0\n"
"v_add_u32 v10,s81,v8\n"
"v_add_u32 v11,s81,v9\n"
"v_mfma_f32_16x16x32_bf16 a[192:195],v[116:119],a[68:71],a[192:195]\n"
"s_mov_b32 s81,s51\n"
"buffer_load_dwordx4 v32,s[40:43],s49 offen offset:0 nt lds\n"
"v_mfma_f32_16x16x32_bf16 a[200:203],v[112:115],a[72:75],a[200:203]\n"
"ds_read_b128 v[96:99],v10 offset:0\n"
"v_mfma_f32_16x16x32_bf16 a[200:203],v[116:119],a[76:79],a[200:203]\n"
"v_mfma_f32_16x16x32_bf16 a[208:211],v[112:115],a[80:83],a[208:211]\n"
"ds_read_b128 v[100:103],v11 offset:0\n"
"v_mfma_f32_16x16x32_bf16 a[208:211],v[116:119],a[84:87],a[208:211]\n"
"buffer_load_dwordx4 v33,s[40:43],s49 offen offset:1024 nt lds\n"
"v_mfma_f32_16x16x32_bf16 a[216:219],v[112:115],a[88:91],a[216:219]\n"
"v_mfma_f32_16x16x32_bf16 a[216:219],v[116:119],a[92:95],a[216:219]\n"
"v_mfma_f32_16x16x32_bf16 a[224:227],v[112:115],a[96:99],a[224:227]\n"
"v_mfma_f32_16x16x32_bf16 a[224:227],v[116:119],a[100:103],a[224:227]\n"
"buffer_load_dwordx4 v34,s[40:43],s49 offen offset:2048 nt lds\n"
"v_mfma_f32_16x16x32_bf16 a[232:235],v[112:115],a[104:107],a[232:235]\n"
"v_mfma_f32_16x16x32_bf16 a[232:235],v[116:119],a[108:111],a[232:235]\n"
"v_mfma_f32_16x16x32_bf16 a[240:243],v[112:115],a[112:115],a[240:243]\n"
"v_mfma_f32_16x16x32_bf16 a[240:243],v[116:119],a[116:119],a[240:243]\n"
"buffer_load_dwordx4 v35,s[40:43],s49 offen offset:3072 nt lds\n"
"v_mfma_f32_16x16x32_bf16 a[248:251],v[112:115],a[120:123],a[248:251]\n"
"v_mfma_f32_16x16x32_bf16 a[248:251],v[116:119],a[124:127],a[248:251]\n"
"v_mfma_f32_16x16x32_bf16 a[196:199],v[120:123],a[64:67],a[196:199]\n"
"s_add_u32 m0,s57,0x1000\n"
"ds_read_b128 v[104:107],v10 offset:2048\n"
"v_mfma_f32_16x16x32_bf16 a[196:199],v[124:127],a[68:71],a[196:199]\n"
"buffer_load_dwordx4 v36,s[40:43],s49 offen offset:0 nt lds\n"
"v_mfma_f32_16x16x32_bf16 a[204:207],v[120:123],a[72:75],a[204:207]\n"
"ds_read_b128 v[108:111],v11 offset:2048\n"
"v_mfma_f32_16x16x32_bf16 a[204:207],v[124:127],a[76:79],a[204:207]\n"
"v_mfma_f32_16x16x32_bf16 a[212:215],v[120:123],a[80:83],a[212:215]\n"
"v_mfma_f32_16x16x32_bf16 a[212:215],v[124:127],a[84:87],a[212:215]\n"
"buffer_load_dwordx4 v37,s[40:43],s49 offen offset:1024 nt lds\n"
"v_mfma_f32_16x16x32_bf16 a[220:223],v[120:123],a[88:91],a[220:223]\n"
"v_mfma_f32_16x16x32_bf16 a[220:223],v[124:127],a[92:95],a[220:223]\n"
"v_mfma_f32_16x16x32_bf16 a[228:231],v[120:123],a[96:99],a[228:231]\n"
"v_mfma_f32_16x16x32_bf16 a[228:231],v[124:127],a[100:103],a[228:231]\n"
"buffer_load_dwordx4 v38,s[40:43],s49 offen offset:2048 nt lds\n"
"v_mfma_f32_16x16x32_bf16 a[236:239],v[120:123],a[104:107],a[236:239]\n"
"v_mfma_f32_16x16x32_bf16 a[236:239],v[124:127],a[108:111],a[236:239]\n"
"v_mfma_f32_16x16x32_bf16 a[244:247],v[120:123],a[112:115],a[244:247]\n"
"v_mfma_f32_16x16x32_bf16 a[244:247],v[124:127],a[116:119],a[244:247]\n"
"buffer_load_dwordx4 v39,s[40:43],s49 offen offset:3072 nt lds\n"
"v_mfma_f32_16x16x32_bf16 a[252:255],v[120:123],a[120:123],a[252:255]\n"
"s_add_u32 s49,s49,0x80000\n"
"v_mfma_f32_16x16x32_bf16 a[252:255],v[124:127],a[124:127],a[252:255]\n"
"s_waitcnt lgkmcnt(2)\n"
"L_kvv_%=:\n"
"v_mfma_f32_16x16x32_bf16 v[128:131],v[96:99],a[0:3],v[128:131]\n"
"ds_read_b128 v[112:115],v10 offset:4096\n"
"v_mfma_f32_16x16x32_bf16 v[128:131],v[100:103],a[4:7],v[128:131]\n"
"ds_read_b64_tr_b16 a[64:65],v12 offset:32768\n"
"v_mfma_f32_16x16x32_bf16 v[136:139],v[96:99],a[8:11],v[136:139]\n"
"ds_read_b128 v[116:119],v11 offset:4096\n"
"v_mfma_f32_16x16x32_bf16 v[136:139],v[100:103],a[12:15],v[136:139]\n"
"ds_read_b64_tr_b16 a[66:67],v12 offset:34816\n"
"v_mfma_f32_16x16x32_bf16 v[144:147],v[96:99],a[16:19],v[144:147]\n"
"v_mfma_f32_16x16x32_bf16 v[144:147],v[100:103],a[20:23],v[144:147]\n"
"global_load_dword v86,v5,s[78:79]\n"
"v_mfma_f32_16x16x32_bf16 v[152:155],v[96:99],a[24:27],v[152:155]\n"
"s_add_u32 m0,s50,s55\n"
"ds_read_b64_tr_b16 a[68:69],v12 offset:49152\n"
"v_mfma_f32_16x16x32_bf16 v[152:155],v[100:103],a[28:31],v[152:155]\n"
"buffer_load_dwordx4 v24,s[36:39],s48 offen offset:0 lds\n"
"v_mfma_f32_16x16x32_bf16 v[160:163],v[96:99],a[32:35],v[160:163]\n"
"ds_read_b64_tr_b16 a[70:71],v12 offset:51200\n"
"v_mfma_f32_16x16x32_bf16 v[160:163],v[100:103],a[36:39],v[160:163]\n"
"v_mfma_f32_16x16x32_bf16 v[168:171],v[96:99],a[40:43],v[168:171]\n"
"ds_read_b64_tr_b16 a[72:73],v13 offset:32768\n"
"v_mfma_f32_16x16x32_bf16 v[168:171],v[100:103],a[44:47],v[168:171]\n"
"v_mfma_f32_16x16x32_bf16 v[176:179],v[96:99],a[48:51],v[176:179]\n"
"v_mfma_f32_16x16x32_bf16 v[176:179],v[100:103],a[52:55],v[176:179]\n"
"buffer_load_dwordx4 v25,s[36:39],s48 offen offset:1024 lds\n"
"v_mfma_f32_16x16x32_bf16 v[184:187],v[96:99],a[56:59],v[184:187]\n"
"ds_read_b64_tr_b16 a[74:75],v13 offset:34816\n"
"v_mfma_f32_16x16x32_bf16 v[184:187],v[100:103],a[60:63],v[184:187]\n"
"s_waitcnt lgkmcnt(8)\n"
"ds_read_b64_tr_b16 a[76:77],v13 offset:49152\n"
"v_mfma_f32_16x16x32_bf16 v[132:135],v[104:107],a[0:3],v[132:135]\n"
"ds_read_b128 v[120:123],v10 offset:6144\n"
"v_mfma_f32_16x16x32_bf16 v[132:135],v[108:111],a[4:7],v[132:135]\n"
"v_mfma_f32_16x16x32_bf16 v[140:143],v[104:107],a[8:11],v[140:143]\n"
"ds_read_b128 v[124:127],v11 offset:6144\n"
"v_mfma_f32_16x16x32_bf16 v[140:143],v[108:111],a[12:15],v[140:143]\n"
"buffer_load_dwordx4 v26,s[36:39],s48 offen offset:2048 lds\n"
"v_mfma_f32_16x16x32_bf16 v[148:151],v[104:107],a[16:19],v[148:151]\n"
"ds_read_b64_tr_b16 a[78:79],v13 offset:51200\n"
"v_mfma_f32_16x16x32_bf16 v[148:151],v[108:111],a[20:23],v[148:151]\n"
"ds_read_b64_tr_b16 a[80:81],v14 offset:32768\n"
"v_mfma_f32_16x16x32_bf16 v[156:159],v[104:107],a[24:27],v[156:159]\n"
"v_mfma_f32_16x16x32_bf16 v[156:159],v[108:111],a[28:31],v[156:159]\n"
"ds_read_b64_tr_b16 a[82:83],v14 offset:34816\n"
"v_mfma_f32_16x16x32_bf16 v[164:167],v[104:107],a[32:35],v[164:167]\n"
"v_mfma_f32_16x16x32_bf16 v[164:167],v[108:111],a[36:39],v[164:167]\n"
"buffer_load_dwordx4 v27,s[36:39],s48 offen offset:3072 lds\n"
"v_mfma_f32_16x16x32_bf16 v[172:175],v[104:107],a[40:43],v[172:175]\n"
"ds_read_b64_tr_b16 a[84:85],v14 offset:49152\n"
"v_mfma_f32_16x16x32_bf16 v[172:175],v[108:111],a[44:47],v[172:175]\n"
"v_mfma_f32_16x16x32_bf16 v[180:183],v[104:107],a[48:51],v[180:183]\n"
"ds_read_b64_tr_b16 a[86:87],v14 offset:51200\n"
"v_mfma_f32_16x16x32_bf16 v[180:183],v[108:111],a[52:55],v[180:183]\n"
"v_mfma_f32_16x16x32_bf16 v[188:191],v[104:107],a[56:59],v[188:191]\n"
"s_add_u32 m0,s50,s56\n"
"v_mfma_f32_16x16x32_bf16 v[188:191],v[108:111],a[60:63],v[188:191]\n"
"s_waitcnt lgkmcnt(13)\n"
"buffer_load_dwordx4 v28,s[36:39],s48 offen offset:0 lds\n"
"v_mfma_f32_16x16x32_bf16 v[192:195],v[112:115],a[0:3],v[192:195]\n"
"ds_read_b128 v[96:99],v10 offset:8192\n"
"v_mfma_f32_16x16x32_bf16 v[192:195],v[116:119],a[4:7],v[192:195]\n"
"ds_read_b64_tr_b16 a[88:89],v15 offset:32768\n"
"v_mfma_f32_16x16x32_bf16 v[200:203],v[112:115],a[8:11],v[200:203]\n"
"ds_read_b128 v[100:103],v11 offset:8192\n"
"v_mfma_f32_16x16x32_bf16 v[200:203],v[116:119],a[12:15],v[200:203]\n"
"ds_read_b64_tr_b16 a[90:91],v15 offset:34816\n"
"v_mfma_f32_16x16x32_bf16 v[208:211],v[112:115],a[16:19],v[208:211]\n"
"ds_read_b64_tr_b16 a[92:93],v15 offset:49152\n"
"v_mfma_f32_16x16x32_bf16 v[208:211],v[116:119],a[20:23],v[208:211]\n"
"buffer_load_dwordx4 v29,s[36:39],s48 offen offset:1024 lds\n"
"v_mfma_f32_16x16x32_bf16 v[216:219],v[112:115],a[24:27],v[216:219]\n"
"v_mfma_f32_16x16x32_bf16 v[216:219],v[116:119],a[28:31],v[216:219]\n"
"ds_read_b64_tr_b16 a[94:95],v15 offset:51200\n"
"v_mfma_f32_16x16x32_bf16 v[224:227],v[112:115],a[32:35],v[224:227]\n"
"v_mfma_f32_16x16x32_bf16 v[224:227],v[116:119],a[36:39],v[224:227]\n"
"v_mfma_f32_16x16x32_bf16 v[232:235],v[112:115],a[40:43],v[232:235]\n"
"ds_read_b64_tr_b16 a[96:97],v16 offset:32768\n"
"v_mfma_f32_16x16x32_bf16 v[232:235],v[116:119],a[44:47],v[232:235]\n"
"buffer_load_dwordx4 v30,s[36:39],s48 offen offset:2048 lds\n"
"v_mfma_f32_16x16x32_bf16 v[240:243],v[112:115],a[48:51],v[240:243]\n"
"ds_read_b64_tr_b16 a[98:99],v16 offset:34816\n"
"v_mfma_f32_16x16x32_bf16 v[240:243],v[116:119],a[52:55],v[240:243]\n"
"v_mfma_f32_16x16x32_bf16 v[248:251],v[112:115],a[56:59],v[248:251]\n"
"v_mfma_f32_16x16x32_bf16 v[248:251],v[116:119],a[60:63],v[248:251]\n"
"s_waitcnt lgkmcnt(13)\n"
"ds_read_b64_tr_b16 a[100:101],v16 offset:49152\n"
"v_mfma_f32_16x16x32_bf16 v[196:199],v[120:123],a[0:3],v[196:199]\n"
"ds_read_b128 v[104:107],v10 offset:10240\n"
"v_mfma_f32_16x16x32_bf16 v[196:199],v[124:127],a[4:7],v[196:199]\n"
"buffer_load_dwordx4 v31,s[36:39],s48 offen offset:3072 lds\n"
"v_mfma_f32_16x16x32_bf16 v[204:207],v[120:123],a[8:11],v[204:207]\n"
"ds_read_b128 v[108:111],v11 offset:10240\n"
"v_mfma_f32_16x16x32_bf16 v[204:207],v[124:127],a[12:15],v[204:207]\n"
"ds_read_b64_tr_b16 a[102:103],v16 offset:51200\n"
"v_mfma_f32_16x16x32_bf16 v[212:215],v[120:123],a[16:19],v[212:215]\n"
"ds_read_b64_tr_b16 a[104:105],v17 offset:32768\n"
"v_mfma_f32_16x16x32_bf16 v[212:215],v[124:127],a[20:23],v[212:215]\n"
"v_mfma_f32_16x16x32_bf16 v[220:223],v[120:123],a[24:27],v[220:223]\n"
"s_add_u32 s48,s48,128\n"
"v_mfma_f32_16x16x32_bf16 v[220:223],v[124:127],a[28:31],v[220:223]\n"
"s_mov_b32 s51,s50\n"
"ds_read_b64_tr_b16 a[106:107],v17 offset:34816\n"
"v_mfma_f32_16x16x32_bf16 v[228:231],v[120:123],a[32:35],v[228:231]\n"
"s_add_u32 s50,s50,0x8000\n"
"v_mfma_f32_16x16x32_bf16 v[228:231],v[124:127],a[36:39],v[228:231]\n"
"s_cmp_eq_u32 s50,0x18000\n"
"ds_read_b64_tr_b16 a[108:109],v17 offset:49152\n"
"v_mfma_f32_16x16x32_bf16 v[236:239],v[120:123],a[40:43],v[236:239]\n"
"s_cselect_b32 s50,0,s50\n"
"v_mfma_f32_16x16x32_bf16 v[236:239],v[124:127],a[44:47],v[236:239]\n"
"v_mfma_f32_16x16x32_bf16 v[244:247],v[120:123],a[48:51],v[244:247]\n"
"ds_read_b64_tr_b16 a[110:111],v17 offset:51200\n"
"v_mfma_f32_16x16x32_bf16 v[244:247],v[124:127],a[52:55],v[244:247]\n"
"v_mfma_f32_16x16x32_bf16 v[252:255],v[120:123],a[56:59],v[252:255]\n"
"v_mfma_f32_16x16x32_bf16 v[252:255],v[124:127],a[60:63],v[252:255]\n"
"s_waitcnt lgkmcnt(13)\n"
"ds_read_b64_tr_b16 a[112:113],v18 offset:32768\n"
"v_mfma_f32_16x16x32_bf16 a[128:131],v[96:99],a[0:3],a[128:131]\n"
"ds_read_b128 v[112:115],v10 offset:12288\n"
"v_mfma_f32_16x16x32_bf16 a[128:131],v[100:103],a[4:7],a[128:131]\n"
"ds_read_b64_tr_b16 a[114:115],v18 offset:34816\n"
"v_mfma_f32_16x16x32_bf16 a[136:139],v[96:99],a[8:11],a[136:139]\n"
"ds_read_b128 v[116:119],v11 offset:12288\n"
"v_mfma_f32_16x16x32_bf16 a[136:139],v[100:103],a[12:15],a[136:139]\n"
"v_mfma_f32_16x16x32_bf16 a[144:147],v[96:99],a[16:19],a[144:147]\n"
"ds_read_b64_tr_b16 a[116:117],v18 offset:49152\n"
"v_mfma_f32_16x16x32_bf16 a[144:147],v[100:103],a[20:23],a[144:147]\n"
"v_mfma_f32_16x16x32_bf16 a[152:155],v[96:99],a[24:27],a[152:155]\n"
"ds_read_b64_tr_b16 a[118:119],v18 offset:51200\n"
"v_mfma_f32_16x16x32_bf16 a[152:155],v[100:103],a[28:31],a[152:155]\n"
"v_mfma_f32_16x16x32_bf16 a[160:163],v[96:99],a[32:35],a[160:163]\n"
"v_mfma_f32_16x16x32_bf16 a[160:163],v[100:103],a[36:39],a[160:163]\n"
"ds_read_b64_tr_b16 a[120:121],v19 offset:32768\n"
"v_mfma_f32_16x16x32_bf16 a[168:171],v[96:99],a[40:43],a[168:171]\n"
"v_mfma_f32_16x16x32_bf16 a[168:171],v[100:103],a[44:47],a[168:171]\n"
"v_mfma_f32_16x16x32_bf16 a[176:179],v[96:99],a[48:51],a[176:179]\n"
"ds_read_b64_tr_b16 a[122:123],v19 offset:34816\n"
"v_mfma_f32_16x16x32_bf16 a[176:179],v[100:103],a[52:55],a[176:179]\n"
"v_mfma_f32_16x16x32_bf16 a[184:187],v[96:99],a[56:59],a[184:187]\n"
"ds_read_b64_tr_b16 a[124:125],v19 offset:49152\n"
"v_mfma_f32_16x16x32_bf16 a[184:187],v[100:103],a[60:63],a[184:187]\n"
"s_waitcnt lgkmcnt(14)\n"
"v_mfma_f32_16x16x32_bf16 a[132:135],v[104:107],a[0:3],a[132:135]\n"
"ds_read_b128 v[120:123],v10 offset:14336\n"
"v_mfma_f32_16x16x32_bf16 a[132:135],v[108:111],a[4:7],a[132:135]\n"
"ds_read_b64_tr_b16 a[126:127],v19 offset:51200\n"
"v_mfma_f32_16x16x32_bf16 a[140:143],v[104:107],a[8:11],a[140:143]\n"
"ds_read_b128 v[124:127],v11 offset:14336\n"
"v_mfma_f32_16x16x32_bf16 a[140:143],v[108:111],a[12:15],a[140:143]\n"
"v_mfma_f32_16x16x32_bf16 a[148:151],v[104:107],a[16:19],a[148:151]\n"
"v_mfma_f32_16x16x32_bf16 a[148:151],v[108:111],a[20:23],a[148:151]\n"
"v_mfma_f32_16x16x32_bf16 a[156:159],v[104:107],a[24:27],a[156:159]\n"
"v_mfma_f32_16x16x32_bf16 a[156:159],v[108:111],a[28:31],a[156:159]\n"
"v_mfma_f32_16x16x32_bf16 a[164:167],v[104:107],a[32:35],a[164:167]\n"
"v_mfma_f32_16x16x32_bf16 a[164:167],v[108:111],a[36:39],a[164:167]\n"
"s_add_u32 s78,s78,384\n"
"v_mfma_f32_16x16x32_bf16 a[172:175],v[104:107],a[40:43],a[172:175]\n"
"s_addc_u32 s79,s79,0\n"
"v_mfma_f32_16x16x32_bf16 a[172:175],v[108:111],a[44:47],a[172:175]\n"
"v_mfma_f32_16x16x32_bf16 a[180:183],v[104:107],a[48:51],a[180:183]\n"
"v_mfma_f32_16x16x32_bf16 a[180:183],v[108:111],a[52:55],a[180:183]\n"
"v_mfma_f32_16x16x32_bf16 a[188:191],v[104:107],a[56:59],a[188:191]\n"
"s_waitcnt lgkmcnt(0)\n"
"v_mfma_f32_16x16x32_bf16 a[188:191],v[108:111],a[60:63],a[188:191]\n"
"s_waitcnt vmcnt(9)\n"
"s_barrier\n"
"v_mfma_f32_16x16x32_bf16 a[192:195],v[112:115],a[0:3],a[192:195]\n"
"s_add_u32 m0,s57,0x8000\n"
"v_add_u32 v10,s81,v8\n"
"v_add_u32 v11,s81,v9\n"
"v_mfma_f32_16x16x32_bf16 a[192:195],v[116:119],a[4:7],a[192:195]\n"
"s_mov_b32 s81,s51\n"
"buffer_load_dwordx4 v32,s[40:43],s49 offen offset:0 nt lds\n"
"v_mfma_f32_16x16x32_bf16 a[200:203],v[112:115],a[8:11],a[200:203]\n"
"ds_read_b128 v[96:99],v10 offset:0\n"
"v_mfma_f32_16x16x32_bf16 a[200:203],v[116:119],a[12:15],a[200:203]\n"
"v_mfma_f32_16x16x32_bf16 a[208:211],v[112:115],a[16:19],a[208:211]\n"
"ds_read_b128 v[100:103],v11 offset:0\n"
"v_mfma_f32_16x16x32_bf16 a[208:211],v[116:119],a[20:23],a[208:211]\n"
"buffer_load_dwordx4 v33,s[40:43],s49 offen offset:1024 nt lds\n"
"v_mfma_f32_16x16x32_bf16 a[216:219],v[112:115],a[24:27],a[216:219]\n"
"v_mfma_f32_16x16x32_bf16 a[216:219],v[116:119],a[28:31],a[216:219]\n"
"v_mfma_f32_16x16x32_bf16 a[224:227],v[112:115],a[32:35],a[224:227]\n"
"v_mfma_f32_16x16x32_bf16 a[224:227],v[116:119],a[36:39],a[224:227]\n"
"buffer_load_dwordx4 v34,s[40:43],s49 offen offset:2048 nt lds\n"
"v_mfma_f32_16x16x32_bf16 a[232:235],v[112:115],a[40:43],a[232:235]\n"
"v_mfma_f32_16x16x32_bf16 a[232:235],v[116:119],a[44:47],a[232:235]\n"
"v_mfma_f32_16x16x32_bf16 a[240:243],v[112:115],a[48:51],a[240:243]\n"
"v_mfma_f32_16x16x32_bf16 a[240:243],v[116:119],a[52:55],a[240:243]\n"
"buffer_load_dwordx4 v35,s[40:43],s49 offen offset:3072 nt lds\n"
"v_mfma_f32_16x16x32_bf16 a[248:251],v[112:115],a[56:59],a[248:251]\n"
"v_mfma_f32_16x16x32_bf16 a[248:251],v[116:119],a[60:63],a[248:251]\n"
"v_mfma_f32_16x16x32_bf16 a[196:199],v[120:123],a[0:3],a[196:199]\n"
"s_add_u32 m0,s57,0x9000\n"
"ds_read_b128 v[104:107],v10 offset:2048\n"
"v_mfma_f32_16x16x32_bf16 a[196:199],v[124:127],a[4:7],a[196:199]\n"
"buffer_load_dwordx4 v36,s[40:43],s49 offen offset:0 nt lds\n"
"v_mfma_f32_16x16x32_bf16 a[204:207],v[120:123],a[8:11],a[204:207]\n"
"ds_read_b128 v[108:111],v11 offset:2048\n"
"v_mfma_f32_16x16x32_bf16 a[204:207],v[124:127],a[12:15],a[204:207]\n"
"v_mfma_f32_16x16x32_bf16 a[212:215],v[120:123],a[16:19],a[212:215]\n"
"v_mfma_f32_16x16x32_bf16 a[212:215],v[124:127],a[20:23],a[212:215]\n"
"buffer_load_dwordx4 v37,s[40:43],s49 offen offset:1024 nt lds\n"
"v_mfma_f32_16x16x32_bf16 a[220:223],v[120:123],a[24:27],a[220:223]\n"
"v_mfma_f32_16x16x32_bf16 a[220:223],v[124:127],a[28:31],a[220:223]\n"
"v_mfma_f32_16x16x32_bf16 a[228:231],v[120:123],a[32:35],a[228:231]\n"
"v_mfma_f32_16x16x32_bf16 a[228:231],v[124:127],a[36:39],a[228:231]\n"
"buffer_load_dwordx4 v38,s[40:43],s49 offen offset:2048 nt lds\n"
"v_mfma_f32_16x16x32_bf16 a[236:239],v[120:123],a[40:43],a[236:239]\n"
"v_mfma_f32_16x16x32_bf16 a[236:239],v[124:127],a[44:47],a[236:239]\n"
"v_mfma_f32_16x16x32_bf16 a[244:247],v[120:123],a[48:51],a[244:247]\n"
"v_mfma_f32_16x16x32_bf16 a[244:247],v[124:127],a[52:55],a[244:247]\n"
"buffer_load_dwordx4 v39,s[40:43],s49 offen offset:3072 nt lds\n"
"v_mfma_f32_16x16x32_bf16 a[252:255],v[120:123],a[56:59],a[252:255]\n"
"s_add_u32 s49,s49,0x80000\n"
"v_mfma_f32_16x16x32_bf16 a[252:255],v[124:127],a[60:63],a[252:255]\n"
"s_waitcnt lgkmcnt(2)\n"
"v_mfma_f32_16x16x32_bf16 v[128:131],v[96:99],a[64:67],v[128:131]\n"
"ds_read_b128 v[112:115],v10 offset:4096\n"
"v_mfma_f32_16x16x32_bf16 v[128:131],v[100:103],a[68:71],v[128:131]\n"
"ds_read_b64_tr_b16 a[0:1],v12 offset:0\n"
"v_mfma_f32_16x16x32_bf16 v[136:139],v[96:99],a[72:75],v[136:139]\n"
"ds_read_b128 v[116:119],v11 offset:4096\n"
"v_mfma_f32_16x16x32_bf16 v[136:139],v[100:103],a[76:79],v[136:139]\n"
"ds_read_b64_tr_b16 a[2:3],v12 offset:2048\n"
"v_mfma_f32_16x16x32_bf16 v[144:147],v[96:99],a[80:83],v[144:147]\n"
"v_mfma_f32_16x16x32_bf16 v[144:147],v[100:103],a[84:87],v[144:147]\n"
"global_load_dword v86,v5,s[78:79]\n"
"v_mfma_f32_16x16x32_bf16 v[152:155],v[96:99],a[88:91],v[152:155]\n"
"s_add_u32 m0,s50,s55\n"
"ds_read_b64_tr_b16 a[4:5],v12 offset:16384\n"
"v_mfma_f32_16x16x32_bf16 v[152:155],v[100:103],a[92:95],v[152:155]\n"
"buffer_load_dwordx4 v24,s[36:39],s48 offen offset:0 lds\n"
"v_mfma_f32_16x16x32_bf16 v[160:163],v[96:99],a[96:99],v[160:163]\n"
"ds_read_b64_tr_b16 a[6:7],v12 offset:18432\n"
"v_mfma_f32_16x16x32_bf16 v[160:163],v[100:103],a[100:103],v[160:163]\n"
"v_mfma_f32_16x16x32_bf16 v[168:171],v[96:99],a[104:107],v[168:171]\n"
"ds_read_b64_tr_b16 a[8:9],v13 offset:0\n"
"v_mfma_f32_16x16x32_bf16 v[168:171],v[100:103],a[108:111],v[168:171]\n"
"v_mfma_f32_16x16x32_bf16 v[176:179],v[96:99],a[112:115],v[176:179]\n"
"v_mfma_f32_16x16x32_bf16 v[176:179],v[100:103],a[116:119],v[176:179]\n"
"buffer_load_dwordx4 v25,s[36:39],s48 offen offset:1024 lds\n"
"v_mfma_f32_16x16x32_bf16 v[184:187],v[96:99],a[120:123],v[184:187]\n"
"ds_read_b64_tr_b16 a[10:11],v13 offset:2048\n"
"v_mfma_f32_16x16x32_bf16 v[184:187],v[100:103],a[124:127],v[184:187]\n"
"s_waitcnt lgkmcnt(8)\n"
"ds_read_b64_tr_b16 a[12:13],v13 offset:16384\n"
"v_mfma_f32_16x16x32_bf16 v[132:135],v[104:107],a[64:67],v[132:135]\n"
"ds_read_b128 v[120:123],v10 offset:6144\n"
"v_mfma_f32_16x16x32_bf16 v[132:135],v[108:111],a[68:71],v[132:135]\n"
"v_mfma_f32_16x16x32_bf16 v[140:143],v[104:107],a[72:75],v[140:143]\n"
"ds_read_b128 v[124:127],v11 offset:6144\n"
"v_mfma_f32_16x16x32_bf16 v[140:143],v[108:111],a[76:79],v[140:143]\n"
"buffer_load_dwordx4 v26,s[36:39],s48 offen offset:2048 lds\n"
"v_mfma_f32_16x16x32_bf16 v[148:151],v[104:107],a[80:83],v[148:151]\n"
"ds_read_b64_tr_b16 a[14:15],v13 offset:18432\n"
"v_mfma_f32_16x16x32_bf16 v[148:151],v[108:111],a[84:87],v[148:151]\n"
"ds_read_b64_tr_b16 a[16:17],v14 offset:0\n"
"v_mfma_f32_16x16x32_bf16 v[156:159],v[104:107],a[88:91],v[156:159]\n"
"v_mfma_f32_16x16x32_bf16 v[156:159],v[108:111],a[92:95],v[156:159]\n"
"ds_read_b64_tr_b16 a[18:19],v14 offset:2048\n"
"v_mfma_f32_16x16x32_bf16 v[164:167],v[104:107],a[96:99],v[164:167]\n"
"v_mfma_f32_16x16x32_bf16 v[164:167],v[108:111],a[100:103],v[164:167]\n"
"buffer_load_dwordx4 v27,s[36:39],s48 offen offset:3072 lds\n"
"v_mfma_f32_16x16x32_bf16 v[172:175],v[104:107],a[104:107],v[172:175]\n"
"ds_read_b64_tr_b16 a[20:21],v14 offset:16384\n"
"v_mfma_f32_16x16x32_bf16 v[172:175],v[108:111],a[108:111],v[172:175]\n"
"v_mfma_f32_16x16x32_bf16 v[180:183],v[104:107],a[112:115],v[180:183]\n"
"ds_read_b64_tr_b16 a[22:23],v14 offset:18432\n"
"v_mfma_f32_16x16x32_bf16 v[180:183],v[108:111],a[116:119],v[180:183]\n"
"v_mfma_f32_16x16x32_bf16 v[188:191],v[104:107],a[120:123],v[188:191]\n"
"s_add_u32 m0,s50,s56\n"
"v_mfma_f32_16x16x32_bf16 v[188:191],v[108:111],a[124:127],v[188:191]\n"
"s_waitcnt lgkmcnt(13)\n"
"buffer_load_dwordx4 v28,s[36:39],s48 offen offset:0 lds\n"
"v_mfma_f32_16x16x32_bf16 v[192:195],v[112:115],a[64:67],v[192:195]\n"
"ds_read_b128 v[96:99],v10 offset:8192\n"
"v_mfma_f32_16x16x32_bf16 v[192:195],v[116:119],a[68:71],v[192:195]\n"
"ds_read_b64_tr_b16 a[24:25],v15 offset:0\n"
"v_mfma_f32_16x16x32_bf16 v[200:203],v[112:115],a[72:75],v[200:203]\n"
"ds_read_b128 v[100:103],v11 offset:8192\n"
"v_mfma_f32_16x16x32_bf16 v[200:203],v[116:119],a[76:79],v[200:203]\n"
"ds_read_b64_tr_b16 a[26:27],v15 offset:2048\n"
"v_mfma_f32_16x16x32_bf16 v[208:211],v[112:115],a[80:83],v[208:211]\n"
"ds_read_b64_tr_b16 a[28:29],v15 offset:16384\n"
"v_mfma_f32_16x16x32_bf16 v[208:211],v[116:119],a[84:87],v[208:211]\n"
"buffer_load_dwordx4 v29,s[36:39],s48 offen offset:1024 lds\n"
"v_mfma_f32_16x16x32_bf16 v[216:219],v[112:115],a[88:91],v[216:219]\n"
"v_mfma_f32_16x16x32_bf16 v[216:219],v[116:119],a[92:95],v[216:219]\n"
"ds_read_b64_tr_b16 a[30:31],v15 offset:18432\n"
"v_mfma_f32_16x16x32_bf16 v[224:227],v[112:115],a[96:99],v[224:227]\n"
"v_mfma_f32_16x16x32_bf16 v[224:227],v[116:119],a[100:103],v[224:227]\n"
"v_mfma_f32_16x16x32_bf16 v[232:235],v[112:115],a[104:107],v[232:235]\n"
"ds_read_b64_tr_b16 a[32:33],v16 offset:0\n"
"v_mfma_f32_16x16x32_bf16 v[232:235],v[116:119],a[108:111],v[232:235]\n"
"buffer_load_dwordx4 v30,s[36:39],s48 offen offset:2048 lds\n"
"v_mfma_f32_16x16x32_bf16 v[240:243],v[112:115],a[112:115],v[240:243]\n"
"ds_read_b64_tr_b16 a[34:35],v16 offset:2048\n"
"v_mfma_f32_16x16x32_bf16 v[240:243],v[116:119],a[116:119],v[240:243]\n"
"v_mfma_f32_16x16x32_bf16 v[248:251],v[112:115],a[120:123],v[248:251]\n"
"v_mfma_f32_16x16x32_bf16 v[248:251],v[116:119],a[124:127],v[248:251]\n"
"s_waitcnt lgkmcnt(13)\n"
"ds_read_b64_tr_b16 a[36:37],v16 offset:16384\n"
"v_mfma_f32_16x16x32_bf16 v[196:199],v[120:123],a[64:67],v[196:199]\n"
"ds_read_b128 v[104:107],v10 offset:10240\n"
"v_mfma_f32_16x16x32_bf16 v[196:199],v[124:127],a[68:71],v[196:199]\n"
"buffer_load_dwordx4 v31,s[36:39],s48 offen offset:3072 lds\n"
"v_mfma_f32_16x16x32_bf16 v[204:207],v[120:123],a[72:75],v[204:207]\n"
"ds_read_b128 v[108:111],v11 offset:10240\n"
"v_mfma_f32_16x16x32_bf16 v[204:207],v[124:127],a[76:79],v[204:207]\n"
"ds_read_b64_tr_b16 a[38:39],v16 offset:18432\n"
"v_mfma_f32_16x16x32_bf16 v[212:215],v[120:123],a[80:83],v[212:215]\n"
"ds_read_b64_tr_b16 a[40:41],v17 offset:0\n"
"v_mfma_f32_16x16x32_bf16 v[212:215],v[124:127],a[84:87],v[212:215]\n"
"v_mfma_f32_16x16x32_bf16 v[220:223],v[120:123],a[88:91],v[220:223]\n"
"s_add_u32 s48,s48,128\n"
"v_mfma_f32_16x16x32_bf16 v[220:223],v[124:127],a[92:95],v[220:223]\n"
"s_mov_b32 s51,s50\n"
"ds_read_b64_tr_b16 a[42:43],v17 offset:2048\n"
"v_mfma_f32_16x16x32_bf16 v[228:231],v[120:123],a[96:99],v[228:231]\n"
"s_add_u32 s50,s50,0x8000\n"
"v_mfma_f32_16x16x32_bf16 v[228:231],v[124:127],a[100:103],v[228:231]\n"
"s_cmp_eq_u32 s50,0x18000\n"
"ds_read_b64_tr_b16 a[44:45],v17 offset:16384\n"
"v_mfma_f32_16x16x32_bf16 v[236:239],v[120:123],a[104:107],v[236:239]\n"
"s_cselect_b32 s50,0,s50\n"
"v_mfma_f32_16x16x32_bf16 v[236:239],v[124:127],a[108:111],v[236:239]\n"
"v_mfma_f32_16x16x32_bf16 v[244:247],v[120:123],a[112:115],v[244:247]\n"
"ds_read_b64_tr_b16 a[46:47],v17 offset:18432\n"
"v_mfma_f32_16x16x32_bf16 v[244:247],v[124:127],a[116:119],v[244:247]\n"
"v_mfma_f32_16x16x32_bf16 v[252:255],v[120:123],a[120:123],v[252:255]\n"
"v_mfma_f32_16x16x32_bf16 v[252:255],v[124:127],a[124:127],v[252:255]\n"
"s_waitcnt lgkmcnt(13)\n"
"ds_read_b64_tr_b16 a[48:49],v18 offset:0\n"
"v_mfma_f32_16x16x32_bf16 a[128:131],v[96:99],a[64:67],a[128:131]\n"
"ds_read_b128 v[112:115],v10 offset:12288\n"
"v_mfma_f32_16x16x32_bf16 a[128:131],v[100:103],a[68:71],a[128:131]\n"
"ds_read_b64_tr_b16 a[50:51],v18 offset:2048\n"
"v_mfma_f32_16x16x32_bf16 a[136:139],v[96:99],a[72:75],a[136:139]\n"
"ds_read_b128 v[116:119],v11 offset:12288\n"
"v_mfma_f32_16x16x32_bf16 a[136:139],v[100:103],a[76:79],a[136:139]\n"
"v_mfma_f32_16x16x32_bf16 a[144:147],v[96:99],a[80:83],a[144:147]\n"
"ds_read_b64_tr_b16 a[52:53],v18 offset:16384\n"
"v_mfma_f32_16x16x32_bf16 a[144:147],v[100:103],a[84:87],a[144:147]\n"
"v_mfma_f32_16x16x32_bf16 a[152:155],v[96:99],a[88:91],a[152:155]\n"
"ds_read_b64_tr_b16 a[54:55],v18 offset:18432\n"
"v_mfma_f32_16x16x32_bf16 a[152:155],v[100:103],a[92:95],a[152:155]\n"
"v_mfma_f32_16x16x32_bf16 a[160:163],v[96:99],a[96:99],a[160:163]\n"
"v_mfma_f32_16x16x32_bf16 a[160:163],v[100:103],a[100:103],a[160:163]\n"
"ds_read_b64_tr_b16 a[56:57],v19 offset:0\n"
"v_mfma_f32_16x16x32_bf16 a[168:171],v[96:99],a[104:107],a[168:171]\n"
"v_mfma_f32_16x16x32_bf16 a[168:171],v[100:103],a[108:111],a[168:171]\n"
"v_mfma_f32_16x16x32_bf16 a[176:179],v[96:99],a[112:115],a[176:179]\n"
"ds_read_b64_tr_b16 a[58:59],v19 offset:2048\n"
"v_mfma_f32_16x16x32_bf16 a[176:179],v[100:103],a[116:119],a[176:179]\n"
"v_mfma_f32_16x16x32_bf16 a[184:187],v[96:99],a[120:123],a[184:187]\n"
"ds_read_b64_tr_b16 a[60:61],v19 offset:16384\n"
"v_mfma_f32_16x16x32_bf16 a[184:187],v[100:103],a[124:127],a[184:187]\n"
"s_waitcnt lgkmcnt(14)\n"
"v_mfma_f32_16x16x32_bf16 a[132:135],v[104:107],a[64:67],a[132:135]\n"
"ds_read_b128 v[120:123],v10 offset:14336\n"
"v_mfma_f32_16x16x32_bf16 a[132:135],v[108:111],a[68:71],a[132:135]\n"
"ds_read_b64_tr_b16 a[62:63],v19 offset:18432\n"
"v_mfma_f32_16x16x32_bf16 a[140:143],v[104:107],a[72:75],a[140:143]\n"
"ds_read_b128 v[124:127],v11 offset:14336\n"
"v_mfma_f32_16x16x32_bf16 a[140:143],v[108:111],a[76:79],a[140:143]\n"
"v_mfma_f32_16x16x32_bf16 a[148:151],v[104:107],a[80:83],a[148:151]\n"
"v_mfma_f32_16x16x32_bf16 a[148:151],v[108:111],a[84:87],a[148:151]\n"
"v_mfma_f32_16x16x32_bf16 a[156:159],v[104:107],a[88:91],a[156:159]\n"
"v_mfma_f32_16x16x32_bf16 a[156:159],v[108:111],a[92:95],a[156:159]\n"
"v_mfma_f32_16x16x32_bf16 a[164:167],v[104:107],a[96:99],a[164:167]\n"
"v_mfma_f32_16x16x32_bf16 a[164:167],v[108:111],a[100:103],a[164:167]\n"
"s_add_u32 s78,s78,384\n"
"v_mfma_f32_16x16x32_bf16 a[172:175],v[104:107],a[104:107],a[172:175]\n"
"s_addc_u32 s79,s79,0\n"
"v_mfma_f32_16x16x32_bf16 a[172:175],v[108:111],a[108:111],a[172:175]\n"
"v_mfma_f32_16x16x32_bf16 a[180:183],v[104:107],a[112:115],a[180:183]\n"
"v_mfma_f32_16x16x32_bf16 a[180:183],v[108:111],a[116:119],a[180:183]\n"
"v_mfma_f32_16x16x32_bf16 a[188:191],v[104:107],a[120:123],a[188:191]\n"
"s_waitcnt lgkmcnt(0)\n"
"v_mfma_f32_16x16x32_bf16 a[188:191],v[108:111],a[124:127],a[188:191]\n"
"s_waitcnt vmcnt(9)\n"
"s_barrier\n"
"v_mfma_f32_16x16x32_bf16 a[192:195],v[112:115],a[64:67],a[192:195]\n"
"s_add_u32 m0,s57,0x0\n"
"v_add_u32 v10,s81,v8\n"
"v_add_u32 v11,s81,v9\n"
"v_mfma_f32_16x16x32_bf16 a[192:195],v[116:119],a[68:71],a[192:195]\n"
"s_mov_b32 s81,s51\n"
"buffer_load_dwordx4 v32,s[40:43],s49 offen offset:0 nt lds\n"
"v_mfma_f32_16x16x32_bf16 a[200:203],v[112:115],a[72:75],a[200:203]\n"
"s_cmp_eq_u32 s52,5\n"
"ds_read_b128 v[96:99],v10 offset:0\n"
"v_mfma_f32_16x16x32_bf16 a[200:203],v[116:119],a[76:79],a[200:203]\n"
"v_mfma_f32_16x16x32_bf16 a[208:211],v[112:115],a[80:83],a[208:211]\n"
"s_cbranch_scc1 L_oncev_%=\n"
"L_oncev_ret_%=:\n"
"ds_read_b128 v[100:103],v11 offset:0\n"
"v_mfma_f32_16x16x32_bf16 a[208:211],v[116:119],a[84:87],a[208:211]\n"
"buffer_load_dwordx4 v33,s[40:43],s49 offen offset:1024 nt lds\n"
"v_mfma_f32_16x16x32_bf16 a[216:219],v[112:115],a[88:91],a[216:219]\n"
"v_mfma_f32_16x16x32_bf16 a[216:219],v[116:119],a[92:95],a[216:219]\n"
"v_mfma_f32_16x16x32_bf16 a[224:227],v[112:115],a[96:99],a[224:227]\n"
"v_mfma_f32_16x16x32_bf16 a[224:227],v[116:119],a[100:103],a[224:227]\n"
"buffer_load_dwordx4 v34,s[40:43],s49 offen offset:2048 nt lds\n"
"v_mfma_f32_16x16x32_bf16 a[232:235],v[112:115],a[104:107],a[232:235]\n"
"v_mfma_f32_16x16x32_bf16 a[232:235],v[116:119],a[108:111],a[232:235]\n"
"v_mfma_f32_16x16x32_bf16 a[240:243],v[112:115],a[112:115],a[240:243]\n"
"v_mfma_f32_16x16x32_bf16 a[240:243],v[116:119],a[116:119],a[240:243]\n"
"buffer_load_dwordx4 v35,s[40:43],s49 offen offset:3072 nt lds\n"
"v_mfma_f32_16x16x32_bf16 a[248:251],v[112:115],a[120:123],a[248:251]\n"
"v_mfma_f32_16x16x32_bf16 a[248:251],v[116:119],a[124:127],a[248:251]\n"
"v_mfma_f32_16x16x32_bf16 a[196:199],v[120:123],a[64:67],a[196:199]\n"
"s_add_u32 m0,s57,0x1000\n"
"ds_read_b128 v[104:107],v10 offset:2048\n"
"v_mfma_f32_16x16x32_bf16 a[196:199],v[124:127],a[68:71],a[196:199]\n"
"buffer_load_dwordx4 v36,s[40:43],s49 offen offset:0 nt lds\n"
"v_mfma_f32_16x16x32_bf16 a[204:207],v[120:123],a[72:75],a[204:207]\n"
"ds_read_b128 v[108:111],v11 offset:2048\n"
"v_mfma_f32_16x16x32_bf16 a[204:207],v[124:127],a[76:79],a[204:207]\n"
"v_mfma_f32_16x16x32_bf16 a[212:215],v[120:123],a[80:83],a[212:215]\n"
"v_mfma_f32_16x16x32_bf16 a[212:215],v[124:127],a[84:87],a[212:215]\n"
"buffer_load_dwordx4 v37,s[40:43],s49 offen offset:1024 nt lds\n"
"v_mfma_f32_16x16x32_bf16 a[220:223],v[120:123],a[88:91],a[220:223]\n"
"v_mfma_f32_16x16x32_bf16 a[220:223],v[124:127],a[92:95],a[220:223]\n"
"v_mfma_f32_16x16x32_bf16 a[228:231],v[120:123],a[96:99],a[228:231]\n"
"v_mfma_f32_16x16x32_bf16 a[228:231],v[124:127],a[100:103],a[228:231]\n"
"buffer_load_dwordx4 v38,s[40:43],s49 offen offset:2048 nt lds\n"
"v_mfma_f32_16x16x32_bf16 a[236:239],v[120:123],a[104:107],a[236:239]\n"
"s_sub_u32 s52,s52,1\n"
"v_mfma_f32_16x16x32_bf16 a[236:239],v[124:127],a[108:111],a[236:239]\n"
"v_mfma_f32_16x16x32_bf16 a[244:247],v[120:123],a[112:115],a[244:247]\n"
"v_mfma_f32_16x16x32_bf16 a[244:247],v[124:127],a[116:119],a[244:247]\n"
"buffer_load_dwordx4 v39,s[40:43],s49 offen offset:3072 nt lds\n"
"v_mfma_f32_16x16x32_bf16 a[252:255],v[120:123],a[120:123],a[252:255]\n"
"s_add_u32 s49,s49,0x80000\n"
"s_cmp_lg_u32 s52,0\n"
"v_mfma_f32_16x16x32_bf16 a[252:255],v[124:127],a[124:127],a[252:255]\n"
"s_waitcnt lgkmcnt(2)\n"
"s_cbranch_scc1 L_kvv_%=\n"
"v_mfma_f32_16x16x32_bf16 v[128:131],v[96:99],a[0:3],v[128:131]\n"
"ds_read_b128 v[112:115],v10 offset:4096\n"
"v_mfma_f32_16x16x32_bf16 v[128:131],v[100:103],a[4:7],v[128:131]\n"
"s_lshl_b32 s76,s54,7\n"
"ds_read_b64_tr_b16 a[64:65],v12 offset:32768\n"
"v_mfma_f32_16x16x32_bf16 v[136:139],v[96:99],a[8:11],v[136:139]\n"
"s_add_u32 s76,s76,0x1000\n"
"ds_read_b128 v[116:119],v11 offset:4096\n"
"v_mfma_f32_16x16x32_bf16 v[136:139],v[100:103],a[12:15],v[136:139]\n"
"s_add_u32 s82,s76,s32\n"
"s_addc_u32 s83,s33,0\n"
"ds_read_b64_tr_b16 a[66:67],v12 offset:34816\n"
"v_mfma_f32_16x16x32_bf16 v[144:147],v[96:99],a[16:19],v[144:147]\n"
"v_mfma_f32_16x16x32_bf16 v[144:147],v[100:103],a[20:23],v[144:147]\n"
"ds_read_b64_tr_b16 a[68:69],v12 offset:49152\n"
"global_load_dwordx2 v[54:55],v53,s[82:83] sc1\n"
"v_mfma_f32_16x16x32_bf16 v[152:155],v[96:99],a[24:27],v[152:155]\n"
"s_add_u32 m0,s50,s55\n"
"v_mfma_f32_16x16x32_bf16 v[152:155],v[100:103],a[28:31],v[152:155]\n"
"buffer_load_dwordx4 v24,s[36:39],s48 offen offset:0 lds\n"
"v_mfma_f32_16x16x32_bf16 v[160:163],v[96:99],a[32:35],v[160:163]\n"
"ds_read_b64_tr_b16 a[70:71],v12 offset:51200\n"
"v_mfma_f32_16x16x32_bf16 v[160:163],v[100:103],a[36:39],v[160:163]\n"
"v_mfma_f32_16x16x32_bf16 v[168:171],v[96:99],a[40:43],v[168:171]\n"
"ds_read_b64_tr_b16 a[72:73],v13 offset:32768\n"
"v_mfma_f32_16x16x32_bf16 v[168:171],v[100:103],a[44:47],v[168:171]\n"
"v_mfma_f32_16x16x32_bf16 v[176:179],v[96:99],a[48:51],v[176:179]\n"
"v_mfma_f32_16x16x32_bf16 v[176:179],v[100:103],a[52:55],v[176:179]\n"
"buffer_load_dwordx4 v25,s[36:39],s48 offen offset:1024 lds\n"
"v_mfma_f32_16x16x32_bf16 v[184:187],v[96:99],a[56:59],v[184:187]\n"
"ds_read_b64_tr_b16 a[74:75],v13 offset:34816\n"
"v_mfma_f32_16x16x32_bf16 v[184:187],v[100:103],a[60:63],v[184:187]\n"
"s_waitcnt lgkmcnt(8)\n"
"ds_read_b64_tr_b16 a[76:77],v13 offset:49152\n"
"v_mfma_f32_16x16x32_bf16 v[132:135],v[104:107],a[0:3],v[132:135]\n"
"ds_read_b128 v[120:123],v10 offset:6144\n"
"v_mfma_f32_16x16x32_bf16 v[132:135],v[108:111],a[4:7],v[132:135]\n"
"v_mfma_f32_16x16x32_bf16 v[140:143],v[104:107],a[8:11],v[140:143]\n"
"ds_read_b128 v[124:127],v11 offset:6144\n"
"v_mfma_f32_16x16x32_bf16 v[140:143],v[108:111],a[12:15],v[140:143]\n"
"buffer_load_dwordx4 v26,s[36:39],s48 offen offset:2048 lds\n"
"v_mfma_f32_16x16x32_bf16 v[148:151],v[104:107],a[16:19],v[148:151]\n"
"ds_read_b64_tr_b16 a[78:79],v13 offset:51200\n"
"v_mfma_f32_16x16x32_bf16 v[148:151],v[108:111],a[20:23],v[148:151]\n"
"ds_read_b64_tr_b16 a[80:81],v14 offset:32768\n"
"v_mfma_f32_16x16x32_bf16 v[156:159],v[104:107],a[24:27],v[156:159]\n"
"v_mfma_f32_16x16x32_bf16 v[156:159],v[108:111],a[28:31],v[156:159]\n"
"ds_read_b64_tr_b16 a[82:83],v14 offset:34816\n"
"v_mfma_f32_16x16x32_bf16 v[164:167],v[104:107],a[32:35],v[164:167]\n"
"v_mfma_f32_16x16x32_bf16 v[164:167],v[108:111],a[36:39],v[164:167]\n"
"buffer_load_dwordx4 v27,s[36:39],s48 offen offset:3072 lds\n"
"v_mfma_f32_16x16x32_bf16 v[172:175],v[104:107],a[40:43],v[172:175]\n"
"ds_read_b64_tr_b16 a[84:85],v14 offset:49152\n"
"v_mfma_f32_16x16x32_bf16 v[172:175],v[108:111],a[44:47],v[172:175]\n"
"v_mfma_f32_16x16x32_bf16 v[180:183],v[104:107],a[48:51],v[180:183]\n"
"ds_read_b64_tr_b16 a[86:87],v14 offset:51200\n"
"v_mfma_f32_16x16x32_bf16 v[180:183],v[108:111],a[52:55],v[180:183]\n"
"v_mfma_f32_16x16x32_bf16 v[188:191],v[104:107],a[56:59],v[188:191]\n"
"s_add_u32 m0,s50,s56\n"
"v_mfma_f32_16x16x32_bf16 v[188:191],v[108:111],a[60:63],v[188:191]\n"
"s_waitcnt lgkmcnt(13)\n"
"buffer_load_dwordx4 v28,s[36:39],s48 offen offset:0 lds\n"
"v_mfma_f32_16x16x32_bf16 v[192:195],v[112:115],a[0:3],v[192:195]\n"
"ds_read_b128 v[96:99],v10 offset:8192\n"
"v_mfma_f32_16x16x32_bf16 v[192:195],v[116:119],a[4:7],v[192:195]\n"
"ds_read_b64_tr_b16 a[88:89],v15 offset:32768\n"
"v_mfma_f32_16x16x32_bf16 v[200:203],v[112:115],a[8:11],v[200:203]\n"
"ds_read_b128 v[100:103],v11 offset:8192\n"
"v_mfma_f32_16x16x32_bf16 v[200:203],v[116:119],a[12:15],v[200:203]\n"
"ds_read_b64_tr_b16 a[90:91],v15 offset:34816\n"
"v_mfma_f32_16x16x32_bf16 v[208:211],v[112:115],a[16:19],v[208:211]\n"
"ds_read_b64_tr_b16 a[92:93],v15 offset:49152\n"
"v_mfma_f32_16x16x32_bf16 v[208:211],v[116:119],a[20:23],v[208:211]\n"
"buffer_load_dwordx4 v29,s[36:39],s48 offen offset:1024 lds\n"
"v_mfma_f32_16x16x32_bf16 v[216:219],v[112:115],a[24:27],v[216:219]\n"
"v_mfma_f32_16x16x32_bf16 v[216:219],v[116:119],a[28:31],v[216:219]\n"
"ds_read_b64_tr_b16 a[94:95],v15 offset:51200\n"
"v_mfma_f32_16x16x32_bf16 v[224:227],v[112:115],a[32:35],v[224:227]\n"
"v_mfma_f32_16x16x32_bf16 v[224:227],v[116:119],a[36:39],v[224:227]\n"
"v_mfma_f32_16x16x32_bf16 v[232:235],v[112:115],a[40:43],v[232:235]\n"
"ds_read_b64_tr_b16 a[96:97],v16 offset:32768\n"
"v_mfma_f32_16x16x32_bf16 v[232:235],v[116:119],a[44:47],v[232:235]\n"
"buffer_load_dwordx4 v30,s[36:39],s48 offen offset:2048 lds\n"
"v_mfma_f32_16x16x32_bf16 v[240:243],v[112:115],a[48:51],v[240:243]\n"
"ds_read_b64_tr_b16 a[98:99],v16 offset:34816\n"
"v_mfma_f32_16x16x32_bf16 v[240:243],v[116:119],a[52:55],v[240:243]\n"
"v_mfma_f32_16x16x32_bf16 v[248:251],v[112:115],a[56:59],v[248:251]\n"
"v_mfma_f32_16x16x32_bf16 v[248:251],v[116:119],a[60:63],v[248:251]\n"
"s_waitcnt lgkmcnt(13)\n"
"ds_read_b64_tr_b16 a[100:101],v16 offset:49152\n"
"v_mfma_f32_16x16x32_bf16 v[196:199],v[120:123],a[0:3],v[196:199]\n"
"ds_read_b128 v[104:107],v10 offset:10240\n"
"v_mfma_f32_16x16x32_bf16 v[196:199],v[124:127],a[4:7],v[196:199]\n"
"buffer_load_dwordx4 v31,s[36:39],s48 offen offset:3072 lds\n"
"v_mfma_f32_16x16x32_bf16 v[204:207],v[120:123],a[8:11],v[204:207]\n"
"ds_read_b128 v[108:111],v11 offset:10240\n"
"v_mfma_f32_16x16x32_bf16 v[204:207],v[124:127],a[12:15],v[204:207]\n"
"ds_read_b64_tr_b16 a[102:103],v16 offset:51200\n"
"v_mfma_f32_16x16x32_bf16 v[212:215],v[120:123],a[16:19],v[212:215]\n"
"ds_read_b64_tr_b16 a[104:105],v17 offset:32768\n"
"v_mfma_f32_16x16x32_bf16 v[212:215],v[124:127],a[20:23],v[212:215]\n"
"v_mfma_f32_16x16x32_bf16 v[220:223],v[120:123],a[24:27],v[220:223]\n"
"s_add_u32 s48,s48,128\n"
"v_mfma_f32_16x16x32_bf16 v[220:223],v[124:127],a[28:31],v[220:223]\n"
"s_mov_b32 s51,s50\n"
"ds_read_b64_tr_b16 a[106:107],v17 offset:34816\n"
"v_mfma_f32_16x16x32_bf16 v[228:231],v[120:123],a[32:35],v[228:231]\n"
"s_add_u32 s50,s50,0x8000\n"
"v_mfma_f32_16x16x32_bf16 v[228:231],v[124:127],a[36:39],v[228:231]\n"
"s_cmp_eq_u32 s50,0x18000\n"
"ds_read_b64_tr_b16 a[108:109],v17 offset:49152\n"
"v_mfma_f32_16x16x32_bf16 v[236:239],v[120:123],a[40:43],v[236:239]\n"
"s_cselect_b32 s50,0,s50\n"
"v_mfma_f32_16x16x32_bf16 v[236:239],v[124:127],a[44:47],v[236:239]\n"
"v_mfma_f32_16x16x32_bf16 v[244:247],v[120:123],a[48:51],v[244:247]\n"
"ds_read_b64_tr_b16 a[110:111],v17 offset:51200\n"
"v_mfma_f32_16x16x32_bf16 v[244:247],v[124:127],a[52:55],v[244:247]\n"
"v_mfma_f32_16x16x32_bf16 v[252:255],v[120:123],a[56:59],v[252:255]\n"
"v_mfma_f32_16x16x32_bf16 v[252:255],v[124:127],a[60:63],v[252:255]\n"
"s_waitcnt lgkmcnt(13)\n"
"ds_read_b64_tr_b16 a[112:113],v18 offset:32768\n"
"v_mfma_f32_16x16x32_bf16 a[128:131],v[96:99],a[0:3],a[128:131]\n"
"ds_read_b128 v[112:115],v10 offset:12288\n"
"v_mfma_f32_16x16x32_bf16 a[128:131],v[100:103],a[4:7],a[128:131]\n"
"ds_read_b64_tr_b16 a[114:115],v18 offset:34816\n"
"v_mfma_f32_16x16x32_bf16 a[136:139],v[96:99],a[8:11],a[136:139]\n"
"ds_read_b128 v[116:119],v11 offset:12288\n"
"v_mfma_f32_16x16x32_bf16 a[136:139],v[100:103],a[12:15],a[136:139]\n"
"v_mfma_f32_16x16x32_bf16 a[144:147],v[96:99],a[16:19],a[144:147]\n"
"ds_read_b64_tr_b16 a[116:117],v18 offset:49152\n"
"v_mfma_f32_16x16x32_bf16 a[144:147],v[100:103],a[20:23],a[144:147]\n"
"v_mfma_f32_16x16x32_bf16 a[152:155],v[96:99],a[24:27],a[152:155]\n"
"ds_read_b64_tr_b16 a[118:119],v18 offset:51200\n"
"v_mfma_f32_16x16x32_bf16 a[152:155],v[100:103],a[28:31],a[152:155]\n"
"v_mfma_f32_16x16x32_bf16 a[160:163],v[96:99],a[32:35],a[160:163]\n"
"v_mfma_f32_16x16x32_bf16 a[160:163],v[100:103],a[36:39],a[160:163]\n"
"ds_read_b64_tr_b16 a[120:121],v19 offset:32768\n"
"v_mfma_f32_16x16x32_bf16 a[168:171],v[96:99],a[40:43],a[168:171]\n"
"v_mfma_f32_16x16x32_bf16 a[168:171],v[100:103],a[44:47],a[168:171]\n"
"v_mfma_f32_16x16x32_bf16 a[176:179],v[96:99],a[48:51],a[176:179]\n"
"ds_read_b64_tr_b16 a[122:123],v19 offset:34816\n"
"v_mfma_f32_16x16x32_bf16 a[176:179],v[100:103],a[52:55],a[176:179]\n"
"v_mfma_f32_16x16x32_bf16 a[184:187],v[96:99],a[56:59],a[184:187]\n"
"ds_read_b64_tr_b16 a[124:125],v19 offset:49152\n"
"v_mfma_f32_16x16x32_bf16 a[184:187],v[100:103],a[60:63],a[184:187]\n"
"s_waitcnt lgkmcnt(14)\n"
"v_mfma_f32_16x16x32_bf16 a[132:135],v[104:107],a[0:3],a[132:135]\n"
"ds_read_b128 v[120:123],v10 offset:14336\n"
"v_mfma_f32_16x16x32_bf16 a[132:135],v[108:111],a[4:7],a[132:135]\n"
"ds_read_b64_tr_b16 a[126:127],v19 offset:51200\n"
"v_mfma_f32_16x16x32_bf16 a[140:143],v[104:107],a[8:11],a[140:143]\n"
"ds_read_b128 v[124:127],v11 offset:14336\n"
"v_mfma_f32_16x16x32_bf16 a[140:143],v[108:111],a[12:15],a[140:143]\n"
"v_mfma_f32_16x16x32_bf16 a[148:151],v[104:107],a[16:19],a[148:151]\n"
"v_mfma_f32_16x16x32_bf16 a[148:151],v[108:111],a[20:23],a[148:151]\n"
"v_mfma_f32_16x16x32_bf16 a[156:159],v[104:107],a[24:27],a[156:159]\n"
"v_mfma_f32_16x16x32_bf16 a[156:159],v[108:111],a[28:31],a[156:159]\n"
"v_mfma_f32_16x16x32_bf16 a[164:167],v[104:107],a[32:35],a[164:167]\n"
"v_mfma_f32_16x16x32_bf16 a[164:167],v[108:111],a[36:39],a[164:167]\n"
"v_mfma_f32_16x16x32_bf16 a[172:175],v[104:107],a[40:43],a[172:175]\n"
"v_mfma_f32_16x16x32_bf16 a[172:175],v[108:111],a[44:47],a[172:175]\n"
"v_mfma_f32_16x16x32_bf16 a[180:183],v[104:107],a[48:51],a[180:183]\n"
"v_mfma_f32_16x16x32_bf16 a[180:183],v[108:111],a[52:55],a[180:183]\n"
"v_mfma_f32_16x16x32_bf16 a[188:191],v[104:107],a[56:59],a[188:191]\n"
"s_waitcnt lgkmcnt(0)\n"
"v_mfma_f32_16x16x32_bf16 a[188:191],v[108:111],a[60:63],a[188:191]\n"
"s_waitcnt vmcnt(9)\n"
"s_barrier\n"
"v_mfma_f32_16x16x32_bf16 a[192:195],v[112:115],a[0:3],a[192:195]\n"
"s_add_u32 m0,s57,0x8000\n"
"v_add_u32 v10,s81,v8\n"
"v_add_u32 v11,s81,v9\n"
"v_mfma_f32_16x16x32_bf16 a[192:195],v[116:119],a[4:7],a[192:195]\n"
"s_mov_b32 s81,s51\n"
"buffer_load_dwordx4 v32,s[40:43],s49 offen offset:0 nt lds\n"
"v_mfma_f32_16x16x32_bf16 a[200:203],v[112:115],a[8:11],a[200:203]\n"
"ds_read_b128 v[96:99],v10 offset:0\n"
"v_mfma_f32_16x16x32_bf16 a[200:203],v[116:119],a[12:15],a[200:203]\n"
"v_mfma_f32_16x16x32_bf16 a[208:211],v[112:115],a[16:19],a[208:211]\n"
"ds_read_b128 v[100:103],v11 offset:0\n"
"v_mfma_f32_16x16x32_bf16 a[208:211],v[116:119],a[20:23],a[208:211]\n"
"buffer_load_dwordx4 v33,s[40:43],s49 offen offset:1024 nt lds\n"
"v_mfma_f32_16x16x32_bf16 a[216:219],v[112:115],a[24:27],a[216:219]\n"
"v_mfma_f32_16x16x32_bf16 a[216:219],v[116:119],a[28:31],a[216:219]\n"
"v_mfma_f32_16x16x32_bf16 a[224:227],v[112:115],a[32:35],a[224:227]\n"
"v_mfma_f32_16x16x32_bf16 a[224:227],v[116:119],a[36:39],a[224:227]\n"
"buffer_load_dwordx4 v34,s[40:43],s49 offen offset:2048 nt lds\n"
"v_mfma_f32_16x16x32_bf16 a[232:235],v[112:115],a[40:43],a[232:235]\n"
"v_mfma_f32_16x16x32_bf16 a[232:235],v[116:119],a[44:47],a[232:235]\n"
"v_mfma_f32_16x16x32_bf16 a[240:243],v[112:115],a[48:51],a[240:243]\n"
"v_mfma_f32_16x16x32_bf16 a[240:243],v[116:119],a[52:55],a[240:243]\n"
"buffer_load_dwordx4 v35,s[40:43],s49 offen offset:3072 nt lds\n"
"v_mfma_f32_16x16x32_bf16 a[248:251],v[112:115],a[56:59],a[248:251]\n"
"v_mfma_f32_16x16x32_bf16 a[248:251],v[116:119],a[60:63],a[248:251]\n"
"v_mfma_f32_16x16x32_bf16 a[196:199],v[120:123],a[0:3],a[196:199]\n"
"s_add_u32 m0,s57,0x9000\n"
"ds_read_b128 v[104:107],v10 offset:2048\n"
"v_mfma_f32_16x16x32_bf16 a[196:199],v[124:127],a[4:7],a[196:199]\n"
"buffer_load_dwordx4 v36,s[40:43],s49 offen offset:0 nt lds\n"
"v_mfma_f32_16x16x32_bf16 a[204:207],v[120:123],a[8:11],a[204:207]\n"
"ds_read_b128 v[108:111],v11 offset:2048\n"
"v_mfma_f32_16x16x32_bf16 a[204:207],v[124:127],a[12:15],a[204:207]\n"
"v_mfma_f32_16x16x32_bf16 a[212:215],v[120:123],a[16:19],a[212:215]\n"
"v_mfma_f32_16x16x32_bf16 a[212:215],v[124:127],a[20:23],a[212:215]\n"
"buffer_load_dwordx4 v37,s[40:43],s49 offen offset:1024 nt lds\n"
"v_mfma_f32_16x16x32_bf16 a[220:223],v[120:123],a[24:27],a[220:223]\n"
"v_mfma_f32_16x16x32_bf16 a[220:223],v[124:127],a[28:31],a[220:223]\n"
"v_mfma_f32_16x16x32_bf16 a[228:231],v[120:123],a[32:35],a[228:231]\n"
"v_mfma_f32_16x16x32_bf16 a[228:231],v[124:127],a[36:39],a[228:231]\n"
"buffer_load_dwordx4 v38,s[40:43],s49 offen offset:2048 nt lds\n"
"v_mfma_f32_16x16x32_bf16 a[236:239],v[120:123],a[40:43],a[236:239]\n"
"v_mfma_f32_16x16x32_bf16 a[236:239],v[124:127],a[44:47],a[236:239]\n"
"v_mfma_f32_16x16x32_bf16 a[244:247],v[120:123],a[48:51],a[244:247]\n"
"v_mfma_f32_16x16x32_bf16 a[244:247],v[124:127],a[52:55],a[244:247]\n"
"buffer_load_dwordx4 v39,s[40:43],s49 offen offset:3072 nt lds\n"
"v_mfma_f32_16x16x32_bf16 a[252:255],v[120:123],a[56:59],a[252:255]\n"
"s_add_u32 s49,s49,0x80000\n"
"v_mfma_f32_16x16x32_bf16 a[252:255],v[124:127],a[60:63],a[252:255]\n"
"s_waitcnt lgkmcnt(2)\n"
"v_mfma_f32_16x16x32_bf16 v[128:131],v[96:99],a[64:67],v[128:131]\n"
"s_add_u32 m0,s50,s55\n"
"ds_read_b128 v[112:115],v10 offset:4096\n"
"v_mfma_f32_16x16x32_bf16 v[128:131],v[100:103],a[68:71],v[128:131]\n"
"s_waitcnt vmcnt(16)\n"
"ds_read_b64_tr_b16 a[0:1],v12 offset:0\n"
"buffer_load_dwordx4 v24,s[36:39],s48 offen offset:0 lds\n"
"v_mfma_f32_16x16x32_bf16 v[136:139],v[96:99],a[72:75],v[136:139]\n"
"ds_read_b128 v[116:119],v11 offset:4096\n"
"v_cmp_ne_u64 vcc,s[34:35],v[54:55]\n"
"v_mfma_f32_16x16x32_bf16 v[136:139],v[100:103],a[76:79],v[136:139]\n"
"ds_read_b64_tr_b16 a[2:3],v12 offset:2048\n"
"v_mfma_f32_16x16x32_bf16 v[144:147],v[96:99],a[80:83],v[144:147]\n"
"s_cbranch_vccnz L_lnsv_%=\n"
"L_lnsv_ret_%=:\n"
"v_mfma_f32_16x16x32_bf16 v[144:147],v[100:103],a[84:87],v[144:147]\n"
"ds_read_b64_tr_b16 a[4:5],v12 offset:16384\n"
"buffer_load_dwordx4 v25,s[36:39],s48 offen offset:1024 lds\n"
"v_mfma_f32_16x16x32_bf16 v[152:155],v[96:99],a[88:91],v[152:155]\n"
"v_mfma_f32_16x16x32_bf16 v[152:155],v[100:103],a[92:95],v[152:155]\n"
"ds_read_b64_tr_b16 a[6:7],v12 offset:18432\n"
"v_mfma_f32_16x16x32_bf16 v[160:163],v[96:99],a[96:99],v[160:163]\n"
"v_mfma_f32_16x16x32_bf16 v[160:163],v[100:103],a[100:103],v[160:163]\n"
"buffer_load_dwordx4 v26,s[36:39],s48 offen offset:2048 lds\n"
"v_mfma_f32_16x16x32_bf16 v[168:171],v[96:99],a[104:107],v[168:171]\n"
"ds_read_b64_tr_b16 a[8:9],v13 offset:0\n"
"v_mfma_f32_16x16x32_bf16 v[168:171],v[100:103],a[108:111],v[168:171]\n"
"v_mfma_f32_16x16x32_bf16 v[176:179],v[96:99],a[112:115],v[176:179]\n"
"v_mfma_f32_16x16x32_bf16 v[176:179],v[100:103],a[116:119],v[176:179]\n"
"ds_read_b64_tr_b16 a[10:11],v13 offset:2048\n"
"buffer_load_dwordx4 v27,s[36:39],s48 offen offset:3072 lds\n"
"v_mfma_f32_16x16x32_bf16 v[184:187],v[96:99],a[120:123],v[184:187]\n"
"v_mfma_f32_16x16x32_bf16 v[184:187],v[100:103],a[124:127],v[184:187]\n"
"s_waitcnt lgkmcnt(8)\n"
"ds_read_b64_tr_b16 a[12:13],v13 offset:16384\n"
"v_mfma_f32_16x16x32_bf16 v[132:135],v[104:107],a[64:67],v[132:135]\n"
"s_add_u32 m0,s50,s56\n"
"ds_read_b128 v[120:123],v10 offset:6144\n"
"v_mfma_f32_16x16x32_bf16 v[132:135],v[108:111],a[68:71],v[132:135]\n"
"buffer_load_dwordx4 v28,s[36:39],s48 offen offset:0 lds\n"
"v_mfma_f32_16x16x32_bf16 v[140:143],v[104:107],a[72:75],v[140:143]\n"
"ds_read_b128 v[124:127],v11 offset:6144\n"
"v_mfma_f32_16x16x32_bf16 v[140:143],v[108:111],a[76:79],v[140:143]\n"
"ds_read_b64_tr_b16 a[14:15],v13 offset:18432\n"
"v_mfma_f32_16x16x32_bf16 v[148:151],v[104:107],a[80:83],v[148:151]\n"
"v_mfma_f32_16x16x32_bf16 v[148:151],v[108:111],a[84:87],v[148:151]\n"
"ds_read_b64_tr_b16 a[16:17],v14 offset:0\n"
"buffer_load_dwordx4 v29,s[36:39],s48 offen offset:1024 lds\n"
"v_mfma_f32_16x16x32_bf16 v[156:159],v[104:107],a[88:91],v[156:159]\n"
"v_mfma_f32_16x16x32_bf16 v[156:159],v[108:111],a[92:95],v[156:159]\n"
"ds_read_b64_tr_b16 a[18:19],v14 offset:2048\n"
"v_mfma_f32_16x16x32_bf16 v[164:167],v[104:107],a[96:99],v[164:167]\n"
"v_mfma_f32_16x16x32_bf16 v[164:167],v[108:111],a[100:103],v[164:167]\n"
"buffer_load_dwordx4 v30,s[36:39],s48 offen offset:2048 lds\n"
"v_mfma_f32_16x16x32_bf16 v[172:175],v[104:107],a[104:107],v[172:175]\n"
"ds_read_b64_tr_b16 a[20:21],v14 offset:16384\n"
"v_mfma_f32_16x16x32_bf16 v[172:175],v[108:111],a[108:111],v[172:175]\n"
"v_mfma_f32_16x16x32_bf16 v[180:183],v[104:107],a[112:115],v[180:183]\n"
"ds_read_b64_tr_b16 a[22:23],v14 offset:18432\n"
"v_mfma_f32_16x16x32_bf16 v[180:183],v[108:111],a[116:119],v[180:183]\n"
"buffer_load_dwordx4 v31,s[36:39],s48 offen offset:3072 lds\n"
"v_mfma_f32_16x16x32_bf16 v[188:191],v[104:107],a[120:123],v[188:191]\n"
"v_mfma_f32_16x16x32_bf16 v[188:191],v[108:111],a[124:127],v[188:191]\n"
"s_waitcnt lgkmcnt(13)\n"
"ds_read_b64_tr_b16 a[24:25],v15 offset:0\n"
"v_mfma_f32_16x16x32_bf16 v[192:195],v[112:115],a[64:67],v[192:195]\n"
"ds_read_b128 v[96:99],v10 offset:8192\n"
"v_mfma_f32_16x16x32_bf16 v[192:195],v[116:119],a[68:71],v[192:195]\n"
"v_mfma_f32_16x16x32_bf16 v[200:203],v[112:115],a[72:75],v[200:203]\n"
"ds_read_b128 v[100:103],v11 offset:8192\n"
"buffer_load_dwordx4 v[72:75],v52,s[60:63],0 offen offset:0 sc1\n"
"v_mfma_f32_16x16x32_bf16 v[200:203],v[116:119],a[76:79],v[200:203]\n"
"ds_read_b64_tr_b16 a[26:27],v15 offset:2048\n"
"v_mfma_f32_16x16x32_bf16 v[208:211],v[112:115],a[80:83],v[208:211]\n"
"ds_read_b64_tr_b16 a[28:29],v15 offset:16384\n"
"v_mfma_f32_16x16x32_bf16 v[208:211],v[116:119],a[84:87],v[208:211]\n"
"v_mfma_f32_16x16x32_bf16 v[216:219],v[112:115],a[88:91],v[216:219]\n"
"v_mfma_f32_16x16x32_bf16 v[216:219],v[116:119],a[92:95],v[216:219]\n"
"ds_read_b64_tr_b16 a[30:31],v15 offset:18432\n"
"buffer_load_dwordx4 v[76:79],v51,s[60:63],0 offen offset:0 sc1\n"
"v_mfma_f32_16x16x32_bf16 v[224:227],v[112:115],a[96:99],v[224:227]\n"
"v_mfma_f32_16x16x32_bf16 v[224:227],v[116:119],a[100:103],v[224:227]\n"
"v_mfma_f32_16x16x32_bf16 v[232:235],v[112:115],a[104:107],v[232:235]\n"
"ds_read_b64_tr_b16 a[32:33],v16 offset:0\n"
"v_mfma_f32_16x16x32_bf16 v[232:235],v[116:119],a[108:111],v[232:235]\n"
"v_mfma_f32_16x16x32_bf16 v[240:243],v[112:115],a[112:115],v[240:243]\n"
"ds_read_b64_tr_b16 a[34:35],v16 offset:2048\n"
"buffer_load_dwordx4 v[80:83],v52,s[60:63],0 offen offset:1024 sc1\n"
"v_mfma_f32_16x16x32_bf16 v[240:243],v[116:119],a[116:119],v[240:243]\n"
"v_mfma_f32_16x16x32_bf16 v[248:251],v[112:115],a[120:123],v[248:251]\n"
"v_mfma_f32_16x16x32_bf16 v[248:251],v[116:119],a[124:127],v[248:251]\n"
"s_waitcnt lgkmcnt(13)\n"
"ds_read_b64_tr_b16 a[36:37],v16 offset:16384\n"
"v_mfma_f32_16x16x32_bf16 v[196:199],v[120:123],a[64:67],v[196:199]\n"
"ds_read_b128 v[104:107],v10 offset:10240\n"
"v_mfma_f32_16x16x32_bf16 v[196:199],v[124:127],a[68:71],v[196:199]\n"
"ds_read_b64_tr_b16 a[38:39],v16 offset:18432\n"
"buffer_load_dwordx4 v[84:87],v51,s[60:63],0 offen offset:1024 sc1\n"
"v_mfma_f32_16x16x32_bf16 v[204:207],v[120:123],a[72:75],v[204:207]\n"
"ds_read_b128 v[108:111],v11 offset:10240\n"
"v_mfma_f32_16x16x32_bf16 v[204:207],v[124:127],a[76:79],v[204:207]\n"
"v_mfma_f32_16x16x32_bf16 v[212:215],v[120:123],a[80:83],v[212:215]\n"
"ds_read_b64_tr_b16 a[40:41],v17 offset:0\n"
"v_mfma_f32_16x16x32_bf16 v[212:215],v[124:127],a[84:87],v[212:215]\n"
"v_mfma_f32_16x16x32_bf16 v[220:223],v[120:123],a[88:91],v[220:223]\n"
"buffer_load_dwordx4 v[88:91],v52,s[60:63],0 offen offset:2048 sc1\n"
"v_mfma_f32_16x16x32_bf16 v[220:223],v[124:127],a[92:95],v[220:223]\n"
"ds_read_b64_tr_b16 a[42:43],v17 offset:2048\n"
"v_mfma_f32_16x16x32_bf16 v[228:231],v[120:123],a[96:99],v[228:231]\n"
"v_mfma_f32_16x16x32_bf16 v[228:231],v[124:127],a[100:103],v[228:231]\n"
"ds_read_b64_tr_b16 a[44:45],v17 offset:16384\n"
"v_mfma_f32_16x16x32_bf16 v[236:239],v[120:123],a[104:107],v[236:239]\n"
"v_mfma_f32_16x16x32_bf16 v[236:239],v[124:127],a[108:111],v[236:239]\n"
"buffer_load_dwordx4 v[92:95],v51,s[60:63],0 offen offset:2048 sc1\n"
"v_mfma_f32_16x16x32_bf16 v[244:247],v[120:123],a[112:115],v[244:247]\n"
"ds_read_b64_tr_b16 a[46:47],v17 offset:18432\n"
"v_mfma_f32_16x16x32_bf16 v[244:247],v[124:127],a[116:119],v[244:247]\n"
"v_mfma_f32_16x16x32_bf16 v[252:255],v[120:123],a[120:123],v[252:255]\n"
"v_mfma_f32_16x16x32_bf16 v[252:255],v[124:127],a[124:127],v[252:255]\n"
"s_waitcnt lgkmcnt(13)\n"
"ds_read_b64_tr_b16 a[48:49],v18 offset:0\n"
"v_mfma_f32_16x16x32_bf16 a[128:131],v[96:99],a[64:67],a[128:131]\n"
"ds_read_b128 v[112:115],v10 offset:12288\n"
"buffer_load_dwordx4 v[24:27],v52,s[60:63],0 offen offset:3072 sc1\n"
"v_mfma_f32_16x16x32_bf16 a[128:131],v[100:103],a[68:71],a[128:131]\n"
"ds_read_b64_tr_b16 a[50:51],v18 offset:2048\n"
"v_mfma_f32_16x16x32_bf16 a[136:139],v[96:99],a[72:75],a[136:139]\n"
"ds_read_b128 v[116:119],v11 offset:12288\n"
"v_mfma_f32_16x16x32_bf16 a[136:139],v[100:103],a[76:79],a[136:139]\n"
"v_mfma_f32_16x16x32_bf16 a[144:147],v[96:99],a[80:83],a[144:147]\n"
"ds_read_b64_tr_b16 a[52:53],v18 offset:16384\n"
"v_mfma_f32_16x16x32_bf16 a[144:147],v[100:103],a[84:87],a[144:147]\n"
"buffer_load_dwordx4 v[28:31],v51,s[60:63],0 offen offset:3072 sc1\n"
"v_mfma_f32_16x16x32_bf16 a[152:155],v[96:99],a[88:91],a[152:155]\n"
"ds_read_b64_tr_b16 a[54:55],v18 offset:18432\n"
"v_mfma_f32_16x16x32_bf16 a[152:155],v[100:103],a[92:95],a[152:155]\n"
"v_mfma_f32_16x16x32_bf16 a[160:163],v[96:99],a[96:99],a[160:163]\n"
"v_mfma_f32_16x16x32_bf16 a[160:163],v[100:103],a[100:103],a[160:163]\n"
"ds_read_b64_tr_b16 a[56:57],v19 offset:0\n"
"v_mfma_f32_16x16x32_bf16 a[168:171],v[96:99],a[104:107],a[168:171]\n"
"buffer_load_dwordx4 v[40:43],v52,s[60:63],s86 offen offset:0 sc1\n"
"v_mfma_f32_16x16x32_bf16 a[168:171],v[100:103],a[108:111],a[168:171]\n"
"v_mfma_f32_16x16x32_bf16 a[176:179],v[96:99],a[112:115],a[176:179]\n"
"ds_read_b64_tr_b16 a[58:59],v19 offset:2048\n"
"v_mfma_f32_16x16x32_bf16 a[176:179],v[100:103],a[116:119],a[176:179]\n"
"v_mfma_f32_16x16x32_bf16 a[184:187],v[96:99],a[120:123],a[184:187]\n"
"ds_read_b64_tr_b16 a[60:61],v19 offset:16384\n"
"v_mfma_f32_16x16x32_bf16 a[184:187],v[100:103],a[124:127],a[184:187]\n"
"s_waitcnt lgkmcnt(13)\n"
"buffer_load_dwordx4 v[44:47],v51,s[60:63],s86 offen offset:0 sc1\n"
"v_mfma_f32_16x16x32_bf16 a[132:135],v[104:107],a[64:67],a[132:135]\n"
"ds_read_b128 v[120:123],v10 offset:14336\n"
"v_mfma_f32_16x16x32_bf16 a[132:135],v[108:111],a[68:71],a[132:135]\n"
"ds_read_b64_tr_b16 a[62:63],v19 offset:18432\n"
"v_mfma_f32_16x16x32_bf16 a[140:143],v[104:107],a[72:75],a[140:143]\n"
"ds_read_b128 v[124:127],v11 offset:14336\n"
"v_mfma_f32_16x16x32_bf16 a[140:143],v[108:111],a[76:79],a[140:143]\n"
"v_mfma_f32_16x16x32_bf16 a[148:151],v[104:107],a[80:83],a[148:151]\n"
"v_mfma_f32_16x16x32_bf16 a[148:151],v[108:111],a[84:87],a[148:151]\n"
"v_mfma_f32_16x16x32_bf16 a[156:159],v[104:107],a[88:91],a[156:159]\n"
"v_mfma_f32_16x16x32_bf16 a[156:159],v[108:111],a[92:95],a[156:159]\n"
"v_mfma_f32_16x16x32_bf16 a[164:167],v[104:107],a[96:99],a[164:167]\n"
"v_mfma_f32_16x16x32_bf16 a[164:167],v[108:111],a[100:103],a[164:167]\n"
"v_mfma_f32_16x16x32_bf16 a[172:175],v[104:107],a[104:107],a[172:175]\n"
"v_mfma_f32_16x16x32_bf16 a[172:175],v[108:111],a[108:111],a[172:175]\n"
"v_mfma_f32_16x16x32_bf16 a[180:183],v[104:107],a[112:115],a[180:183]\n"
"v_mfma_f32_16x16x32_bf16 a[180:183],v[108:111],a[116:119],a[180:183]\n"
"v_mfma_f32_16x16x32_bf16 a[188:191],v[104:107],a[120:123],a[188:191]\n"
"s_waitcnt lgkmcnt(0)\n"
"v_mfma_f32_16x16x32_bf16 a[188:191],v[108:111],a[124:127],a[188:191]\n"
"s_waitcnt vmcnt(26)\n"
"s_barrier\n"
"v_mfma_f32_16x16x32_bf16 a[192:195],v[112:115],a[64:67],a[192:195]\n"
"s_mov_b32 s53,0x0\n"
"v_add_u32 v10,s81,v8\n"
"v_add_u32 v11,s81,v9\n"
"v_mfma_f32_16x16x32_bf16 a[192:195],v[116:119],a[68:71],a[192:195]\n"
"s_add_u32 m0,s57,0x0\n"
"v_mfma_f32_16x16x32_bf16 a[200:203],v[112:115],a[72:75],a[200:203]\n"
"ds_read_b128 v[96:99],v10 offset:0\n"
"buffer_load_dwordx4 v32,s[64:67],s53 offen offset:0 lds\n"
"v_mfma_f32_16x16x32_bf16 a[200:203],v[116:119],a[76:79],a[200:203]\n"
"v_mfma_f32_16x16x32_bf16 a[208:211],v[112:115],a[80:83],a[208:211]\n"
"ds_read_b128 v[100:103],v11 offset:0\n"
"v_mfma_f32_16x16x32_bf16 a[208:211],v[116:119],a[84:87],a[208:211]\n"
"v_mfma_f32_16x16x32_bf16 a[216:219],v[112:115],a[88:91],a[216:219]\n"
"buffer_load_dwordx4 v33,s[64:67],s53 offen offset:1024 lds\n"
"v_mfma_f32_16x16x32_bf16 a[216:219],v[116:119],a[92:95],a[216:219]\n"
"v_mfma_f32_16x16x32_bf16 a[224:227],v[112:115],a[96:99],a[224:227]\n"
"v_mfma_f32_16x16x32_bf16 a[224:227],v[116:119],a[100:103],a[224:227]\n"
"v_mfma_f32_16x16x32_bf16 a[232:235],v[112:115],a[104:107],a[232:235]\n"
"buffer_load_dwordx4 v34,s[64:67],s53 offen offset:2048 lds\n"
"v_mfma_f32_16x16x32_bf16 a[232:235],v[116:119],a[108:111],a[232:235]\n"
"v_mfma_f32_16x16x32_bf16 a[240:243],v[112:115],a[112:115],a[240:243]\n"
"v_mfma_f32_16x16x32_bf16 a[240:243],v[116:119],a[116:119],a[240:243]\n"
"v_mfma_f32_16x16x32_bf16 a[248:251],v[112:115],a[120:123],a[248:251]\n"
"buffer_load_dwordx4 v35,s[64:67],s53 offen offset:3072 lds\n"
"v_mfma_f32_16x16x32_bf16 a[248:251],v[116:119],a[124:127],a[248:251]\n"
"s_waitcnt vmcnt(14)\n"
"s_barrier\n"
"v_mfma_f32_16x16x32_bf16 a[196:199],v[120:123],a[64:67],a[196:199]\n"
"ds_read_b128 v[104:107],v8 offset:0\n"
"v_mfma_f32_16x16x32_bf16 a[196:199],v[124:127],a[68:71],a[196:199]\n"
"s_add_u32 m0,s57,0x1000\n"
"ds_read_b128 v[108:111],v9 offset:0\n"
"v_mfma_f32_16x16x32_bf16 a[204:207],v[120:123],a[72:75],a[204:207]\n"
"ds_read_b64_tr_b16 a[64:65],v12 offset:32768\n"
"buffer_load_dwordx4 v36,s[64:67],s53 offen offset:0 lds\n"
"v_mfma_f32_16x16x32_bf16 a[204:207],v[124:127],a[76:79],a[204:207]\n"
"ds_read_b64_tr_b16 a[66:67],v12 offset:34816\n"
"v_mfma_f32_16x16x32_bf16 a[212:215],v[120:123],a[80:83],a[212:215]\n"
"ds_read_b64_tr_b16 a[68:69],v12 offset:49152\n"
"v_mfma_f32_16x16x32_bf16 a[212:215],v[124:127],a[84:87],a[212:215]\n"
"ds_read_b64_tr_b16 a[70:71],v12 offset:51200\n"
"v_mfma_f32_16x16x32_bf16 a[220:223],v[120:123],a[88:91],a[220:223]\n"
"ds_read_b64_tr_b16 a[72:73],v13 offset:32768\n"
"buffer_load_dwordx4 v37,s[64:67],s53 offen offset:1024 lds\n"
"v_mfma_f32_16x16x32_bf16 a[220:223],v[124:127],a[92:95],a[220:223]\n"
"ds_read_b64_tr_b16 a[74:75],v13 offset:34816\n"
"v_mfma_f32_16x16x32_bf16 a[228:231],v[120:123],a[96:99],a[228:231]\n"
"ds_read_b64_tr_b16 a[76:77],v13 offset:49152\n"
"v_mfma_f32_16x16x32_bf16 a[228:231],v[124:127],a[100:103],a[228:231]\n"
"ds_read_b64_tr_b16 a[78:79],v13 offset:51200\n"
"v_mfma_f32_16x16x32_bf16 a[236:239],v[120:123],a[104:107],a[236:239]\n"
"ds_read_b64_tr_b16 a[80:81],v14 offset:32768\n"
"buffer_load_dwordx4 v38,s[64:67],s53 offen offset:2048 lds\n"
"v_mfma_f32_16x16x32_bf16 a[236:239],v[124:127],a[108:111],a[236:239]\n"
"ds_read_b64_tr_b16 a[82:83],v14 offset:34816\n"
"v_mfma_f32_16x16x32_bf16 a[244:247],v[120:123],a[112:115],a[244:247]\n"
"ds_read_b64_tr_b16 a[84:85],v14 offset:49152\n"
"v_mfma_f32_16x16x32_bf16 a[244:247],v[124:127],a[116:119],a[244:247]\n"
"ds_read_b64_tr_b16 a[86:87],v14 offset:51200\n"
"v_mfma_f32_16x16x32_bf16 a[252:255],v[120:123],a[120:123],a[252:255]\n"
"ds_read_b64_tr_b16 a[88:89],v15 offset:32768\n"
"buffer_load_dwordx4 v39,s[64:67],s53 offen offset:3072 lds\n"
"v_mfma_f32_16x16x32_bf16 a[252:255],v[124:127],a[124:127],a[252:255]\n"
"s_waitcnt lgkmcnt(15)\n"
"ds_read_b64_tr_b16 a[90:91],v15 offset:34816\n"
"v_mfma_f32_16x16x32_bf16 v[128:131],v[96:99],a[0:3],v[128:131]\n"
"ds_read_b64_tr_b16 a[92:93],v15 offset:49152\n"
"v_mfma_f32_16x16x32_bf16 v[128:131],v[100:103],a[4:7],v[128:131]\n"
"s_mov_b32 s53,0x100000\n"
"ds_read_b64_tr_b16 a[94:95],v15 offset:51200\n"
"v_mfma_f32_16x16x32_bf16 v[136:139],v[96:99],a[8:11],v[136:139]\n"
"s_add_u32 m0,s57,0x18000\n"
"ds_read_b64_tr_b16 a[96:97],v16 offset:32768\n"
"v_mfma_f32_16x16x32_bf16 v[136:139],v[100:103],a[12:15],v[136:139]\n"
"ds_read_b64_tr_b16 a[98:99],v16 offset:34816\n"
"buffer_load_dwordx4 v32,s[64:67],s53 offen offset:0 lds\n"
"v_mfma_f32_16x16x32_bf16 v[144:147],v[96:99],a[16:19],v[144:147]\n"
"ds_read_b64_tr_b16 a[100:101],v16 offset:49152\n"
"v_mfma_f32_16x16x32_bf16 v[144:147],v[100:103],a[20:23],v[144:147]\n"
"ds_read_b64_tr_b16 a[102:103],v16 offset:51200\n"
"v_mfma_f32_16x16x32_bf16 v[152:155],v[96:99],a[24:27],v[152:155]\n"
"ds_read_b64_tr_b16 a[104:105],v17 offset:32768\n"
"v_mfma_f32_16x16x32_bf16 v[152:155],v[100:103],a[28:31],v[152:155]\n"
"ds_read_b64_tr_b16 a[106:107],v17 offset:34816\n"
"buffer_load_dwordx4 v33,s[64:67],s53 offen offset:1024 lds\n"
"v_mfma_f32_16x16x32_bf16 v[160:163],v[96:99],a[32:35],v[160:163]\n"
"ds_read_b64_tr_b16 a[108:109],v17 offset:49152\n"
"v_mfma_f32_16x16x32_bf16 v[160:163],v[100:103],a[36:39],v[160:163]\n"
"ds_read_b64_tr_b16 a[110:111],v17 offset:51200\n"
"v_mfma_f32_16x16x32_bf16 v[168:171],v[96:99],a[40:43],v[168:171]\n"
"ds_read_b64_tr_b16 a[112:113],v18 offset:32768\n"
"v_mfma_f32_16x16x32_bf16 v[168:171],v[100:103],a[44:47],v[168:171]\n"
"ds_read_b64_tr_b16 a[114:115],v18 offset:34816\n"
"buffer_load_dwordx4 v34,s[64:67],s53 offen offset:2048 lds\n"
"v_mfma_f32_16x16x32_bf16 v[176:179],v[96:99],a[48:51],v[176:179]\n"
"ds_read_b64_tr_b16 a[116:117],v18 offset:49152\n"
"v_mfma_f32_16x16x32_bf16 v[176:179],v[100:103],a[52:55],v[176:179]\n"
"ds_read_b64_tr_b16 a[118:119],v18 offset:51200\n"
"v_mfma_f32_16x16x32_bf16 v[184:187],v[96:99],a[56:59],v[184:187]\n"
"ds_read_b64_tr_b16 a[120:121],v19 offset:32768\n"
"v_mfma_f32_16x16x32_bf16 v[184:187],v[100:103],a[60:63],v[184:187]\n"
"s_waitcnt lgkmcnt(13)\n"
"ds_read_b64_tr_b16 a[122:123],v19 offset:34816\n"
"buffer_load_dwordx4 v35,s[64:67],s53 offen offset:3072 lds\n"
"v_mfma_f32_16x16x32_bf16 v[128:131],v[104:107],a[64:67],v[128:131]\n"
"ds_read_b64_tr_b16 a[124:125],v19 offset:49152\n"
"v_mfma_f32_16x16x32_bf16 v[128:131],v[108:111],a[68:71],v[128:131]\n"
"ds_read_b64_tr_b16 a[126:127],v19 offset:51200\n"
"v_mfma_f32_16x16x32_bf16 v[136:139],v[104:107],a[72:75],v[136:139]\n"
"s_add_u32 m0,s57,0x19000\n"
"ds_read_b128 v[112:115],v10 offset:2048\n"
"v_mfma_f32_16x16x32_bf16 v[136:139],v[108:111],a[76:79],v[136:139]\n"
"ds_read_b128 v[116:119],v11 offset:2048\n"
"buffer_load_dwordx4 v36,s[64:67],s53 offen offset:0 lds\n"
"v_mfma_f32_16x16x32_bf16 v[144:147],v[104:107],a[80:83],v[144:147]\n"
"ds_read_b128 v[120:123],v8 offset:2048\n"
"v_mfma_f32_16x16x32_bf16 v[144:147],v[108:111],a[84:87],v[144:147]\n"
"ds_read_b128 v[124:127],v9 offset:2048\n"
"v_mfma_f32_16x16x32_bf16 v[152:155],v[104:107],a[88:91],v[152:155]\n"
"v_subrev_u32 v12,s80,v12\n"
"v_mfma_f32_16x16x32_bf16 v[152:155],v[108:111],a[92:95],v[152:155]\n"
"s_waitcnt lgkmcnt(8)\n"
"buffer_load_dwordx4 v37,s[64:67],s53 offen offset:1024 lds\n"
"v_mfma_f32_16x16x32_bf16 v[160:163],v[104:107],a[96:99],v[160:163]\n"
"v_subrev_u32 v13,s80,v13\n"
"v_mfma_f32_16x16x32_bf16 v[160:163],v[108:111],a[100:103],v[160:163]\n"
"v_mfma_f32_16x16x32_bf16 v[168:171],v[104:107],a[104:107],v[168:171]\n"
"v_subrev_u32 v14,s80,v14\n"
"v_mfma_f32_16x16x32_bf16 v[168:171],v[108:111],a[108:111],v[168:171]\n"
"buffer_load_dwordx4 v38,s[64:67],s53 offen offset:2048 lds\n"
"v_mfma_f32_16x16x32_bf16 v[176:179],v[104:107],a[112:115],v[176:179]\n"
"v_subrev_u32 v15,s80,v15\n"
"v_mfma_f32_16x16x32_bf16 v[176:179],v[108:111],a[116:119],v[176:179]\n"
"s_waitcnt lgkmcnt(4)\n"
"v_mfma_f32_16x16x32_bf16 v[184:187],v[104:107],a[120:123],v[184:187]\n"
"v_subrev_u32 v16,s80,v16\n"
"v_mfma_f32_16x16x32_bf16 v[184:187],v[108:111],a[124:127],v[184:187]\n"
"s_waitcnt lgkmcnt(0)\n"
"buffer_load_dwordx4 v39,s[64:67],s53 offen offset:3072 lds\n"
"v_mfma_f32_16x16x32_bf16 v[132:135],v[112:115],a[0:3],v[132:135]\n"
"ds_read_b128 v[96:99],v10 offset:4096\n"
"v_subrev_u32 v17,s80,v17\n"
"v_mfma_f32_16x16x32_bf16 v[132:135],v[116:119],a[4:7],v[132:135]\n"
"v_mfma_f32_16x16x32_bf16 v[132:135],v[120:123],a[64:67],v[132:135]\n"
"ds_read_b128 v[100:103],v11 offset:4096\n"
"v_subrev_u32 v18,s80,v18\n"
"v_mfma_f32_16x16x32_bf16 v[132:135],v[124:127],a[68:71],v[132:135]\n"
"v_mfma_f32_16x16x32_bf16 v[140:143],v[112:115],a[8:11],v[140:143]\n"
"ds_read_b128 v[104:107],v8 offset:4096\n"
"v_subrev_u32 v19,s80,v19\n"
"v_mfma_f32_16x16x32_bf16 v[140:143],v[116:119],a[12:15],v[140:143]\n"
"v_mfma_f32_16x16x32_bf16 v[140:143],v[120:123],a[72:75],v[140:143]\n"
"ds_read_b128 v[108:111],v9 offset:4096\n"
"v_mfma_f32_16x16x32_bf16 v[140:143],v[124:127],a[76:79],v[140:143]\n"
"v_mfma_f32_16x16x32_bf16 v[148:151],v[112:115],a[16:19],v[148:151]\n"
"s_barrier\n"
"v_mfma_f32_16x16x32_bf16 v[148:151],v[116:119],a[20:23],v[148:151]\n"
"v_mfma_f32_16x16x32_bf16 v[148:151],v[120:123],a[80:83],v[148:151]\n"
"s_mov_b32 s53,0x80000\n"
"v_mfma_f32_16x16x32_bf16 v[148:151],v[124:127],a[84:87],v[148:151]\n"
"s_add_u32 m0,s57,0x8000\n"
"v_mfma_f32_16x16x32_bf16 v[156:159],v[112:115],a[24:27],v[156:159]\n"
"buffer_load_dwordx4 v32,s[64:67],s53 offen offset:0 lds\n"
"v_mfma_f32_16x16x32_bf16 v[156:159],v[116:119],a[28:31],v[156:159]\n"
"v_mfma_f32_16x16x32_bf16 v[156:159],v[120:123],a[88:91],v[156:159]\n"
"v_mfma_f32_16x16x32_bf16 v[156:159],v[124:127],a[92:95],v[156:159]\n"
"v_mfma_f32_16x16x32_bf16 v[164:167],v[112:115],a[32:35],v[164:167]\n"
"buffer_load_dwordx4 v33,s[64:67],s53 offen offset:1024 lds\n"
"v_mfma_f32_16x16x32_bf16 v[164:167],v[116:119],a[36:39],v[164:167]\n"
"v_mfma_f32_16x16x32_bf16 v[164:167],v[120:123],a[96:99],v[164:167]\n"
"v_mfma_f32_16x16x32_bf16 v[164:167],v[124:127],a[100:103],v[164:167]\n"
"v_mfma_f32_16x16x32_bf16 v[172:175],v[112:115],a[40:43],v[172:175]\n"
"buffer_load_dwordx4 v34,s[64:67],s53 offen offset:2048 lds\n"
"v_mfma_f32_16x16x32_bf16 v[172:175],v[116:119],a[44:47],v[172:175]\n"
"v_mfma_f32_16x16x32_bf16 v[172:175],v[120:123],a[104:107],v[172:175]\n"
"v_mfma_f32_16x16x32_bf16 v[172:175],v[124:127],a[108:111],v[172:175]\n"
"v_mfma_f32_16x16x32_bf16 v[180:183],v[112:115],a[48:51],v[180:183]\n"
"buffer_load_dwordx4 v35,s[64:67],s53 offen offset:3072 lds\n"
"v_mfma_f32_16x16x32_bf16 v[180:183],v[116:119],a[52:55],v[180:183]\n"
"v_mfma_f32_16x16x32_bf16 v[180:183],v[120:123],a[112:115],v[180:183]\n"
"v_mfma_f32_16x16x32_bf16 v[180:183],v[124:127],a[116:119],v[180:183]\n"
"s_add_u32 m0,s57,0x9000\n"
"v_mfma_f32_16x16x32_bf16 v[188:191],v[112:115],a[56:59],v[188:191]\n"
"buffer_load_dwordx4 v36,s[64:67],s53 offen offset:0 lds\n"
"v_mfma_f32_16x16x32_bf16 v[188:191],v[116:119],a[60:63],v[188:191]\n"
"v_mfma_f32_16x16x32_bf16 v[188:191],v[120:123],a[120:123],v[188:191]\n"
"v_mfma_f32_16x16x32_bf16 v[188:191],v[124:127],a[124:127],v[188:191]\n"
"s_waitcnt lgkmcnt(0)\n"
"v_mfma_f32_16x16x32_bf16 v[192:195],v[96:99],a[0:3],v[192:195]\n"
"ds_read_b128 v[112:115],v10 offset:6144\n"
"buffer_load_dwordx4 v37,s[64:67],s53 offen offset:1024 lds\n"
"v_mfma_f32_16x16x32_bf16 v[192:195],v[100:103],a[4:7],v[192:195]\n"
"v_mfma_f32_16x16x32_bf16 v[192:195],v[104:107],a[64:67],v[192:195]\n"
"ds_read_b128 v[116:119],v11 offset:6144\n"
"v_mfma_f32_16x16x32_bf16 v[192:195],v[108:111],a[68:71],v[192:195]\n"
"v_cvt_pk_bf16_f32 v128,v128,v129\n"
"v_cvt_pk_bf16_f32 v129,v130,v131\n"
"v_mfma_f32_16x16x32_bf16 v[200:203],v[96:99],a[8:11],v[200:203]\n"
"ds_read_b128 v[120:123],v8 offset:6144\n"
"buffer_load_dwordx4 v38,s[64:67],s53 offen offset:2048 lds\n"
"v_mfma_f32_16x16x32_bf16 v[200:203],v[100:103],a[12:15],v[200:203]\n"
"v_cvt_pk_bf16_f32 v130,v132,v133\n"
"v_cvt_pk_bf16_f32 v131,v134,v135\n"
"v_mfma_f32_16x16x32_bf16 v[200:203],v[104:107],a[72:75],v[200:203]\n"
"ds_read_b128 v[124:127],v9 offset:6144\n"
"v_cvt_pk_bf16_f32 v136,v136,v137\n"
"v_mfma_f32_16x16x32_bf16 v[200:203],v[108:111],a[76:79],v[200:203]\n"
"s_mov_b32 s59,0x0\n"
"v_cvt_pk_bf16_f32 v137,v138,v139\n"
"v_cvt_pk_bf16_f32 v138,v140,v141\n"
"v_mfma_f32_16x16x32_bf16 v[208:211],v[96:99],a[16:19],v[208:211]\n"
"buffer_store_dwordx4 v[128:131],v52,s[44:47],s59 offen offset:0 sc1\n"
"v_mfma_f32_16x16x32_bf16 v[208:211],v[100:103],a[20:23],v[208:211]\n"
"v_cvt_pk_bf16_f32 v139,v142,v143\n"
"v_mfma_f32_16x16x32_bf16 v[208:211],v[104:107],a[80:83],v[208:211]\n"
"v_mfma_f32_16x16x32_bf16 v[208:211],v[108:111],a[84:87],v[208:211]\n"
"v_mfma_f32_16x16x32_bf16 v[216:219],v[96:99],a[24:27],v[216:219]\n"
"buffer_load_dwordx4 v39,s[64:67],s53 offen offset:3072 lds\n"
"v_mfma_f32_16x16x32_bf16 v[216:219],v[100:103],a[28:31],v[216:219]\n"
"v_cvt_pk_bf16_f32 v144,v144,v145\n"
"v_cvt_pk_bf16_f32 v145,v146,v147\n"
"v_mfma_f32_16x16x32_bf16 v[216:219],v[104:107],a[88:91],v[216:219]\n"
"v_cvt_pk_bf16_f32 v146,v148,v149\n"
"v_cvt_pk_bf16_f32 v147,v150,v151\n"
"v_mfma_f32_16x16x32_bf16 v[216:219],v[108:111],a[92:95],v[216:219]\n"
"v_mfma_f32_16x16x32_bf16 v[224:227],v[96:99],a[32:35],v[224:227]\n"
"v_mfma_f32_16x16x32_bf16 v[224:227],v[100:103],a[36:39],v[224:227]\n"
"v_mfma_f32_16x16x32_bf16 v[224:227],v[104:107],a[96:99],v[224:227]\n"
"v_mfma_f32_16x16x32_bf16 v[224:227],v[108:111],a[100:103],v[224:227]\n"
"v_mfma_f32_16x16x32_bf16 v[232:235],v[96:99],a[40:43],v[232:235]\n"
"v_mfma_f32_16x16x32_bf16 v[232:235],v[100:103],a[44:47],v[232:235]\n"
"s_mov_b32 s59,0x0\n"
"v_mfma_f32_16x16x32_bf16 v[232:235],v[104:107],a[104:107],v[232:235]\n"
"buffer_store_dwordx4 v[136:139],v52,s[44:47],s59 offen offset:1024 sc1\n"
"v_mfma_f32_16x16x32_bf16 v[232:235],v[108:111],a[108:111],v[232:235]\n"
"v_mfma_f32_16x16x32_bf16 v[240:243],v[96:99],a[48:51],v[240:243]\n"
"v_mfma_f32_16x16x32_bf16 v[240:243],v[100:103],a[52:55],v[240:243]\n"
"v_mfma_f32_16x16x32_bf16 v[240:243],v[104:107],a[112:115],v[240:243]\n"
"v_mfma_f32_16x16x32_bf16 v[240:243],v[108:111],a[116:119],v[240:243]\n"
"v_mfma_f32_16x16x32_bf16 v[248:251],v[96:99],a[56:59],v[248:251]\n"
"v_cvt_pk_bf16_f32 v152,v152,v153\n"
"v_cvt_pk_bf16_f32 v153,v154,v155\n"
"v_mfma_f32_16x16x32_bf16 v[248:251],v[100:103],a[60:63],v[248:251]\n"
"v_cvt_pk_bf16_f32 v154,v156,v157\n"
"v_cvt_pk_bf16_f32 v155,v158,v159\n"
"v_mfma_f32_16x16x32_bf16 v[248:251],v[104:107],a[120:123],v[248:251]\n"
"v_mfma_f32_16x16x32_bf16 v[248:251],v[108:111],a[124:127],v[248:251]\n"
"s_waitcnt lgkmcnt(0)\n"
"v_mfma_f32_16x16x32_bf16 v[196:199],v[112:115],a[0:3],v[196:199]\n"
"ds_read_b128 v[96:99],v10 offset:8192\n"
"v_mfma_f32_16x16x32_bf16 v[196:199],v[116:119],a[4:7],v[196:199]\n"
"v_mfma_f32_16x16x32_bf16 v[196:199],v[120:123],a[64:67],v[196:199]\n"
"ds_read_b128 v[100:103],v11 offset:8192\n"
"v_mfma_f32_16x16x32_bf16 v[196:199],v[124:127],a[68:71],v[196:199]\n"
"v_mfma_f32_16x16x32_bf16 v[204:207],v[112:115],a[8:11],v[204:207]\n"
"s_mov_b32 s59,0x0\n"
"ds_read_b128 v[104:107],v8 offset:8192\n"
"v_mfma_f32_16x16x32_bf16 v[204:207],v[116:119],a[12:15],v[204:207]\n"
"buffer_store_dwordx4 v[144:147],v52,s[44:47],s59 offen offset:2048 sc1\n"
"v_mfma_f32_16x16x32_bf16 v[204:207],v[120:123],a[72:75],v[204:207]\n"
"ds_read_b128 v[108:111],v9 offset:8192\n"
"v_mfma_f32_16x16x32_bf16 v[204:207],v[124:127],a[76:79],v[204:207]\n"
"v_mfma_f32_16x16x32_bf16 v[212:215],v[112:115],a[16:19],v[212:215]\n"
"v_mfma_f32_16x16x32_bf16 v[212:215],v[116:119],a[20:23],v[212:215]\n"
"v_mfma_f32_16x16x32_bf16 v[212:215],v[120:123],a[80:83],v[212:215]\n"
"v_mfma_f32_16x16x32_bf16 v[212:215],v[124:127],a[84:87],v[212:215]\n"
"v_cvt_pk_bf16_f32 v160,v160,v161\n"
"v_cvt_pk_bf16_f32 v161,v162,v163\n"
"v_mfma_f32_16x16x32_bf16 v[220:223],v[112:115],a[24:27],v[220:223]\n"
"v_cvt_pk_bf16_f32 v162,v164,v165\n"
"v_cvt_pk_bf16_f32 v163,v166,v167\n"
"v_mfma_f32_16x16x32_bf16 v[220:223],v[116:119],a[28:31],v[220:223]\n"
"v_mfma_f32_16x16x32_bf16 v[220:223],v[120:123],a[88:91],v[220:223]\n"
"v_mfma_f32_16x16x32_bf16 v[220:223],v[124:127],a[92:95],v[220:223]\n"
"v_mfma_f32_16x16x32_bf16 v[228:231],v[112:115],a[32:35],v[228:231]\n"
"v_mfma_f32_16x16x32_bf16 v[228:231],v[116:119],a[36:39],v[228:231]\n"
"v_mfma_f32_16x16x32_bf16 v[228:231],v[120:123],a[96:99],v[228:231]\n"
"v_mfma_f32_16x16x32_bf16 v[228:231],v[124:127],a[100:103],v[228:231]\n"
"s_mov_b32 s59,0x0\n"
"v_mfma_f32_16x16x32_bf16 v[236:239],v[112:115],a[40:43],v[236:239]\n"
"buffer_store_dwordx4 v[152:155],v52,s[44:47],s59 offen offset:3072 sc1\n"
"v_mfma_f32_16x16x32_bf16 v[236:239],v[116:119],a[44:47],v[236:239]\n"
"v_mfma_f32_16x16x32_bf16 v[236:239],v[120:123],a[104:107],v[236:239]\n"
"v_mfma_f32_16x16x32_bf16 v[236:239],v[124:127],a[108:111],v[236:239]\n"
"v_mfma_f32_16x16x32_bf16 v[244:247],v[112:115],a[48:51],v[244:247]\n"
"v_mfma_f32_16x16x32_bf16 v[244:247],v[116:119],a[52:55],v[244:247]\n"
"v_mfma_f32_16x16x32_bf16 v[244:247],v[120:123],a[112:115],v[244:247]\n"
"v_cvt_pk_bf16_f32 v168,v168,v169\n"
"v_cvt_pk_bf16_f32 v169,v170,v171\n"
"v_mfma_f32_16x16x32_bf16 v[244:247],v[124:127],a[116:119],v[244:247]\n"
"v_cvt_pk_bf16_f32 v170,v172,v173\n"
"v_cvt_pk_bf16_f32 v171,v174,v175\n"
"v_mfma_f32_16x16x32_bf16 v[252:255],v[112:115],a[56:59],v[252:255]\n"
"v_mfma_f32_16x16x32_bf16 v[252:255],v[116:119],a[60:63],v[252:255]\n"
"v_mfma_f32_16x16x32_bf16 v[252:255],v[120:123],a[120:123],v[252:255]\n"
"v_mfma_f32_16x16x32_bf16 v[252:255],v[124:127],a[124:127],v[252:255]\n"
"s_waitcnt lgkmcnt(0)\n"
"v_mfma_f32_16x16x32_bf16 a[128:131],v[96:99],a[0:3],a[128:131]\n"
"ds_read_b128 v[112:115],v10 offset:10240\n"
"v_mfma_f32_16x16x32_bf16 a[128:131],v[100:103],a[4:7],a[128:131]\n"
"v_mfma_f32_16x16x32_bf16 a[128:131],v[104:107],a[64:67],a[128:131]\n"
"s_mov_b32 s59,0x1000\n"
"ds_read_b128 v[116:119],v11 offset:10240\n"
"v_mfma_f32_16x16x32_bf16 a[128:131],v[108:111],a[68:71],a[128:131]\n"
"buffer_store_dwordx4 v[160:163],v52,s[44:47],s59 offen offset:0 sc1\n"
"v_mfma_f32_16x16x32_bf16 a[136:139],v[96:99],a[8:11],a[136:139]\n"
"ds_read_b128 v[120:123],v8 offset:10240\n"
"v_mfma_f32_16x16x32_bf16 a[136:139],v[100:103],a[12:15],a[136:139]\n"
"v_mfma_f32_16x16x32_bf16 a[136:139],v[104:107],a[72:75],a[136:139]\n"
"ds_read_b128 v[124:127],v9 offset:10240\n"
"v_mfma_f32_16x16x32_bf16 a[136:139],v[108:111],a[76:79],a[136:139]\n"
"v_mfma_f32_16x16x32_bf16 a[144:147],v[96:99],a[16:19],a[144:147]\n"
"v_mfma_f32_16x16x32_bf16 a[144:147],v[100:103],a[20:23],a[144:147]\n"
"v_cvt_pk_bf16_f32 v176,v176,v177\n"
"v_cvt_pk_bf16_f32 v177,v178,v179\n"
"v_mfma_f32_16x16x32_bf16 a[144:147],v[104:107],a[80:83],a[144:147]\n"
"v_cvt_pk_bf16_f32 v178,v180,v181\n"
"v_cvt_pk_bf16_f32 v179,v182,v183\n"
"v_mfma_f32_16x16x32_bf16 a[144:147],v[108:111],a[84:87],a[144:147]\n"
"v_mfma_f32_16x16x32_bf16 a[152:155],v[96:99],a[24:27],a[152:155]\n"
"v_mfma_f32_16x16x32_bf16 a[152:155],v[100:103],a[28:31],a[152:155]\n"
"v_mfma_f32_16x16x32_bf16 a[152:155],v[104:107],a[88:91],a[152:155]\n"
"v_mfma_f32_16x16x32_bf16 a[152:155],v[108:111],a[92:95],a[152:155]\n"
"v_mfma_f32_16x16x32_bf16 a[160:163],v[96:99],a[32:35],a[160:163]\n"
"v_mfma_f32_16x16x32_bf16 a[160:163],v[100:103],a[36:39],a[160:163]\n"
"s_mov_b32 s59,0x1000\n"
"v_mfma_f32_16x16x32_bf16 a[160:163],v[104:107],a[96:99],a[160:163]\n"
"buffer_store_dwordx4 v[168:171],v52,s[44:47],s59 offen offset:1024 sc1\n"
"v_mfma_f32_16x16x32_bf16 a[160:163],v[108:111],a[100:103],a[160:163]\n"
"v_mfma_f32_16x16x32_bf16 a[168:171],v[96:99],a[40:43],a[168:171]\n"
"v_mfma_f32_16x16x32_bf16 a[168:171],v[100:103],a[44:47],a[168:171]\n"
"v_mfma_f32_16x16x32_bf16 a[168:171],v[104:107],a[104:107],a[168:171]\n"
"v_mfma_f32_16x16x32_bf16 a[168:171],v[108:111],a[108:111],a[168:171]\n"
"v_cvt_pk_bf16_f32 v184,v184,v185\n"
"v_cvt_pk_bf16_f32 v185,v186,v187\n"
"v_mfma_f32_16x16x32_bf16 a[176:179],v[96:99],a[48:51],a[176:179]\n"
"v_cvt_pk_bf16_f32 v186,v188,v189\n"
"v_cvt_pk_bf16_f32 v187,v190,v191\n"
"v_mfma_f32_16x16x32_bf16 a[176:179],v[100:103],a[52:55],a[176:179]\n"
"v_mfma_f32_16x16x32_bf16 a[176:179],v[104:107],a[112:115],a[176:179]\n"
"v_mfma_f32_16x16x32_bf16 a[176:179],v[108:111],a[116:119],a[176:179]\n"
"v_mfma_f32_16x16x32_bf16 a[184:187],v[96:99],a[56:59],a[184:187]\n"
"v_mfma_f32_16x16x32_bf16 a[184:187],v[100:103],a[60:63],a[184:187]\n"
"v_mfma_f32_16x16x32_bf16 a[184:187],v[104:107],a[120:123],a[184:187]\n"
"v_mfma_f32_16x16x32_bf16 a[184:187],v[108:111],a[124:127],a[184:187]\n"
"s_waitcnt lgkmcnt(0)\n"
"v_mfma_f32_16x16x32_bf16 a[132:135],v[112:115],a[0:3],a[132:135]\n"
"s_mov_b32 s59,0x1000\n"
"ds_read_b128 v[96:99],v10 offset:12288\n"
"v_mfma_f32_16x16x32_bf16 a[132:135],v[116:119],a[4:7],a[132:135]\n"
"buffer_store_dwordx4 v[176:179],v52,s[44:47],s59 offen offset:2048 sc1\n"
"v_mfma_f32_16x16x32_bf16 a[132:135],v[120:123],a[64:67],a[132:135]\n"
"ds_read_b128 v[100:103],v11 offset:12288\n"
"v_mfma_f32_16x16x32_bf16 a[132:135],v[124:127],a[68:71],a[132:135]\n"
"v_mfma_f32_16x16x32_bf16 a[140:143],v[112:115],a[8:11],a[140:143]\n"
"ds_read_b128 v[104:107],v8 offset:12288\n"
"v_mfma_f32_16x16x32_bf16 a[140:143],v[116:119],a[12:15],a[140:143]\n"
"v_mfma_f32_16x16x32_bf16 a[140:143],v[120:123],a[72:75],a[140:143]\n"
"ds_read_b128 v[108:111],v9 offset:12288\n"
"v_cvt_pk_bf16_f32 v192,v192,v193\n"
"v_mfma_f32_16x16x32_bf16 a[140:143],v[124:127],a[76:79],a[140:143]\n"
"v_cvt_pk_bf16_f32 v193,v194,v195\n"
"v_cvt_pk_bf16_f32 v194,v196,v197\n"
"v_mfma_f32_16x16x32_bf16 a[148:151],v[112:115],a[16:19],a[148:151]\n"
"v_cvt_pk_bf16_f32 v195,v198,v199\n"
"v_mfma_f32_16x16x32_bf16 a[148:151],v[116:119],a[20:23],a[148:151]\n"
"v_mfma_f32_16x16x32_bf16 a[148:151],v[120:123],a[80:83],a[148:151]\n"
"v_mfma_f32_16x16x32_bf16 a[148:151],v[124:127],a[84:87],a[148:151]\n"
"v_mfma_f32_16x16x32_bf16 a[156:159],v[112:115],a[24:27],a[156:159]\n"
"v_mfma_f32_16x16x32_bf16 a[156:159],v[116:119],a[28:31],a[156:159]\n"
"v_mfma_f32_16x16x32_bf16 a[156:159],v[120:123],a[88:91],a[156:159]\n"
"s_mov_b32 s59,0x1000\n"
"v_mfma_f32_16x16x32_bf16 a[156:159],v[124:127],a[92:95],a[156:159]\n"
"buffer_store_dwordx4 v[184:187],v52,s[44:47],s59 offen offset:3072 sc1\n"
"v_mfma_f32_16x16x32_bf16 a[164:167],v[112:115],a[32:35],a[164:167]\n"
"v_mfma_f32_16x16x32_bf16 a[164:167],v[116:119],a[36:39],a[164:167]\n"
"v_mfma_f32_16x16x32_bf16 a[164:167],v[120:123],a[96:99],a[164:167]\n"
"v_mfma_f32_16x16x32_bf16 a[164:167],v[124:127],a[100:103],a[164:167]\n"
"v_mfma_f32_16x16x32_bf16 a[172:175],v[112:115],a[40:43],a[172:175]\n"
"v_mfma_f32_16x16x32_bf16 a[172:175],v[116:119],a[44:47],a[172:175]\n"
"v_cvt_pk_bf16_f32 v200,v200,v201\n"
"v_cvt_pk_bf16_f32 v201,v202,v203\n"
"v_mfma_f32_16x16x32_bf16 a[172:175],v[120:123],a[104:107],a[172:175]\n"
"v_cvt_pk_bf16_f32 v202,v204,v205\n"
"v_cvt_pk_bf16_f32 v203,v206,v207\n"
"v_mfma_f32_16x16x32_bf16 a[172:175],v[124:127],a[108:111],a[172:175]\n"
"v_mfma_f32_16x16x32_bf16 a[180:183],v[112:115],a[48:51],a[180:183]\n"
"v_mfma_f32_16x16x32_bf16 a[180:183],v[116:119],a[52:55],a[180:183]\n"
"v_mfma_f32_16x16x32_bf16 a[180:183],v[120:123],a[112:115],a[180:183]\n"
"v_mfma_f32_16x16x32_bf16 a[180:183],v[124:127],a[116:119],a[180:183]\n"
"v_mfma_f32_16x16x32_bf16 a[188:191],v[112:115],a[56:59],a[188:191]\n"
"v_mfma_f32_16x16x32_bf16 a[188:191],v[116:119],a[60:63],a[188:191]\n"
"s_mov_b32 s59,0x2000\n"
"v_mfma_f32_16x16x32_bf16 a[188:191],v[120:123],a[120:123],a[188:191]\n"
"buffer_store_dwordx4 v[192:195],v52,s[44:47],s59 offen offset:0 sc1\n"
"v_mfma_f32_16x16x32_bf16 a[188:191],v[124:127],a[124:127],a[188:191]\n"
"s_waitcnt lgkmcnt(0)\n"
"v_mfma_f32_16x16x32_bf16 a[192:195],v[96:99],a[0:3],a[192:195]\n"
"ds_read_b128 v[112:115],v10 offset:14336\n"
"v_mfma_f32_16x16x32_bf16 a[192:195],v[100:103],a[4:7],a[192:195]\n"
"v_mfma_f32_16x16x32_bf16 a[192:195],v[104:107],a[64:67],a[192:195]\n"
"ds_read_b128 v[116:119],v11 offset:14336\n"
"v_mfma_f32_16x16x32_bf16 a[192:195],v[108:111],a[68:71],a[192:195]\n"
"v_mfma_f32_16x16x32_bf16 a[200:203],v[96:99],a[8:11],a[200:203]\n"
"ds_read_b128 v[120:123],v8 offset:14336\n"
"v_cvt_pk_bf16_f32 v208,v208,v209\n"
"v_mfma_f32_16x16x32_bf16 a[200:203],v[100:103],a[12:15],a[200:203]\n"
"v_cvt_pk_bf16_f32 v209,v210,v211\n"
"v_cvt_pk_bf16_f32 v210,v212,v213\n"
"v_mfma_f32_16x16x32_bf16 a[200:203],v[104:107],a[72:75],a[200:203]\n"
"ds_read_b128 v[124:127],v9 offset:14336\n"
"v_cvt_pk_bf16_f32 v211,v214,v215\n"
"v_mfma_f32_16x16x32_bf16 a[200:203],v[108:111],a[76:79],a[200:203]\n"
"v_mfma_f32_16x16x32_bf16 a[208:211],v[96:99],a[16:19],a[208:211]\n"
"v_mfma_f32_16x16x32_bf16 a[208:211],v[100:103],a[20:23],a[208:211]\n"
"v_mfma_f32_16x16x32_bf16 a[208:211],v[104:107],a[80:83],a[208:211]\n"
"v_mfma_f32_16x16x32_bf16 a[208:211],v[108:111],a[84:87],a[208:211]\n"
"v_mfma_f32_16x16x32_bf16 a[216:219],v[96:99],a[24:27],a[216:219]\n"
"s_mov_b32 s59,0x2000\n"
"v_mfma_f32_16x16x32_bf16 a[216:219],v[100:103],a[28:31],a[216:219]\n"
"buffer_store_dwordx4 v[200:203],v52,s[44:47],s59 offen offset:1024 sc1\n"
"v_mfma_f32_16x16x32_bf16 a[216:219],v[104:107],a[88:91],a[216:219]\n"
"v_mfma_f32_16x16x32_bf16 a[216:219],v[108:111],a[92:95],a[216:219]\n"
"v_mfma_f32_16x16x32_bf16 a[224:227],v[96:99],a[32:35],a[224:227]\n"
"v_mfma_f32_16x16x32_bf16 a[224:227],v[100:103],a[36:39],a[224:227]\n"
"s_waitcnt lgkmcnt(0)\n"
"v_mfma_f32_16x16x32_bf16 a[224:227],v[104:107],a[96:99],a[224:227]\n"
"s_waitcnt vmcnt(26)\n"
"s_barrier\n"
"v_mfma_f32_16x16x32_bf16 a[224:227],v[108:111],a[100:103],a[224:227]\n"
"v_cvt_pk_bf16_f32 v216,v216,v217\n"
"v_cvt_pk_bf16_f32 v217,v218,v219\n"
"v_mfma_f32_16x16x32_bf16 a[232:235],v[96:99],a[40:43],a[232:235]\n"
"s_mov_b32 s53,0x180000\n"
"v_cvt_pk_bf16_f32 v218,v220,v221\n"
"v_cvt_pk_bf16_f32 v219,v222,v223\n"
"v_mfma_f32_16x16x32_bf16 a[232:235],v[100:103],a[44:47],a[232:235]\n"
"s_add_u32 m0,s57,0x20000\n"
"v_mfma_f32_16x16x32_bf16 a[232:235],v[104:107],a[104:107],a[232:235]\n"
"buffer_load_dwordx4 v32,s[64:67],s53 offen offset:0 lds\n"
"v_mfma_f32_16x16x32_bf16 a[232:235],v[108:111],a[108:111],a[232:235]\n"
"v_mfma_f32_16x16x32_bf16 a[240:243],v[96:99],a[48:51],a[240:243]\n"
"v_mfma_f32_16x16x32_bf16 a[240:243],v[100:103],a[52:55],a[240:243]\n"
"buffer_load_dwordx4 v33,s[64:67],s53 offen offset:1024 lds\n"
"v_mfma_f32_16x16x32_bf16 a[240:243],v[104:107],a[112:115],a[240:243]\n"
"v_mfma_f32_16x16x32_bf16 a[240:243],v[108:111],a[116:119],a[240:243]\n"
"s_mov_b32 s59,0x2000\n"
"v_mfma_f32_16x16x32_bf16 a[248:251],v[96:99],a[56:59],a[248:251]\n"
"buffer_store_dwordx4 v[208:211],v52,s[44:47],s59 offen offset:2048 sc1\n"
"v_mfma_f32_16x16x32_bf16 a[248:251],v[100:103],a[60:63],a[248:251]\n"
"v_mfma_f32_16x16x32_bf16 a[248:251],v[104:107],a[120:123],a[248:251]\n"
"v_mfma_f32_16x16x32_bf16 a[248:251],v[108:111],a[124:127],a[248:251]\n"
"buffer_load_dwordx4 v34,s[64:67],s53 offen offset:2048 lds\n"
"v_mfma_f32_16x16x32_bf16 a[196:199],v[112:115],a[0:3],a[196:199]\n"
"v_mfma_f32_16x16x32_bf16 a[196:199],v[116:119],a[4:7],a[196:199]\n"
"v_mfma_f32_16x16x32_bf16 a[196:199],v[120:123],a[64:67],a[196:199]\n"
"buffer_load_dwordx4 v35,s[64:67],s53 offen offset:3072 lds\n"
"v_mfma_f32_16x16x32_bf16 a[196:199],v[124:127],a[68:71],a[196:199]\n"
"v_cvt_pk_bf16_f32 v224,v224,v225\n"
"v_cvt_pk_bf16_f32 v225,v226,v227\n"
"v_mfma_f32_16x16x32_bf16 a[204:207],v[112:115],a[8:11],a[204:207]\n"
"s_add_u32 m0,s57,0x21000\n"
"v_cvt_pk_bf16_f32 v226,v228,v229\n"
"v_cvt_pk_bf16_f32 v227,v230,v231\n"
"v_mfma_f32_16x16x32_bf16 a[204:207],v[116:119],a[12:15],a[204:207]\n"
"buffer_load_dwordx4 v36,s[64:67],s53 offen offset:0 lds\n"
"v_mfma_f32_16x16x32_bf16 a[204:207],v[120:123],a[72:75],a[204:207]\n"
"v_mfma_f32_16x16x32_bf16 a[204:207],v[124:127],a[76:79],a[204:207]\n"
"v_mfma_f32_16x16x32_bf16 a[212:215],v[112:115],a[16:19],a[212:215]\n"
"buffer_load_dwordx4 v37,s[64:67],s53 offen offset:1024 lds\n"
"v_mfma_f32_16x16x32_bf16 a[212:215],v[116:119],a[20:23],a[212:215]\n"
"v_mfma_f32_16x16x32_bf16 a[212:215],v[120:123],a[80:83],a[212:215]\n"
"s_mov_b32 s59,0x2000\n"
"v_mfma_f32_16x16x32_bf16 a[212:215],v[124:127],a[84:87],a[212:215]\n"
"buffer_store_dwordx4 v[216:219],v52,s[44:47],s59 offen offset:3072 sc1\n"
"v_mfma_f32_16x16x32_bf16 a[220:223],v[112:115],a[24:27],a[220:223]\n"
"v_mfma_f32_16x16x32_bf16 a[220:223],v[116:119],a[28:31],a[220:223]\n"
"v_mfma_f32_16x16x32_bf16 a[220:223],v[120:123],a[88:91],a[220:223]\n"
"buffer_load_dwordx4 v38,s[64:67],s53 offen offset:2048 lds\n"
"v_mfma_f32_16x16x32_bf16 a[220:223],v[124:127],a[92:95],a[220:223]\n"
"v_mfma_f32_16x16x32_bf16 a[228:231],v[112:115],a[32:35],a[228:231]\n"
"ds_read_b64_tr_b16 a[0:1],v12 offset:0\n"
"v_mfma_f32_16x16x32_bf16 a[228:231],v[116:119],a[36:39],a[228:231]\n"
"ds_read_b64_tr_b16 a[2:3],v12 offset:2048\n"
"buffer_load_dwordx4 v39,s[64:67],s53 offen offset:3072 lds\n"
"v_mfma_f32_16x16x32_bf16 a[228:231],v[120:123],a[96:99],a[228:231]\n"
"ds_read_b64_tr_b16 a[4:5],v12 offset:16384\n"
"v_mfma_f32_16x16x32_bf16 a[228:231],v[124:127],a[100:103],a[228:231]\n"
"ds_read_b64_tr_b16 a[6:7],v12 offset:18432\n"
"v_mfma_f32_16x16x32_bf16 a[236:239],v[112:115],a[40:43],a[236:239]\n"
"ds_read_b64_tr_b16 a[8:9],v13 offset:0\n"
"v_mfma_f32_16x16x32_bf16 a[236:239],v[116:119],a[44:47],a[236:239]\n"
"ds_read_b64_tr_b16 a[10:11],v13 offset:2048\n"
"v_mfma_f32_16x16x32_bf16 a[236:239],v[120:123],a[104:107],a[236:239]\n"
"ds_read_b64_tr_b16 a[12:13],v13 offset:16384\n"
"v_mfma_f32_16x16x32_bf16 a[236:239],v[124:127],a[108:111],a[236:239]\n"
"ds_read_b64_tr_b16 a[14:15],v13 offset:18432\n"
"v_mfma_f32_16x16x32_bf16 a[244:247],v[112:115],a[48:51],a[244:247]\n"
"ds_read_b64_tr_b16 a[16:17],v14 offset:0\n"
"v_mfma_f32_16x16x32_bf16 a[244:247],v[116:119],a[52:55],a[244:247]\n"
"s_mov_b32 s59,0x3000\n"
"ds_read_b64_tr_b16 a[18:19],v14 offset:2048\n"
"v_mfma_f32_16x16x32_bf16 a[244:247],v[120:123],a[112:115],a[244:247]\n"
"ds_read_b64_tr_b16 a[20:21],v14 offset:16384\n"
"buffer_store_dwordx4 v[224:227],v52,s[44:47],s59 offen offset:0 sc1\n"
"v_mfma_f32_16x16x32_bf16 a[244:247],v[124:127],a[116:119],a[244:247]\n"
"ds_read_b64_tr_b16 a[22:23],v14 offset:18432\n"
"v_mfma_f32_16x16x32_bf16 a[252:255],v[112:115],a[56:59],a[252:255]\n"
"ds_read_b64_tr_b16 a[24:25],v15 offset:0\n"
"v_mfma_f32_16x16x32_bf16 a[252:255],v[116:119],a[60:63],a[252:255]\n"
"ds_read_b64_tr_b16 a[26:27],v15 offset:2048\n"
"v_mfma_f32_16x16x32_bf16 a[252:255],v[120:123],a[120:123],a[252:255]\n"
"ds_read_b64_tr_b16 a[28:29],v15 offset:16384\n"
"v_mfma_f32_16x16x32_bf16 a[252:255],v[124:127],a[124:127],a[252:255]\n"
"s_waitcnt lgkmcnt(11)\n"
"ds_read_b64_tr_b16 a[30:31],v15 offset:18432\n"
"v_mfma_f32_16x16x32_bf16 v[128:131],a[0:3],v[72:75],0\n"
"ds_read_b64_tr_b16 a[32:33],v16 offset:0\n"
"v_cvt_pk_bf16_f32 v232,v232,v233\n"
"v_mfma_f32_16x16x32_bf16 v[128:131],a[4:7],v[80:83],v[128:131]\n"
"ds_read_b64_tr_b16 a[34:35],v16 offset:2048\n"
"buffer_load_dwordx4 a[40:43],v52,s[60:63],s86 offen offset:1024 sc1\n"
"v_mfma_f32_16x16x32_bf16 v[136:139],a[0:3],v[76:79],0\n"
"ds_read_b64_tr_b16 a[36:37],v16 offset:16384\n"
"v_cvt_pk_bf16_f32 v233,v234,v235\n"
"v_mfma_f32_16x16x32_bf16 v[136:139],a[4:7],v[84:87],v[136:139]\n"
"s_waitcnt lgkmcnt(11)\n"
"ds_read_b64_tr_b16 a[38:39],v16 offset:18432\n"
"v_cvt_pk_bf16_f32 v234,v236,v237\n"
"v_mfma_f32_16x16x32_bf16 v[132:135],a[8:11],v[72:75],0\n"
"ds_read_b64_tr_b16 a[0:1],v17 offset:0\n"
"v_cvt_pk_bf16_f32 v235,v238,v239\n"
"v_mfma_f32_16x16x32_bf16 v[132:135],a[12:15],v[80:83],v[132:135]\n"
"ds_read_b64_tr_b16 a[2:3],v17 offset:2048\n"
"buffer_load_dwordx4 a[44:47],v51,s[60:63],s86 offen offset:1024 sc1\n"
"v_mfma_f32_16x16x32_bf16 v[140:143],a[8:11],v[76:79],0\n"
"ds_read_b64_tr_b16 a[4:5],v17 offset:16384\n"
"v_cvt_pk_bf16_f32 v240,v240,v241\n"
"v_mfma_f32_16x16x32_bf16 v[140:143],a[12:15],v[84:87],v[140:143]\n"
"s_waitcnt lgkmcnt(11)\n"
"ds_read_b64_tr_b16 a[6:7],v17 offset:18432\n"
"v_cvt_pk_bf16_f32 v241,v242,v243\n"
"v_mfma_f32_16x16x32_bf16 v[144:147],a[16:19],v[72:75],0\n"
"s_mov_b32 s59,0x3000\n"
"ds_read_b64_tr_b16 a[8:9],v18 offset:0\n"
"v_cvt_pk_bf16_f32 v242,v244,v245\n"
"v_mfma_f32_16x16x32_bf16 v[144:147],a[20:23],v[80:83],v[144:147]\n"
"ds_read_b64_tr_b16 a[10:11],v18 offset:2048\n"
"buffer_store_dwordx4 v[232:235],v52,s[44:47],s59 offen offset:1024 sc1\n"
"v_mfma_f32_16x16x32_bf16 v[152:155],a[16:19],v[76:79],0\n"
"ds_read_b64_tr_b16 a[12:13],v18 offset:16384\n"
"v_cvt_pk_bf16_f32 v243,v246,v247\n"
"v_mfma_f32_16x16x32_bf16 v[152:155],a[20:23],v[84:87],v[152:155]\n"
"s_waitcnt lgkmcnt(11)\n"
"ds_read_b64_tr_b16 a[14:15],v18 offset:18432\n"
"v_mfma_f32_16x16x32_bf16 v[148:151],a[24:27],v[72:75],0\n"
"ds_read_b64_tr_b16 a[16:17],v19 offset:0\n"
"v_mfma_f32_16x16x32_bf16 v[148:151],a[28:31],v[80:83],v[148:151]\n"
"ds_read_b64_tr_b16 a[18:19],v19 offset:2048\n"
"buffer_load_dwordx4 a[48:51],v52,s[60:63],s86 offen offset:2048 sc1\n"
"v_mfma_f32_16x16x32_bf16 v[156:159],a[24:27],v[76:79],0\n"
"ds_read_b64_tr_b16 a[20:21],v19 offset:16384\n"
"v_mfma_f32_16x16x32_bf16 v[156:159],a[28:31],v[84:87],v[156:159]\n"
"s_waitcnt lgkmcnt(11)\n"
"ds_read_b64_tr_b16 a[22:23],v19 offset:18432\n"
"v_mfma_f32_16x16x32_bf16 v[160:163],a[32:35],v[72:75],0\n"
"ds_read_b64_tr_b16 a[24:25],v12 offset:256\n"
"v_mfma_f32_16x16x32_bf16 v[160:163],a[36:39],v[80:83],v[160:163]\n"
"ds_read_b64_tr_b16 a[26:27],v12 offset:2304\n"
"buffer_load_dwordx4 a[52:55],v51,s[60:63],s86 offen offset:2048 sc1\n"
"v_mfma_f32_16x16x32_bf16 v[168:171],a[32:35],v[76:79],0\n"
"ds_read_b64_tr_b16 a[28:29],v12 offset:16640\n"
"v_mfma_f32_16x16x32_bf16 v[168:171],a[36:39],v[84:87],v[168:171]\n"
"s_waitcnt lgkmcnt(11)\n"
"ds_read_b64_tr_b16 a[30:31],v12 offset:18688\n"
"v_cvt_pk_bf16_f32 v248,v248,v249\n"
"v_mfma_f32_16x16x32_bf16 v[164:167],a[0:3],v[72:75],0\n"
"ds_read_b64_tr_b16 a[32:33],v13 offset:256\n"
"v_cvt_pk_bf16_f32 v249,v250,v251\n"
"v_mfma_f32_16x16x32_bf16 v[164:167],a[4:7],v[80:83],v[164:167]\n"
"ds_read_b64_tr_b16 a[34:35],v13 offset:2304\n"
"buffer_load_dwordx4 a[56:59],v52,s[60:63],s86 offen offset:3072 sc1\n"
"v_mfma_f32_16x16x32_bf16 v[172:175],a[0:3],v[76:79],0\n"
"ds_read_b64_tr_b16 a[36:37],v13 offset:16640\n"
"v_cvt_pk_bf16_f32 v250,v252,v253\n"
"v_mfma_f32_16x16x32_bf16 v[172:175],a[4:7],v[84:87],v[172:175]\n"
"s_waitcnt lgkmcnt(11)\n"
"ds_read_b64_tr_b16 a[38:39],v13 offset:18688\n"
"v_cvt_pk_bf16_f32 v251,v254,v255\n"
"v_mfma_f32_16x16x32_bf16 v[176:179],a[8:11],v[72:75],0\n"
"ds_read_b64_tr_b16 a[0:1],v14 offset:256\n"
"v_mfma_f32_16x16x32_bf16 v[176:179],a[12:15],v[80:83],v[176:179]\n"
"s_mov_b32 s59,0x3000\n"
"ds_read_b64_tr_b16 a[2:3],v14 offset:2304\n"
"v_mfma_f32_16x16x32_bf16 v[184:187],a[8:11],v[76:79],0\n"
"ds_read_b64_tr_b16 a[4:5],v14 offset:16640\n"
"buffer_store_dwordx4 v[240:243],v52,s[44:47],s59 offen offset:2048 sc1\n"
"v_mfma_f32_16x16x32_bf16 v[184:187],a[12:15],v[84:87],v[184:187]\n"
"s_waitcnt lgkmcnt(11)\n"
"ds_read_b64_tr_b16 a[6:7],v14 offset:18688\n"
"v_mfma_f32_16x16x32_bf16 v[180:183],a[16:19],v[72:75],0\n"
"ds_read_b64_tr_b16 a[8:9],v15 offset:256\n"
"v_mfma_f32_16x16x32_bf16 v[180:183],a[20:23],v[80:83],v[180:183]\n"
"ds_read_b64_tr_b16 a[10:11],v15 offset:2304\n"
"v_mfma_f32_16x16x32_bf16 v[188:191],a[16:19],v[76:79],0\n"
"ds_read_b64_tr_b16 a[12:13],v15 offset:16640\n"
"buffer_load_dwordx4 a[60:63],v51,s[60:63],s86 offen offset:3072 sc1\n"
"v_mfma_f32_16x16x32_bf16 v[188:191],a[20:23],v[84:87],v[188:191]\n"
"s_waitcnt lgkmcnt(11)\n"
"ds_read_b64_tr_b16 a[14:15],v15 offset:18688\n"
"v_mfma_f32_16x16x32_bf16 v[192:195],a[24:27],v[72:75],0\n"
"s_mov_b32 s53,0x200000\n"
"ds_read_b64_tr_b16 a[16:17],v16 offset:256\n"
"v_mfma_f32_16x16x32_bf16 v[192:195],a[28:31],v[80:83],v[192:195]\n"
"s_add_u32 m0,s57,0x10000\n"
"ds_read_b64_tr_b16 a[18:19],v16 offset:2304\n"
"v_mfma_f32_16x16x32_bf16 v[200:203],a[24:27],v[76:79],0\n"
"ds_read_b64_tr_b16 a[20:21],v16 offset:16640\n"
"buffer_load_dwordx4 v32,s[64:67],s53 offen offset:0 lds\n"
"v_mfma_f32_16x16x32_bf16 v[200:203],a[28:31],v[84:87],v[200:203]\n"
"s_waitcnt lgkmcnt(11)\n"
"ds_read_b64_tr_b16 a[22:23],v16 offset:18688\n"
"v_mfma_f32_16x16x32_bf16 v[196:199],a[32:35],v[72:75],0\n"
"ds_read_b64_tr_b16 a[24:25],v17 offset:256\n"
"v_accvgpr_read_b32 v56,a128\n"
"v_mfma_f32_16x16x32_bf16 v[196:199],a[36:39],v[80:83],v[196:199]\n"
"ds_read_b64_tr_b16 a[26:27],v17 offset:2304\n"
"v_accvgpr_read_b32 v57,a129\n"
"v_mfma_f32_16x16x32_bf16 v[204:207],a[32:35],v[76:79],0\n"
"ds_read_b64_tr_b16 a[28:29],v17 offset:16640\n"
"buffer_load_dwordx4 v33,s[64:67],s53 offen offset:1024 lds\n"
"v_mfma_f32_16x16x32_bf16 v[204:207],a[36:39],v[84:87],v[204:207]\n"
"s_waitcnt lgkmcnt(11)\n"
"ds_read_b64_tr_b16 a[30:31],v17 offset:18688\n"
"v_accvgpr_read_b32 v58,a130\n"
"v_mfma_f32_16x16x32_bf16 v[208:211],a[0:3],v[72:75],0\n"
"ds_read_b64_tr_b16 a[32:33],v18 offset:256\n"
"v_accvgpr_read_b32 v59,a131\n"
"v_mfma_f32_16x16x32_bf16 v[208:211],a[4:7],v[80:83],v[208:211]\n"
"ds_read_b64_tr_b16 a[34:35],v18 offset:2304\n"
"v_accvgpr_read_b32 v60,a132\n"
"v_mfma_f32_16x16x32_bf16 v[216:219],a[0:3],v[76:79],0\n"
"s_mov_b32 s59,0x3000\n"
"ds_read_b64_tr_b16 a[36:37],v18 offset:16640\n"
"v_accvgpr_read_b32 v61,a133\n"
"v_mfma_f32_16x16x32_bf16 v[216:219],a[4:7],v[84:87],v[216:219]\n"
"s_waitcnt lgkmcnt(11)\n"
"ds_read_b64_tr_b16 a[38:39],v18 offset:18688\n"
"buffer_store_dwordx4 v[248:251],v52,s[44:47],s59 offen offset:3072 sc1\n"
"v_mfma_f32_16x16x32_bf16 v[212:215],a[8:11],v[72:75],0\n"
"ds_read_b64_tr_b16 a[0:1],v19 offset:256\n"
"v_accvgpr_read_b32 v62,a134\n"
"v_mfma_f32_16x16x32_bf16 v[212:215],a[12:15],v[80:83],v[212:215]\n"
"ds_read_b64_tr_b16 a[2:3],v19 offset:2304\n"
"v_accvgpr_read_b32 v63,a135\n"
"v_mfma_f32_16x16x32_bf16 v[220:223],a[8:11],v[76:79],0\n"
"s_waitcnt vmcnt(31)\n"
"s_barrier\n"
"ds_read_b64_tr_b16 a[4:5],v19 offset:16640\n"
"v_cvt_pk_bf16_f32 v56,v56,v57\n"
"v_mfma_f32_16x16x32_bf16 v[220:223],a[12:15],v[84:87],v[220:223]\n"
"s_waitcnt lgkmcnt(11)\n"
"ds_read_b64_tr_b16 a[6:7],v19 offset:18688\n"
"buffer_load_dwordx4 v34,s[64:67],s53 offen offset:2048 lds\n"
"v_mfma_f32_16x16x32_bf16 v[224:227],a[16:19],v[72:75],0\n"
"ds_read_b64_tr_b16 a[8:9],v12 offset:32768\n"
"v_cvt_pk_bf16_f32 v57,v58,v59\n"
"v_mfma_f32_16x16x32_bf16 v[224:227],a[20:23],v[80:83],v[224:227]\n"
"ds_read_b64_tr_b16 a[10:11],v12 offset:34816\n"
"v_cvt_pk_bf16_f32 v58,v60,v61\n"
"v_mfma_f32_16x16x32_bf16 v[232:235],a[16:19],v[76:79],0\n"
"ds_read_b64_tr_b16 a[12:13],v12 offset:49152\n"
"v_cvt_pk_bf16_f32 v59,v62,v63\n"
"v_mfma_f32_16x16x32_bf16 v[232:235],a[20:23],v[84:87],v[232:235]\n"
"s_waitcnt lgkmcnt(11)\n"
"ds_read_b64_tr_b16 a[14:15],v12 offset:51200\n"
"buffer_load_dwordx4 v35,s[64:67],s53 offen offset:3072 lds\n"
"v_mfma_f32_16x16x32_bf16 v[228:231],a[24:27],v[72:75],0\n"
"ds_read_b64_tr_b16 a[16:17],v13 offset:32768\n"
"v_mfma_f32_16x16x32_bf16 v[228:231],a[28:31],v[80:83],v[228:231]\n"
"ds_read_b64_tr_b16 a[18:19],v13 offset:34816\n"
"v_accvgpr_read_b32 v64,a136\n"
"v_mfma_f32_16x16x32_bf16 v[236:239],a[24:27],v[76:79],0\n"
"s_add_u32 m0,s57,0x11000\n"
"ds_read_b64_tr_b16 a[20:21],v13 offset:49152\n"
"v_accvgpr_read_b32 v65,a137\n"
"v_mfma_f32_16x16x32_bf16 v[236:239],a[28:31],v[84:87],v[236:239]\n"
"s_waitcnt lgkmcnt(11)\n"
"ds_read_b64_tr_b16 a[22:23],v13 offset:51200\n"
"buffer_load_dwordx4 v36,s[64:67],s53 offen offset:0 lds\n"
"v_mfma_f32_16x16x32_bf16 v[240:243],a[32:35],v[72:75],0\n"
"ds_read_b64_tr_b16 a[24:25],v14 offset:32768\n"
"v_accvgpr_read_b32 v66,a138\n"
"v_mfma_f32_16x16x32_bf16 v[240:243],a[36:39],v[80:83],v[240:243]\n"
"ds_read_b64_tr_b16 a[26:27],v14 offset:34816\n"
"v_accvgpr_read_b32 v67,a139\n"
"v_mfma_f32_16x16x32_bf16 v[248:251],a[32:35],v[76:79],0\n"
"s_mov_b32 s59,0x10000\n"
"ds_read_b64_tr_b16 a[28:29],v14 offset:49152\n"
"v_accvgpr_read_b32 v68,a140\n"
"v_mfma_f32_16x16x32_bf16 v[248:251],a[36:39],v[84:87],v[248:251]\n"
"s_waitcnt lgkmcnt(11)\n"
"ds_read_b64_tr_b16 a[30:31],v14 offset:51200\n"
"v_accvgpr_read_b32 v69,a141\n"
"v_mfma_f32_16x16x32_bf16 v[244:247],a[0:3],v[72:75],0\n"
"ds_read_b64_tr_b16 a[32:33],v15 offset:32768\n"
"buffer_store_dwordx4 v[56:59],v52,s[44:47],s59 offen offset:0 sc1\n"
"v_mfma_f32_16x16x32_bf16 v[244:247],a[4:7],v[80:83],v[244:247]\n"
"ds_read_b64_tr_b16 a[34:35],v15 offset:34816\n"
"v_accvgpr_read_b32 v70,a142\n"
"v_mfma_f32_16x16x32_bf16 v[252:255],a[0:3],v[76:79],0\n"
"ds_read_b64_tr_b16 a[36:37],v15 offset:49152\n"
"v_accvgpr_read_b32 v71,a143\n"
"v_mfma_f32_16x16x32_bf16 v[252:255],a[4:7],v[84:87],v[252:255]\n"
"s_waitcnt lgkmcnt(11)\n"
"ds_read_b64_tr_b16 a[38:39],v15 offset:51200\n"
"v_cvt_pk_bf16_f32 v64,v64,v65\n"
"v_mfma_f32_16x16x32_bf16 v[128:131],a[8:11],v[88:91],v[128:131]\n"
"ds_read_b64_tr_b16 a[0:1],v16 offset:32768\n"
"buffer_load_dwordx4 v37,s[64:67],s53 offen offset:1024 lds\n"
"v_mfma_f32_16x16x32_bf16 v[128:131],a[12:15],v[24:27],v[128:131]\n"
"ds_read_b64_tr_b16 a[2:3],v16 offset:34816\n"
"v_cvt_pk_bf16_f32 v65,v66,v67\n"
"v_mfma_f32_16x16x32_bf16 v[136:139],a[8:11],v[92:95],v[136:139]\n"
"ds_read_b64_tr_b16 a[4:5],v16 offset:49152\n"
"v_cvt_pk_bf16_f32 v66,v68,v69\n"
"v_mfma_f32_16x16x32_bf16 v[136:139],a[12:15],v[28:31],v[136:139]\n"
"s_waitcnt lgkmcnt(11)\n"
"ds_read_b64_tr_b16 a[6:7],v16 offset:51200\n"
"v_cvt_pk_bf16_f32 v67,v70,v71\n"
"v_mfma_f32_16x16x32_bf16 v[132:135],a[16:19],v[88:91],v[132:135]\n"
"ds_read_b64_tr_b16 a[8:9],v17 offset:32768\n"
"buffer_load_dwordx4 v38,s[64:67],s53 offen offset:2048 lds\n"
"v_mfma_f32_16x16x32_bf16 v[132:135],a[20:23],v[24:27],v[132:135]\n"
"ds_read_b64_tr_b16 a[10:11],v17 offset:34816\n"
"v_mfma_f32_16x16x32_bf16 v[140:143],a[16:19],v[92:95],v[140:143]\n"
"ds_read_b64_tr_b16 a[12:13],v17 offset:49152\n"
"v_mfma_f32_16x16x32_bf16 v[140:143],a[20:23],v[28:31],v[140:143]\n"
"s_waitcnt lgkmcnt(11)\n"
"ds_read_b64_tr_b16 a[14:15],v17 offset:51200\n"
"v_accvgpr_read_b32 v56,a144\n"
"v_mfma_f32_16x16x32_bf16 v[144:147],a[24:27],v[88:91],v[144:147]\n"
"ds_read_b64_tr_b16 a[16:17],v18 offset:32768\n"
"buffer_load_dwordx4 v39,s[64:67],s53 offen offset:3072 lds\n"
"v_mfma_f32_16x16x32_bf16 v[144:147],a[28:31],v[24:27],v[144:147]\n"
"ds_read_b64_tr_b16 a[18:19],v18 offset:34816\n"
"v_accvgpr_read_b32 v57,a145\n"
"v_mfma_f32_16x16x32_bf16 v[152:155],a[24:27],v[92:95],v[152:155]\n"
"ds_read_b64_tr_b16 a[20:21],v18 offset:49152\n"
"v_accvgpr_read_b32 v58,a146\n"
"v_mfma_f32_16x16x32_bf16 v[152:155],a[28:31],v[28:31],v[152:155]\n"
"s_waitcnt lgkmcnt(11)\n"
"ds_read_b64_tr_b16 a[22:23],v18 offset:51200\n"
"v_accvgpr_read_b32 v59,a147\n"
"v_mfma_f32_16x16x32_bf16 v[148:151],a[32:35],v[88:91],v[148:151]\n"
"s_mov_b32 s59,0x10000\n"
"ds_read_b64_tr_b16 a[24:25],v19 offset:32768\n"
"v_accvgpr_read_b32 v60,a148\n"
"v_mfma_f32_16x16x32_bf16 v[148:151],a[36:39],v[24:27],v[148:151]\n"
"ds_read_b64_tr_b16 a[26:27],v19 offset:34816\n"
"buffer_store_dwordx4 v[64:67],v52,s[44:47],s59 offen offset:1024 sc1\n"
"v_mfma_f32_16x16x32_bf16 v[156:159],a[32:35],v[92:95],v[156:159]\n"
"ds_read_b64_tr_b16 a[28:29],v19 offset:49152\n"
"v_accvgpr_read_b32 v61,a149\n"
"v_mfma_f32_16x16x32_bf16 v[156:159],a[36:39],v[28:31],v[156:159]\n"
"s_waitcnt lgkmcnt(11)\n"
"ds_read_b64_tr_b16 a[30:31],v19 offset:51200\n"
"v_accvgpr_read_b32 v62,a150\n"
"v_mfma_f32_16x16x32_bf16 v[160:163],a[0:3],v[88:91],v[160:163]\n"
"ds_read_b64_tr_b16 a[32:33],v12 offset:33024\n"
"v_accvgpr_read_b32 v63,a151\n"
"v_mfma_f32_16x16x32_bf16 v[160:163],a[4:7],v[24:27],v[160:163]\n"
"ds_read_b64_tr_b16 a[34:35],v12 offset:35072\n"
"buffer_load_dwordx4 a[64:67],v52,s[60:63],s87 offen offset:0 sc1\n"
"v_mfma_f32_16x16x32_bf16 v[168:171],a[0:3],v[92:95],v[168:171]\n"
"ds_read_b64_tr_b16 a[36:37],v12 offset:49408\n"
"v_cvt_pk_bf16_f32 v56,v56,v57\n"
"v_mfma_f32_16x16x32_bf16 v[168:171],a[4:7],v[28:31],v[168:171]\n"
"s_waitcnt lgkmcnt(11)\n"
"ds_read_b64_tr_b16 a[38:39],v12 offset:51456\n"
"v_cvt_pk_bf16_f32 v57,v58,v59\n"
"v_mfma_f32_16x16x32_bf16 v[164:167],a[8:11],v[88:91],v[164:167]\n"
"ds_read_b64_tr_b16 a[0:1],v13 offset:33024\n"
"v_cvt_pk_bf16_f32 v58,v60,v61\n"
"v_mfma_f32_16x16x32_bf16 v[164:167],a[12:15],v[24:27],v[164:167]\n"
"ds_read_b64_tr_b16 a[2:3],v13 offset:35072\n"
"buffer_load_dwordx4 a[68:71],v51,s[60:63],s87 offen offset:0 sc1\n"
"v_mfma_f32_16x16x32_bf16 v[172:175],a[8:11],v[92:95],v[172:175]\n"
"ds_read_b64_tr_b16 a[4:5],v13 offset:49408\n"
"v_add_u32 v12,0x10000,v12\n"
"v_mfma_f32_16x16x32_bf16 v[172:175],a[12:15],v[28:31],v[172:175]\n"
"s_waitcnt lgkmcnt(11)\n"
"ds_read_b64_tr_b16 a[6:7],v13 offset:51456\n"
"v_cvt_pk_bf16_f32 v59,v62,v63\n"
"v_mfma_f32_16x16x32_bf16 v[176:179],a[16:19],v[88:91],v[176:179]\n"
"ds_read_b64_tr_b16 a[8:9],v14 offset:33024\n"
"v_accvgpr_read_b32 v64,a152\n"
"v_mfma_f32_16x16x32_bf16 v[176:179],a[20:23],v[24:27],v[176:179]\n"
"ds_read_b64_tr_b16 a[10:11],v14 offset:35072\n"
"buffer_load_dwordx4 a[72:75],v52,s[60:63],s87 offen offset:1024 sc1\n"
"v_mfma_f32_16x16x32_bf16 v[184:187],a[16:19],v[92:95],v[184:187]\n"
"ds_read_b64_tr_b16 a[12:13],v14 offset:49408\n"
"v_add_u32 v13,0x10000,v13\n"
"v_mfma_f32_16x16x32_bf16 v[184:187],a[20:23],v[28:31],v[184:187]\n"
"s_waitcnt lgkmcnt(11)\n"
"ds_read_b64_tr_b16 a[14:15],v14 offset:51456\n"
"v_accvgpr_read_b32 v65,a153\n"
"v_mfma_f32_16x16x32_bf16 v[180:183],a[24:27],v[88:91],v[180:183]\n"
"ds_read_b64_tr_b16 a[16:17],v15 offset:33024\n"
"v_accvgpr_read_b32 v66,a154\n"
"v_mfma_f32_16x16x32_bf16 v[180:183],a[28:31],v[24:27],v[180:183]\n"
"ds_read_b64_tr_b16 a[18:19],v15 offset:35072\n"
"v_accvgpr_read_b32 v67,a155\n"
"v_mfma_f32_16x16x32_bf16 v[188:191],a[24:27],v[92:95],v[188:191]\n"
"s_mov_b32 s59,0x10000\n"
"ds_read_b64_tr_b16 a[20:21],v15 offset:49408\n"
"v_add_u32 v14,0x10000,v14\n"
"v_mfma_f32_16x16x32_bf16 v[188:191],a[28:31],v[28:31],v[188:191]\n"
"s_waitcnt lgkmcnt(11)\n"
"ds_read_b64_tr_b16 a[22:23],v15 offset:51456\n"
"buffer_store_dwordx4 v[56:59],v52,s[44:47],s59 offen offset:2048 sc1\n"
"v_mfma_f32_16x16x32_bf16 v[192:195],a[32:35],v[88:91],v[192:195]\n"
"ds_read_b64_tr_b16 a[24:25],v16 offset:33024\n"
"v_accvgpr_read_b32 v68,a156\n"
"v_mfma_f32_16x16x32_bf16 v[192:195],a[36:39],v[24:27],v[192:195]\n"
"ds_read_b64_tr_b16 a[26:27],v16 offset:35072\n"
"v_accvgpr_read_b32 v69,a157\n"
"v_mfma_f32_16x16x32_bf16 v[200:203],a[32:35],v[92:95],v[200:203]\n"
"ds_read_b64_tr_b16 a[28:29],v16 offset:49408\n"
"v_add_u32 v15,0x10000,v15\n"
"v_mfma_f32_16x16x32_bf16 v[200:203],a[36:39],v[28:31],v[200:203]\n"
"s_waitcnt lgkmcnt(11)\n"
"ds_read_b64_tr_b16 a[30:31],v16 offset:51456\n"
"buffer_load_dwordx4 a[76:79],v51,s[60:63],s87 offen offset:1024 sc1\n"
"v_mfma_f32_16x16x32_bf16 v[196:199],a[0:3],v[88:91],v[196:199]\n"
"ds_read_b64_tr_b16 a[32:33],v17 offset:33024\n"
"v_accvgpr_read_b32 v70,a158\n"
"v_mfma_f32_16x16x32_bf16 v[196:199],a[4:7],v[24:27],v[196:199]\n"
"ds_read_b64_tr_b16 a[34:35],v17 offset:35072\n"
"v_accvgpr_read_b32 v71,a159\n"
"v_mfma_f32_16x16x32_bf16 v[204:207],a[0:3],v[92:95],v[204:207]\n"
"ds_read_b64_tr_b16 a[36:37],v17 offset:49408\n"
"v_add_u32 v16,0x10000,v16\n"
"v_mfma_f32_16x16x32_bf16 v[204:207],a[4:7],v[28:31],v[204:207]\n"
"s_waitcnt lgkmcnt(11)\n"
"ds_read_b64_tr_b16 a[38:39],v17 offset:51456\n"
"buffer_load_dwordx4 a[80:83],v52,s[60:63],s87 offen offset:2048 sc1\n"
"v_mfma_f32_16x16x32_bf16 v[208:211],a[8:11],v[88:91],v[208:211]\n"
"ds_read_b64_tr_b16 a[0:1],v18 offset:33024\n"
"v_cvt_pk_bf16_f32 v64,v64,v65\n"
"v_mfma_f32_16x16x32_bf16 v[208:211],a[12:15],v[24:27],v[208:211]\n"
"ds_read_b64_tr_b16 a[2:3],v18 offset:35072\n"
"v_cvt_pk_bf16_f32 v65,v66,v67\n"
"v_mfma_f32_16x16x32_bf16 v[216:219],a[8:11],v[92:95],v[216:219]\n"
"ds_read_b64_tr_b16 a[4:5],v18 offset:49408\n"
"v_add_u32 v17,0x10000,v17\n"
"v_mfma_f32_16x16x32_bf16 v[216:219],a[12:15],v[28:31],v[216:219]\n"
"s_waitcnt lgkmcnt(11)\n"
"ds_read_b64_tr_b16 a[6:7],v18 offset:51456\n"
"buffer_load_dwordx4 a[84:87],v51,s[60:63],s87 offen offset:2048 sc1\n"
"v_mfma_f32_16x16x32_bf16 v[212:215],a[16:19],v[88:91],v[212:215]\n"
"ds_read_b64_tr_b16 a[8:9],v19 offset:33024\n"
"v_cvt_pk_bf16_f32 v66,v68,v69\n"
"v_mfma_f32_16x16x32_bf16 v[212:215],a[20:23],v[24:27],v[212:215]\n"
"s_mov_b32 s59,0x10000\n"
"ds_read_b64_tr_b16 a[10:11],v19 offset:35072\n"
"v_cvt_pk_bf16_f32 v67,v70,v71\n"
"v_mfma_f32_16x16x32_bf16 v[220:223],a[16:19],v[92:95],v[220:223]\n"
"s_barrier\n"
"ds_read_b64_tr_b16 a[12:13],v19 offset:49408\n"
"v_add_u32 v18,0x10000,v18\n"
"v_mfma_f32_16x16x32_bf16 v[220:223],a[20:23],v[28:31],v[220:223]\n"
"s_waitcnt lgkmcnt(11)\n"
"ds_read_b64_tr_b16 a[14:15],v19 offset:51456\n"
"buffer_store_dwordx4 v[64:67],v52,s[44:47],s59 offen offset:3072 sc1\n"
"v_mfma_f32_16x16x32_bf16 v[224:227],a[24:27],v[88:91],v[224:227]\n"
"ds_read_b64_tr_b16 a[16:17],v12 offset:32768\n"
"v_accvgpr_read_b32 v56,a160\n"
"v_mfma_f32_16x16x32_bf16 v[224:227],a[28:31],v[24:27],v[224:227]\n"
"ds_read_b64_tr_b16 a[18:19],v12 offset:34816\n"
"v_accvgpr_read_b32 v57,a161\n"
"v_mfma_f32_16x16x32_bf16 v[232:235],a[24:27],v[92:95],v[232:235]\n"
"ds_read_b64_tr_b16 a[20:21],v12 offset:49152\n"
"v_add_u32 v19,0x10000,v19\n"
"v_mfma_f32_16x16x32_bf16 v[232:235],a[28:31],v[28:31],v[232:235]\n"
"s_waitcnt lgkmcnt(11)\n"
"ds_read_b64_tr_b16 a[22:23],v12 offset:51200\n"
"buffer_load_dwordx4 a[88:91],v52,s[60:63],s87 offen offset:3072 sc1\n"
"v_mfma_f32_16x16x32_bf16 v[228:231],a[32:35],v[88:91],v[228:231]\n"
"ds_read_b64_tr_b16 a[24:25],v13 offset:32768\n"
"v_accvgpr_read_b32 v58,a162\n"
"v_mfma_f32_16x16x32_bf16 v[228:231],a[36:39],v[24:27],v[228:231]\n"
"ds_read_b64_tr_b16 a[26:27],v13 offset:34816\n"
"v_accvgpr_read_b32 v59,a163\n"
"v_mfma_f32_16x16x32_bf16 v[236:239],a[32:35],v[92:95],v[236:239]\n"
"ds_read_b64_tr_b16 a[28:29],v13 offset:49152\n"
"v_accvgpr_read_b32 v60,a164\n"
"v_mfma_f32_16x16x32_bf16 v[236:239],a[36:39],v[28:31],v[236:239]\n"
"s_waitcnt lgkmcnt(11)\n"
"ds_read_b64_tr_b16 a[30:31],v13 offset:51200\n"
"buffer_load_dwordx4 a[92:95],v51,s[60:63],s87 offen offset:3072 sc1\n"
"v_mfma_f32_16x16x32_bf16 v[240:243],a[0:3],v[88:91],v[240:243]\n"
"ds_read_b64_tr_b16 a[32:33],v14 offset:32768\n"
"v_accvgpr_read_b32 v61,a165\n"
"v_mfma_f32_16x16x32_bf16 v[240:243],a[4:7],v[24:27],v[240:243]\n"
"ds_read_b64_tr_b16 a[34:35],v14 offset:34816\n"
"v_accvgpr_read_b32 v62,a166\n"
"v_mfma_f32_16x16x32_bf16 v[248:251],a[0:3],v[92:95],v[248:251]\n"
"ds_read_b64_tr_b16 a[36:37],v14 offset:49152\n"
"v_accvgpr_read_b32 v63,a167\n"
"v_mfma_f32_16x16x32_bf16 v[248:251],a[4:7],v[28:31],v[248:251]\n"
"s_waitcnt lgkmcnt(11)\n"
"ds_read_b64_tr_b16 a[38:39],v14 offset:51200\n"
"buffer_load_dwordx4 a[96:99],v52,s[60:63],s88 offen offset:0 sc1\n"
"v_mfma_f32_16x16x32_bf16 v[244:247],a[8:11],v[88:91],v[244:247]\n"
"ds_read_b64_tr_b16 a[0:1],v15 offset:32768\n"
"v_cvt_pk_bf16_f32 v56,v56,v57\n"
"v_mfma_f32_16x16x32_bf16 v[244:247],a[12:15],v[24:27],v[244:247]\n"
"s_mov_b32 s59,0x11000\n"
"ds_read_b64_tr_b16 a[2:3],v15 offset:34816\n"
"v_cvt_pk_bf16_f32 v57,v58,v59\n"
"v_mfma_f32_16x16x32_bf16 v[252:255],a[8:11],v[92:95],v[252:255]\n"
"s_waitcnt vmcnt(28)\n"
"ds_read_b64_tr_b16 a[4:5],v15 offset:49152\n"
"v_cvt_pk_bf16_f32 v58,v60,v61\n"
"v_mfma_f32_16x16x32_bf16 v[252:255],a[12:15],v[28:31],v[252:255]\n"
"s_waitcnt lgkmcnt(11)\n"
"ds_read_b64_tr_b16 a[6:7],v15 offset:51200\n"
"v_cvt_pk_bf16_f32 v59,v62,v63\n"
"v_mfma_f32_16x16x32_bf16 v[128:131],a[16:19],v[40:43],v[128:131]\n"
"ds_read_b64_tr_b16 a[8:9],v16 offset:32768\n"
"buffer_store_dwordx4 v[56:59],v52,s[44:47],s59 offen offset:0 sc1\n"
"v_mfma_f32_16x16x32_bf16 v[128:131],a[20:23],a[40:43],v[128:131]\n"
"ds_read_b64_tr_b16 a[10:11],v16 offset:34816\n"
"v_accvgpr_read_b32 v64,a168\n"
"v_mfma_f32_16x16x32_bf16 v[136:139],a[16:19],v[44:47],v[136:139]\n"
"ds_read_b64_tr_b16 a[12:13],v16 offset:49152\n"
"v_accvgpr_read_b32 v65,a169\n"
"v_mfma_f32_16x16x32_bf16 v[136:139],a[20:23],a[44:47],v[136:139]\n"
"s_waitcnt lgkmcnt(11)\n"
"ds_read_b64_tr_b16 a[14:15],v16 offset:51200\n"
"v_accvgpr_read_b32 v66,a170\n"
"v_mfma_f32_16x16x32_bf16 v[132:135],a[24:27],v[40:43],v[132:135]\n"
"ds_read_b64_tr_b16 a[16:17],v17 offset:32768\n"
"buffer_load_dwordx4 a[100:103],v51,s[60:63],s88 offen offset:0 sc1\n"
"v_mfma_f32_16x16x32_bf16 v[132:135],a[28:31],a[40:43],v[132:135]\n"
"ds_read_b64_tr_b16 a[18:19],v17 offset:34816\n"
"v_accvgpr_read_b32 v67,a171\n"
"v_mfma_f32_16x16x32_bf16 v[140:143],a[24:27],v[44:47],v[140:143]\n"
"ds_read_b64_tr_b16 a[20:21],v17 offset:49152\n"
"v_accvgpr_read_b32 v68,a172\n"
"v_mfma_f32_16x16x32_bf16 v[140:143],a[28:31],a[44:47],v[140:143]\n"
"s_waitcnt lgkmcnt(11)\n"
"ds_read_b64_tr_b16 a[22:23],v17 offset:51200\n"
"v_accvgpr_read_b32 v69,a173\n"
"v_mfma_f32_16x16x32_bf16 v[144:147],a[32:35],v[40:43],v[144:147]\n"
"ds_read_b64_tr_b16 a[24:25],v18 offset:32768\n"
"buffer_load_dwordx4 a[104:107],v52,s[60:63],s88 offen offset:1024 sc1\n"
"v_mfma_f32_16x16x32_bf16 v[144:147],a[36:39],a[40:43],v[144:147]\n"
"ds_read_b64_tr_b16 a[26:27],v18 offset:34816\n"
"v_accvgpr_read_b32 v70,a174\n"
"v_mfma_f32_16x16x32_bf16 v[152:155],a[32:35],v[44:47],v[152:155]\n"
"ds_read_b64_tr_b16 a[28:29],v18 offset:49152\n"
"v_accvgpr_read_b32 v71,a175\n"
"v_mfma_f32_16x16x32_bf16 v[152:155],a[36:39],a[44:47],v[152:155]\n"
"s_waitcnt lgkmcnt(11)\n"
"ds_read_b64_tr_b16 a[30:31],v18 offset:51200\n"
"v_cvt_pk_bf16_f32 v64,v64,v65\n"
"v_mfma_f32_16x16x32_bf16 v[148:151],a[0:3],v[40:43],v[148:151]\n"
"ds_read_b64_tr_b16 a[32:33],v19 offset:32768\n"
"buffer_load_dwordx4 a[108:111],v51,s[60:63],s88 offen offset:1024 sc1\n"
"v_mfma_f32_16x16x32_bf16 v[148:151],a[4:7],a[40:43],v[148:151]\n"
"ds_read_b64_tr_b16 a[34:35],v19 offset:34816\n"
"v_cvt_pk_bf16_f32 v65,v66,v67\n"
"v_mfma_f32_16x16x32_bf16 v[156:159],a[0:3],v[44:47],v[156:159]\n"
"ds_read_b64_tr_b16 a[36:37],v19 offset:49152\n"
"v_cvt_pk_bf16_f32 v66,v68,v69\n"
"v_mfma_f32_16x16x32_bf16 v[156:159],a[4:7],a[44:47],v[156:159]\n"
"s_waitcnt lgkmcnt(11)\n"
"ds_read_b64_tr_b16 a[38:39],v19 offset:51200\n"
"v_cvt_pk_bf16_f32 v67,v70,v71\n"
"v_accvgpr_read_b32 v56,a176\n"
"v_mfma_f32_16x16x32_bf16 v[160:163],a[8:11],v[40:43],v[160:163]\n"
"s_mov_b32 s59,0x11000\n"
"ds_read_b64_tr_b16 a[0:1],v12 offset:33024\n"
"v_accvgpr_read_b32 v57,a177\n"
"v_mfma_f32_16x16x32_bf16 v[160:163],a[12:15],a[40:43],v[160:163]\n"
"ds_read_b64_tr_b16 a[2:3],v12 offset:35072\n"
"buffer_store_dwordx4 v[64:67],v52,s[44:47],s59 offen offset:1024 sc1\n"
"v_mfma_f32_16x16x32_bf16 v[168:171],a[8:11],v[44:47],v[168:171]\n"
"s_mov_b32 s53,0x280000\n"
"ds_read_b64_tr_b16 a[4:5],v12 offset:49408\n"
"v_accvgpr_read_b32 v58,a178\n"
"v_mfma_f32_16x16x32_bf16 v[168:171],a[12:15],a[44:47],v[168:171]\n"
"s_waitcnt lgkmcnt(11)\n"
"ds_read_b64_tr_b16 a[6:7],v12 offset:51456\n"
"v_accvgpr_read_b32 v59,a179\n"
"v_mfma_f32_16x16x32_bf16 v[164:167],a[16:19],v[40:43],v[164:167]\n"
"s_add_u32 m0,s57,0x0\n"
"ds_read_b64_tr_b16 a[8:9],v13 offset:33024\n"
"v_accvgpr_read_b32 v60,a180\n"
"v_mfma_f32_16x16x32_bf16 v[164:167],a[20:23],a[40:43],v[164:167]\n"
"ds_read_b64_tr_b16 a[10:11],v13 offset:35072\n"
"buffer_load_dwordx4 v32,s[64:67],s53 offen offset:0 lds\n"
"v_mfma_f32_16x16x32_bf16 v[172:175],a[16:19],v[44:47],v[172:175]\n"
"ds_read_b64_tr_b16 a[12:13],v13 offset:49408\n"
"v_add_u32 v12,0x10000,v12\n"
"v_mfma_f32_16x16x32_bf16 v[172:175],a[20:23],a[44:47],v[172:175]\n"
"s_waitcnt lgkmcnt(11)\n"
"ds_read_b64_tr_b16 a[14:15],v13 offset:51456\n"
"v_accvgpr_read_b32 v61,a181\n"
"v_mfma_f32_16x16x32_bf16 v[176:179],a[24:27],v[40:43],v[176:179]\n"
"ds_read_b64_tr_b16 a[16:17],v14 offset:33024\n"
"v_accvgpr_read_b32 v62,a182\n"
"v_mfma_f32_16x16x32_bf16 v[176:179],a[28:31],a[40:43],v[176:179]\n"
"ds_read_b64_tr_b16 a[18:19],v14 offset:35072\n"
"buffer_load_dwordx4 v33,s[64:67],s53 offen offset:1024 lds\n"
"v_mfma_f32_16x16x32_bf16 v[184:187],a[24:27],v[44:47],v[184:187]\n"
"ds_read_b64_tr_b16 a[20:21],v14 offset:49408\n"
"v_add_u32 v13,0x10000,v13\n"
"v_mfma_f32_16x16x32_bf16 v[184:187],a[28:31],a[44:47],v[184:187]\n"
"s_waitcnt lgkmcnt(11)\n"
"ds_read_b64_tr_b16 a[22:23],v14 offset:51456\n"
"v_accvgpr_read_b32 v63,a183\n"
"v_mfma_f32_16x16x32_bf16 v[180:183],a[32:35],v[40:43],v[180:183]\n"
"ds_read_b64_tr_b16 a[24:25],v15 offset:33024\n"
"v_cvt_pk_bf16_f32 v56,v56,v57\n"
"v_mfma_f32_16x16x32_bf16 v[180:183],a[36:39],a[40:43],v[180:183]\n"
"ds_read_b64_tr_b16 a[26:27],v15 offset:35072\n"
"buffer_load_dwordx4 v34,s[64:67],s53 offen offset:2048 lds\n"
"v_mfma_f32_16x16x32_bf16 v[188:191],a[32:35],v[44:47],v[188:191]\n"
"ds_read_b64_tr_b16 a[28:29],v15 offset:49408\n"
"v_add_u32 v14,0x10000,v14\n"
"v_mfma_f32_16x16x32_bf16 v[188:191],a[36:39],a[44:47],v[188:191]\n"
"s_waitcnt lgkmcnt(11)\n"
"ds_read_b64_tr_b16 a[30:31],v15 offset:51456\n"
"v_cvt_pk_bf16_f32 v57,v58,v59\n"
"v_mfma_f32_16x16x32_bf16 v[192:195],a[0:3],v[40:43],v[192:195]\n"
"ds_read_b64_tr_b16 a[32:33],v16 offset:33024\n"
"v_cvt_pk_bf16_f32 v58,v60,v61\n"
"v_mfma_f32_16x16x32_bf16 v[192:195],a[4:7],a[40:43],v[192:195]\n"
"ds_read_b64_tr_b16 a[34:35],v16 offset:35072\n"
"v_cvt_pk_bf16_f32 v59,v62,v63\n"
"v_accvgpr_read_b32 v64,a184\n"
"v_mfma_f32_16x16x32_bf16 v[200:203],a[0:3],v[44:47],v[200:203]\n"
"s_mov_b32 s59,0x11000\n"
"ds_read_b64_tr_b16 a[36:37],v16 offset:49408\n"
"v_add_u32 v15,0x10000,v15\n"
"v_accvgpr_read_b32 v65,a185\n"
"v_mfma_f32_16x16x32_bf16 v[200:203],a[4:7],a[44:47],v[200:203]\n"
"s_waitcnt lgkmcnt(11)\n"
"ds_read_b64_tr_b16 a[38:39],v16 offset:51456\n"
"buffer_store_dwordx4 v[56:59],v52,s[44:47],s59 offen offset:2048 sc1\n"
"v_mfma_f32_16x16x32_bf16 v[196:199],a[8:11],v[40:43],v[196:199]\n"
"ds_read_b64_tr_b16 a[0:1],v17 offset:33024\n"
"v_accvgpr_read_b32 v66,a186\n"
"v_accvgpr_read_b32 v67,a187\n"
"v_mfma_f32_16x16x32_bf16 v[196:199],a[12:15],a[40:43],v[196:199]\n"
"ds_read_b64_tr_b16 a[2:3],v17 offset:35072\n"
"v_accvgpr_read_b32 v68,a188\n"
"v_accvgpr_read_b32 v69,a189\n"
"v_mfma_f32_16x16x32_bf16 v[204:207],a[8:11],v[44:47],v[204:207]\n"
"ds_read_b64_tr_b16 a[4:5],v17 offset:49408\n"
"v_add_u32 v16,0x10000,v16\n"
"v_mfma_f32_16x16x32_bf16 v[204:207],a[12:15],a[44:47],v[204:207]\n"
"s_waitcnt lgkmcnt(11)\n"
"ds_read_b64_tr_b16 a[6:7],v17 offset:51456\n"
"buffer_load_dwordx4 v35,s[64:67],s53 offen offset:3072 lds\n"
"v_mfma_f32_16x16x32_bf16 v[208:211],a[16:19],v[40:43],v[208:211]\n"
"ds_read_b64_tr_b16 a[8:9],v18 offset:33024\n"
"v_accvgpr_read_b32 v70,a190\n"
"v_mfma_f32_16x16x32_bf16 v[208:211],a[20:23],a[40:43],v[208:211]\n"
"ds_read_b64_tr_b16 a[10:11],v18 offset:35072\n"
"v_accvgpr_read_b32 v71,a191\n"
"v_mfma_f32_16x16x32_bf16 v[216:219],a[16:19],v[44:47],v[216:219]\n"
"s_add_u32 m0,s57,0x1000\n"
"ds_read_b64_tr_b16 a[12:13],v18 offset:49408\n"
"v_add_u32 v17,0x10000,v17\n"
"v_mfma_f32_16x16x32_bf16 v[216:219],a[20:23],a[44:47],v[216:219]\n"
"s_waitcnt lgkmcnt(11)\n"
"ds_read_b64_tr_b16 a[14:15],v18 offset:51456\n"
"buffer_load_dwordx4 v36,s[64:67],s53 offen offset:0 lds\n"
"v_mfma_f32_16x16x32_bf16 v[212:215],a[24:27],v[40:43],v[212:215]\n"
"ds_read_b64_tr_b16 a[16:17],v19 offset:33024\n"
"v_cvt_pk_bf16_f32 v64,v64,v65\n"
"v_mfma_f32_16x16x32_bf16 v[212:215],a[28:31],a[40:43],v[212:215]\n"
"ds_read_b64_tr_b16 a[18:19],v19 offset:35072\n"
"v_cvt_pk_bf16_f32 v65,v66,v67\n"
"v_mfma_f32_16x16x32_bf16 v[220:223],a[24:27],v[44:47],v[220:223]\n"
"s_barrier\n"
"ds_read_b64_tr_b16 a[20:21],v19 offset:49408\n"
"v_add_u32 v18,0x10000,v18\n"
"v_mfma_f32_16x16x32_bf16 v[220:223],a[28:31],a[44:47],v[220:223]\n"
"s_waitcnt lgkmcnt(11)\n"
"ds_read_b64_tr_b16 a[22:23],v19 offset:51456\n"
"buffer_load_dwordx4 v37,s[64:67],s53 offen offset:1024 lds\n"
"v_mfma_f32_16x16x32_bf16 v[224:227],a[32:35],v[40:43],v[224:227]\n"
"ds_read_b64_tr_b16 a[24:25],v12 offset:0\n"
"v_cvt_pk_bf16_f32 v66,v68,v69\n"
"v_mfma_f32_16x16x32_bf16 v[224:227],a[36:39],a[40:43],v[224:227]\n"
"ds_read_b64_tr_b16 a[26:27],v12 offset:2048\n"
"v_cvt_pk_bf16_f32 v67,v70,v71\n"
"v_mfma_f32_16x16x32_bf16 v[232:235],a[32:35],v[44:47],v[232:235]\n"
"s_mov_b32 s59,0x11000\n"
"ds_read_b64_tr_b16 a[28:29],v12 offset:16384\n"
"v_add_u32 v19,0x10000,v19\n"
"v_mfma_f32_16x16x32_bf16 v[232:235],a[36:39],a[44:47],v[232:235]\n"
"s_waitcnt lgkmcnt(11)\n"
"ds_read_b64_tr_b16 a[30:31],v12 offset:18432\n"
"buffer_store_dwordx4 v[64:67],v52,s[44:47],s59 offen offset:3072 sc1\n"
"v_mfma_f32_16x16x32_bf16 v[228:231],a[0:3],v[40:43],v[228:231]\n"
"ds_read_b64_tr_b16 a[32:33],v13 offset:0\n"
"v_accvgpr_read_b32 v56,a192\n"
"v_mfma_f32_16x16x32_bf16 v[228:231],a[4:7],a[40:43],v[228:231]\n"
"ds_read_b64_tr_b16 a[34:35],v13 offset:2048\n"
"v_accvgpr_read_b32 v57,a193\n"
"v_mfma_f32_16x16x32_bf16 v[236:239],a[0:3],v[44:47],v[236:239]\n"
"ds_read_b64_tr_b16 a[36:37],v13 offset:16384\n"
"v_accvgpr_read_b32 v58,a194\n"
"v_mfma_f32_16x16x32_bf16 v[236:239],a[4:7],a[44:47],v[236:239]\n"
"s_waitcnt lgkmcnt(11)\n"
"ds_read_b64_tr_b16 a[38:39],v13 offset:18432\n"
"buffer_load_dwordx4 v38,s[64:67],s53 offen offset:2048 lds\n"
"v_mfma_f32_16x16x32_bf16 v[240:243],a[8:11],v[40:43],v[240:243]\n"
"ds_read_b64_tr_b16 a[0:1],v14 offset:0\n"
"v_accvgpr_read_b32 v59,a195\n"
"v_mfma_f32_16x16x32_bf16 v[240:243],a[12:15],a[40:43],v[240:243]\n"
"ds_read_b64_tr_b16 a[2:3],v14 offset:2048\n"
"v_accvgpr_read_b32 v60,a196\n"
"v_mfma_f32_16x16x32_bf16 v[248:251],a[8:11],v[44:47],v[248:251]\n"
"ds_read_b64_tr_b16 a[4:5],v14 offset:16384\n"
"v_accvgpr_read_b32 v61,a197\n"
"v_mfma_f32_16x16x32_bf16 v[248:251],a[12:15],a[44:47],v[248:251]\n"
"s_waitcnt lgkmcnt(11)\n"
"ds_read_b64_tr_b16 a[6:7],v14 offset:18432\n"
"buffer_load_dwordx4 v39,s[64:67],s53 offen offset:3072 lds\n"
"v_mfma_f32_16x16x32_bf16 v[244:247],a[16:19],v[40:43],v[244:247]\n"
"s_mov_b32 s53,0x300000\n"
"ds_read_b64_tr_b16 a[8:9],v15 offset:0\n"
"v_accvgpr_read_b32 v62,a198\n"
"v_mfma_f32_16x16x32_bf16 v[244:247],a[20:23],a[40:43],v[244:247]\n"
"s_add_u32 m0,s57,0x8000\n"
"ds_read_b64_tr_b16 a[10:11],v15 offset:2048\n"
"v_accvgpr_read_b32 v63,a199\n"
"v_mfma_f32_16x16x32_bf16 v[252:255],a[16:19],v[44:47],v[252:255]\n"
"s_waitcnt vmcnt(37)\n"
"ds_read_b64_tr_b16 a[12:13],v15 offset:16384\n"
"v_cvt_pk_bf16_f32 v56,v56,v57\n"
"v_mfma_f32_16x16x32_bf16 v[252:255],a[20:23],a[44:47],v[252:255]\n"
"s_waitcnt lgkmcnt(11)\n"
"ds_read_b64_tr_b16 a[14:15],v15 offset:18432\n"
"buffer_load_dwordx4 v32,s[64:67],s53 offen offset:0 lds\n"
"v_mfma_f32_16x16x32_bf16 v[128:131],a[24:27],a[48:51],v[128:131]\n"
"ds_read_b64_tr_b16 a[16:17],v16 offset:0\n"
"v_cvt_pk_bf16_f32 v57,v58,v59\n"
"v_mfma_f32_16x16x32_bf16 v[128:131],a[28:31],a[56:59],v[128:131]\n"
"ds_read_b64_tr_b16 a[18:19],v16 offset:2048\n"
"v_cvt_pk_bf16_f32 v58,v60,v61\n"
"v_mfma_f32_16x16x32_bf16 v[136:139],a[24:27],a[52:55],v[136:139]\n"
"s_mov_b32 s59,0x12000\n"
"ds_read_b64_tr_b16 a[20:21],v16 offset:16384\n"
"v_cvt_pk_bf16_f32 v59,v62,v63\n"
"v_mfma_f32_16x16x32_bf16 v[136:139],a[28:31],a[60:63],v[136:139]\n"
"s_waitcnt lgkmcnt(11)\n"
"ds_read_b64_tr_b16 a[22:23],v16 offset:18432\n"
"v_accvgpr_read_b32 v64,a200\n"
"v_mfma_f32_16x16x32_bf16 v[132:135],a[32:35],a[48:51],v[132:135]\n"
"ds_read_b64_tr_b16 a[24:25],v17 offset:0\n"
"buffer_store_dwordx4 v[56:59],v52,s[44:47],s59 offen offset:0 sc1\n"
"v_mfma_f32_16x16x32_bf16 v[132:135],a[36:39],a[56:59],v[132:135]\n"
"ds_read_b64_tr_b16 a[26:27],v17 offset:2048\n"
"v_accvgpr_read_b32 v65,a201\n"
"v_mfma_f32_16x16x32_bf16 v[140:143],a[32:35],a[52:55],v[140:143]\n"
"ds_read_b64_tr_b16 a[28:29],v17 offset:16384\n"
"v_accvgpr_read_b32 v66,a202\n"
"v_mfma_f32_16x16x32_bf16 v[140:143],a[36:39],a[60:63],v[140:143]\n"
"s_waitcnt lgkmcnt(11)\n"
"ds_read_b64_tr_b16 a[30:31],v17 offset:18432\n"
"v_accvgpr_read_b32 v67,a203\n"
"v_mfma_f32_16x16x32_bf16 v[144:147],a[0:3],a[48:51],v[144:147]\n"
"ds_read_b64_tr_b16 a[32:33],v18 offset:0\n"
"buffer_load_dwordx4 v33,s[64:67],s53 offen offset:1024 lds\n"
"v_mfma_f32_16x16x32_bf16 v[144:147],a[4:7],a[56:59],v[144:147]\n"
"ds_read_b64_tr_b16 a[34:35],v18 offset:2048\n"
"v_accvgpr_read_b32 v68,a204\n"
"v_mfma_f32_16x16x32_bf16 v[152:155],a[0:3],a[52:55],v[152:155]\n"
"ds_read_b64_tr_b16 a[36:37],v18 offset:16384\n"
"v_accvgpr_read_b32 v69,a205\n"
"v_mfma_f32_16x16x32_bf16 v[152:155],a[4:7],a[60:63],v[152:155]\n"
"s_waitcnt lgkmcnt(11)\n"
"ds_read_b64_tr_b16 a[38:39],v18 offset:18432\n"
"v_accvgpr_read_b32 v70,a206\n"
"v_mfma_f32_16x16x32_bf16 v[148:151],a[8:11],a[48:51],v[148:151]\n"
"ds_read_b64_tr_b16 a[0:1],v19 offset:0\n"
"buffer_load_dwordx4 v34,s[64:67],s53 offen offset:2048 lds\n"
"v_mfma_f32_16x16x32_bf16 v[148:151],a[12:15],a[56:59],v[148:151]\n"
"ds_read_b64_tr_b16 a[2:3],v19 offset:2048\n"
"v_accvgpr_read_b32 v71,a207\n"
"v_mfma_f32_16x16x32_bf16 v[156:159],a[8:11],a[52:55],v[156:159]\n"
"ds_read_b64_tr_b16 a[4:5],v19 offset:16384\n"
"v_cvt_pk_bf16_f32 v64,v64,v65\n"
"v_mfma_f32_16x16x32_bf16 v[156:159],a[12:15],a[60:63],v[156:159]\n"
"s_waitcnt lgkmcnt(11)\n"
"ds_read_b64_tr_b16 a[6:7],v19 offset:18432\n"
"v_cvt_pk_bf16_f32 v65,v66,v67\n"
"v_mfma_f32_16x16x32_bf16 v[160:163],a[16:19],a[48:51],v[160:163]\n"
"ds_read_b64_tr_b16 a[8:9],v12 offset:256\n"
"buffer_load_dwordx4 v35,s[64:67],s53 offen offset:3072 lds\n"
"v_mfma_f32_16x16x32_bf16 v[160:163],a[20:23],a[56:59],v[160:163]\n"
"ds_read_b64_tr_b16 a[10:11],v12 offset:2304\n"
"v_cvt_pk_bf16_f32 v66,v68,v69\n"
"v_mfma_f32_16x16x32_bf16 v[168:171],a[16:19],a[52:55],v[168:171]\n"
"ds_read_b64_tr_b16 a[12:13],v12 offset:16640\n"
"v_cvt_pk_bf16_f32 v67,v70,v71\n"
"v_accvgpr_read_b32 v56,a208\n"
"v_mfma_f32_16x16x32_bf16 v[168:171],a[20:23],a[60:63],v[168:171]\n"
"s_waitcnt lgkmcnt(11)\n"
"ds_read_b64_tr_b16 a[14:15],v12 offset:18688\n"
"v_accvgpr_read_b32 v57,a209\n"
"v_mfma_f32_16x16x32_bf16 v[164:167],a[24:27],a[48:51],v[164:167]\n"
"s_mov_b32 s59,0x12000\n"
"ds_read_b64_tr_b16 a[16:17],v13 offset:256\n"
"v_accvgpr_read_b32 v58,a210\n"
"v_mfma_f32_16x16x32_bf16 v[164:167],a[28:31],a[56:59],v[164:167]\n"
"ds_read_b64_tr_b16 a[18:19],v13 offset:2304\n"
"buffer_store_dwordx4 v[64:67],v52,s[44:47],s59 offen offset:1024 sc1\n"
"v_mfma_f32_16x16x32_bf16 v[172:175],a[24:27],a[52:55],v[172:175]\n"
"ds_read_b64_tr_b16 a[20:21],v13 offset:16640\n"
"v_add_u32 v12,0xffff0000,v12\n"
"v_mfma_f32_16x16x32_bf16 v[172:175],a[28:31],a[60:63],v[172:175]\n"
"s_waitcnt lgkmcnt(11)\n"
"ds_read_b64_tr_b16 a[22:23],v13 offset:18688\n"
"v_accvgpr_read_b32 v59,a211\n"
"v_mfma_f32_16x16x32_bf16 v[176:179],a[32:35],a[48:51],v[176:179]\n"
"s_add_u32 m0,s57,0x9000\n"
"ds_read_b64_tr_b16 a[24:25],v14 offset:256\n"
"v_accvgpr_read_b32 v60,a212\n"
"v_mfma_f32_16x16x32_bf16 v[176:179],a[36:39],a[56:59],v[176:179]\n"
"ds_read_b64_tr_b16 a[26:27],v14 offset:2304\n"
"buffer_load_dwordx4 v36,s[64:67],s53 offen offset:0 lds\n"
"v_mfma_f32_16x16x32_bf16 v[184:187],a[32:35],a[52:55],v[184:187]\n"
"ds_read_b64_tr_b16 a[28:29],v14 offset:16640\n"
"v_add_u32 v13,0xffff0000,v13\n"
"v_mfma_f32_16x16x32_bf16 v[184:187],a[36:39],a[60:63],v[184:187]\n"
"s_waitcnt lgkmcnt(11)\n"
"ds_read_b64_tr_b16 a[30:31],v14 offset:18688\n"
"v_accvgpr_read_b32 v61,a213\n"
"v_mfma_f32_16x16x32_bf16 v[180:183],a[0:3],a[48:51],v[180:183]\n"
"ds_read_b64_tr_b16 a[32:33],v15 offset:256\n"
"v_accvgpr_read_b32 v62,a214\n"
"v_mfma_f32_16x16x32_bf16 v[180:183],a[4:7],a[56:59],v[180:183]\n"
"ds_read_b64_tr_b16 a[34:35],v15 offset:2304\n"
"buffer_load_dwordx4 v37,s[64:67],s53 offen offset:1024 lds\n"
"v_mfma_f32_16x16x32_bf16 v[188:191],a[0:3],a[52:55],v[188:191]\n"
"ds_read_b64_tr_b16 a[36:37],v15 offset:16640\n"
"v_add_u32 v14,0xffff0000,v14\n"
"v_mfma_f32_16x16x32_bf16 v[188:191],a[4:7],a[60:63],v[188:191]\n"
"s_waitcnt lgkmcnt(11)\n"
"ds_read_b64_tr_b16 a[38:39],v15 offset:18688\n"
"v_accvgpr_read_b32 v63,a215\n"
"v_mfma_f32_16x16x32_bf16 v[192:195],a[8:11],a[48:51],v[192:195]\n"
"ds_read_b64_tr_b16 a[0:1],v16 offset:256\n"
"v_cvt_pk_bf16_f32 v56,v56,v57\n"
"v_mfma_f32_16x16x32_bf16 v[192:195],a[12:15],a[56:59],v[192:195]\n"
"ds_read_b64_tr_b16 a[2:3],v16 offset:2304\n"
"buffer_load_dwordx4 v38,s[64:67],s53 offen offset:2048 lds\n"
"v_mfma_f32_16x16x32_bf16 v[200:203],a[8:11],a[52:55],v[200:203]\n"
"ds_read_b64_tr_b16 a[4:5],v16 offset:16640\n"
"v_add_u32 v15,0xffff0000,v15\n"
"v_mfma_f32_16x16x32_bf16 v[200:203],a[12:15],a[60:63],v[200:203]\n"
"s_waitcnt lgkmcnt(11)\n"
"ds_read_b64_tr_b16 a[6:7],v16 offset:18688\n"
"v_cvt_pk_bf16_f32 v57,v58,v59\n"
"v_mfma_f32_16x16x32_bf16 v[196:199],a[16:19],a[48:51],v[196:199]\n"
"ds_read_b64_tr_b16 a[8:9],v17 offset:256\n"
"v_cvt_pk_bf16_f32 v58,v60,v61\n"
"v_mfma_f32_16x16x32_bf16 v[196:199],a[20:23],a[56:59],v[196:199]\n"
"ds_read_b64_tr_b16 a[10:11],v17 offset:2304\n"
"v_cvt_pk_bf16_f32 v59,v62,v63\n"
"v_accvgpr_read_b32 v64,a216\n"
"v_mfma_f32_16x16x32_bf16 v[204:207],a[16:19],a[52:55],v[204:207]\n"
"s_mov_b32 s59,0x12000\n"
"ds_read_b64_tr_b16 a[12:13],v17 offset:16640\n"
"v_add_u32 v16,0xffff0000,v16\n"
"v_mfma_f32_16x16x32_bf16 v[204:207],a[20:23],a[60:63],v[204:207]\n"
"s_waitcnt lgkmcnt(11)\n"
"ds_read_b64_tr_b16 a[14:15],v17 offset:18688\n"
"buffer_store_dwordx4 v[56:59],v52,s[44:47],s59 offen offset:2048 sc1\n"
"v_mfma_f32_16x16x32_bf16 v[208:211],a[24:27],a[48:51],v[208:211]\n"
"ds_read_b64_tr_b16 a[16:17],v18 offset:256\n"
"v_accvgpr_read_b32 v65,a217\n"
"v_mfma_f32_16x16x32_bf16 v[208:211],a[28:31],a[56:59],v[208:211]\n"
"ds_read_b64_tr_b16 a[18:19],v18 offset:2304\n"
"v_accvgpr_read_b32 v66,a218\n"
"v_mfma_f32_16x16x32_bf16 v[216:219],a[24:27],a[52:55],v[216:219]\n"
"ds_read_b64_tr_b16 a[20:21],v18 offset:16640\n"
"v_add_u32 v17,0xffff0000,v17\n"
"v_mfma_f32_16x16x32_bf16 v[216:219],a[28:31],a[60:63],v[216:219]\n"
"s_waitcnt lgkmcnt(11)\n"
"ds_read_b64_tr_b16 a[22:23],v18 offset:18688\n"
"buffer_load_dwordx4 v39,s[64:67],s53 offen offset:3072 lds\n"
"v_mfma_f32_16x16x32_bf16 v[212:215],a[32:35],a[48:51],v[212:215]\n"
"ds_read_b64_tr_b16 a[24:25],v19 offset:256\n"
"v_accvgpr_read_b32 v67,a219\n"
"v_mfma_f32_16x16x32_bf16 v[212:215],a[36:39],a[56:59],v[212:215]\n"
"s_add_u32 s36,s32,0x1400000\n"
"ds_read_b64_tr_b16 a[26:27],v19 offset:2304\n"
"v_accvgpr_read_b32 v68,a220\n"
"v_mfma_f32_16x16x32_bf16 v[220:223],a[32:35],a[52:55],v[220:223]\n"
"s_waitcnt vmcnt(38)\n"
"s_barrier\n"
"ds_read_b64_tr_b16 a[28:29],v19 offset:16640\n"
"v_add_u32 v18,0xffff0000,v18\n"
"v_mfma_f32_16x16x32_bf16 v[220:223],a[36:39],a[60:63],v[220:223]\n"
"s_waitcnt lgkmcnt(11)\n"
"ds_read_b64_tr_b16 a[30:31],v19 offset:18688\n"
"v_accvgpr_read_b32 v69,a221\n"
"v_mfma_f32_16x16x32_bf16 v[224:227],a[0:3],a[48:51],v[224:227]\n"
"s_addc_u32 s37,s33,0\n"
"ds_read_b64_tr_b16 a[32:33],v12 offset:0\n"
"v_accvgpr_read_b32 v70,a222\n"
"v_mfma_f32_16x16x32_bf16 v[224:227],a[4:7],a[56:59],v[224:227]\n"
"s_and_b32 s37,s37,0xffff\n"
"ds_read_b64_tr_b16 a[34:35],v12 offset:2048\n"
"v_accvgpr_read_b32 v71,a223\n"
"v_mfma_f32_16x16x32_bf16 v[232:235],a[0:3],a[52:55],v[232:235]\n"
"s_mov_b32 s38,0x2000000\n"
"ds_read_b64_tr_b16 a[36:37],v12 offset:16384\n"
"v_add_u32 v19,0xffff0000,v19\n"
"v_mfma_f32_16x16x32_bf16 v[232:235],a[4:7],a[60:63],v[232:235]\n"
"s_waitcnt lgkmcnt(11)\n"
"ds_read_b64_tr_b16 a[38:39],v12 offset:18432\n"
"v_cvt_pk_bf16_f32 v64,v64,v65\n"
"v_mfma_f32_16x16x32_bf16 v[228:231],a[8:11],a[48:51],v[228:231]\n"
"s_mov_b32 s39,0x20000\n"
"ds_read_b64_tr_b16 a[0:1],v13 offset:0\n"
"v_cvt_pk_bf16_f32 v65,v66,v67\n"
"v_lshlrev_b32 v8,4,v0\n"
"v_mfma_f32_16x16x32_bf16 v[228:231],a[12:15],a[56:59],v[228:231]\n"
"ds_read_b64_tr_b16 a[2:3],v13 offset:2048\n"
"v_cvt_pk_bf16_f32 v66,v68,v69\n"
"v_add_u32 v10,s57,v8\n"
"v_mfma_f32_16x16x32_bf16 v[236:239],a[8:11],a[52:55],v[236:239]\n"
"s_mov_b32 s59,0x12000\n"
"ds_read_b64_tr_b16 a[4:5],v13 offset:16384\n"
"v_cvt_pk_bf16_f32 v67,v70,v71\n"
"v_add_u32 v7,0x1000,v10\n"
"v_mfma_f32_16x16x32_bf16 v[236:239],a[12:15],a[60:63],v[236:239]\n"
"s_waitcnt lgkmcnt(11)\n"
"ds_read_b64_tr_b16 a[6:7],v13 offset:18432\n"
"buffer_store_dwordx4 v[64:67],v52,s[44:47],s59 offen offset:3072 sc1\n"
"v_mfma_f32_16x16x32_bf16 v[240:243],a[16:19],a[48:51],v[240:243]\n"
"ds_read_b64_tr_b16 a[8:9],v14 offset:0\n"
"v_accvgpr_read_b32 v56,a224\n"
"v_add_u32 v9,0x10000,v8\n"
"v_mfma_f32_16x16x32_bf16 v[240:243],a[20:23],a[56:59],v[240:243]\n"
"s_lshl_b32 s78,s92,21\n"
"ds_read_b64_tr_b16 a[10:11],v14 offset:2048\n"
"v_accvgpr_read_b32 v57,a225\n"
"v_mfma_f32_16x16x32_bf16 v[248:251],a[16:19],a[52:55],v[248:251]\n"
"s_cmp_lt_u32 s93,6\n"
"ds_read_b64_tr_b16 a[12:13],v14 offset:16384\n"
"v_accvgpr_read_b32 v58,a226\n"
"v_mfma_f32_16x16x32_bf16 v[248:251],a[20:23],a[60:63],v[248:251]\n"
"s_waitcnt lgkmcnt(11)\n"
"ds_read_b64_tr_b16 a[14:15],v14 offset:18432\n"
"v_accvgpr_read_b32 v59,a227\n"
"v_mfma_f32_16x16x32_bf16 v[244:247],a[24:27],a[48:51],v[244:247]\n"
"s_cselect_b32 s56,s94,2\n"
"ds_read_b64_tr_b16 a[16:17],v15 offset:0\n"
"v_accvgpr_read_b32 v60,a228\n"
"v_mfma_f32_16x16x32_bf16 v[244:247],a[28:31],a[56:59],v[244:247]\n"
"s_lshl_b32 s79,s54,1\n"
"ds_read_b64_tr_b16 a[18:19],v15 offset:2048\n"
"v_accvgpr_read_b32 v61,a229\n"
"v_mfma_f32_16x16x32_bf16 v[252:255],a[24:27],a[52:55],v[252:255]\n"
"s_waitcnt vmcnt(33)\n"
"ds_read_b64_tr_b16 a[20:21],v15 offset:16384\n"
"v_accvgpr_read_b32 v62,a230\n"
"v_mfma_f32_16x16x32_bf16 v[252:255],a[28:31],a[60:63],v[252:255]\n"
"s_waitcnt lgkmcnt(11)\n"
"ds_read_b64_tr_b16 a[22:23],v15 offset:18432\n"
"v_accvgpr_read_b32 v63,a231\n"
"v_mfma_f32_16x16x32_bf16 v[128:131],a[32:35],a[64:67],v[128:131]\n"
"s_add_u32 s79,s79,2\n"
"ds_read_b64_tr_b16 a[24:25],v16 offset:0\n"
"v_cvt_pk_bf16_f32 v56,v56,v57\n"
"v_mfma_f32_16x16x32_bf16 v[128:131],a[36:39],a[72:75],v[128:131]\n"
"s_add_u32 s55,s78,0x140000\n"
"ds_read_b64_tr_b16 a[26:27],v16 offset:2048\n"
"v_cvt_pk_bf16_f32 v57,v58,v59\n"
"v_mfma_f32_16x16x32_bf16 v[136:139],a[32:35],a[68:71],v[136:139]\n"
"s_cmp_eq_u32 s56,1\n"
"ds_read_b64_tr_b16 a[28:29],v16 offset:16384\n"
"v_cvt_pk_bf16_f32 v58,v60,v61\n"
"v_mfma_f32_16x16x32_bf16 v[136:139],a[36:39],a[76:79],v[136:139]\n"
"s_waitcnt lgkmcnt(11)\n"
"ds_read_b64_tr_b16 a[30:31],v16 offset:18432\n"
"v_cvt_pk_bf16_f32 v59,v62,v63\n"
"v_mfma_f32_16x16x32_bf16 v[132:135],a[0:3],a[64:67],v[132:135]\n"
"s_cselect_b32 s55,s55,s78\n"
"ds_read_b64_tr_b16 a[32:33],v17 offset:0\n"
"v_accvgpr_read_b32 v64,a232\n"
"v_mfma_f32_16x16x32_bf16 v[132:135],a[4:7],a[72:75],v[132:135]\n"
"s_cmp_eq_u32 s56,0\n"
"ds_read_b64_tr_b16 a[34:35],v17 offset:2048\n"
"v_accvgpr_read_b32 v65,a233\n"
"v_mfma_f32_16x16x32_bf16 v[140:143],a[0:3],a[68:71],v[140:143]\n"
"s_mov_b32 s59,0x13000\n"
"ds_read_b64_tr_b16 a[36:37],v17 offset:16384\n"
"v_accvgpr_read_b32 v66,a234\n"
"v_mfma_f32_16x16x32_bf16 v[140:143],a[4:7],a[76:79],v[140:143]\n"
"s_waitcnt lgkmcnt(11)\n"
"ds_read_b64_tr_b16 a[38:39],v17 offset:18432\n"
"v_accvgpr_read_b32 v67,a235\n"
"v_mfma_f32_16x16x32_bf16 v[144:147],a[8:11],a[64:67],v[144:147]\n"
"s_cselect_b32 s79,19,s79\n"
"ds_read_b64_tr_b16 a[0:1],v18 offset:0\n"
"buffer_store_dwordx4 v[56:59],v52,s[44:47],s59 offen offset:0 sc1\n"
"v_mfma_f32_16x16x32_bf16 v[144:147],a[12:15],a[72:75],v[144:147]\n"
"s_cselect_b32 s52,-1,1\n"
"ds_read_b64_tr_b16 a[2:3],v18 offset:2048\n"
"v_accvgpr_read_b32 v68,a236\n"
"v_mfma_f32_16x16x32_bf16 v[152:155],a[8:11],a[68:71],v[152:155]\n"
"s_cselect_b32 s58,1,0\n"
"ds_read_b64_tr_b16 a[4:5],v18 offset:16384\n"
"v_accvgpr_read_b32 v69,a237\n"
"v_mfma_f32_16x16x32_bf16 v[152:155],a[12:15],a[76:79],v[152:155]\n"
"s_waitcnt lgkmcnt(11)\n"
"ds_read_b64_tr_b16 a[6:7],v18 offset:18432\n"
"v_accvgpr_read_b32 v70,a238\n"
"v_mfma_f32_16x16x32_bf16 v[148:151],a[16:19],a[64:67],v[148:151]\n"
"s_lshl_b32 s90,s58,15\n"
"ds_read_b64_tr_b16 a[8:9],v19 offset:0\n"
"v_accvgpr_read_b32 v71,a239\n"
"v_mfma_f32_16x16x32_bf16 v[148:151],a[20:23],a[72:75],v[148:151]\n"
"s_xor_b32 s91,s90,0x18000\n"
"ds_read_b64_tr_b16 a[10:11],v19 offset:2048\n"
"v_cvt_pk_bf16_f32 v64,v64,v65\n"
"v_mfma_f32_16x16x32_bf16 v[156:159],a[16:19],a[68:71],v[156:159]\n"
"s_add_u32 s90,s90,0x10000\n"
"ds_read_b64_tr_b16 a[12:13],v19 offset:16384\n"
"v_cvt_pk_bf16_f32 v65,v66,v67\n"
"v_mfma_f32_16x16x32_bf16 v[156:159],a[20:23],a[76:79],v[156:159]\n"
"s_waitcnt lgkmcnt(11)\n"
"ds_read_b64_tr_b16 a[14:15],v19 offset:18432\n"
"v_cvt_pk_bf16_f32 v66,v68,v69\n"
"v_mfma_f32_16x16x32_bf16 v[160:163],a[24:27],a[64:67],v[160:163]\n"
"s_lshl_b32 s96,s92,7\n"
"ds_read_b64_tr_b16 a[16:17],v12 offset:256\n"
"v_cvt_pk_bf16_f32 v67,v70,v71\n"
"v_mfma_f32_16x16x32_bf16 v[160:163],a[28:31],a[72:75],v[160:163]\n"
"s_add_u32 s82,s96,0x2000\n"
"ds_read_b64_tr_b16 a[18:19],v12 offset:2304\n"
"v_mfma_f32_16x16x32_bf16 v[168:171],a[24:27],a[68:71],v[168:171]\n"
"s_add_u32 s82,s82,s32\n"
"ds_read_b64_tr_b16 a[20:21],v12 offset:16640\n"
"v_mfma_f32_16x16x32_bf16 v[168:171],a[28:31],a[76:79],v[168:171]\n"
"s_waitcnt lgkmcnt(11)\n"
"ds_read_b64_tr_b16 a[22:23],v12 offset:18688\n"
"v_accvgpr_read_b32 v56,a240\n"
"v_mfma_f32_16x16x32_bf16 v[164:167],a[32:35],a[64:67],v[164:167]\n"
"s_addc_u32 s83,s33,0\n"
"ds_read_b64_tr_b16 a[24:25],v13 offset:256\n"
"v_accvgpr_read_b32 v57,a241\n"
"v_and_b32 v5,7,v0\n"
"v_mfma_f32_16x16x32_bf16 v[164:167],a[36:39],a[72:75],v[164:167]\n"
"ds_read_b64_tr_b16 a[26:27],v13 offset:2304\n"
"v_accvgpr_read_b32 v58,a242\n"
"v_lshlrev_b32 v5,3,v5\n"
"v_mfma_f32_16x16x32_bf16 v[172:175],a[32:35],a[68:71],v[172:175]\n"
"s_add_u32 s49,s78,0x8000\n"
"ds_read_b64_tr_b16 a[28:29],v13 offset:16640\n"
"v_add_u32 v12,0xffff0000,v12\n"
"v_mfma_f32_16x16x32_bf16 v[172:175],a[36:39],a[76:79],v[172:175]\n"
"s_waitcnt lgkmcnt(11)\n"
"ds_read_b64_tr_b16 a[30:31],v13 offset:18688\n"
"v_accvgpr_read_b32 v59,a243\n"
"v_mfma_f32_16x16x32_bf16 v[176:179],a[0:3],a[64:67],v[176:179]\n"
"s_mov_b32 s59,0x13000\n"
"ds_read_b64_tr_b16 a[32:33],v14 offset:256\n"
"v_accvgpr_read_b32 v60,a244\n"
"v_mfma_f32_16x16x32_bf16 v[176:179],a[4:7],a[72:75],v[176:179]\n"
"s_add_u32 s48,s78,0x10000\n"
"ds_read_b64_tr_b16 a[34:35],v14 offset:2304\n"
"buffer_store_dwordx4 v[64:67],v52,s[44:47],s59 offen offset:1024 sc1\n"
"v_mfma_f32_16x16x32_bf16 v[184:187],a[0:3],a[68:71],v[184:187]\n"
"ds_read_b64_tr_b16 a[36:37],v14 offset:16640\n"
"v_add_u32 v13,0xffff0000,v13\n"
"v_mfma_f32_16x16x32_bf16 v[184:187],a[4:7],a[76:79],v[184:187]\n"
"s_waitcnt lgkmcnt(11)\n"
"ds_read_b64_tr_b16 a[38:39],v14 offset:18688\n"
"v_accvgpr_read_b32 v61,a245\n"
"v_mfma_f32_16x16x32_bf16 v[180:183],a[8:11],a[64:67],v[180:183]\n"
"ds_read_b64_tr_b16 a[0:1],v15 offset:256\n"
"v_accvgpr_read_b32 v62,a246\n"
"v_mfma_f32_16x16x32_bf16 v[180:183],a[12:15],a[72:75],v[180:183]\n"
"ds_read_b64_tr_b16 a[2:3],v15 offset:2304\n"
"v_accvgpr_read_b32 v63,a247\n"
"v_mfma_f32_16x16x32_bf16 v[188:191],a[8:11],a[68:71],v[188:191]\n"
"ds_read_b64_tr_b16 a[4:5],v15 offset:16640\n"
"v_add_u32 v14,0xffff0000,v14\n"
"v_mfma_f32_16x16x32_bf16 v[188:191],a[12:15],a[76:79],v[188:191]\n"
"s_waitcnt lgkmcnt(11)\n"
"ds_read_b64_tr_b16 a[6:7],v15 offset:18688\n"
"v_cvt_pk_bf16_f32 v56,v56,v57\n"
"v_mfma_f32_16x16x32_bf16 v[192:195],a[16:19],a[64:67],v[192:195]\n"
"ds_read_b64_tr_b16 a[8:9],v16 offset:256\n"
"v_cvt_pk_bf16_f32 v57,v58,v59\n"
"v_mfma_f32_16x16x32_bf16 v[192:195],a[20:23],a[72:75],v[192:195]\n"
"ds_read_b64_tr_b16 a[10:11],v16 offset:2304\n"
"v_cvt_pk_bf16_f32 v58,v60,v61\n"
"v_mfma_f32_16x16x32_bf16 v[200:203],a[16:19],a[68:71],v[200:203]\n"
"ds_read_b64_tr_b16 a[12:13],v16 offset:16640\n"
"v_add_u32 v15,0xffff0000,v15\n"
"v_mfma_f32_16x16x32_bf16 v[200:203],a[20:23],a[76:79],v[200:203]\n"
"s_waitcnt lgkmcnt(11)\n"
"ds_read_b64_tr_b16 a[14:15],v16 offset:18688\n"
"v_cvt_pk_bf16_f32 v59,v62,v63\n"
"v_mfma_f32_16x16x32_bf16 v[196:199],a[24:27],a[64:67],v[196:199]\n"
"ds_read_b64_tr_b16 a[16:17],v17 offset:256\n"
"v_accvgpr_read_b32 v64,a248\n"
"v_mfma_f32_16x16x32_bf16 v[196:199],a[28:31],a[72:75],v[196:199]\n"
"ds_read_b64_tr_b16 a[18:19],v17 offset:2304\n"
"v_accvgpr_read_b32 v65,a249\n"
"v_mfma_f32_16x16x32_bf16 v[204:207],a[24:27],a[68:71],v[204:207]\n"
"ds_read_b64_tr_b16 a[20:21],v17 offset:16640\n"
"v_add_u32 v16,0xffff0000,v16\n"
"v_mfma_f32_16x16x32_bf16 v[204:207],a[28:31],a[76:79],v[204:207]\n"
"s_waitcnt lgkmcnt(11)\n"
"ds_read_b64_tr_b16 a[22:23],v17 offset:18688\n"
"v_accvgpr_read_b32 v66,a250\n"
"v_mfma_f32_16x16x32_bf16 v[208:211],a[32:35],a[64:67],v[208:211]\n"
"ds_read_b64_tr_b16 a[24:25],v18 offset:256\n"
"v_accvgpr_read_b32 v67,a251\n"
"v_mfma_f32_16x16x32_bf16 v[208:211],a[36:39],a[72:75],v[208:211]\n"
"ds_read_b64_tr_b16 a[26:27],v18 offset:2304\n"
"v_accvgpr_read_b32 v68,a252\n"
"v_mfma_f32_16x16x32_bf16 v[216:219],a[32:35],a[68:71],v[216:219]\n"
"s_mov_b32 s59,0x13000\n"
"ds_read_b64_tr_b16 a[28:29],v18 offset:16640\n"
"v_add_u32 v17,0xffff0000,v17\n"
"v_mfma_f32_16x16x32_bf16 v[216:219],a[36:39],a[76:79],v[216:219]\n"
"s_waitcnt lgkmcnt(11)\n"
"ds_read_b64_tr_b16 a[30:31],v18 offset:18688\n"
"buffer_store_dwordx4 v[56:59],v52,s[44:47],s59 offen offset:2048 sc1\n"
"v_mfma_f32_16x16x32_bf16 v[212:215],a[0:3],a[64:67],v[212:215]\n"
"ds_read_b64_tr_b16 a[32:33],v19 offset:256\n"
"v_accvgpr_read_b32 v69,a253\n"
"v_mfma_f32_16x16x32_bf16 v[212:215],a[4:7],a[72:75],v[212:215]\n"
"ds_read_b64_tr_b16 a[34:35],v19 offset:2304\n"
"v_accvgpr_read_b32 v70,a254\n"
"v_mfma_f32_16x16x32_bf16 v[220:223],a[0:3],a[68:71],v[220:223]\n"
"s_waitcnt vmcnt(15)\n"
"s_barrier\n"
"ds_read_b64_tr_b16 a[36:37],v19 offset:16640\n"
"v_add_u32 v18,0xffff0000,v18\n"
"v_mfma_f32_16x16x32_bf16 v[220:223],a[4:7],a[76:79],v[220:223]\n"
"s_waitcnt lgkmcnt(11)\n"
"ds_read_b64_tr_b16 a[38:39],v19 offset:18688\n"
"v_accvgpr_read_b32 v71,a255\n"
"v_mfma_f32_16x16x32_bf16 v[224:227],a[8:11],a[64:67],v[224:227]\n"
"ds_read_b64_tr_b16 a[0:1],v12 offset:0\n"
"v_cvt_pk_bf16_f32 v64,v64,v65\n"
"v_mfma_f32_16x16x32_bf16 v[224:227],a[12:15],a[72:75],v[224:227]\n"
"ds_read_b64_tr_b16 a[2:3],v12 offset:2048\n"
"v_cvt_pk_bf16_f32 v65,v66,v67\n"
"v_mfma_f32_16x16x32_bf16 v[232:235],a[8:11],a[68:71],v[232:235]\n"
"ds_read_b64_tr_b16 a[4:5],v12 offset:16384\n"
"v_add_u32 v19,0xffff0000,v19\n"
"v_mfma_f32_16x16x32_bf16 v[232:235],a[12:15],a[76:79],v[232:235]\n"
"s_waitcnt lgkmcnt(11)\n"
"ds_read_b64_tr_b16 a[6:7],v12 offset:18432\n"
"v_cvt_pk_bf16_f32 v66,v68,v69\n"
"v_mfma_f32_16x16x32_bf16 v[228:231],a[16:19],a[64:67],v[228:231]\n"
"ds_read_b64_tr_b16 a[8:9],v13 offset:0\n"
"v_cvt_pk_bf16_f32 v67,v70,v71\n"
"v_mfma_f32_16x16x32_bf16 v[228:231],a[20:23],a[72:75],v[228:231]\n"
"ds_read_b64_tr_b16 a[10:11],v13 offset:2048\n"
"v_mfma_f32_16x16x32_bf16 v[236:239],a[16:19],a[68:71],v[236:239]\n"
"ds_read_b64_tr_b16 a[12:13],v13 offset:16384\n"
"v_mfma_f32_16x16x32_bf16 v[236:239],a[20:23],a[76:79],v[236:239]\n"
"s_waitcnt lgkmcnt(11)\n"
"ds_read_b64_tr_b16 a[14:15],v13 offset:18432\n"
"v_mfma_f32_16x16x32_bf16 v[240:243],a[24:27],a[64:67],v[240:243]\n"
"ds_read_b64_tr_b16 a[16:17],v14 offset:0\n"
"v_mfma_f32_16x16x32_bf16 v[240:243],a[28:31],a[72:75],v[240:243]\n"
"ds_read_b64_tr_b16 a[18:19],v14 offset:2048\n"
"v_mfma_f32_16x16x32_bf16 v[248:251],a[24:27],a[68:71],v[248:251]\n"
"s_mov_b32 s59,0x13000\n"
"ds_read_b64_tr_b16 a[20:21],v14 offset:16384\n"
"v_mfma_f32_16x16x32_bf16 v[248:251],a[28:31],a[76:79],v[248:251]\n"
"s_waitcnt lgkmcnt(11)\n"
"ds_read_b64_tr_b16 a[22:23],v14 offset:18432\n"
"buffer_store_dwordx4 v[64:67],v52,s[44:47],s59 offen offset:3072 sc1\n"
"v_mfma_f32_16x16x32_bf16 v[244:247],a[32:35],a[64:67],v[244:247]\n"
"ds_read_b64_tr_b16 a[24:25],v15 offset:0\n"
"v_mfma_f32_16x16x32_bf16 v[244:247],a[36:39],a[72:75],v[244:247]\n"
"ds_read_b64_tr_b16 a[26:27],v15 offset:2048\n"
"v_mfma_f32_16x16x32_bf16 v[252:255],a[32:35],a[68:71],v[252:255]\n"
"ds_read_b64_tr_b16 a[28:29],v15 offset:16384\n"
"v_mfma_f32_16x16x32_bf16 v[252:255],a[36:39],a[76:79],v[252:255]\n"
"s_waitcnt lgkmcnt(11)\n"
"ds_read_b64_tr_b16 a[30:31],v15 offset:18432\n"
"v_mfma_f32_16x16x32_bf16 v[128:131],a[0:3],a[80:83],v[128:131]\n"
"ds_read_b64_tr_b16 a[32:33],v16 offset:0\n"
"v_mfma_f32_16x16x32_bf16 v[128:131],a[4:7],a[88:91],v[128:131]\n"
"ds_read_b64_tr_b16 a[34:35],v16 offset:2048\n"
"v_mfma_f32_16x16x32_bf16 v[136:139],a[0:3],a[84:87],v[136:139]\n"
"ds_read_b64_tr_b16 a[36:37],v16 offset:16384\n"
"v_mfma_f32_16x16x32_bf16 v[136:139],a[4:7],a[92:95],v[136:139]\n"
"s_waitcnt lgkmcnt(11)\n"
"ds_read_b64_tr_b16 a[38:39],v16 offset:18432\n"
"v_mfma_f32_16x16x32_bf16 v[132:135],a[8:11],a[80:83],v[132:135]\n"
"ds_read_b64_tr_b16 a[0:1],v17 offset:0\n"
"v_mfma_f32_16x16x32_bf16 v[132:135],a[12:15],a[88:91],v[132:135]\n"
"ds_read_b64_tr_b16 a[2:3],v17 offset:2048\n"
"v_mfma_f32_16x16x32_bf16 v[140:143],a[8:11],a[84:87],v[140:143]\n"
"ds_read_b64_tr_b16 a[4:5],v17 offset:16384\n"
"v_mfma_f32_16x16x32_bf16 v[140:143],a[12:15],a[92:95],v[140:143]\n"
"s_waitcnt lgkmcnt(11)\n"
"ds_read_b64_tr_b16 a[6:7],v17 offset:18432\n"
"v_mfma_f32_16x16x32_bf16 v[144:147],a[16:19],a[80:83],v[144:147]\n"
"ds_read_b64_tr_b16 a[8:9],v18 offset:0\n"
"v_mfma_f32_16x16x32_bf16 v[144:147],a[20:23],a[88:91],v[144:147]\n"
"ds_read_b64_tr_b16 a[10:11],v18 offset:2048\n"
"v_mfma_f32_16x16x32_bf16 v[152:155],a[16:19],a[84:87],v[152:155]\n"
"ds_read_b64_tr_b16 a[12:13],v18 offset:16384\n"
"v_mfma_f32_16x16x32_bf16 v[152:155],a[20:23],a[92:95],v[152:155]\n"
"s_waitcnt lgkmcnt(11)\n"
"ds_read_b64_tr_b16 a[14:15],v18 offset:18432\n"
"v_mfma_f32_16x16x32_bf16 v[148:151],a[24:27],a[80:83],v[148:151]\n"
"ds_read_b64_tr_b16 a[16:17],v19 offset:0\n"
"v_mfma_f32_16x16x32_bf16 v[148:151],a[28:31],a[88:91],v[148:151]\n"
"ds_read_b64_tr_b16 a[18:19],v19 offset:2048\n"
"v_mfma_f32_16x16x32_bf16 v[156:159],a[24:27],a[84:87],v[156:159]\n"
"ds_read_b64_tr_b16 a[20:21],v19 offset:16384\n"
"v_mfma_f32_16x16x32_bf16 v[156:159],a[28:31],a[92:95],v[156:159]\n"
"s_waitcnt lgkmcnt(11)\n"
"ds_read_b64_tr_b16 a[22:23],v19 offset:18432\n"
"v_mfma_f32_16x16x32_bf16 v[160:163],a[32:35],a[80:83],v[160:163]\n"
"ds_read_b64_tr_b16 a[24:25],v12 offset:256\n"
"v_mfma_f32_16x16x32_bf16 v[160:163],a[36:39],a[88:91],v[160:163]\n"
"ds_read_b64_tr_b16 a[26:27],v12 offset:2304\n"
"v_mfma_f32_16x16x32_bf16 v[168:171],a[32:35],a[84:87],v[168:171]\n"
"ds_read_b64_tr_b16 a[28:29],v12 offset:16640\n"
"v_mfma_f32_16x16x32_bf16 v[168:171],a[36:39],a[92:95],v[168:171]\n"
"s_waitcnt lgkmcnt(11)\n"
"ds_read_b64_tr_b16 a[30:31],v12 offset:18688\n"
"v_mfma_f32_16x16x32_bf16 v[164:167],a[0:3],a[80:83],v[164:167]\n"
"ds_read_b64_tr_b16 a[32:33],v13 offset:256\n"
"v_mfma_f32_16x16x32_bf16 v[164:167],a[4:7],a[88:91],v[164:167]\n"
"ds_read_b64_tr_b16 a[34:35],v13 offset:2304\n"
"global_load_dwordx2 v[2:3],v5,s[82:83] sc1\n"
"v_mfma_f32_16x16x32_bf16 v[172:175],a[0:3],a[84:87],v[172:175]\n"
"ds_read_b64_tr_b16 a[36:37],v13 offset:16640\n"
"v_mfma_f32_16x16x32_bf16 v[172:175],a[4:7],a[92:95],v[172:175]\n"
"s_waitcnt lgkmcnt(11)\n"
"ds_read_b64_tr_b16 a[38:39],v13 offset:18688\n"
"v_mfma_f32_16x16x32_bf16 v[176:179],a[8:11],a[80:83],v[176:179]\n"
"ds_read_b64_tr_b16 a[0:1],v14 offset:256\n"
"v_mfma_f32_16x16x32_bf16 v[176:179],a[12:15],a[88:91],v[176:179]\n"
"ds_read_b64_tr_b16 a[2:3],v14 offset:2304\n"
"v_mfma_f32_16x16x32_bf16 v[184:187],a[8:11],a[84:87],v[184:187]\n"
"ds_read_b64_tr_b16 a[4:5],v14 offset:16640\n"
"v_mfma_f32_16x16x32_bf16 v[184:187],a[12:15],a[92:95],v[184:187]\n"
"s_waitcnt lgkmcnt(11)\n"
"ds_read_b64_tr_b16 a[6:7],v14 offset:18688\n"
"v_mfma_f32_16x16x32_bf16 v[180:183],a[16:19],a[80:83],v[180:183]\n"
"ds_read_b64_tr_b16 a[8:9],v15 offset:256\n"
"v_mfma_f32_16x16x32_bf16 v[180:183],a[20:23],a[88:91],v[180:183]\n"
"ds_read_b64_tr_b16 a[10:11],v15 offset:2304\n"
"v_mfma_f32_16x16x32_bf16 v[188:191],a[16:19],a[84:87],v[188:191]\n"
"ds_read_b64_tr_b16 a[12:13],v15 offset:16640\n"
"v_mfma_f32_16x16x32_bf16 v[188:191],a[20:23],a[92:95],v[188:191]\n"
"s_waitcnt lgkmcnt(11)\n"
"ds_read_b64_tr_b16 a[14:15],v15 offset:18688\n"
"v_mfma_f32_16x16x32_bf16 v[192:195],a[24:27],a[80:83],v[192:195]\n"
"ds_read_b64_tr_b16 a[16:17],v16 offset:256\n"
"v_mfma_f32_16x16x32_bf16 v[192:195],a[28:31],a[88:91],v[192:195]\n"
"ds_read_b64_tr_b16 a[18:19],v16 offset:2304\n"
"v_mfma_f32_16x16x32_bf16 v[200:203],a[24:27],a[84:87],v[200:203]\n"
"ds_read_b64_tr_b16 a[20:21],v16 offset:16640\n"
"v_mfma_f32_16x16x32_bf16 v[200:203],a[28:31],a[92:95],v[200:203]\n"
"s_waitcnt lgkmcnt(11)\n"
"ds_read_b64_tr_b16 a[22:23],v16 offset:18688\n"
"v_mfma_f32_16x16x32_bf16 v[196:199],a[32:35],a[80:83],v[196:199]\n"
"ds_read_b64_tr_b16 a[24:25],v17 offset:256\n"
"v_mfma_f32_16x16x32_bf16 v[196:199],a[36:39],a[88:91],v[196:199]\n"
"ds_read_b64_tr_b16 a[26:27],v17 offset:2304\n"
"v_mfma_f32_16x16x32_bf16 v[204:207],a[32:35],a[84:87],v[204:207]\n"
"ds_read_b64_tr_b16 a[28:29],v17 offset:16640\n"
"v_mfma_f32_16x16x32_bf16 v[204:207],a[36:39],a[92:95],v[204:207]\n"
"s_waitcnt lgkmcnt(11)\n"
"ds_read_b64_tr_b16 a[30:31],v17 offset:18688\n"
"v_mfma_f32_16x16x32_bf16 v[208:211],a[0:3],a[80:83],v[208:211]\n"
"ds_read_b64_tr_b16 a[32:33],v18 offset:256\n"
"v_mfma_f32_16x16x32_bf16 v[208:211],a[4:7],a[88:91],v[208:211]\n"
"ds_read_b64_tr_b16 a[34:35],v18 offset:2304\n"
"v_mfma_f32_16x16x32_bf16 v[216:219],a[0:3],a[84:87],v[216:219]\n"
"ds_read_b64_tr_b16 a[36:37],v18 offset:16640\n"
"v_mfma_f32_16x16x32_bf16 v[216:219],a[4:7],a[92:95],v[216:219]\n"
"s_waitcnt lgkmcnt(11)\n"
"ds_read_b64_tr_b16 a[38:39],v18 offset:18688\n"
"v_mfma_f32_16x16x32_bf16 v[212:215],a[8:11],a[80:83],v[212:215]\n"
"ds_read_b64_tr_b16 a[0:1],v19 offset:256\n"
"v_mfma_f32_16x16x32_bf16 v[212:215],a[12:15],a[88:91],v[212:215]\n"
"ds_read_b64_tr_b16 a[2:3],v19 offset:2304\n"
"v_mfma_f32_16x16x32_bf16 v[220:223],a[8:11],a[84:87],v[220:223]\n"
"s_waitcnt vmcnt(1)\n"
"s_barrier\n"
"ds_read_b64_tr_b16 a[4:5],v19 offset:16640\n"
"v_mfma_f32_16x16x32_bf16 v[220:223],a[12:15],a[92:95],v[220:223]\n"
"s_waitcnt lgkmcnt(11)\n"
"s_branch L_pubkvv_%=\n"
"L_pubkvv_ret_%=:\n"
"ds_read_b64_tr_b16 a[6:7],v19 offset:18688\n"
"v_mfma_f32_16x16x32_bf16 v[224:227],a[16:19],a[80:83],v[224:227]\n"
"s_waitcnt vmcnt(0)\n"
"ds_read_b64_tr_b16 a[8:9],v12 offset:32768\n"
"v_mfma_f32_16x16x32_bf16 v[224:227],a[20:23],a[88:91],v[224:227]\n"
"ds_read_b64_tr_b16 a[10:11],v12 offset:34816\n"
"v_cmp_ne_u64 vcc,s[34:35],v[2:3]\n"
"v_mfma_f32_16x16x32_bf16 v[232:235],a[16:19],a[84:87],v[232:235]\n"
"ds_read_b64_tr_b16 a[12:13],v12 offset:49152\n"
"v_mfma_f32_16x16x32_bf16 v[232:235],a[20:23],a[92:95],v[232:235]\n"
"s_waitcnt lgkmcnt(11)\n"
"ds_read_b64_tr_b16 a[14:15],v12 offset:51200\n"
"v_mfma_f32_16x16x32_bf16 v[228:231],a[24:27],a[80:83],v[228:231]\n"
"s_cbranch_vccnz L_kfv_%=\n"
"L_kfv_ret_%=:\n"
"ds_read_b64_tr_b16 a[16:17],v13 offset:32768\n"
"v_mfma_f32_16x16x32_bf16 v[228:231],a[28:31],a[88:91],v[228:231]\n"
"ds_read_b64_tr_b16 a[18:19],v13 offset:34816\n"
"v_mfma_f32_16x16x32_bf16 v[236:239],a[24:27],a[84:87],v[236:239]\n"
"s_add_u32 m0,s57,s90\n"
"ds_read_b64_tr_b16 a[20:21],v13 offset:49152\n"
"v_mfma_f32_16x16x32_bf16 v[236:239],a[28:31],a[92:95],v[236:239]\n"
"s_waitcnt lgkmcnt(11)\n"
"ds_read_b64_tr_b16 a[22:23],v13 offset:51200\n"
"buffer_load_dwordx4 v10,s[36:39],s78 offen offset:0 sc1 lds\n"
"v_mfma_f32_16x16x32_bf16 v[240:243],a[32:35],a[80:83],v[240:243]\n"
"ds_read_b64_tr_b16 a[24:25],v14 offset:32768\n"
"v_mfma_f32_16x16x32_bf16 v[240:243],a[36:39],a[88:91],v[240:243]\n"
"ds_read_b64_tr_b16 a[26:27],v14 offset:34816\n"
"v_mfma_f32_16x16x32_bf16 v[248:251],a[32:35],a[84:87],v[248:251]\n"
"ds_read_b64_tr_b16 a[28:29],v14 offset:49152\n"
"v_mfma_f32_16x16x32_bf16 v[248:251],a[36:39],a[92:95],v[248:251]\n"
"s_waitcnt lgkmcnt(11)\n"
"ds_read_b64_tr_b16 a[30:31],v14 offset:51200\n"
"buffer_load_dwordx4 v10,s[36:39],s78 offen offset:1024 sc1 lds\n"
"v_mfma_f32_16x16x32_bf16 v[244:247],a[0:3],a[80:83],v[244:247]\n"
"ds_read_b64_tr_b16 a[32:33],v15 offset:32768\n"
"v_mfma_f32_16x16x32_bf16 v[244:247],a[4:7],a[88:91],v[244:247]\n"
"ds_read_b64_tr_b16 a[34:35],v15 offset:34816\n"
"v_mfma_f32_16x16x32_bf16 v[252:255],a[0:3],a[84:87],v[252:255]\n"
"ds_read_b64_tr_b16 a[36:37],v15 offset:49152\n"
"v_mfma_f32_16x16x32_bf16 v[252:255],a[4:7],a[92:95],v[252:255]\n"
"s_waitcnt lgkmcnt(11)\n"
"ds_read_b64_tr_b16 a[38:39],v15 offset:51200\n"
"buffer_load_dwordx4 v10,s[36:39],s78 offen offset:2048 sc1 lds\n"
"v_mfma_f32_16x16x32_bf16 v[128:131],a[8:11],a[96:99],v[128:131]\n"
"ds_read_b64_tr_b16 a[0:1],v16 offset:32768\n"
"v_mfma_f32_16x16x32_bf16 v[128:131],a[12:15],a[104:107],v[128:131]\n"
"ds_read_b64_tr_b16 a[2:3],v16 offset:34816\n"
"v_mfma_f32_16x16x32_bf16 v[136:139],a[8:11],a[100:103],v[136:139]\n"
"ds_read_b64_tr_b16 a[4:5],v16 offset:49152\n"
"v_mfma_f32_16x16x32_bf16 v[136:139],a[12:15],a[108:111],v[136:139]\n"
"s_waitcnt lgkmcnt(11)\n"
"ds_read_b64_tr_b16 a[6:7],v16 offset:51200\n"
"buffer_load_dwordx4 v10,s[36:39],s78 offen offset:3072 sc1 lds\n"
"v_mfma_f32_16x16x32_bf16 v[132:135],a[16:19],a[96:99],v[132:135]\n"
"ds_read_b64_tr_b16 a[8:9],v17 offset:32768\n"
"v_mfma_f32_16x16x32_bf16 v[132:135],a[20:23],a[104:107],v[132:135]\n"
"ds_read_b64_tr_b16 a[10:11],v17 offset:34816\n"
"v_mfma_f32_16x16x32_bf16 v[140:143],a[16:19],a[100:103],v[140:143]\n"
"s_add_u32 m0,m0,0x1000\n"
"ds_read_b64_tr_b16 a[12:13],v17 offset:49152\n"
"v_mfma_f32_16x16x32_bf16 v[140:143],a[20:23],a[108:111],v[140:143]\n"
"s_waitcnt lgkmcnt(11)\n"
"ds_read_b64_tr_b16 a[14:15],v17 offset:51200\n"
"buffer_load_dwordx4 v7,s[36:39],s78 offen offset:0 sc1 lds\n"
"v_mfma_f32_16x16x32_bf16 v[144:147],a[24:27],a[96:99],v[144:147]\n"
"ds_read_b64_tr_b16 a[16:17],v18 offset:32768\n"
"v_mfma_f32_16x16x32_bf16 v[144:147],a[28:31],a[104:107],v[144:147]\n"
"ds_read_b64_tr_b16 a[18:19],v18 offset:34816\n"
"v_cvt_pk_bf16_f32 v128,v128,v129\n"
"v_mfma_f32_16x16x32_bf16 v[152:155],a[24:27],a[100:103],v[152:155]\n"
"ds_read_b64_tr_b16 a[20:21],v18 offset:49152\n"
"v_cvt_pk_bf16_f32 v129,v130,v131\n"
"v_mfma_f32_16x16x32_bf16 v[152:155],a[28:31],a[108:111],v[152:155]\n"
"s_waitcnt lgkmcnt(11)\n"
"ds_read_b64_tr_b16 a[22:23],v18 offset:51200\n"
"buffer_load_dwordx4 v7,s[36:39],s78 offen offset:1024 sc1 lds\n"
"v_mfma_f32_16x16x32_bf16 v[148:151],a[32:35],a[96:99],v[148:151]\n"
"ds_read_b64_tr_b16 a[24:25],v19 offset:32768\n"
"v_cvt_pk_bf16_f32 v130,v132,v133\n"
"v_mfma_f32_16x16x32_bf16 v[148:151],a[36:39],a[104:107],v[148:151]\n"
"ds_read_b64_tr_b16 a[26:27],v19 offset:34816\n"
"v_cvt_pk_bf16_f32 v131,v134,v135\n"
"v_mfma_f32_16x16x32_bf16 v[156:159],a[32:35],a[100:103],v[156:159]\n"
"ds_read_b64_tr_b16 a[28:29],v19 offset:49152\n"
"v_accvgpr_write_b32 a128,v128\n"
"v_mfma_f32_16x16x32_bf16 v[156:159],a[36:39],a[108:111],v[156:159]\n"
"s_waitcnt lgkmcnt(11)\n"
"ds_read_b64_tr_b16 a[30:31],v19 offset:51200\n"
"buffer_load_dwordx4 v7,s[36:39],s78 offen offset:2048 sc1 lds\n"
"v_mfma_f32_16x16x32_bf16 v[160:163],a[0:3],a[96:99],v[160:163]\n"
"ds_read_b64_tr_b16 a[32:33],v12 offset:33024\n"
"v_accvgpr_write_b32 a129,v129\n"
"v_mfma_f32_16x16x32_bf16 v[160:163],a[4:7],a[104:107],v[160:163]\n"
"ds_read_b64_tr_b16 a[34:35],v12 offset:35072\n"
"v_accvgpr_write_b32 a130,v130\n"
"v_mfma_f32_16x16x32_bf16 v[168:171],a[0:3],a[100:103],v[168:171]\n"
"ds_read_b64_tr_b16 a[36:37],v12 offset:49408\n"
"v_accvgpr_write_b32 a131,v131\n"
"v_mfma_f32_16x16x32_bf16 v[168:171],a[4:7],a[108:111],v[168:171]\n"
"s_waitcnt lgkmcnt(11)\n"
"ds_read_b64_tr_b16 a[38:39],v12 offset:51456\n"
"buffer_load_dwordx4 v7,s[36:39],s78 offen offset:3072 sc1 lds\n"
"v_mfma_f32_16x16x32_bf16 v[164:167],a[8:11],a[96:99],v[164:167]\n"
"ds_read_b64_tr_b16 a[0:1],v13 offset:33024\n"
"v_cvt_pk_bf16_f32 v136,v136,v137\n"
"v_mfma_f32_16x16x32_bf16 v[164:167],a[12:15],a[104:107],v[164:167]\n"
"ds_read_b64_tr_b16 a[2:3],v13 offset:35072\n"
"v_cvt_pk_bf16_f32 v137,v138,v139\n"
"v_mfma_f32_16x16x32_bf16 v[172:175],a[8:11],a[100:103],v[172:175]\n"
"s_add_u32 m0,s57,s91\n"
"ds_read_b64_tr_b16 a[4:5],v13 offset:49408\n"
"v_cvt_pk_bf16_f32 v138,v140,v141\n"
"v_mfma_f32_16x16x32_bf16 v[172:175],a[12:15],a[108:111],v[172:175]\n"
"s_waitcnt lgkmcnt(11)\n"
"ds_read_b64_tr_b16 a[6:7],v13 offset:51456\n"
"buffer_load_dwordx4 v10,s[36:39],s48 offen offset:0 sc1 lds\n"
"v_mfma_f32_16x16x32_bf16 v[176:179],a[16:19],a[96:99],v[176:179]\n"
"ds_read_b64_tr_b16 a[8:9],v14 offset:33024\n"
"v_cvt_pk_bf16_f32 v139,v142,v143\n"
"v_mfma_f32_16x16x32_bf16 v[176:179],a[20:23],a[104:107],v[176:179]\n"
"ds_read_b64_tr_b16 a[10:11],v14 offset:35072\n"
"v_accvgpr_write_b32 a160,v136\n"
"v_mfma_f32_16x16x32_bf16 v[184:187],a[16:19],a[100:103],v[184:187]\n"
"ds_read_b64_tr_b16 a[12:13],v14 offset:49408\n"
"v_accvgpr_write_b32 a161,v137\n"
"v_mfma_f32_16x16x32_bf16 v[184:187],a[20:23],a[108:111],v[184:187]\n"
"s_waitcnt lgkmcnt(11)\n"
"ds_read_b64_tr_b16 a[14:15],v14 offset:51456\n"
"buffer_load_dwordx4 v10,s[36:39],s48 offen offset:1024 sc1 lds\n"
"v_mfma_f32_16x16x32_bf16 v[180:183],a[24:27],a[96:99],v[180:183]\n"
"ds_read_b64_tr_b16 a[16:17],v15 offset:33024\n"
"v_accvgpr_write_b32 a162,v138\n"
"v_mfma_f32_16x16x32_bf16 v[180:183],a[28:31],a[104:107],v[180:183]\n"
"ds_read_b64_tr_b16 a[18:19],v15 offset:35072\n"
"v_accvgpr_write_b32 a163,v139\n"
"v_mfma_f32_16x16x32_bf16 v[188:191],a[24:27],a[100:103],v[188:191]\n"
"ds_read_b64_tr_b16 a[20:21],v15 offset:49408\n"
"v_cvt_pk_bf16_f32 v144,v144,v145\n"
"v_mfma_f32_16x16x32_bf16 v[188:191],a[28:31],a[108:111],v[188:191]\n"
"s_waitcnt lgkmcnt(11)\n"
"ds_read_b64_tr_b16 a[22:23],v15 offset:51456\n"
"buffer_load_dwordx4 v10,s[36:39],s48 offen offset:2048 sc1 lds\n"
"v_mfma_f32_16x16x32_bf16 v[192:195],a[32:35],a[96:99],v[192:195]\n"
"ds_read_b64_tr_b16 a[24:25],v16 offset:33024\n"
"v_cvt_pk_bf16_f32 v145,v146,v147\n"
"v_mfma_f32_16x16x32_bf16 v[192:195],a[36:39],a[104:107],v[192:195]\n"
"ds_read_b64_tr_b16 a[26:27],v16 offset:35072\n"
"v_cvt_pk_bf16_f32 v146,v148,v149\n"
"v_mfma_f32_16x16x32_bf16 v[200:203],a[32:35],a[100:103],v[200:203]\n"
"ds_read_b64_tr_b16 a[28:29],v16 offset:49408\n"
"v_cvt_pk_bf16_f32 v147,v150,v151\n"
"v_mfma_f32_16x16x32_bf16 v[200:203],a[36:39],a[108:111],v[200:203]\n"
"s_waitcnt lgkmcnt(11)\n"
"ds_read_b64_tr_b16 a[30:31],v16 offset:51456\n"
"buffer_load_dwordx4 v10,s[36:39],s48 offen offset:3072 sc1 lds\n"
"v_mfma_f32_16x16x32_bf16 v[196:199],a[0:3],a[96:99],v[196:199]\n"
"ds_read_b64_tr_b16 a[32:33],v17 offset:33024\n"
"v_accvgpr_write_b32 a132,v144\n"
"v_mfma_f32_16x16x32_bf16 v[196:199],a[4:7],a[104:107],v[196:199]\n"
"ds_read_b64_tr_b16 a[34:35],v17 offset:35072\n"
"v_accvgpr_write_b32 a133,v145\n"
"v_mfma_f32_16x16x32_bf16 v[204:207],a[0:3],a[100:103],v[204:207]\n"
"s_add_u32 m0,m0,0x1000\n"
"ds_read_b64_tr_b16 a[36:37],v17 offset:49408\n"
"v_accvgpr_write_b32 a134,v146\n"
"v_mfma_f32_16x16x32_bf16 v[204:207],a[4:7],a[108:111],v[204:207]\n"
"s_waitcnt lgkmcnt(11)\n"
"ds_read_b64_tr_b16 a[38:39],v17 offset:51456\n"
"buffer_load_dwordx4 v7,s[36:39],s48 offen offset:0 sc1 lds\n"
"v_mfma_f32_16x16x32_bf16 v[208:211],a[8:11],a[96:99],v[208:211]\n"
"ds_read_b64_tr_b16 a[0:1],v18 offset:33024\n"
"v_accvgpr_write_b32 a135,v147\n"
"v_mfma_f32_16x16x32_bf16 v[208:211],a[12:15],a[104:107],v[208:211]\n"
"ds_read_b64_tr_b16 a[2:3],v18 offset:35072\n"
"v_cvt_pk_bf16_f32 v152,v152,v153\n"
"v_mfma_f32_16x16x32_bf16 v[216:219],a[8:11],a[100:103],v[216:219]\n"
"ds_read_b64_tr_b16 a[4:5],v18 offset:49408\n"
"v_cvt_pk_bf16_f32 v153,v154,v155\n"
"v_mfma_f32_16x16x32_bf16 v[216:219],a[12:15],a[108:111],v[216:219]\n"
"s_waitcnt lgkmcnt(11)\n"
"ds_read_b64_tr_b16 a[6:7],v18 offset:51456\n"
"buffer_load_dwordx4 v7,s[36:39],s48 offen offset:1024 sc1 lds\n"
"v_mfma_f32_16x16x32_bf16 v[212:215],a[16:19],a[96:99],v[212:215]\n"
"ds_read_b64_tr_b16 a[8:9],v19 offset:33024\n"
"v_cvt_pk_bf16_f32 v154,v156,v157\n"
"v_mfma_f32_16x16x32_bf16 v[212:215],a[20:23],a[104:107],v[212:215]\n"
"ds_read_b64_tr_b16 a[10:11],v19 offset:35072\n"
"v_cvt_pk_bf16_f32 v155,v158,v159\n"
"v_mfma_f32_16x16x32_bf16 v[220:223],a[16:19],a[100:103],v[220:223]\n"
"ds_read_b64_tr_b16 a[12:13],v19 offset:49408\n"
"v_accvgpr_write_b32 a164,v152\n"
"v_mfma_f32_16x16x32_bf16 v[220:223],a[20:23],a[108:111],v[220:223]\n"
"s_waitcnt lgkmcnt(11)\n"
"ds_read_b64_tr_b16 a[14:15],v19 offset:51456\n"
"buffer_load_dwordx4 v7,s[36:39],s48 offen offset:2048 sc1 lds\n"
"v_mfma_f32_16x16x32_bf16 v[224:227],a[24:27],a[96:99],v[224:227]\n"
"v_accvgpr_write_b32 a165,v153\n"
"v_accvgpr_write_b32 a166,v154\n"
"v_mfma_f32_16x16x32_bf16 v[224:227],a[28:31],a[104:107],v[224:227]\n"
"v_accvgpr_write_b32 a167,v155\n"
"v_cvt_pk_bf16_f32 v160,v160,v161\n"
"v_mfma_f32_16x16x32_bf16 v[232:235],a[24:27],a[100:103],v[232:235]\n"
"v_cvt_pk_bf16_f32 v161,v162,v163\n"
"v_cvt_pk_bf16_f32 v162,v164,v165\n"
"v_mfma_f32_16x16x32_bf16 v[232:235],a[28:31],a[108:111],v[232:235]\n"
"s_waitcnt lgkmcnt(8)\n"
"buffer_load_dwordx4 v7,s[36:39],s48 offen offset:3072 sc1 lds\n"
"v_mfma_f32_16x16x32_bf16 v[228:231],a[32:35],a[96:99],v[228:231]\n"
"v_cvt_pk_bf16_f32 v163,v166,v167\n"
"v_accvgpr_write_b32 a136,v160\n"
"v_mfma_f32_16x16x32_bf16 v[228:231],a[36:39],a[104:107],v[228:231]\n"
"v_accvgpr_write_b32 a137,v161\n"
"v_accvgpr_write_b32 a138,v162\n"
"v_mfma_f32_16x16x32_bf16 v[236:239],a[32:35],a[100:103],v[236:239]\n"
"v_accvgpr_write_b32 a139,v163\n"
"v_cvt_pk_bf16_f32 v168,v168,v169\n"
"v_mfma_f32_16x16x32_bf16 v[236:239],a[36:39],a[108:111],v[236:239]\n"
"s_waitcnt lgkmcnt(4)\n"
"v_cvt_pk_bf16_f32 v169,v170,v171\n"
"v_cvt_pk_bf16_f32 v170,v172,v173\n"
"v_mfma_f32_16x16x32_bf16 v[240:243],a[0:3],a[96:99],v[240:243]\n"
"v_cvt_pk_bf16_f32 v171,v174,v175\n"
"v_accvgpr_write_b32 a168,v168\n"
"v_mfma_f32_16x16x32_bf16 v[240:243],a[4:7],a[104:107],v[240:243]\n"
"v_accvgpr_write_b32 a169,v169\n"
"v_accvgpr_write_b32 a170,v170\n"
"v_mfma_f32_16x16x32_bf16 v[248:251],a[0:3],a[100:103],v[248:251]\n"
"v_accvgpr_write_b32 a171,v171\n"
"v_cvt_pk_bf16_f32 v176,v176,v177\n"
"v_mfma_f32_16x16x32_bf16 v[248:251],a[4:7],a[108:111],v[248:251]\n"
"s_waitcnt lgkmcnt(0)\n"
"v_cvt_pk_bf16_f32 v177,v178,v179\n"
"v_cvt_pk_bf16_f32 v178,v180,v181\n"
"v_mfma_f32_16x16x32_bf16 v[244:247],a[8:11],a[96:99],v[244:247]\n"
"v_cvt_pk_bf16_f32 v179,v182,v183\n"
"v_accvgpr_write_b32 a140,v176\n"
"v_mfma_f32_16x16x32_bf16 v[244:247],a[12:15],a[104:107],v[244:247]\n"
"v_accvgpr_write_b32 a141,v177\n"
"v_accvgpr_write_b32 a142,v178\n"
"v_mfma_f32_16x16x32_bf16 v[252:255],a[8:11],a[100:103],v[252:255]\n"
"v_accvgpr_write_b32 a143,v179\n"
"v_cvt_pk_bf16_f32 v184,v184,v185\n"
"v_mfma_f32_16x16x32_bf16 v[252:255],a[12:15],a[108:111],v[252:255]\n"
"v_cvt_pk_bf16_f32 v185,v186,v187\n"
"v_cvt_pk_bf16_f32 v186,v188,v189\n"
"v_cvt_pk_bf16_f32 v187,v190,v191\n"
"v_accvgpr_write_b32 a172,v184\n"
"v_accvgpr_write_b32 a173,v185\n"
"v_accvgpr_write_b32 a174,v186\n"
"v_accvgpr_write_b32 a175,v187\n"
"v_cvt_pk_bf16_f32 v192,v192,v193\n"
"v_cvt_pk_bf16_f32 v193,v194,v195\n"
"v_cvt_pk_bf16_f32 v194,v196,v197\n"
"v_cvt_pk_bf16_f32 v195,v198,v199\n"
"v_accvgpr_write_b32 a144,v192\n"
"v_accvgpr_write_b32 a145,v193\n"
"v_accvgpr_write_b32 a146,v194\n"
"v_accvgpr_write_b32 a147,v195\n"
"v_cvt_pk_bf16_f32 v200,v200,v201\n"
"v_cvt_pk_bf16_f32 v201,v202,v203\n"
"v_cvt_pk_bf16_f32 v202,v204,v205\n"
"v_cvt_pk_bf16_f32 v203,v206,v207\n"
"v_accvgpr_write_b32 a176,v200\n"
"v_accvgpr_write_b32 a177,v201\n"
"v_accvgpr_write_b32 a178,v202\n"
"v_accvgpr_write_b32 a179,v203\n"
"v_cvt_pk_bf16_f32 v208,v208,v209\n"
"v_cvt_pk_bf16_f32 v209,v210,v211\n"
"v_cvt_pk_bf16_f32 v210,v212,v213\n"
"v_cvt_pk_bf16_f32 v211,v214,v215\n"
"v_accvgpr_write_b32 a148,v208\n"
"v_accvgpr_write_b32 a149,v209\n"
"v_accvgpr_write_b32 a150,v210\n"
"v_accvgpr_write_b32 a151,v211\n"
"v_cvt_pk_bf16_f32 v216,v216,v217\n"
"v_cvt_pk_bf16_f32 v217,v218,v219\n"
"v_cvt_pk_bf16_f32 v218,v220,v221\n"
"v_cvt_pk_bf16_f32 v219,v222,v223\n"
"v_accvgpr_write_b32 a180,v216\n"
"v_accvgpr_write_b32 a181,v217\n"
"v_accvgpr_write_b32 a182,v218\n"
"v_accvgpr_write_b32 a183,v219\n"
"v_cvt_pk_bf16_f32 v224,v224,v225\n"
"v_cvt_pk_bf16_f32 v225,v226,v227\n"
"v_cvt_pk_bf16_f32 v226,v228,v229\n"
"v_cvt_pk_bf16_f32 v227,v230,v231\n"
"v_accvgpr_write_b32 a152,v224\n"
"v_accvgpr_write_b32 a153,v225\n"
"v_accvgpr_write_b32 a154,v226\n"
"v_accvgpr_write_b32 a155,v227\n"
"v_cvt_pk_bf16_f32 v232,v232,v233\n"
"v_cvt_pk_bf16_f32 v233,v234,v235\n"
"v_cvt_pk_bf16_f32 v234,v236,v237\n"
"v_cvt_pk_bf16_f32 v235,v238,v239\n"
"v_accvgpr_write_b32 a184,v232\n"
"v_accvgpr_write_b32 a185,v233\n"
"v_accvgpr_write_b32 a186,v234\n"
"v_accvgpr_write_b32 a187,v235\n"
"v_cvt_pk_bf16_f32 v240,v240,v241\n"
"v_cvt_pk_bf16_f32 v241,v242,v243\n"
"v_cvt_pk_bf16_f32 v242,v244,v245\n"
"v_cvt_pk_bf16_f32 v243,v246,v247\n"
"v_accvgpr_write_b32 a156,v240\n"
"v_accvgpr_write_b32 a157,v241\n"
"v_accvgpr_write_b32 a158,v242\n"
"v_accvgpr_write_b32 a159,v243\n"
"v_cvt_pk_bf16_f32 v248,v248,v249\n"
"v_cvt_pk_bf16_f32 v249,v250,v251\n"
"v_cvt_pk_bf16_f32 v250,v252,v253\n"
"v_cvt_pk_bf16_f32 v251,v254,v255\n"
"v_accvgpr_write_b32 a188,v248\n"
"v_accvgpr_write_b32 a189,v249\n"
"v_accvgpr_write_b32 a190,v250\n"
"v_accvgpr_write_b32 a191,v251\n"
"L_qdone_%=:\n"
"s_add_u32 s48,s48,0x10000\n"
"s_mul_i32 s96,s92,229376\n"
"s_add_u32 s40,s22,s96\n"
"s_addc_u32 s41,s23,0\n"
"s_and_b32 s41,s41,0xffff\n"
"s_mov_b32 s42,0x38000\n"
"s_mov_b32 s43,0x20000\n"
"v_lshrrev_b32 v1,3,v0\n"
"v_lshrrev_b32 v6,4,v0\n"
"v_bfe_u32 v11,v0,1,2\n"
"v_xor_b32 v11,v11,v6\n"
"v_and_b32 v4,1,v0\n"
"v_lshlrev_b32 v11,5,v11\n"
"v_lshl_add_u32 v11,v4,4,v11\n"
"s_lshl_b32 s96,s11,6\n"
"v_add_u32 v4,s96,v1\n"
"v_mul_u32_u24 v4,896,v4\n"
"v_add_u32 v4,v4,v11\n"
"v_add_u32 v32,0x0,v4\n"
"v_add_u32 v33,0x1800,v4\n"
"v_add_u32 v34,0x3000,v4\n"
"v_add_u32 v35,0x4800,v4\n"
"v_add_u32 v36,0x7000,v4\n"
"v_add_u32 v37,0x8800,v4\n"
"v_add_u32 v38,0xa000,v4\n"
"v_add_u32 v39,0xb800,v4\n"
"s_lshl_b32 s96,s92,3\n"
"s_add_u32 s96,s96,s93\n"
"s_lshl_b32 s97,s96,16\n"
"s_lshl_b32 s59,s11,14\n"
"s_add_u32 s97,s97,s59\n"
"s_add_u32 s60,s32,s97\n"
"s_addc_u32 s61,s33,0\n"
"s_add_u32 s60,s60,0x400000\n"
"s_addc_u32 s61,s61,0\n"
"s_and_b32 s61,s61,0xffff\n"
"s_mov_b32 s62,0x4000\n"
"s_mov_b32 s63,0x20000\n"
"s_mul_i32 s97,s96,0x12000\n"
"s_add_u32 s64,s32,s97\n"
"s_addc_u32 s65,s33,0\n"
"s_add_u32 s64,s64,0x5000000\n"
"s_addc_u32 s65,s65,0\n"
"s_and_b32 s65,s65,0xffff\n"
"s_mov_b32 s66,0x12000\n"
"s_mov_b32 s67,0x20000\n"
"s_lshl_b32 s96,s96,3\n"
"s_add_u32 s84,s96,0x2800\n"
"s_add_u32 s84,s84,s32\n"
"s_addc_u32 s85,s33,0\n"
"s_mov_b32 s76,0\n"
"s_mov_b32 s80,0xff800000\n"
"s_mov_b32 s81,0x41000000\n"
"s_sub_u32 s50,s79,1\n"
"s_lshl_b32 s53,s54,7\n"
"s_lshl_b32 s59,s11,5\n"
"s_add_u32 s53,s53,s59\n"
"s_sub_u32 s53,0,s53\n"
"v_lshrrev_b32 v2,4,v0\n"
"v_bfe_u32 v1,v0,2,4\n"
"v_bfe_u32 v3,v0,3,2\n"
"v_and_b32 v4,3,v0\n"
"v_lshlrev_b32 v1,7,v1\n"
"v_lshl_add_u32 v1,v4,3,v1\n"
"v_add_u32 v1,0x20000,v1\n"
"v_xor_b32 v4,0,v3\n"
"v_lshl_add_u32 v28,v4,5,v1\n"
"v_xor_b32 v4,1,v3\n"
"v_lshl_add_u32 v29,v4,5,v1\n"
"v_xor_b32 v4,2,v3\n"
"v_lshl_add_u32 v30,v4,5,v1\n"
"v_xor_b32 v4,3,v3\n"
"v_lshl_add_u32 v31,v4,5,v1\n"
"v_and_b32 v1,15,v0\n"
"v_lshlrev_b32 v4,2,v2\n"
"v_sub_u32 v26,v4,v1\n"
"v_and_b32 v1,7,v0\n"
"v_mul_u32_u24 v1,896,v1\n"
"v_lshl_add_u32 v1,v2,4,v1\n"
"v_bfe_u32 v4,v0,3,1\n"
"v_lshl_add_u32 v6,v4,6,v1\n"
"v_mov_b32 v27,0xff800000\n"
"s_and_b32 s95,s92,1\n"
"s_lshl_b32 s95,s95,3\n"
"s_add_u32 s95,s95,s93\n"
"s_bfe_u32 s96,s92,0x10001\n"
"s_mul_i32 s96,s96,14\n"
"s_min_u32 s97,s95,13\n"
"s_add_u32 s96,s96,s97\n"
"s_lshl_b32 s96,s96,7\n"
"s_sub_u32 s97,0x188000,s96\n"
"s_add_u32 s68,s24,s96\n"
"s_addc_u32 s69,s25,0\n"
"s_and_b32 s69,s69,0xffff\n"
"s_mov_b32 s70,s97\n"
"s_mov_b32 s71,0x20000\n"
"s_add_u32 s72,s26,s96\n"
"s_addc_u32 s73,s27,0\n"
"s_and_b32 s73,s73,0xffff\n"
"s_mov_b32 s74,s97\n"
"s_mov_b32 s75,0x20000\n"
"s_lshl_b32 s96,s92,3\n"
"s_add_u32 s96,s96,s93\n"
"s_lshl_b32 s96,s96,2\n"
"s_add_u32 s96,s96,s11\n"
"s_mul_i32 s97,s96,7168\n"
"s_add_u32 s8,s22,s97\n"
"s_addc_u32 s9,s23,0\n"
"s_mul_i32 s97,s96,7168\n"
"s_add_u32 s97,s97,3584\n"
"s_add_u32 s14,s22,s97\n"
"s_addc_u32 s15,s23,0\n"
"s_mul_i32 s97,s96,3136\n"
"s_min_u32 s97,s97,1602048\n"
"s_add_u32 s16,s24,s97\n"
"s_addc_u32 s17,s25,0\n"
"s_mul_i32 s97,s96,3136\n"
"s_min_u32 s97,s97,1602048\n"
"s_add_u32 s18,s26,s97\n"
"s_addc_u32 s19,s27,0\n"
"s_mul_i32 s97,s96,3136\n"
"s_min_u32 s97,s97,1602048\n"
"s_add_u32 s20,s28,s97\n"
"s_addc_u32 s21,s29,0\n"
"v_lshlrev_b32 v4,7,v0\n"
"v_min_u32 v4,3580,v4\n"
"v_mul_u32_u24 v4,s94,v4\n"
"v_cmp_lt_i32 s[86:87],0,v26\n"
"v_cmp_lt_i32 s[88:89],-1,v26\n"
"v_cmp_lt_i32 s[90:91],-2,v26\n"
"v_cmp_lt_i32 s[22:23],-3,v26\n"
"L_item_%=:\n"
"v_mov_b32 v12,0\n"
"v_mov_b32 v13,0\n"
"v_mov_b32 v14,0\n"
"v_mov_b32 v15,0\n"
"v_mov_b32 v16,0\n"
"v_mov_b32 v17,0\n"
"v_mov_b32 v18,0\n"
"v_mov_b32 v19,0\n"
"v_mov_b32 v20,0\n"
"v_mov_b32 v21,0\n"
"v_mov_b32 v22,0\n"
"v_mov_b32 v23,0\n"
"s_waitcnt vmcnt(8)\n"
"s_barrier\n"
"s_bitcmp1_b32 s79,0\n"
"s_cbranch_scc1 L_pro1_%=\n"
"global_load_dwordx2 v[2:3],v5,s[82:83] offset:64 sc1\n"
"ds_read_b128 a[192:195],v9 offset:0\n"
"ds_read_b128 a[196:199],v9 offset:1024\n"
"ds_read_b128 a[200:203],v9 offset:2048\n"
"ds_read_b128 a[204:207],v9 offset:3072\n"
"ds_read_b128 a[208:211],v9 offset:16384\n"
"ds_read_b128 a[212:215],v9 offset:17408\n"
"ds_read_b128 a[216:219],v9 offset:18432\n"
"ds_read_b128 a[220:223],v9 offset:19456\n"
"ds_read_b128 a[224:227],v9 offset:4096\n"
"ds_read_b128 a[228:231],v9 offset:5120\n"
"s_waitcnt lgkmcnt(6)\n"
"v_mfma_f32_16x16x32_bf16 v[64:67],a[192:195],a[128:131],v[16:19]\n"
"ds_read_b128 a[232:235],v9 offset:6144\n"
"v_mov_b32 v128,0\n"
"v_mfma_f32_16x16x32_bf16 v[64:67],a[196:199],a[132:135],v[64:67]\n"
"v_mov_b32 v129,0\n"
"v_mov_b32 v130,0\n"
"v_mfma_f32_16x16x32_bf16 v[64:67],a[200:203],a[136:139],v[64:67]\n"
"ds_read_b128 a[236:239],v9 offset:7168\n"
"v_mov_b32 v131,0\n"
"v_mfma_f32_16x16x32_bf16 v[64:67],a[204:207],a[140:143],v[64:67]\n"
"s_waitcnt lgkmcnt(4)\n"
"v_mov_b32 v132,0\n"
"v_mov_b32 v133,0\n"
"v_mfma_f32_16x16x32_bf16 v[64:67],a[208:211],a[144:147],v[64:67]\n"
"ds_read_b128 a[240:243],v9 offset:20480\n"
"v_mov_b32 v134,0\n"
"v_mfma_f32_16x16x32_bf16 v[64:67],a[212:215],a[148:151],v[64:67]\n"
"v_mov_b32 v135,0\n"
"v_mov_b32 v136,0\n"
"v_mfma_f32_16x16x32_bf16 v[64:67],a[216:219],a[152:155],v[64:67]\n"
"ds_read_b128 a[244:247],v9 offset:21504\n"
"v_mov_b32 v137,0\n"
"v_mfma_f32_16x16x32_bf16 v[64:67],a[220:223],a[156:159],v[64:67]\n"
"v_mov_b32 v138,0\n"
"v_mov_b32 v139,0\n"
"v_mfma_f32_16x16x32_bf16 v[80:83],a[192:195],a[160:163],v[20:23]\n"
"ds_read_b128 a[248:251],v9 offset:22528\n"
"v_mov_b32 v140,0\n"
"v_mfma_f32_16x16x32_bf16 v[80:83],a[196:199],a[164:167],v[80:83]\n"
"v_mov_b32 v141,0\n"
"v_mov_b32 v142,0\n"
"v_mfma_f32_16x16x32_bf16 v[80:83],a[200:203],a[168:171],v[80:83]\n"
"ds_read_b128 a[252:255],v9 offset:23552\n"
"v_mov_b32 v143,0\n"
"v_mfma_f32_16x16x32_bf16 v[80:83],a[204:207],a[172:175],v[80:83]\n"
"v_mov_b32 v144,0\n"
"v_mov_b32 v145,0\n"
"v_mfma_f32_16x16x32_bf16 v[80:83],a[208:211],a[176:179],v[80:83]\n"
"ds_read_b128 a[192:195],v9 offset:8192\n"
"v_mov_b32 v146,0\n"
"v_mfma_f32_16x16x32_bf16 v[80:83],a[212:215],a[180:183],v[80:83]\n"
"v_mov_b32 v147,0\n"
"v_mov_b32 v148,0\n"
"v_mfma_f32_16x16x32_bf16 v[80:83],a[216:219],a[184:187],v[80:83]\n"
"ds_read_b128 a[196:199],v9 offset:9216\n"
"v_mov_b32 v149,0\n"
"v_mfma_f32_16x16x32_bf16 v[80:83],a[220:223],a[188:191],v[80:83]\n"
"s_waitcnt lgkmcnt(6)\n"
"v_mov_b32 v150,0\n"
"v_mov_b32 v151,0\n"
"v_mfma_f32_16x16x32_bf16 v[68:71],a[224:227],a[128:131],v[16:19]\n"
"ds_read_b128 a[200:203],v9 offset:10240\n"
"v_mov_b32 v152,0\n"
"v_mfma_f32_16x16x32_bf16 v[68:71],a[228:231],a[132:135],v[68:71]\n"
"v_mov_b32 v153,0\n"
"v_mov_b32 v154,0\n"
"v_mfma_f32_16x16x32_bf16 v[68:71],a[232:235],a[136:139],v[68:71]\n"
"ds_read_b128 a[204:207],v9 offset:11264\n"
"v_mov_b32 v155,0\n"
"v_mfma_f32_16x16x32_bf16 v[68:71],a[236:239],a[140:143],v[68:71]\n"
"s_waitcnt lgkmcnt(4)\n"
"v_mov_b32 v156,0\n"
"v_mov_b32 v157,0\n"
"v_mfma_f32_16x16x32_bf16 v[68:71],a[240:243],a[144:147],v[68:71]\n"
"ds_read_b128 a[208:211],v9 offset:24576\n"
"v_mov_b32 v158,0\n"
"v_mfma_f32_16x16x32_bf16 v[68:71],a[244:247],a[148:151],v[68:71]\n"
"v_mov_b32 v159,0\n"
"v_mov_b32 v160,0\n"
"v_mfma_f32_16x16x32_bf16 v[68:71],a[248:251],a[152:155],v[68:71]\n"
"ds_read_b128 a[212:215],v9 offset:25600\n"
"v_mov_b32 v161,0\n"
"v_mfma_f32_16x16x32_bf16 v[68:71],a[252:255],a[156:159],v[68:71]\n"
"v_mov_b32 v162,0\n"
"v_mov_b32 v163,0\n"
"v_mfma_f32_16x16x32_bf16 v[84:87],a[224:227],a[160:163],v[20:23]\n"
"s_waitcnt vmcnt(0)\n"
"ds_read_b128 a[216:219],v9 offset:26624\n"
"v_mov_b32 v164,0\n"
"v_mfma_f32_16x16x32_bf16 v[84:87],a[228:231],a[164:167],v[84:87]\n"
"v_cmp_ne_u64 vcc,s[34:35],v[2:3]\n"
"v_mov_b32 v165,0\n"
"v_mfma_f32_16x16x32_bf16 v[84:87],a[232:235],a[168:171],v[84:87]\n"
"ds_read_b128 a[220:223],v9 offset:27648\n"
"v_mov_b32 v166,0\n"
"v_mfma_f32_16x16x32_bf16 v[84:87],a[236:239],a[172:175],v[84:87]\n"
"v_mov_b32 v167,0\n"
"v_mov_b32 v168,0\n"
"v_mfma_f32_16x16x32_bf16 v[84:87],a[240:243],a[176:179],v[84:87]\n"
"s_cbranch_vccnz L_vf0_%=\n"
"L_vf0_ret_%=:\n"
"ds_read_b128 a[224:227],v9 offset:12288\n"
"v_mov_b32 v169,0\n"
"v_mfma_f32_16x16x32_bf16 v[84:87],a[244:247],a[180:183],v[84:87]\n"
"v_mov_b32 v170,0\n"
"v_mov_b32 v171,0\n"
"v_mfma_f32_16x16x32_bf16 v[84:87],a[248:251],a[184:187],v[84:87]\n"
"ds_read_b128 a[228:231],v9 offset:13312\n"
"v_mov_b32 v172,0\n"
"v_mfma_f32_16x16x32_bf16 v[84:87],a[252:255],a[188:191],v[84:87]\n"
"s_waitcnt lgkmcnt(6)\n"
"v_mov_b32 v173,0\n"
"v_mov_b32 v174,0\n"
"v_mfma_f32_16x16x32_bf16 v[72:75],a[192:195],a[128:131],v[16:19]\n"
"s_add_u32 m0,s57,0x0\n"
"ds_read_b128 a[232:235],v9 offset:14336\n"
"v_mov_b32 v175,0\n"
"v_mfma_f32_16x16x32_bf16 v[72:75],a[196:199],a[132:135],v[72:75]\n"
"buffer_load_dwordx4 v10,s[36:39],s49 offen offset:0 sc1 lds\n"
"v_mfma_f32_16x16x32_bf16 v[72:75],a[200:203],a[136:139],v[72:75]\n"
"ds_read_b128 a[236:239],v9 offset:15360\n"
"v_mov_b32 v176,0\n"
"v_mfma_f32_16x16x32_bf16 v[72:75],a[204:207],a[140:143],v[72:75]\n"
"s_waitcnt lgkmcnt(4)\n"
"v_mov_b32 v177,0\n"
"v_mov_b32 v178,0\n"
"v_mfma_f32_16x16x32_bf16 v[72:75],a[208:211],a[144:147],v[72:75]\n"
"ds_read_b128 a[240:243],v9 offset:28672\n"
"v_mov_b32 v179,0\n"
"v_mfma_f32_16x16x32_bf16 v[72:75],a[212:215],a[148:151],v[72:75]\n"
"buffer_load_dwordx4 v10,s[36:39],s49 offen offset:1024 sc1 lds\n"
"v_mfma_f32_16x16x32_bf16 v[72:75],a[216:219],a[152:155],v[72:75]\n"
"ds_read_b128 a[244:247],v9 offset:29696\n"
"v_mov_b32 v180,0\n"
"v_mfma_f32_16x16x32_bf16 v[72:75],a[220:223],a[156:159],v[72:75]\n"
"v_mov_b32 v181,0\n"
"v_mov_b32 v182,0\n"
"v_mfma_f32_16x16x32_bf16 v[88:91],a[192:195],a[160:163],v[20:23]\n"
"ds_read_b128 a[248:251],v9 offset:30720\n"
"v_mov_b32 v183,0\n"
"v_mfma_f32_16x16x32_bf16 v[88:91],a[196:199],a[164:167],v[88:91]\n"
"buffer_load_dwordx4 v10,s[36:39],s49 offen offset:2048 sc1 lds\n"
"v_mfma_f32_16x16x32_bf16 v[88:91],a[200:203],a[168:171],v[88:91]\n"
"s_waitcnt lgkmcnt(3)\n"
"ds_read_b128 a[252:255],v9 offset:31744\n"
"v_mov_b32 v184,0\n"
"v_mfma_f32_16x16x32_bf16 v[88:91],a[204:207],a[172:175],v[88:91]\n"
"v_mov_b32 v185,0\n"
"v_mov_b32 v186,0\n"
"v_mfma_f32_16x16x32_bf16 v[88:91],a[208:211],a[176:179],v[88:91]\n"
"v_mov_b32 v187,0\n"
"v_mov_b32 v188,0\n"
"v_mfma_f32_16x16x32_bf16 v[88:91],a[212:215],a[180:183],v[88:91]\n"
"buffer_load_dwordx4 v10,s[36:39],s49 offen offset:3072 sc1 lds\n"
"v_mfma_f32_16x16x32_bf16 v[88:91],a[216:219],a[184:187],v[88:91]\n"
"v_mov_b32 v189,0\n"
"v_mov_b32 v190,0\n"
"v_mfma_f32_16x16x32_bf16 v[88:91],a[220:223],a[188:191],v[88:91]\n"
"v_mov_b32 v191,0\n"
"v_mov_b32 v192,0\n"
"v_mfma_f32_16x16x32_bf16 v[76:79],a[224:227],a[128:131],v[16:19]\n"
"s_add_u32 m0,m0,0x1000\n"
"v_mov_b32 v193,0\n"
"v_mov_b32 v194,0\n"
"v_mfma_f32_16x16x32_bf16 v[76:79],a[228:231],a[132:135],v[76:79]\n"
"buffer_load_dwordx4 v7,s[36:39],s49 offen offset:0 sc1 lds\n"
"v_mfma_f32_16x16x32_bf16 v[76:79],a[232:235],a[136:139],v[76:79]\n"
"v_mov_b32 v195,0\n"
"v_mov_b32 v196,0\n"
"v_mfma_f32_16x16x32_bf16 v[76:79],a[236:239],a[140:143],v[76:79]\n"
"s_waitcnt lgkmcnt(0)\n"
"v_mov_b32 v197,0\n"
"v_mov_b32 v198,0\n"
"v_mfma_f32_16x16x32_bf16 v[76:79],a[240:243],a[144:147],v[76:79]\n"
"v_mov_b32 v199,0\n"
"v_mov_b32 v200,0\n"
"v_mfma_f32_16x16x32_bf16 v[76:79],a[244:247],a[148:151],v[76:79]\n"
"buffer_load_dwordx4 v7,s[36:39],s49 offen offset:1024 sc1 lds\n"
"v_mfma_f32_16x16x32_bf16 v[76:79],a[248:251],a[152:155],v[76:79]\n"
"v_mov_b32 v201,0\n"
"v_mov_b32 v202,0\n"
"v_mfma_f32_16x16x32_bf16 v[76:79],a[252:255],a[156:159],v[76:79]\n"
"v_mov_b32 v203,0\n"
"v_mov_b32 v204,0\n"
"v_mfma_f32_16x16x32_bf16 v[92:95],a[224:227],a[160:163],v[20:23]\n"
"v_mov_b32 v205,0\n"
"v_mov_b32 v206,0\n"
"v_mfma_f32_16x16x32_bf16 v[92:95],a[228:231],a[164:167],v[92:95]\n"
"buffer_load_dwordx4 v7,s[36:39],s49 offen offset:2048 sc1 lds\n"
"v_mfma_f32_16x16x32_bf16 v[92:95],a[232:235],a[168:171],v[92:95]\n"
"v_mov_b32 v207,0\n"
"v_mov_b32 v208,0\n"
"v_mfma_f32_16x16x32_bf16 v[92:95],a[236:239],a[172:175],v[92:95]\n"
"v_mov_b32 v209,0\n"
"v_mov_b32 v210,0\n"
"v_mfma_f32_16x16x32_bf16 v[92:95],a[240:243],a[176:179],v[92:95]\n"
"v_mov_b32 v211,0\n"
"v_mov_b32 v212,0\n"
"v_mfma_f32_16x16x32_bf16 v[92:95],a[244:247],a[180:183],v[92:95]\n"
"buffer_load_dwordx4 v7,s[36:39],s49 offen offset:3072 sc1 lds\n"
"v_mfma_f32_16x16x32_bf16 v[92:95],a[248:251],a[184:187],v[92:95]\n"
"v_mov_b32 v213,0\n"
"v_mov_b32 v214,0\n"
"v_mfma_f32_16x16x32_bf16 v[92:95],a[252:255],a[188:191],v[92:95]\n"
"v_mov_b32 v215,0\n"
"v_mov_b32 v216,0\n"
"s_cmp_eq_u32 s50,1\n"
"s_cselect_b32 s48,s55,s48\n"
"s_barrier\n"
"s_add_u32 m0,s57,0x10000\n"
"s_nop 0\n"
"buffer_load_dwordx4 v10,s[36:39],s48 offen offset:0 sc1 lds\n"
"v_mov_b32 v217,0\n"
"v_pk_mov_b32 v[218:219],0,0\n"
"v_pk_mov_b32 v[220:221],0,0\n"
"v_pk_mov_b32 v[222:223],0,0\n"
"v_pk_mov_b32 v[224:225],0,0\n"
"v_pk_mov_b32 v[226:227],0,0\n"
"v_pk_mov_b32 v[228:229],0,0\n"
"v_pk_mov_b32 v[230:231],0,0\n"
"v_pk_mov_b32 v[232:233],0,0\n"
"v_pk_mov_b32 v[234:235],0,0\n"
"v_pk_mov_b32 v[236:237],0,0\n"
"v_pk_mov_b32 v[238:239],0,0\n"
"v_pk_mov_b32 v[240:241],0,0\n"
"v_pk_mov_b32 v[242:243],0,0\n"
"v_pk_mov_b32 v[244:245],0,0\n"
"v_pk_mov_b32 v[246:247],0,0\n"
"v_pk_mov_b32 v[248:249],0,0\n"
"v_pk_mov_b32 v[250:251],0,0\n"
"v_pk_mov_b32 v[252:253],0,0\n"
"v_pk_mov_b32 v[254:255],0,0\n"
"s_branch L_slow_pro0_%=\n"
"L_slowret_pro0_%=:\n"
"buffer_load_dwordx4 v10,s[36:39],s48 offen offset:1024 sc1 lds\n"
"v_exp_f32 v64,v64\n"
"v_exp_f32 v65,v65\n"
"v_add_f32 v12,v12,v64\n"
"v_exp_f32 v66,v66\n"
"buffer_load_dwordx4 v10,s[36:39],s48 offen offset:2048 sc1 lds\n"
"v_add_f32 v12,v12,v65\n"
"v_exp_f32 v67,v67\n"
"v_add_f32 v12,v12,v66\n"
"v_exp_f32 v68,v68\n"
"buffer_load_dwordx4 v10,s[36:39],s48 offen offset:3072 sc1 lds\n"
"v_add_f32 v12,v12,v67\n"
"v_exp_f32 v69,v69\n"
"v_add_f32 v12,v12,v68\n"
"v_exp_f32 v70,v70\n"
"s_add_u32 m0,m0,0x1000\n"
"s_nop 0\n"
"buffer_load_dwordx4 v7,s[36:39],s48 offen offset:0 sc1 lds\n"
"v_add_f32 v12,v12,v69\n"
"v_exp_f32 v71,v71\n"
"v_add_f32 v12,v12,v70\n"
"v_exp_f32 v72,v72\n"
"v_add_f32 v12,v12,v71\n"
"v_exp_f32 v73,v73\n"
"v_add_f32 v12,v12,v72\n"
"v_exp_f32 v74,v74\n"
"v_add_f32 v12,v12,v73\n"
"v_exp_f32 v75,v75\n"
"v_add_f32 v12,v12,v74\n"
"v_exp_f32 v76,v76\n"
"v_add_f32 v12,v12,v75\n"
"ds_read_b128 a[192:195],v9 offset:32768\n"
"v_exp_f32 v77,v77\n"
"v_add_f32 v12,v12,v76\n"
"ds_read_b128 a[196:199],v9 offset:33792\n"
"v_exp_f32 v78,v78\n"
"v_add_f32 v12,v12,v77\n"
"ds_read_b128 a[200:203],v9 offset:34816\n"
"v_exp_f32 v79,v79\n"
"v_add_f32 v12,v12,v78\n"
"ds_read_b128 a[204:207],v9 offset:35840\n"
"v_exp_f32 v80,v80\n"
"v_add_f32 v12,v12,v79\n"
"ds_read_b128 a[208:211],v9 offset:49152\n"
"v_exp_f32 v81,v81\n"
"v_add_f32 v13,v13,v80\n"
"ds_read_b128 a[212:215],v9 offset:50176\n"
"v_exp_f32 v82,v82\n"
"v_add_f32 v13,v13,v81\n"
"ds_read_b128 a[216:219],v9 offset:51200\n"
"v_exp_f32 v83,v83\n"
"v_add_f32 v13,v13,v82\n"
"ds_read_b128 a[220:223],v9 offset:52224\n"
"v_exp_f32 v84,v84\n"
"v_add_f32 v13,v13,v83\n"
"ds_read_b128 a[224:227],v9 offset:36864\n"
"v_exp_f32 v85,v85\n"
"v_add_f32 v13,v13,v84\n"
"ds_read_b128 a[228:231],v9 offset:37888\n"
"s_add_u32 s49,s49,0x10000\n"
"s_add_u32 s53,s53,64\n"
"s_sub_u32 s50,s50,1\n"
"s_cmp_eq_u32 s50,0\n"
"s_cbranch_scc1 L_last_%=\n"
"L_loop_%=:\n"
"s_waitcnt lgkmcnt(9)\n"
"v_mfma_f32_16x16x32_bf16 v[96:99],a[192:195],a[128:131],v[16:19]\n"
"ds_read_b128 a[232:235],v9 offset:38912\n"
"v_add_f32 v13,v13,v85\n"
"s_waitcnt lgkmcnt(9)\n"
"v_mfma_f32_16x16x32_bf16 v[96:99],a[196:199],a[132:135],v[96:99]\n"
"buffer_load_dwordx4 v7,s[36:39],s48 offen offset:1024 sc1 lds\n"
"s_waitcnt lgkmcnt(8)\n"
"v_mfma_f32_16x16x32_bf16 v[96:99],a[200:203],a[136:139],v[96:99]\n"
"ds_read_b128 a[236:239],v9 offset:39936\n"
"v_cvt_pk_bf16_f32 v40,v64,v65\n"
"s_waitcnt lgkmcnt(8)\n"
"v_mfma_f32_16x16x32_bf16 v[96:99],a[204:207],a[140:143],v[96:99]\n"
"s_waitcnt lgkmcnt(4)\n"
"v_exp_f32 v86,v86\n"
"v_mfma_f32_16x16x32_bf16 v[96:99],a[208:211],a[144:147],v[96:99]\n"
"ds_read_b128 a[240:243],v9 offset:53248\n"
"v_cvt_pk_bf16_f32 v41,v66,v67\n"
"v_mfma_f32_16x16x32_bf16 v[96:99],a[212:215],a[148:151],v[96:99]\n"
"buffer_load_dwordx4 v7,s[36:39],s48 offen offset:2048 sc1 lds\n"
"v_mfma_f32_16x16x32_bf16 v[96:99],a[216:219],a[152:155],v[96:99]\n"
"ds_read_b128 a[244:247],v9 offset:54272\n"
"v_cvt_pk_bf16_f32 v42,v68,v69\n"
"v_mfma_f32_16x16x32_bf16 v[96:99],a[220:223],a[156:159],v[96:99]\n"
"v_exp_f32 v87,v87\n"
"v_mfma_f32_16x16x32_bf16 v[112:115],a[192:195],a[160:163],v[20:23]\n"
"ds_read_b128 a[248:251],v9 offset:55296\n"
"v_cvt_pk_bf16_f32 v43,v70,v71\n"
"v_mfma_f32_16x16x32_bf16 v[112:115],a[196:199],a[164:167],v[112:115]\n"
"buffer_load_dwordx4 v7,s[36:39],s48 offen offset:3072 sc1 lds\n"
"v_mfma_f32_16x16x32_bf16 v[112:115],a[200:203],a[168:171],v[112:115]\n"
"s_add_u32 s48,s48,0x10000\n"
"ds_read_b128 a[252:255],v9 offset:56320\n"
"v_cvt_pk_bf16_f32 v44,v72,v73\n"
"v_mfma_f32_16x16x32_bf16 v[112:115],a[204:207],a[172:175],v[112:115]\n"
"v_exp_f32 v88,v88\n"
"v_mfma_f32_16x16x32_bf16 v[112:115],a[208:211],a[176:179],v[112:115]\n"
"ds_read_b128 a[192:195],v9 offset:40960\n"
"v_max3_f32 v11,v96,v97,v98\n"
"v_mfma_f32_16x16x32_bf16 v[112:115],a[212:215],a[180:183],v[112:115]\n"
"v_cvt_pk_bf16_f32 v45,v74,v75\n"
"v_cvt_pk_bf16_f32 v46,v76,v77\n"
"v_mfma_f32_16x16x32_bf16 v[112:115],a[216:219],a[184:187],v[112:115]\n"
"ds_read_b128 a[196:199],v9 offset:41984\n"
"v_max_f32 v11,v11,v99\n"
"v_mfma_f32_16x16x32_bf16 v[112:115],a[220:223],a[188:191],v[112:115]\n"
"s_waitcnt lgkmcnt(6)\n"
"v_exp_f32 v89,v89\n"
"v_mfma_f32_16x16x32_bf16 v[100:103],a[224:227],a[128:131],v[16:19]\n"
"ds_read_b128 a[200:203],v9 offset:43008\n"
"v_cvt_pk_bf16_f32 v47,v78,v79\n"
"v_mfma_f32_16x16x32_bf16 v[100:103],a[228:231],a[132:135],v[100:103]\n"
"v_exp_f32 v90,v90\n"
"v_mfma_f32_16x16x32_bf16 v[100:103],a[232:235],a[136:139],v[100:103]\n"
"ds_read_b128 a[204:207],v9 offset:44032\n"
"v_cvt_pk_bf16_f32 v48,v80,v81\n"
"v_mfma_f32_16x16x32_bf16 v[100:103],a[236:239],a[140:143],v[100:103]\n"
"s_waitcnt lgkmcnt(4)\n"
"v_exp_f32 v91,v91\n"
"v_mfma_f32_16x16x32_bf16 v[100:103],a[240:243],a[144:147],v[100:103]\n"
"ds_read_b128 a[208:211],v9 offset:57344\n"
"v_max3_f32 v11,v11,v112,v113\n"
"v_mfma_f32_16x16x32_bf16 v[100:103],a[244:247],a[148:151],v[100:103]\n"
"v_exp_f32 v92,v92\n"
"v_mfma_f32_16x16x32_bf16 v[100:103],a[248:251],a[152:155],v[100:103]\n"
"ds_read_b128 a[212:215],v9 offset:58368\n"
"v_max3_f32 v11,v11,v114,v115\n"
"v_mfma_f32_16x16x32_bf16 v[100:103],a[252:255],a[156:159],v[100:103]\n"
"v_exp_f32 v93,v93\n"
"v_mfma_f32_16x16x32_bf16 v[116:119],a[224:227],a[160:163],v[20:23]\n"
"ds_read_b128 a[216:219],v9 offset:59392\n"
"v_cvt_pk_bf16_f32 v49,v82,v83\n"
"v_mfma_f32_16x16x32_bf16 v[116:119],a[228:231],a[164:167],v[116:119]\n"
"v_exp_f32 v94,v94\n"
"v_mfma_f32_16x16x32_bf16 v[116:119],a[232:235],a[168:171],v[116:119]\n"
"ds_read_b128 a[220:223],v9 offset:60416\n"
"v_cvt_pk_bf16_f32 v50,v84,v85\n"
"v_mfma_f32_16x16x32_bf16 v[116:119],a[236:239],a[172:175],v[116:119]\n"
"v_exp_f32 v95,v95\n"
"v_mfma_f32_16x16x32_bf16 v[116:119],a[240:243],a[176:179],v[116:119]\n"
"ds_read_b128 a[224:227],v9 offset:45056\n"
"v_max3_f32 v11,v11,v100,v101\n"
"v_mfma_f32_16x16x32_bf16 v[116:119],a[244:247],a[180:183],v[116:119]\n"
"global_load_dword v1,v4,s[8:9]\n"
"v_mfma_f32_16x16x32_bf16 v[116:119],a[248:251],a[184:187],v[116:119]\n"
"s_mov_b64 s[8:9],s[14:15]\n"
"ds_read_b128 a[228:231],v9 offset:46080\n"
"v_max3_f32 v11,v11,v102,v103\n"
"v_mfma_f32_16x16x32_bf16 v[116:119],a[252:255],a[188:191],v[116:119]\n"
"s_waitcnt lgkmcnt(6)\n"
"v_add_f32 v13,v13,v86\n"
"v_add_f32 v13,v13,v87\n"
"v_mfma_f32_16x16x32_bf16 v[104:107],a[192:195],a[128:131],v[16:19]\n"
"s_mov_b64 s[14:15],s[16:17]\n"
"ds_read_b128 a[232:235],v9 offset:47104\n"
"v_cvt_pk_bf16_f32 v51,v86,v87\n"
"v_mfma_f32_16x16x32_bf16 v[104:107],a[196:199],a[132:135],v[104:107]\n"
"v_add_f32 v13,v13,v88\n"
"v_add_f32 v13,v13,v89\n"
"v_mfma_f32_16x16x32_bf16 v[104:107],a[200:203],a[136:139],v[104:107]\n"
"s_mov_b64 s[16:17],s[18:19]\n"
"ds_read_b128 a[236:239],v9 offset:48128\n"
"v_cvt_pk_bf16_f32 v52,v88,v89\n"
"v_mfma_f32_16x16x32_bf16 v[104:107],a[204:207],a[140:143],v[104:107]\n"
"s_waitcnt lgkmcnt(4)\n"
"v_add_f32 v13,v13,v90\n"
"v_add_f32 v13,v13,v91\n"
"v_mfma_f32_16x16x32_bf16 v[104:107],a[208:211],a[144:147],v[104:107]\n"
"s_mov_b64 s[18:19],s[20:21]\n"
"ds_read_b128 a[240:243],v9 offset:61440\n"
"v_max3_f32 v11,v11,v116,v117\n"
"v_mfma_f32_16x16x32_bf16 v[104:107],a[212:215],a[148:151],v[104:107]\n"
"v_cvt_pk_bf16_f32 v53,v90,v91\n"
"v_add_f32 v13,v13,v92\n"
"v_mfma_f32_16x16x32_bf16 v[104:107],a[216:219],a[152:155],v[104:107]\n"
"ds_read_b128 a[244:247],v9 offset:62464\n"
"v_max3_f32 v11,v11,v118,v119\n"
"v_mfma_f32_16x16x32_bf16 v[104:107],a[220:223],a[156:159],v[104:107]\n"
"v_add_f32 v13,v13,v93\n"
"v_cvt_pk_bf16_f32 v54,v92,v93\n"
"v_mfma_f32_16x16x32_bf16 v[120:123],a[192:195],a[160:163],v[20:23]\n"
"ds_read_b128 a[248:251],v9 offset:63488\n"
"v_add_f32 v13,v13,v94\n"
"v_mfma_f32_16x16x32_bf16 v[120:123],a[196:199],a[164:167],v[120:123]\n"
"v_add_f32 v13,v13,v95\n"
"v_cvt_pk_bf16_f32 v55,v94,v95\n"
"v_mfma_f32_16x16x32_bf16 v[120:123],a[200:203],a[168:171],v[120:123]\n"
"s_waitcnt lgkmcnt(3)\n"
"ds_read_b128 a[252:255],v9 offset:64512\n"
"v_mfma_f32_16x16x32_bf16 v[120:123],a[204:207],a[172:175],v[120:123]\n"
"s_waitcnt vmcnt(9)\n"
"s_barrier\n"
"v_mfma_f32_16x16x32_bf16 v[120:123],a[208:211],a[176:179],v[120:123]\n"
"s_add_u32 m0,s57,0x8000\n"
"ds_read_b128 a[192:195],v8 offset:0\n"
"v_max3_f32 v11,v11,v104,v105\n"
"v_mfma_f32_16x16x32_bf16 v[120:123],a[212:215],a[180:183],v[120:123]\n"
"buffer_load_dwordx4 v10,s[36:39],s49 offen offset:0 sc1 lds\n"
"v_mfma_f32_16x16x32_bf16 v[120:123],a[216:219],a[184:187],v[120:123]\n"
"ds_read_b128 a[196:199],v8 offset:8192\n"
"v_max3_f32 v11,v11,v106,v107\n"
"v_mfma_f32_16x16x32_bf16 v[120:123],a[220:223],a[188:191],v[120:123]\n"
"v_mfma_f32_16x16x32_bf16 v[108:111],a[224:227],a[128:131],v[16:19]\n"
"ds_read_b128 a[200:203],v8 offset:1024\n"
"v_mfma_f32_16x16x32_bf16 v[108:111],a[228:231],a[132:135],v[108:111]\n"
"buffer_load_dwordx4 v10,s[36:39],s49 offen offset:1024 sc1 lds\n"
"v_mfma_f32_16x16x32_bf16 v[108:111],a[232:235],a[136:139],v[108:111]\n"
"ds_read_b128 a[204:207],v8 offset:9216\n"
"v_mfma_f32_16x16x32_bf16 v[108:111],a[236:239],a[140:143],v[108:111]\n"
"s_waitcnt lgkmcnt(4)\n"
"v_mfma_f32_16x16x32_bf16 v[108:111],a[240:243],a[144:147],v[108:111]\n"
"ds_read_b128 a[208:211],v8 offset:2048\n"
"v_max3_f32 v11,v11,v120,v121\n"
"v_mfma_f32_16x16x32_bf16 v[108:111],a[244:247],a[148:151],v[108:111]\n"
"buffer_load_dwordx4 v10,s[36:39],s49 offen offset:2048 sc1 lds\n"
"v_mfma_f32_16x16x32_bf16 v[108:111],a[248:251],a[152:155],v[108:111]\n"
"ds_read_b128 a[212:215],v8 offset:10240\n"
"v_max3_f32 v11,v11,v122,v123\n"
"v_mfma_f32_16x16x32_bf16 v[108:111],a[252:255],a[156:159],v[108:111]\n"
"v_mfma_f32_16x16x32_bf16 v[124:127],a[224:227],a[160:163],v[20:23]\n"
"ds_read_b128 a[216:219],v8 offset:3072\n"
"v_mfma_f32_16x16x32_bf16 v[124:127],a[228:231],a[164:167],v[124:127]\n"
"buffer_load_dwordx4 v10,s[36:39],s49 offen offset:3072 sc1 lds\n"
"v_mfma_f32_16x16x32_bf16 v[124:127],a[232:235],a[168:171],v[124:127]\n"
"ds_read_b128 a[220:223],v8 offset:11264\n"
"v_mfma_f32_16x16x32_bf16 v[124:127],a[236:239],a[172:175],v[124:127]\n"
"v_mfma_f32_16x16x32_bf16 v[124:127],a[240:243],a[176:179],v[124:127]\n"
"s_add_u32 m0,m0,0x1000\n"
"ds_read_b128 a[224:227],v8 offset:4096\n"
"v_max3_f32 v11,v11,v108,v109\n"
"v_mfma_f32_16x16x32_bf16 v[124:127],a[244:247],a[180:183],v[124:127]\n"
"buffer_load_dwordx4 v7,s[36:39],s49 offen offset:0 sc1 lds\n"
"v_mfma_f32_16x16x32_bf16 v[124:127],a[248:251],a[184:187],v[124:127]\n"
"s_cmp_le_i32 s50,s52\n"
"ds_read_b128 a[228:231],v8 offset:12288\n"
"v_max3_f32 v11,v11,v110,v111\n"
"v_mfma_f32_16x16x32_bf16 v[124:127],a[252:255],a[188:191],v[124:127]\n"
"s_waitcnt lgkmcnt(6)\n"
"v_mfma_f32_16x16x32_bf16 v[128:131],a[192:195],v[40:43],v[128:131]\n"
"s_cselect_b32 s51,s80,s81\n"
"ds_read_b128 a[232:235],v8 offset:5120\n"
"v_mfma_f32_16x16x32_bf16 v[128:131],a[196:199],v[44:47],v[128:131]\n"
"buffer_load_dwordx4 v7,s[36:39],s49 offen offset:1024 sc1 lds\n"
"v_mfma_f32_16x16x32_bf16 v[192:195],a[192:195],v[48:51],v[192:195]\n"
"ds_read_b128 a[236:239],v8 offset:13312\n"
"v_mfma_f32_16x16x32_bf16 v[192:195],a[196:199],v[52:55],v[192:195]\n"
"v_mfma_f32_16x16x32_bf16 v[132:135],a[200:203],v[40:43],v[132:135]\n"
"ds_read_b128 a[240:243],v8 offset:6144\n"
"v_max3_f32 v11,v11,v124,v125\n"
"v_mfma_f32_16x16x32_bf16 v[132:135],a[204:207],v[44:47],v[132:135]\n"
"v_max3_f32 v11,v11,v126,v127\n"
"v_cmp_nge_f32 vcc,s51,v11\n"
"v_mfma_f32_16x16x32_bf16 v[196:199],a[200:203],v[48:51],v[196:199]\n"
"s_waitcnt lgkmcnt(5)\n"
"ds_read_b128 a[244:247],v8 offset:14336\n"
"v_mfma_f32_16x16x32_bf16 v[196:199],a[204:207],v[52:55],v[196:199]\n"
"s_cbranch_vccnz L_slow_odd_%=\n"
"L_slowret_odd_%=:\n"
"v_exp_f32 v96,v96\n"
"v_mfma_f32_16x16x32_bf16 v[136:139],a[208:211],v[40:43],v[136:139]\n"
"ds_read_b128 a[248:251],v8 offset:7168\n"
"v_add_f32 v12,v12,v96\n"
"v_mfma_f32_16x16x32_bf16 v[136:139],a[212:215],v[44:47],v[136:139]\n"
"buffer_load_dwordx4 v7,s[36:39],s49 offen offset:2048 sc1 lds\n"
"v_mfma_f32_16x16x32_bf16 v[200:203],a[208:211],v[48:51],v[200:203]\n"
"ds_read_b128 a[252:255],v8 offset:15360\n"
"v_mfma_f32_16x16x32_bf16 v[200:203],a[212:215],v[52:55],v[200:203]\n"
"v_exp_f32 v97,v97\n"
"v_mfma_f32_16x16x32_bf16 v[140:143],a[216:219],v[40:43],v[140:143]\n"
"ds_read_b128 a[192:195],v8 offset:16384\n"
"v_add_f32 v12,v12,v97\n"
"v_mfma_f32_16x16x32_bf16 v[140:143],a[220:223],v[44:47],v[140:143]\n"
"buffer_load_dwordx4 v7,s[36:39],s49 offen offset:3072 sc1 lds\n"
"v_mfma_f32_16x16x32_bf16 v[204:207],a[216:219],v[48:51],v[204:207]\n"
"s_add_u32 s49,s49,0x10000\n"
"ds_read_b128 a[196:199],v8 offset:24576\n"
"v_mfma_f32_16x16x32_bf16 v[204:207],a[220:223],v[52:55],v[204:207]\n"
"s_waitcnt lgkmcnt(6)\n"
"v_exp_f32 v98,v98\n"
"v_mfma_f32_16x16x32_bf16 v[144:147],a[224:227],v[40:43],v[144:147]\n"
"ds_read_b128 a[200:203],v8 offset:17408\n"
"v_add_f32 v12,v12,v98\n"
"v_mfma_f32_16x16x32_bf16 v[144:147],a[228:231],v[44:47],v[144:147]\n"
"v_exp_f32 v99,v99\n"
"v_mfma_f32_16x16x32_bf16 v[208:211],a[224:227],v[48:51],v[208:211]\n"
"ds_read_b128 a[204:207],v8 offset:25600\n"
"v_add_f32 v12,v12,v99\n"
"v_mfma_f32_16x16x32_bf16 v[208:211],a[228:231],v[52:55],v[208:211]\n"
"v_exp_f32 v100,v100\n"
"v_mfma_f32_16x16x32_bf16 v[148:151],a[232:235],v[40:43],v[148:151]\n"
"ds_read_b128 a[208:211],v8 offset:18432\n"
"v_add_f32 v12,v12,v100\n"
"v_mfma_f32_16x16x32_bf16 v[148:151],a[236:239],v[44:47],v[148:151]\n"
"v_exp_f32 v101,v101\n"
"v_mfma_f32_16x16x32_bf16 v[212:215],a[232:235],v[48:51],v[212:215]\n"
"ds_read_b128 a[212:215],v8 offset:26624\n"
"v_add_f32 v12,v12,v101\n"
"v_mfma_f32_16x16x32_bf16 v[212:215],a[236:239],v[52:55],v[212:215]\n"
"s_waitcnt lgkmcnt(6)\n"
"v_exp_f32 v102,v102\n"
"v_mfma_f32_16x16x32_bf16 v[152:155],a[240:243],v[40:43],v[152:155]\n"
"ds_read_b128 a[216:219],v8 offset:19456\n"
"v_add_f32 v12,v12,v102\n"
"v_mfma_f32_16x16x32_bf16 v[152:155],a[244:247],v[44:47],v[152:155]\n"
"v_exp_f32 v103,v103\n"
"v_mfma_f32_16x16x32_bf16 v[216:219],a[240:243],v[48:51],v[216:219]\n"
"ds_read_b128 a[220:223],v8 offset:27648\n"
"v_add_f32 v12,v12,v103\n"
"v_mfma_f32_16x16x32_bf16 v[216:219],a[244:247],v[52:55],v[216:219]\n"
"v_exp_f32 v104,v104\n"
"v_mfma_f32_16x16x32_bf16 v[156:159],a[248:251],v[40:43],v[156:159]\n"
"ds_read_b128 a[224:227],v8 offset:20480\n"
"v_add_f32 v12,v12,v104\n"
"v_mfma_f32_16x16x32_bf16 v[156:159],a[252:255],v[44:47],v[156:159]\n"
"v_exp_f32 v105,v105\n"
"v_mfma_f32_16x16x32_bf16 v[220:223],a[248:251],v[48:51],v[220:223]\n"
"ds_read_b128 a[228:231],v8 offset:28672\n"
"v_add_f32 v12,v12,v105\n"
"v_mfma_f32_16x16x32_bf16 v[220:223],a[252:255],v[52:55],v[220:223]\n"
"s_waitcnt lgkmcnt(6)\n"
"v_exp_f32 v106,v106\n"
"v_mfma_f32_16x16x32_bf16 v[160:163],a[192:195],v[40:43],v[160:163]\n"
"ds_read_b128 a[232:235],v8 offset:21504\n"
"v_add_f32 v12,v12,v106\n"
"v_mfma_f32_16x16x32_bf16 v[160:163],a[196:199],v[44:47],v[160:163]\n"
"v_exp_f32 v107,v107\n"
"v_mfma_f32_16x16x32_bf16 v[224:227],a[192:195],v[48:51],v[224:227]\n"
"ds_read_b128 a[236:239],v8 offset:29696\n"
"v_add_f32 v12,v12,v107\n"
"v_mfma_f32_16x16x32_bf16 v[224:227],a[196:199],v[52:55],v[224:227]\n"
"v_exp_f32 v108,v108\n"
"v_mfma_f32_16x16x32_bf16 v[164:167],a[200:203],v[40:43],v[164:167]\n"
"ds_read_b128 a[240:243],v8 offset:22528\n"
"v_add_f32 v12,v12,v108\n"
"v_mfma_f32_16x16x32_bf16 v[164:167],a[204:207],v[44:47],v[164:167]\n"
"v_exp_f32 v109,v109\n"
"v_mfma_f32_16x16x32_bf16 v[228:231],a[200:203],v[48:51],v[228:231]\n"
"s_add_u32 s53,s53,64\n"
"ds_read_b128 a[244:247],v8 offset:30720\n"
"v_add_f32 v12,v12,v109\n"
"v_mfma_f32_16x16x32_bf16 v[228:231],a[204:207],v[52:55],v[228:231]\n"
"s_waitcnt lgkmcnt(6)\n"
"v_exp_f32 v110,v110\n"
"v_mfma_f32_16x16x32_bf16 v[168:171],a[208:211],v[40:43],v[168:171]\n"
"s_sub_u32 s50,s50,1\n"
"ds_read_b128 a[248:251],v8 offset:23552\n"
"v_add_f32 v12,v12,v110\n"
"v_mfma_f32_16x16x32_bf16 v[168:171],a[212:215],v[44:47],v[168:171]\n"
"v_exp_f32 v111,v111\n"
"v_mfma_f32_16x16x32_bf16 v[232:235],a[208:211],v[48:51],v[232:235]\n"
"ds_read_b128 a[252:255],v8 offset:31744\n"
"v_add_f32 v12,v12,v111\n"
"v_mfma_f32_16x16x32_bf16 v[232:235],a[212:215],v[52:55],v[232:235]\n"
"s_waitcnt vmcnt(9)\n"
"s_barrier\n"
"v_exp_f32 v112,v112\n"
"v_mfma_f32_16x16x32_bf16 v[172:175],a[216:219],v[40:43],v[172:175]\n"
"s_add_u32 m0,s57,0x18000\n"
"ds_read_b128 a[192:195],v9 offset:0\n"
"v_add_f32 v13,v13,v112\n"
"v_mfma_f32_16x16x32_bf16 v[172:175],a[220:223],v[44:47],v[172:175]\n"
"buffer_load_dwordx4 v10,s[36:39],s48 offen offset:0 sc1 lds\n"
"v_mfma_f32_16x16x32_bf16 v[236:239],a[216:219],v[48:51],v[236:239]\n"
"ds_read_b128 a[196:199],v9 offset:1024\n"
"v_mfma_f32_16x16x32_bf16 v[236:239],a[220:223],v[52:55],v[236:239]\n"
"s_waitcnt lgkmcnt(6)\n"
"v_exp_f32 v113,v113\n"
"v_mfma_f32_16x16x32_bf16 v[176:179],a[224:227],v[40:43],v[176:179]\n"
"ds_read_b128 a[200:203],v9 offset:2048\n"
"v_add_f32 v13,v13,v113\n"
"v_mfma_f32_16x16x32_bf16 v[176:179],a[228:231],v[44:47],v[176:179]\n"
"buffer_load_dwordx4 v10,s[36:39],s48 offen offset:1024 sc1 lds\n"
"v_mfma_f32_16x16x32_bf16 v[240:243],a[224:227],v[48:51],v[240:243]\n"
"ds_read_b128 a[204:207],v9 offset:3072\n"
"v_mfma_f32_16x16x32_bf16 v[240:243],a[228:231],v[52:55],v[240:243]\n"
"v_exp_f32 v114,v114\n"
"v_mfma_f32_16x16x32_bf16 v[180:183],a[232:235],v[40:43],v[180:183]\n"
"ds_read_b128 a[208:211],v9 offset:16384\n"
"v_add_f32 v13,v13,v114\n"
"v_mfma_f32_16x16x32_bf16 v[180:183],a[236:239],v[44:47],v[180:183]\n"
"buffer_load_dwordx4 v10,s[36:39],s48 offen offset:2048 sc1 lds\n"
"v_mfma_f32_16x16x32_bf16 v[244:247],a[232:235],v[48:51],v[244:247]\n"
"ds_read_b128 a[212:215],v9 offset:17408\n"
"v_mfma_f32_16x16x32_bf16 v[244:247],a[236:239],v[52:55],v[244:247]\n"
"s_waitcnt lgkmcnt(6)\n"
"v_exp_f32 v115,v115\n"
"v_mfma_f32_16x16x32_bf16 v[184:187],a[240:243],v[40:43],v[184:187]\n"
"ds_read_b128 a[216:219],v9 offset:18432\n"
"v_add_f32 v13,v13,v115\n"
"v_mfma_f32_16x16x32_bf16 v[184:187],a[244:247],v[44:47],v[184:187]\n"
"buffer_load_dwordx4 v10,s[36:39],s48 offen offset:3072 sc1 lds\n"
"v_mfma_f32_16x16x32_bf16 v[248:251],a[240:243],v[48:51],v[248:251]\n"
"ds_read_b128 a[220:223],v9 offset:19456\n"
"v_mfma_f32_16x16x32_bf16 v[248:251],a[244:247],v[52:55],v[248:251]\n"
"s_add_u32 m0,m0,0x1000\n"
"v_exp_f32 v116,v116\n"
"v_mfma_f32_16x16x32_bf16 v[188:191],a[248:251],v[40:43],v[188:191]\n"
"s_cmp_lg_u32 s76,0\n"
"ds_read_b128 a[224:227],v9 offset:4096\n"
"v_add_f32 v13,v13,v116\n"
"v_mfma_f32_16x16x32_bf16 v[188:191],a[252:255],v[44:47],v[188:191]\n"
"buffer_load_dwordx4 v7,s[36:39],s48 offen offset:0 sc1 lds\n"
"v_mfma_f32_16x16x32_bf16 v[252:255],a[248:251],v[48:51],v[252:255]\n"
"s_waitcnt lgkmcnt(5)\n"
"ds_read_b128 a[228:231],v9 offset:5120\n"
"v_mfma_f32_16x16x32_bf16 v[252:255],a[252:255],v[52:55],v[252:255]\n"
"s_cbranch_scc1 L_resc_odd_%=\n"
"L_rescret_odd_%=:\n"
"v_exp_f32 v117,v117\n"
"L_even_%=:\n"
"s_waitcnt lgkmcnt(9)\n"
"v_mfma_f32_16x16x32_bf16 v[64:67],a[192:195],a[128:131],v[16:19]\n"
"ds_read_b128 a[232:235],v9 offset:6144\n"
"v_add_f32 v13,v13,v117\n"
"s_waitcnt lgkmcnt(9)\n"
"v_mfma_f32_16x16x32_bf16 v[64:67],a[196:199],a[132:135],v[64:67]\n"
"buffer_load_dwordx4 v7,s[36:39],s48 offen offset:1024 sc1 lds\n"
"s_waitcnt lgkmcnt(8)\n"
"v_mfma_f32_16x16x32_bf16 v[64:67],a[200:203],a[136:139],v[64:67]\n"
"ds_read_b128 a[236:239],v9 offset:7168\n"
"v_cvt_pk_bf16_f32 v40,v96,v97\n"
"s_waitcnt lgkmcnt(8)\n"
"v_mfma_f32_16x16x32_bf16 v[64:67],a[204:207],a[140:143],v[64:67]\n"
"s_waitcnt lgkmcnt(4)\n"
"v_exp_f32 v118,v118\n"
"v_mfma_f32_16x16x32_bf16 v[64:67],a[208:211],a[144:147],v[64:67]\n"
"ds_read_b128 a[240:243],v9 offset:20480\n"
"v_cvt_pk_bf16_f32 v41,v98,v99\n"
"v_mfma_f32_16x16x32_bf16 v[64:67],a[212:215],a[148:151],v[64:67]\n"
"buffer_load_dwordx4 v7,s[36:39],s48 offen offset:2048 sc1 lds\n"
"v_mfma_f32_16x16x32_bf16 v[64:67],a[216:219],a[152:155],v[64:67]\n"
"ds_read_b128 a[244:247],v9 offset:21504\n"
"v_cvt_pk_bf16_f32 v42,v100,v101\n"
"v_mfma_f32_16x16x32_bf16 v[64:67],a[220:223],a[156:159],v[64:67]\n"
"v_exp_f32 v119,v119\n"
"v_mfma_f32_16x16x32_bf16 v[80:83],a[192:195],a[160:163],v[20:23]\n"
"ds_read_b128 a[248:251],v9 offset:22528\n"
"v_cvt_pk_bf16_f32 v43,v102,v103\n"
"v_mfma_f32_16x16x32_bf16 v[80:83],a[196:199],a[164:167],v[80:83]\n"
"buffer_load_dwordx4 v7,s[36:39],s48 offen offset:3072 sc1 lds\n"
"v_mfma_f32_16x16x32_bf16 v[80:83],a[200:203],a[168:171],v[80:83]\n"
"s_add_u32 s48,s48,0x10000\n"
"ds_read_b128 a[252:255],v9 offset:23552\n"
"v_cvt_pk_bf16_f32 v44,v104,v105\n"
"v_mfma_f32_16x16x32_bf16 v[80:83],a[204:207],a[172:175],v[80:83]\n"
"v_exp_f32 v120,v120\n"
"v_mfma_f32_16x16x32_bf16 v[80:83],a[208:211],a[176:179],v[80:83]\n"
"ds_read_b128 a[192:195],v9 offset:8192\n"
"v_max3_f32 v11,v64,v65,v66\n"
"v_mfma_f32_16x16x32_bf16 v[80:83],a[212:215],a[180:183],v[80:83]\n"
"v_cvt_pk_bf16_f32 v45,v106,v107\n"
"v_cvt_pk_bf16_f32 v46,v108,v109\n"
"v_mfma_f32_16x16x32_bf16 v[80:83],a[216:219],a[184:187],v[80:83]\n"
"ds_read_b128 a[196:199],v9 offset:9216\n"
"v_max_f32 v11,v11,v67\n"
"v_mfma_f32_16x16x32_bf16 v[80:83],a[220:223],a[188:191],v[80:83]\n"
"s_waitcnt lgkmcnt(6)\n"
"v_exp_f32 v121,v121\n"
"v_mfma_f32_16x16x32_bf16 v[68:71],a[224:227],a[128:131],v[16:19]\n"
"ds_read_b128 a[200:203],v9 offset:10240\n"
"v_cvt_pk_bf16_f32 v47,v110,v111\n"
"v_mfma_f32_16x16x32_bf16 v[68:71],a[228:231],a[132:135],v[68:71]\n"
"v_exp_f32 v122,v122\n"
"v_mfma_f32_16x16x32_bf16 v[68:71],a[232:235],a[136:139],v[68:71]\n"
"ds_read_b128 a[204:207],v9 offset:11264\n"
"v_cvt_pk_bf16_f32 v48,v112,v113\n"
"v_mfma_f32_16x16x32_bf16 v[68:71],a[236:239],a[140:143],v[68:71]\n"
"s_waitcnt lgkmcnt(4)\n"
"v_exp_f32 v123,v123\n"
"v_mfma_f32_16x16x32_bf16 v[68:71],a[240:243],a[144:147],v[68:71]\n"
"ds_read_b128 a[208:211],v9 offset:24576\n"
"v_max3_f32 v11,v11,v80,v81\n"
"v_mfma_f32_16x16x32_bf16 v[68:71],a[244:247],a[148:151],v[68:71]\n"
"v_exp_f32 v124,v124\n"
"v_mfma_f32_16x16x32_bf16 v[68:71],a[248:251],a[152:155],v[68:71]\n"
"ds_read_b128 a[212:215],v9 offset:25600\n"
"v_max3_f32 v11,v11,v82,v83\n"
"v_mfma_f32_16x16x32_bf16 v[68:71],a[252:255],a[156:159],v[68:71]\n"
"v_exp_f32 v125,v125\n"
"v_mfma_f32_16x16x32_bf16 v[84:87],a[224:227],a[160:163],v[20:23]\n"
"ds_read_b128 a[216:219],v9 offset:26624\n"
"v_cvt_pk_bf16_f32 v49,v114,v115\n"
"v_mfma_f32_16x16x32_bf16 v[84:87],a[228:231],a[164:167],v[84:87]\n"
"v_exp_f32 v126,v126\n"
"v_mfma_f32_16x16x32_bf16 v[84:87],a[232:235],a[168:171],v[84:87]\n"
"ds_read_b128 a[220:223],v9 offset:27648\n"
"v_cvt_pk_bf16_f32 v50,v116,v117\n"
"v_mfma_f32_16x16x32_bf16 v[84:87],a[236:239],a[172:175],v[84:87]\n"
"v_exp_f32 v127,v127\n"
"v_mfma_f32_16x16x32_bf16 v[84:87],a[240:243],a[176:179],v[84:87]\n"
"ds_read_b128 a[224:227],v9 offset:12288\n"
"v_max3_f32 v11,v11,v68,v69\n"
"v_mfma_f32_16x16x32_bf16 v[84:87],a[244:247],a[180:183],v[84:87]\n"
"v_add_f32 v13,v13,v118\n"
"v_add_f32 v13,v13,v119\n"
"v_mfma_f32_16x16x32_bf16 v[84:87],a[248:251],a[184:187],v[84:87]\n"
"ds_read_b128 a[228:231],v9 offset:13312\n"
"v_max3_f32 v11,v11,v70,v71\n"
"v_mfma_f32_16x16x32_bf16 v[84:87],a[252:255],a[188:191],v[84:87]\n"
"s_waitcnt lgkmcnt(6)\n"
"v_cvt_pk_bf16_f32 v51,v118,v119\n"
"v_add_f32 v13,v13,v120\n"
"v_mfma_f32_16x16x32_bf16 v[72:75],a[192:195],a[128:131],v[16:19]\n"
"ds_read_b128 a[232:235],v9 offset:14336\n"
"v_add_f32 v13,v13,v121\n"
"v_mfma_f32_16x16x32_bf16 v[72:75],a[196:199],a[132:135],v[72:75]\n"
"v_cvt_pk_bf16_f32 v52,v120,v121\n"
"v_add_f32 v13,v13,v122\n"
"v_mfma_f32_16x16x32_bf16 v[72:75],a[200:203],a[136:139],v[72:75]\n"
"ds_read_b128 a[236:239],v9 offset:15360\n"
"v_add_f32 v13,v13,v123\n"
"v_mfma_f32_16x16x32_bf16 v[72:75],a[204:207],a[140:143],v[72:75]\n"
"s_waitcnt lgkmcnt(4)\n"
"v_cvt_pk_bf16_f32 v53,v122,v123\n"
"v_add_f32 v13,v13,v124\n"
"v_mfma_f32_16x16x32_bf16 v[72:75],a[208:211],a[144:147],v[72:75]\n"
"ds_read_b128 a[240:243],v9 offset:28672\n"
"v_max3_f32 v11,v11,v84,v85\n"
"v_mfma_f32_16x16x32_bf16 v[72:75],a[212:215],a[148:151],v[72:75]\n"
"v_add_f32 v13,v13,v125\n"
"v_cvt_pk_bf16_f32 v54,v124,v125\n"
"v_mfma_f32_16x16x32_bf16 v[72:75],a[216:219],a[152:155],v[72:75]\n"
"ds_read_b128 a[244:247],v9 offset:29696\n"
"v_max3_f32 v11,v11,v86,v87\n"
"v_mfma_f32_16x16x32_bf16 v[72:75],a[220:223],a[156:159],v[72:75]\n"
"v_add_f32 v13,v13,v126\n"
"v_add_f32 v13,v13,v127\n"
"v_mfma_f32_16x16x32_bf16 v[88:91],a[192:195],a[160:163],v[20:23]\n"
"ds_read_b128 a[248:251],v9 offset:30720\n"
"v_cvt_pk_bf16_f32 v55,v126,v127\n"
"v_mfma_f32_16x16x32_bf16 v[88:91],a[196:199],a[164:167],v[88:91]\n"
"v_mfma_f32_16x16x32_bf16 v[88:91],a[200:203],a[168:171],v[88:91]\n"
"s_waitcnt lgkmcnt(3)\n"
"ds_read_b128 a[252:255],v9 offset:31744\n"
"v_mfma_f32_16x16x32_bf16 v[88:91],a[204:207],a[172:175],v[88:91]\n"
"s_waitcnt vmcnt(8)\n"
"s_barrier\n"
"v_mfma_f32_16x16x32_bf16 v[88:91],a[208:211],a[176:179],v[88:91]\n"
"s_add_u32 m0,s57,0x0\n"
"ds_read_b128 a[192:195],v8 offset:32768\n"
"v_max3_f32 v11,v11,v72,v73\n"
"v_mfma_f32_16x16x32_bf16 v[88:91],a[212:215],a[180:183],v[88:91]\n"
"buffer_load_dwordx4 v10,s[36:39],s49 offen offset:0 sc1 lds\n"
"v_mfma_f32_16x16x32_bf16 v[88:91],a[216:219],a[184:187],v[88:91]\n"
"s_cmp_eq_u32 s58,0\n"
"ds_read_b128 a[196:199],v8 offset:40960\n"
"v_max3_f32 v11,v11,v74,v75\n"
"v_mfma_f32_16x16x32_bf16 v[88:91],a[220:223],a[188:191],v[88:91]\n"
"s_cbranch_scc0 L_pubq_%=\n"
"L_pubq_ret_%=:\n"
"v_mfma_f32_16x16x32_bf16 v[76:79],a[224:227],a[128:131],v[16:19]\n"
"ds_read_b128 a[200:203],v8 offset:33792\n"
"v_mfma_f32_16x16x32_bf16 v[76:79],a[228:231],a[132:135],v[76:79]\n"
"buffer_load_dwordx4 v10,s[36:39],s49 offen offset:1024 sc1 lds\n"
"v_mfma_f32_16x16x32_bf16 v[76:79],a[232:235],a[136:139],v[76:79]\n"
"ds_read_b128 a[204:207],v8 offset:41984\n"
"v_mfma_f32_16x16x32_bf16 v[76:79],a[236:239],a[140:143],v[76:79]\n"
"s_waitcnt lgkmcnt(4)\n"
"v_mfma_f32_16x16x32_bf16 v[76:79],a[240:243],a[144:147],v[76:79]\n"
"ds_read_b128 a[208:211],v8 offset:34816\n"
"v_max3_f32 v11,v11,v88,v89\n"
"v_mfma_f32_16x16x32_bf16 v[76:79],a[244:247],a[148:151],v[76:79]\n"
"buffer_load_dwordx4 v10,s[36:39],s49 offen offset:2048 sc1 lds\n"
"v_mfma_f32_16x16x32_bf16 v[76:79],a[248:251],a[152:155],v[76:79]\n"
"ds_read_b128 a[212:215],v8 offset:43008\n"
"v_max3_f32 v11,v11,v90,v91\n"
"v_mfma_f32_16x16x32_bf16 v[76:79],a[252:255],a[156:159],v[76:79]\n"
"v_mfma_f32_16x16x32_bf16 v[92:95],a[224:227],a[160:163],v[20:23]\n"
"ds_read_b128 a[216:219],v8 offset:35840\n"
"v_mfma_f32_16x16x32_bf16 v[92:95],a[228:231],a[164:167],v[92:95]\n"
"buffer_load_dwordx4 v10,s[36:39],s49 offen offset:3072 sc1 lds\n"
"v_mfma_f32_16x16x32_bf16 v[92:95],a[232:235],a[168:171],v[92:95]\n"
"ds_read_b128 a[220:223],v8 offset:44032\n"
"v_mfma_f32_16x16x32_bf16 v[92:95],a[236:239],a[172:175],v[92:95]\n"
"v_mfma_f32_16x16x32_bf16 v[92:95],a[240:243],a[176:179],v[92:95]\n"
"s_add_u32 m0,m0,0x1000\n"
"ds_read_b128 a[224:227],v8 offset:36864\n"
"v_max3_f32 v11,v11,v76,v77\n"
"v_mfma_f32_16x16x32_bf16 v[92:95],a[244:247],a[180:183],v[92:95]\n"
"buffer_load_dwordx4 v7,s[36:39],s49 offen offset:0 sc1 lds\n"
"v_mfma_f32_16x16x32_bf16 v[92:95],a[248:251],a[184:187],v[92:95]\n"
"s_cmp_le_i32 s50,s52\n"
"ds_read_b128 a[228:231],v8 offset:45056\n"
"v_max3_f32 v11,v11,v78,v79\n"
"v_mfma_f32_16x16x32_bf16 v[92:95],a[252:255],a[188:191],v[92:95]\n"
"s_waitcnt lgkmcnt(6)\n"
"v_mfma_f32_16x16x32_bf16 v[128:131],a[192:195],v[40:43],v[128:131]\n"
"s_cselect_b32 s51,s80,s81\n"
"ds_read_b128 a[232:235],v8 offset:37888\n"
"v_mfma_f32_16x16x32_bf16 v[128:131],a[196:199],v[44:47],v[128:131]\n"
"buffer_load_dwordx4 v7,s[36:39],s49 offen offset:1024 sc1 lds\n"
"v_mfma_f32_16x16x32_bf16 v[192:195],a[192:195],v[48:51],v[192:195]\n"
"ds_read_b128 a[236:239],v8 offset:46080\n"
"v_mfma_f32_16x16x32_bf16 v[192:195],a[196:199],v[52:55],v[192:195]\n"
"v_mfma_f32_16x16x32_bf16 v[132:135],a[200:203],v[40:43],v[132:135]\n"
"ds_read_b128 a[240:243],v8 offset:38912\n"
"v_max3_f32 v11,v11,v92,v93\n"
"v_mfma_f32_16x16x32_bf16 v[132:135],a[204:207],v[44:47],v[132:135]\n"
"v_max3_f32 v11,v11,v94,v95\n"
"v_cmp_nge_f32 vcc,s51,v11\n"
"v_mfma_f32_16x16x32_bf16 v[196:199],a[200:203],v[48:51],v[196:199]\n"
"s_waitcnt lgkmcnt(5)\n"
"ds_read_b128 a[244:247],v8 offset:47104\n"
"v_mfma_f32_16x16x32_bf16 v[196:199],a[204:207],v[52:55],v[196:199]\n"
"s_cbranch_vccnz L_slow_even_%=\n"
"L_slowret_even_%=:\n"
"v_exp_f32 v64,v64\n"
"v_mfma_f32_16x16x32_bf16 v[136:139],a[208:211],v[40:43],v[136:139]\n"
"ds_read_b128 a[248:251],v8 offset:39936\n"
"v_add_f32 v12,v12,v64\n"
"v_mfma_f32_16x16x32_bf16 v[136:139],a[212:215],v[44:47],v[136:139]\n"
"buffer_load_dwordx4 v7,s[36:39],s49 offen offset:2048 sc1 lds\n"
"v_mfma_f32_16x16x32_bf16 v[200:203],a[208:211],v[48:51],v[200:203]\n"
"ds_read_b128 a[252:255],v8 offset:48128\n"
"v_mfma_f32_16x16x32_bf16 v[200:203],a[212:215],v[52:55],v[200:203]\n"
"v_exp_f32 v65,v65\n"
"v_mfma_f32_16x16x32_bf16 v[140:143],a[216:219],v[40:43],v[140:143]\n"
"ds_read_b128 a[192:195],v8 offset:49152\n"
"v_add_f32 v12,v12,v65\n"
"v_mfma_f32_16x16x32_bf16 v[140:143],a[220:223],v[44:47],v[140:143]\n"
"buffer_load_dwordx4 v7,s[36:39],s49 offen offset:3072 sc1 lds\n"
"v_mfma_f32_16x16x32_bf16 v[204:207],a[216:219],v[48:51],v[204:207]\n"
"s_add_u32 s49,s49,0x10000\n"
"ds_read_b128 a[196:199],v8 offset:57344\n"
"v_mfma_f32_16x16x32_bf16 v[204:207],a[220:223],v[52:55],v[204:207]\n"
"s_waitcnt lgkmcnt(6)\n"
"v_exp_f32 v66,v66\n"
"v_mfma_f32_16x16x32_bf16 v[144:147],a[224:227],v[40:43],v[144:147]\n"
"ds_read_b128 a[200:203],v8 offset:50176\n"
"v_add_f32 v12,v12,v66\n"
"v_mfma_f32_16x16x32_bf16 v[144:147],a[228:231],v[44:47],v[144:147]\n"
"v_exp_f32 v67,v67\n"
"v_mfma_f32_16x16x32_bf16 v[208:211],a[224:227],v[48:51],v[208:211]\n"
"ds_read_b128 a[204:207],v8 offset:58368\n"
"v_add_f32 v12,v12,v67\n"
"v_mfma_f32_16x16x32_bf16 v[208:211],a[228:231],v[52:55],v[208:211]\n"
"v_exp_f32 v68,v68\n"
"v_mfma_f32_16x16x32_bf16 v[148:151],a[232:235],v[40:43],v[148:151]\n"
"ds_read_b128 a[208:211],v8 offset:51200\n"
"v_add_f32 v12,v12,v68\n"
"v_mfma_f32_16x16x32_bf16 v[148:151],a[236:239],v[44:47],v[148:151]\n"
"v_exp_f32 v69,v69\n"
"v_mfma_f32_16x16x32_bf16 v[212:215],a[232:235],v[48:51],v[212:215]\n"
"ds_read_b128 a[212:215],v8 offset:59392\n"
"v_add_f32 v12,v12,v69\n"
"v_mfma_f32_16x16x32_bf16 v[212:215],a[236:239],v[52:55],v[212:215]\n"
"s_waitcnt lgkmcnt(6)\n"
"v_exp_f32 v70,v70\n"
"v_mfma_f32_16x16x32_bf16 v[152:155],a[240:243],v[40:43],v[152:155]\n"
"ds_read_b128 a[216:219],v8 offset:52224\n"
"v_add_f32 v12,v12,v70\n"
"v_mfma_f32_16x16x32_bf16 v[152:155],a[244:247],v[44:47],v[152:155]\n"
"v_exp_f32 v71,v71\n"
"v_mfma_f32_16x16x32_bf16 v[216:219],a[240:243],v[48:51],v[216:219]\n"
"ds_read_b128 a[220:223],v8 offset:60416\n"
"v_add_f32 v12,v12,v71\n"
"v_mfma_f32_16x16x32_bf16 v[216:219],a[244:247],v[52:55],v[216:219]\n"
"v_exp_f32 v72,v72\n"
"v_mfma_f32_16x16x32_bf16 v[156:159],a[248:251],v[40:43],v[156:159]\n"
"ds_read_b128 a[224:227],v8 offset:53248\n"
"v_add_f32 v12,v12,v72\n"
"v_mfma_f32_16x16x32_bf16 v[156:159],a[252:255],v[44:47],v[156:159]\n"
"v_exp_f32 v73,v73\n"
"v_mfma_f32_16x16x32_bf16 v[220:223],a[248:251],v[48:51],v[220:223]\n"
"ds_read_b128 a[228:231],v8 offset:61440\n"
"v_add_f32 v12,v12,v73\n"
"v_mfma_f32_16x16x32_bf16 v[220:223],a[252:255],v[52:55],v[220:223]\n"
"s_waitcnt lgkmcnt(6)\n"
"v_exp_f32 v74,v74\n"
"v_mfma_f32_16x16x32_bf16 v[160:163],a[192:195],v[40:43],v[160:163]\n"
"ds_read_b128 a[232:235],v8 offset:54272\n"
"v_add_f32 v12,v12,v74\n"
"v_mfma_f32_16x16x32_bf16 v[160:163],a[196:199],v[44:47],v[160:163]\n"
"v_exp_f32 v75,v75\n"
"v_mfma_f32_16x16x32_bf16 v[224:227],a[192:195],v[48:51],v[224:227]\n"
"ds_read_b128 a[236:239],v8 offset:62464\n"
"v_add_f32 v12,v12,v75\n"
"v_mfma_f32_16x16x32_bf16 v[224:227],a[196:199],v[52:55],v[224:227]\n"
"v_exp_f32 v76,v76\n"
"v_mfma_f32_16x16x32_bf16 v[164:167],a[200:203],v[40:43],v[164:167]\n"
"s_cmp_eq_u32 s50,1\n"
"ds_read_b128 a[240:243],v8 offset:55296\n"
"v_add_f32 v12,v12,v76\n"
"v_mfma_f32_16x16x32_bf16 v[164:167],a[204:207],v[44:47],v[164:167]\n"
"s_cselect_b32 s48,s55,s48\n"
"v_exp_f32 v77,v77\n"
"v_mfma_f32_16x16x32_bf16 v[228:231],a[200:203],v[48:51],v[228:231]\n"
"s_add_u32 s53,s53,64\n"
"ds_read_b128 a[244:247],v8 offset:63488\n"
"v_add_f32 v12,v12,v77\n"
"v_mfma_f32_16x16x32_bf16 v[228:231],a[204:207],v[52:55],v[228:231]\n"
"s_waitcnt lgkmcnt(6)\n"
"v_exp_f32 v78,v78\n"
"v_mfma_f32_16x16x32_bf16 v[168:171],a[208:211],v[40:43],v[168:171]\n"
"s_sub_u32 s50,s50,1\n"
"ds_read_b128 a[248:251],v8 offset:56320\n"
"v_add_f32 v12,v12,v78\n"
"v_mfma_f32_16x16x32_bf16 v[168:171],a[212:215],v[44:47],v[168:171]\n"
"v_exp_f32 v79,v79\n"
"v_mfma_f32_16x16x32_bf16 v[232:235],a[208:211],v[48:51],v[232:235]\n"
"ds_read_b128 a[252:255],v8 offset:64512\n"
"v_add_f32 v12,v12,v79\n"
"v_mfma_f32_16x16x32_bf16 v[232:235],a[212:215],v[52:55],v[232:235]\n"
"s_waitcnt vmcnt(8)\n"
"s_barrier\n"
"v_exp_f32 v80,v80\n"
"v_mfma_f32_16x16x32_bf16 v[172:175],a[216:219],v[40:43],v[172:175]\n"
"s_add_u32 m0,s57,0x10000\n"
"ds_read_b128 a[192:195],v9 offset:32768\n"
"v_add_f32 v13,v13,v80\n"
"v_mfma_f32_16x16x32_bf16 v[172:175],a[220:223],v[44:47],v[172:175]\n"
"buffer_load_dwordx4 v10,s[36:39],s48 offen offset:0 sc1 lds\n"
"v_mfma_f32_16x16x32_bf16 v[236:239],a[216:219],v[48:51],v[236:239]\n"
"ds_read_b128 a[196:199],v9 offset:33792\n"
"v_mfma_f32_16x16x32_bf16 v[236:239],a[220:223],v[52:55],v[236:239]\n"
"s_waitcnt lgkmcnt(6)\n"
"v_exp_f32 v81,v81\n"
"v_mfma_f32_16x16x32_bf16 v[176:179],a[224:227],v[40:43],v[176:179]\n"
"ds_read_b128 a[200:203],v9 offset:34816\n"
"v_add_f32 v13,v13,v81\n"
"v_mfma_f32_16x16x32_bf16 v[176:179],a[228:231],v[44:47],v[176:179]\n"
"buffer_load_dwordx4 v10,s[36:39],s48 offen offset:1024 sc1 lds\n"
"v_mfma_f32_16x16x32_bf16 v[240:243],a[224:227],v[48:51],v[240:243]\n"
"ds_read_b128 a[204:207],v9 offset:35840\n"
"v_mfma_f32_16x16x32_bf16 v[240:243],a[228:231],v[52:55],v[240:243]\n"
"v_exp_f32 v82,v82\n"
"v_mfma_f32_16x16x32_bf16 v[180:183],a[232:235],v[40:43],v[180:183]\n"
"ds_read_b128 a[208:211],v9 offset:49152\n"
"v_add_f32 v13,v13,v82\n"
"v_mfma_f32_16x16x32_bf16 v[180:183],a[236:239],v[44:47],v[180:183]\n"
"buffer_load_dwordx4 v10,s[36:39],s48 offen offset:2048 sc1 lds\n"
"v_mfma_f32_16x16x32_bf16 v[244:247],a[232:235],v[48:51],v[244:247]\n"
"ds_read_b128 a[212:215],v9 offset:50176\n"
"v_mfma_f32_16x16x32_bf16 v[244:247],a[236:239],v[52:55],v[244:247]\n"
"s_waitcnt lgkmcnt(6)\n"
"v_exp_f32 v83,v83\n"
"v_mfma_f32_16x16x32_bf16 v[184:187],a[240:243],v[40:43],v[184:187]\n"
"ds_read_b128 a[216:219],v9 offset:51200\n"
"v_add_f32 v13,v13,v83\n"
"v_mfma_f32_16x16x32_bf16 v[184:187],a[244:247],v[44:47],v[184:187]\n"
"buffer_load_dwordx4 v10,s[36:39],s48 offen offset:3072 sc1 lds\n"
"v_mfma_f32_16x16x32_bf16 v[248:251],a[240:243],v[48:51],v[248:251]\n"
"ds_read_b128 a[220:223],v9 offset:52224\n"
"v_mfma_f32_16x16x32_bf16 v[248:251],a[244:247],v[52:55],v[248:251]\n"
"s_add_u32 m0,m0,0x1000\n"
"v_exp_f32 v84,v84\n"
"v_mfma_f32_16x16x32_bf16 v[188:191],a[248:251],v[40:43],v[188:191]\n"
"s_cmp_lg_u32 s76,0\n"
"ds_read_b128 a[224:227],v9 offset:36864\n"
"v_add_f32 v13,v13,v84\n"
"v_mfma_f32_16x16x32_bf16 v[188:191],a[252:255],v[44:47],v[188:191]\n"
"buffer_load_dwordx4 v7,s[36:39],s48 offen offset:0 sc1 lds\n"
"v_mfma_f32_16x16x32_bf16 v[252:255],a[248:251],v[48:51],v[252:255]\n"
"s_waitcnt lgkmcnt(5)\n"
"ds_read_b128 a[228:231],v9 offset:37888\n"
"v_mfma_f32_16x16x32_bf16 v[252:255],a[252:255],v[52:55],v[252:255]\n"
"s_cbranch_scc1 L_resc_even_%=\n"
"L_rescret_even_%=:\n"
"v_exp_f32 v85,v85\n"
"s_cmp_eq_u32 s50,0\n"
"s_cbranch_scc0 L_loop_%=\n"
"L_last_%=:\n"
"s_cmp_eq_u32 s56,1\n"
"s_cbranch_scc1 L_lastb_%=\n"
"s_waitcnt lgkmcnt(9)\n"
"v_mfma_f32_16x16x32_bf16 v[96:99],a[192:195],a[128:131],v[16:19]\n"
"ds_read_b128 a[232:235],v9 offset:38912\n"
"v_add_f32 v13,v13,v85\n"
"s_waitcnt lgkmcnt(9)\n"
"v_mfma_f32_16x16x32_bf16 v[96:99],a[196:199],a[132:135],v[96:99]\n"
"buffer_load_dwordx4 v7,s[36:39],s48 offen offset:1024 sc1 lds\n"
"s_waitcnt lgkmcnt(8)\n"
"v_mfma_f32_16x16x32_bf16 v[96:99],a[200:203],a[136:139],v[96:99]\n"
"ds_read_b128 a[236:239],v9 offset:39936\n"
"v_cvt_pk_bf16_f32 v40,v64,v65\n"
"s_waitcnt lgkmcnt(8)\n"
"v_mfma_f32_16x16x32_bf16 v[96:99],a[204:207],a[140:143],v[96:99]\n"
"s_waitcnt lgkmcnt(4)\n"
"v_exp_f32 v86,v86\n"
"v_mfma_f32_16x16x32_bf16 v[96:99],a[208:211],a[144:147],v[96:99]\n"
"ds_read_b128 a[240:243],v9 offset:53248\n"
"v_cvt_pk_bf16_f32 v41,v66,v67\n"
"v_mfma_f32_16x16x32_bf16 v[96:99],a[212:215],a[148:151],v[96:99]\n"
"buffer_load_dwordx4 v7,s[36:39],s48 offen offset:2048 sc1 lds\n"
"v_mfma_f32_16x16x32_bf16 v[96:99],a[216:219],a[152:155],v[96:99]\n"
"ds_read_b128 a[244:247],v9 offset:54272\n"
"v_cvt_pk_bf16_f32 v42,v68,v69\n"
"v_mfma_f32_16x16x32_bf16 v[96:99],a[220:223],a[156:159],v[96:99]\n"
"v_exp_f32 v87,v87\n"
"v_mfma_f32_16x16x32_bf16 v[112:115],a[192:195],a[160:163],v[20:23]\n"
"ds_read_b128 a[248:251],v9 offset:55296\n"
"v_cvt_pk_bf16_f32 v43,v70,v71\n"
"v_mfma_f32_16x16x32_bf16 v[112:115],a[196:199],a[164:167],v[112:115]\n"
"buffer_load_dwordx4 v7,s[36:39],s48 offen offset:3072 sc1 lds\n"
"v_mfma_f32_16x16x32_bf16 v[112:115],a[200:203],a[168:171],v[112:115]\n"
"s_add_u32 s48,s48,0x10000\n"
"ds_read_b128 a[252:255],v9 offset:56320\n"
"v_cvt_pk_bf16_f32 v44,v72,v73\n"
"v_mfma_f32_16x16x32_bf16 v[112:115],a[204:207],a[172:175],v[112:115]\n"
"v_exp_f32 v88,v88\n"
"v_mfma_f32_16x16x32_bf16 v[112:115],a[208:211],a[176:179],v[112:115]\n"
"s_add_u32 s84,s84,0x400\n"
"ds_read_b128 a[192:195],v9 offset:40960\n"
"v_max3_f32 v11,v96,v97,v98\n"
"v_mfma_f32_16x16x32_bf16 v[112:115],a[212:215],a[180:183],v[112:115]\n"
"s_addc_u32 s85,s85,0\n"
"v_mov_b32 v1,0\n"
"v_cvt_pk_bf16_f32 v45,v74,v75\n"
"v_mfma_f32_16x16x32_bf16 v[112:115],a[216:219],a[184:187],v[112:115]\n"
"ds_read_b128 a[196:199],v9 offset:41984\n"
"v_max_f32 v11,v11,v99\n"
"v_mfma_f32_16x16x32_bf16 v[112:115],a[220:223],a[188:191],v[112:115]\n"
"s_waitcnt lgkmcnt(6)\n"
"v_exp_f32 v89,v89\n"
"v_mfma_f32_16x16x32_bf16 v[100:103],a[224:227],a[128:131],v[16:19]\n"
"ds_read_b128 a[200:203],v9 offset:43008\n"
"v_cvt_pk_bf16_f32 v46,v76,v77\n"
"v_mfma_f32_16x16x32_bf16 v[100:103],a[228:231],a[132:135],v[100:103]\n"
"v_exp_f32 v90,v90\n"
"v_mfma_f32_16x16x32_bf16 v[100:103],a[232:235],a[136:139],v[100:103]\n"
"ds_read_b128 a[204:207],v9 offset:44032\n"
"v_cvt_pk_bf16_f32 v47,v78,v79\n"
"v_mfma_f32_16x16x32_bf16 v[100:103],a[236:239],a[140:143],v[100:103]\n"
"s_waitcnt lgkmcnt(4)\n"
"v_exp_f32 v91,v91\n"
"v_mfma_f32_16x16x32_bf16 v[100:103],a[240:243],a[144:147],v[100:103]\n"
"ds_read_b128 a[208:211],v9 offset:57344\n"
"v_max3_f32 v11,v11,v112,v113\n"
"v_mfma_f32_16x16x32_bf16 v[100:103],a[244:247],a[148:151],v[100:103]\n"
"v_exp_f32 v92,v92\n"
"v_mfma_f32_16x16x32_bf16 v[100:103],a[248:251],a[152:155],v[100:103]\n"
"ds_read_b128 a[212:215],v9 offset:58368\n"
"v_max3_f32 v11,v11,v114,v115\n"
"v_mfma_f32_16x16x32_bf16 v[100:103],a[252:255],a[156:159],v[100:103]\n"
"v_exp_f32 v93,v93\n"
"v_mfma_f32_16x16x32_bf16 v[116:119],a[224:227],a[160:163],v[20:23]\n"
"ds_read_b128 a[216:219],v9 offset:59392\n"
"v_cvt_pk_bf16_f32 v48,v80,v81\n"
"v_mfma_f32_16x16x32_bf16 v[116:119],a[228:231],a[164:167],v[116:119]\n"
"v_exp_f32 v94,v94\n"
"v_mfma_f32_16x16x32_bf16 v[116:119],a[232:235],a[168:171],v[116:119]\n"
"ds_read_b128 a[220:223],v9 offset:60416\n"
"v_cvt_pk_bf16_f32 v49,v82,v83\n"
"v_mfma_f32_16x16x32_bf16 v[116:119],a[236:239],a[172:175],v[116:119]\n"
"v_exp_f32 v95,v95\n"
"v_mfma_f32_16x16x32_bf16 v[116:119],a[240:243],a[176:179],v[116:119]\n"
"ds_read_b128 a[224:227],v9 offset:45056\n"
"v_max3_f32 v11,v11,v100,v101\n"
"v_mfma_f32_16x16x32_bf16 v[116:119],a[244:247],a[180:183],v[116:119]\n"
"v_cvt_pk_bf16_f32 v50,v84,v85\n"
"v_add_f32 v13,v13,v86\n"
"v_mfma_f32_16x16x32_bf16 v[116:119],a[248:251],a[184:187],v[116:119]\n"
"ds_read_b128 a[228:231],v9 offset:46080\n"
"v_max3_f32 v11,v11,v102,v103\n"
"v_mfma_f32_16x16x32_bf16 v[116:119],a[252:255],a[188:191],v[116:119]\n"
"s_waitcnt lgkmcnt(6)\n"
"v_add_f32 v13,v13,v87\n"
"v_cvt_pk_bf16_f32 v51,v86,v87\n"
"v_mfma_f32_16x16x32_bf16 v[104:107],a[192:195],a[128:131],v[16:19]\n"
"ds_read_b128 a[232:235],v9 offset:47104\n"
"v_add_f32 v13,v13,v88\n"
"v_mfma_f32_16x16x32_bf16 v[104:107],a[196:199],a[132:135],v[104:107]\n"
"v_add_f32 v13,v13,v89\n"
"v_cvt_pk_bf16_f32 v52,v88,v89\n"
"v_mfma_f32_16x16x32_bf16 v[104:107],a[200:203],a[136:139],v[104:107]\n"
"ds_read_b128 a[236:239],v9 offset:48128\n"
"v_add_f32 v13,v13,v90\n"
"v_mfma_f32_16x16x32_bf16 v[104:107],a[204:207],a[140:143],v[104:107]\n"
"s_waitcnt lgkmcnt(4)\n"
"v_add_f32 v13,v13,v91\n"
"v_cvt_pk_bf16_f32 v53,v90,v91\n"
"v_mfma_f32_16x16x32_bf16 v[104:107],a[208:211],a[144:147],v[104:107]\n"
"ds_read_b128 a[240:243],v9 offset:61440\n"
"v_max3_f32 v11,v11,v116,v117\n"
"v_mfma_f32_16x16x32_bf16 v[104:107],a[212:215],a[148:151],v[104:107]\n"
"v_add_f32 v13,v13,v92\n"
"v_add_f32 v13,v13,v93\n"
"v_mfma_f32_16x16x32_bf16 v[104:107],a[216:219],a[152:155],v[104:107]\n"
"ds_read_b128 a[244:247],v9 offset:62464\n"
"v_max3_f32 v11,v11,v118,v119\n"
"v_mfma_f32_16x16x32_bf16 v[104:107],a[220:223],a[156:159],v[104:107]\n"
"v_cvt_pk_bf16_f32 v54,v92,v93\n"
"v_add_f32 v13,v13,v94\n"
"v_mfma_f32_16x16x32_bf16 v[120:123],a[192:195],a[160:163],v[20:23]\n"
"ds_read_b128 a[248:251],v9 offset:63488\n"
"v_add_f32 v13,v13,v95\n"
"v_mfma_f32_16x16x32_bf16 v[120:123],a[196:199],a[164:167],v[120:123]\n"
"v_cvt_pk_bf16_f32 v55,v94,v95\n"
"v_mfma_f32_16x16x32_bf16 v[120:123],a[200:203],a[168:171],v[120:123]\n"
"s_waitcnt lgkmcnt(3)\n"
"ds_read_b128 a[252:255],v9 offset:64512\n"
"v_mfma_f32_16x16x32_bf16 v[120:123],a[204:207],a[172:175],v[120:123]\n"
"s_waitcnt vmcnt(8)\n"
"s_barrier\n"
"v_mfma_f32_16x16x32_bf16 v[120:123],a[208:211],a[176:179],v[120:123]\n"
"s_add_u32 m0,s57,0x8000\n"
"ds_read_b128 a[192:195],v8 offset:0\n"
"v_max3_f32 v11,v11,v104,v105\n"
"v_mfma_f32_16x16x32_bf16 v[120:123],a[212:215],a[180:183],v[120:123]\n"
"buffer_load_dwordx4 v10,s[36:39],s49 offen offset:0 sc1 lds\n"
"v_mfma_f32_16x16x32_bf16 v[120:123],a[216:219],a[184:187],v[120:123]\n"
"ds_read_b128 a[196:199],v8 offset:8192\n"
"v_max3_f32 v11,v11,v106,v107\n"
"v_mfma_f32_16x16x32_bf16 v[120:123],a[220:223],a[188:191],v[120:123]\n"
"v_mfma_f32_16x16x32_bf16 v[108:111],a[224:227],a[128:131],v[16:19]\n"
"ds_read_b128 a[200:203],v8 offset:1024\n"
"v_mfma_f32_16x16x32_bf16 v[108:111],a[228:231],a[132:135],v[108:111]\n"
"buffer_load_dwordx4 v10,s[36:39],s49 offen offset:1024 sc1 lds\n"
"v_mfma_f32_16x16x32_bf16 v[108:111],a[232:235],a[136:139],v[108:111]\n"
"ds_read_b128 a[204:207],v8 offset:9216\n"
"v_mfma_f32_16x16x32_bf16 v[108:111],a[236:239],a[140:143],v[108:111]\n"
"s_waitcnt lgkmcnt(4)\n"
"v_mfma_f32_16x16x32_bf16 v[108:111],a[240:243],a[144:147],v[108:111]\n"
"ds_read_b128 a[208:211],v8 offset:2048\n"
"v_max3_f32 v11,v11,v120,v121\n"
"v_mfma_f32_16x16x32_bf16 v[108:111],a[244:247],a[148:151],v[108:111]\n"
"buffer_load_dwordx4 v10,s[36:39],s49 offen offset:2048 sc1 lds\n"
"v_mfma_f32_16x16x32_bf16 v[108:111],a[248:251],a[152:155],v[108:111]\n"
"ds_read_b128 a[212:215],v8 offset:10240\n"
"v_max3_f32 v11,v11,v122,v123\n"
"v_mfma_f32_16x16x32_bf16 v[108:111],a[252:255],a[156:159],v[108:111]\n"
"v_mfma_f32_16x16x32_bf16 v[124:127],a[224:227],a[160:163],v[20:23]\n"
"ds_read_b128 a[216:219],v8 offset:3072\n"
"v_mfma_f32_16x16x32_bf16 v[124:127],a[228:231],a[164:167],v[124:127]\n"
"buffer_load_dwordx4 v10,s[36:39],s49 offen offset:3072 sc1 lds\n"
"v_mfma_f32_16x16x32_bf16 v[124:127],a[232:235],a[168:171],v[124:127]\n"
"ds_read_b128 a[220:223],v8 offset:11264\n"
"v_mfma_f32_16x16x32_bf16 v[124:127],a[236:239],a[172:175],v[124:127]\n"
"v_mfma_f32_16x16x32_bf16 v[124:127],a[240:243],a[176:179],v[124:127]\n"
"s_add_u32 m0,m0,0x1000\n"
"ds_read_b128 a[224:227],v8 offset:4096\n"
"v_max3_f32 v11,v11,v108,v109\n"
"v_mfma_f32_16x16x32_bf16 v[124:127],a[244:247],a[180:183],v[124:127]\n"
"buffer_load_dwordx4 v7,s[36:39],s49 offen offset:0 sc1 lds\n"
"v_mfma_f32_16x16x32_bf16 v[124:127],a[248:251],a[184:187],v[124:127]\n"
"s_cmp_le_i32 s50,s52\n"
"ds_read_b128 a[228:231],v8 offset:12288\n"
"v_max3_f32 v11,v11,v110,v111\n"
"v_mfma_f32_16x16x32_bf16 v[124:127],a[252:255],a[188:191],v[124:127]\n"
"s_waitcnt lgkmcnt(6)\n"
"v_mfma_f32_16x16x32_bf16 v[128:131],a[192:195],v[40:43],v[128:131]\n"
"s_cselect_b32 s51,s80,s81\n"
"ds_read_b128 a[232:235],v8 offset:5120\n"
"v_mfma_f32_16x16x32_bf16 v[128:131],a[196:199],v[44:47],v[128:131]\n"
"buffer_load_dwordx4 v7,s[36:39],s49 offen offset:1024 sc1 lds\n"
"v_mfma_f32_16x16x32_bf16 v[192:195],a[192:195],v[48:51],v[192:195]\n"
"ds_read_b128 a[236:239],v8 offset:13312\n"
"v_mfma_f32_16x16x32_bf16 v[192:195],a[196:199],v[52:55],v[192:195]\n"
"v_mfma_f32_16x16x32_bf16 v[132:135],a[200:203],v[40:43],v[132:135]\n"
"ds_read_b128 a[240:243],v8 offset:6144\n"
"v_max3_f32 v11,v11,v124,v125\n"
"v_mfma_f32_16x16x32_bf16 v[132:135],a[204:207],v[44:47],v[132:135]\n"
"v_max3_f32 v11,v11,v126,v127\n"
"v_cmp_nge_f32 vcc,s51,v11\n"
"v_mfma_f32_16x16x32_bf16 v[196:199],a[200:203],v[48:51],v[196:199]\n"
"s_waitcnt lgkmcnt(5)\n"
"ds_read_b128 a[244:247],v8 offset:14336\n"
"v_mfma_f32_16x16x32_bf16 v[196:199],a[204:207],v[52:55],v[196:199]\n"
"s_cbranch_vccnz L_slow_lastf_%=\n"
"L_slowret_lastf_%=:\n"
"v_exp_f32 v96,v96\n"
"v_mfma_f32_16x16x32_bf16 v[136:139],a[208:211],v[40:43],v[136:139]\n"
"ds_read_b128 a[248:251],v8 offset:7168\n"
"v_add_f32 v12,v12,v96\n"
"v_mfma_f32_16x16x32_bf16 v[136:139],a[212:215],v[44:47],v[136:139]\n"
"buffer_load_dwordx4 v7,s[36:39],s49 offen offset:2048 sc1 lds\n"
"v_mfma_f32_16x16x32_bf16 v[200:203],a[208:211],v[48:51],v[200:203]\n"
"ds_read_b128 a[252:255],v8 offset:15360\n"
"v_mfma_f32_16x16x32_bf16 v[200:203],a[212:215],v[52:55],v[200:203]\n"
"v_exp_f32 v97,v97\n"
"v_mfma_f32_16x16x32_bf16 v[140:143],a[216:219],v[40:43],v[140:143]\n"
"ds_read_b128 a[192:195],v8 offset:16384\n"
"v_add_f32 v12,v12,v97\n"
"v_mfma_f32_16x16x32_bf16 v[140:143],a[220:223],v[44:47],v[140:143]\n"
"buffer_load_dwordx4 v7,s[36:39],s49 offen offset:3072 sc1 lds\n"
"v_mfma_f32_16x16x32_bf16 v[204:207],a[216:219],v[48:51],v[204:207]\n"
"s_add_u32 s49,s49,0x10000\n"
"ds_read_b128 a[196:199],v8 offset:24576\n"
"v_mfma_f32_16x16x32_bf16 v[204:207],a[220:223],v[52:55],v[204:207]\n"
"s_waitcnt lgkmcnt(6)\n"
"v_exp_f32 v98,v98\n"
"v_mfma_f32_16x16x32_bf16 v[144:147],a[224:227],v[40:43],v[144:147]\n"
"s_mov_b32 s77,0\n"
"s_add_u32 m0,s57,0x20000\n"
"ds_read_b128 a[200:203],v8 offset:17408\n"
"v_add_f32 v12,v12,v98\n"
"v_mfma_f32_16x16x32_bf16 v[144:147],a[228:231],v[44:47],v[144:147]\n"
"buffer_load_dwordx4 v32,s[40:43],s77 offen offset:0 lds\n"
"v_mfma_f32_16x16x32_bf16 v[208:211],a[224:227],v[48:51],v[208:211]\n"
"ds_read_b128 a[204:207],v8 offset:25600\n"
"v_mfma_f32_16x16x32_bf16 v[208:211],a[228:231],v[52:55],v[208:211]\n"
"v_exp_f32 v99,v99\n"
"v_mfma_f32_16x16x32_bf16 v[148:151],a[232:235],v[40:43],v[148:151]\n"
"ds_read_b128 a[208:211],v8 offset:18432\n"
"v_add_f32 v12,v12,v99\n"
"v_mfma_f32_16x16x32_bf16 v[148:151],a[236:239],v[44:47],v[148:151]\n"
"v_exp_f32 v100,v100\n"
"v_mfma_f32_16x16x32_bf16 v[212:215],a[232:235],v[48:51],v[212:215]\n"
"ds_read_b128 a[212:215],v8 offset:26624\n"
"v_add_f32 v12,v12,v100\n"
"v_mfma_f32_16x16x32_bf16 v[212:215],a[236:239],v[52:55],v[212:215]\n"
"s_waitcnt lgkmcnt(6)\n"
"v_exp_f32 v101,v101\n"
"v_mfma_f32_16x16x32_bf16 v[152:155],a[240:243],v[40:43],v[152:155]\n"
"ds_read_b128 a[216:219],v8 offset:19456\n"
"v_add_f32 v12,v12,v101\n"
"v_mfma_f32_16x16x32_bf16 v[152:155],a[244:247],v[44:47],v[152:155]\n"
"buffer_load_dwordx4 v33,s[40:43],s77 offen offset:1024 lds\n"
"v_mfma_f32_16x16x32_bf16 v[216:219],a[240:243],v[48:51],v[216:219]\n"
"ds_read_b128 a[220:223],v8 offset:27648\n"
"v_mfma_f32_16x16x32_bf16 v[216:219],a[244:247],v[52:55],v[216:219]\n"
"v_exp_f32 v102,v102\n"
"v_mfma_f32_16x16x32_bf16 v[156:159],a[248:251],v[40:43],v[156:159]\n"
"ds_read_b128 a[224:227],v8 offset:20480\n"
"v_add_f32 v12,v12,v102\n"
"v_mfma_f32_16x16x32_bf16 v[156:159],a[252:255],v[44:47],v[156:159]\n"
"v_exp_f32 v103,v103\n"
"v_mfma_f32_16x16x32_bf16 v[220:223],a[248:251],v[48:51],v[220:223]\n"
"ds_read_b128 a[228:231],v8 offset:28672\n"
"v_add_f32 v12,v12,v103\n"
"v_mfma_f32_16x16x32_bf16 v[220:223],a[252:255],v[52:55],v[220:223]\n"
"s_waitcnt lgkmcnt(6)\n"
"v_exp_f32 v104,v104\n"
"v_mfma_f32_16x16x32_bf16 v[160:163],a[192:195],v[40:43],v[160:163]\n"
"ds_read_b128 a[232:235],v8 offset:21504\n"
"v_add_f32 v12,v12,v104\n"
"v_mfma_f32_16x16x32_bf16 v[160:163],a[196:199],v[44:47],v[160:163]\n"
"buffer_load_dwordx4 v34,s[40:43],s77 offen offset:2048 lds\n"
"v_mfma_f32_16x16x32_bf16 v[224:227],a[192:195],v[48:51],v[224:227]\n"
"ds_read_b128 a[236:239],v8 offset:29696\n"
"v_mfma_f32_16x16x32_bf16 v[224:227],a[196:199],v[52:55],v[224:227]\n"
"v_exp_f32 v105,v105\n"
"v_mfma_f32_16x16x32_bf16 v[164:167],a[200:203],v[40:43],v[164:167]\n"
"ds_read_b128 a[240:243],v8 offset:22528\n"
"v_add_f32 v12,v12,v105\n"
"v_mfma_f32_16x16x32_bf16 v[164:167],a[204:207],v[44:47],v[164:167]\n"
"v_exp_f32 v106,v106\n"
"v_mfma_f32_16x16x32_bf16 v[228:231],a[200:203],v[48:51],v[228:231]\n"
"s_add_u32 s53,s53,64\n"
"ds_read_b128 a[244:247],v8 offset:30720\n"
"v_add_f32 v12,v12,v106\n"
"v_mfma_f32_16x16x32_bf16 v[228:231],a[204:207],v[52:55],v[228:231]\n"
"s_waitcnt lgkmcnt(6)\n"
"v_exp_f32 v107,v107\n"
"v_mfma_f32_16x16x32_bf16 v[168:171],a[208:211],v[40:43],v[168:171]\n"
"s_sub_u32 s50,s50,1\n"
"ds_read_b128 a[248:251],v8 offset:23552\n"
"v_add_f32 v12,v12,v107\n"
"v_mfma_f32_16x16x32_bf16 v[168:171],a[212:215],v[44:47],v[168:171]\n"
"buffer_load_dwordx4 v35,s[40:43],s77 offen offset:3072 lds\n"
"v_mfma_f32_16x16x32_bf16 v[232:235],a[208:211],v[48:51],v[232:235]\n"
"ds_read_b128 a[252:255],v8 offset:31744\n"
"v_mfma_f32_16x16x32_bf16 v[232:235],a[212:215],v[52:55],v[232:235]\n"
"v_exp_f32 v108,v108\n"
"v_mfma_f32_16x16x32_bf16 v[172:175],a[216:219],v[40:43],v[172:175]\n"
"v_exp_f32 v109,v109\n"
"v_mfma_f32_16x16x32_bf16 v[172:175],a[220:223],v[44:47],v[172:175]\n"
"v_exp_f32 v110,v110\n"
"v_mfma_f32_16x16x32_bf16 v[236:239],a[216:219],v[48:51],v[236:239]\n"
"v_exp_f32 v111,v111\n"
"v_mfma_f32_16x16x32_bf16 v[236:239],a[220:223],v[52:55],v[236:239]\n"
"s_waitcnt lgkmcnt(4)\n"
"v_exp_f32 v112,v112\n"
"v_mfma_f32_16x16x32_bf16 v[176:179],a[224:227],v[40:43],v[176:179]\n"
"v_exp_f32 v113,v113\n"
"v_mfma_f32_16x16x32_bf16 v[176:179],a[228:231],v[44:47],v[176:179]\n"
"s_waitcnt vmcnt(4)\n"
"s_barrier\n"
"v_add_f32 v12,v12,v108\n"
"v_add_f32 v12,v12,v109\n"
"v_mfma_f32_16x16x32_bf16 v[240:243],a[224:227],v[48:51],v[240:243]\n"
"ds_read_b128 a[192:195],v8 offset:32768\n"
"v_add_f32 v12,v12,v110\n"
"v_mfma_f32_16x16x32_bf16 v[240:243],a[228:231],v[52:55],v[240:243]\n"
"v_exp_f32 v114,v114\n"
"v_mfma_f32_16x16x32_bf16 v[180:183],a[232:235],v[40:43],v[180:183]\n"
"s_add_u32 m0,m0,0x1000\n"
"ds_read_b128 a[196:199],v8 offset:40960\n"
"v_add_f32 v12,v12,v111\n"
"v_mfma_f32_16x16x32_bf16 v[180:183],a[236:239],v[44:47],v[180:183]\n"
"buffer_load_dwordx4 v36,s[40:43],s77 offen offset:0 lds\n"
"v_mfma_f32_16x16x32_bf16 v[244:247],a[232:235],v[48:51],v[244:247]\n"
"ds_read_b128 a[200:203],v8 offset:33792\n"
"v_add_f32 v13,v13,v112\n"
"v_mfma_f32_16x16x32_bf16 v[244:247],a[236:239],v[52:55],v[244:247]\n"
"s_waitcnt lgkmcnt(3)\n"
"v_exp_f32 v115,v115\n"
"v_mfma_f32_16x16x32_bf16 v[184:187],a[240:243],v[40:43],v[184:187]\n"
"ds_read_b128 a[204:207],v8 offset:41984\n"
"v_add_f32 v13,v13,v113\n"
"v_mfma_f32_16x16x32_bf16 v[184:187],a[244:247],v[44:47],v[184:187]\n"
"buffer_load_dwordx4 v37,s[40:43],s77 offen offset:1024 lds\n"
"v_mfma_f32_16x16x32_bf16 v[248:251],a[240:243],v[48:51],v[248:251]\n"
"ds_read_b128 a[208:211],v8 offset:34816\n"
"v_add_f32 v13,v13,v114\n"
"v_mfma_f32_16x16x32_bf16 v[248:251],a[244:247],v[52:55],v[248:251]\n"
"v_exp_f32 v116,v116\n"
"v_mfma_f32_16x16x32_bf16 v[188:191],a[248:251],v[40:43],v[188:191]\n"
"s_cmp_lg_u32 s76,0\n"
"ds_read_b128 a[212:215],v8 offset:43008\n"
"v_add_f32 v13,v13,v115\n"
"v_mfma_f32_16x16x32_bf16 v[188:191],a[252:255],v[44:47],v[188:191]\n"
"global_load_dwordx2 v[2:3],v1,s[84:85] sc1\n"
"v_mfma_f32_16x16x32_bf16 v[252:255],a[248:251],v[48:51],v[252:255]\n"
"s_waitcnt lgkmcnt(4)\n"
"ds_read_b128 a[216:219],v8 offset:35840\n"
"v_add_f32 v13,v13,v116\n"
"v_mfma_f32_16x16x32_bf16 v[252:255],a[252:255],v[52:55],v[252:255]\n"
"s_cbranch_scc1 L_resc_lastf_%=\n"
"L_rescret_lastf_%=:\n"
"v_exp_f32 v117,v117\n"
"s_cmp_eq_u32 s56,0\n"
"s_cbranch_scc0 L_notA_%=\n"
"v_exp_f32 v118,v118\n"
"v_exp_f32 v119,v119\n"
"v_exp_f32 v120,v120\n"
"v_exp_f32 v121,v121\n"
"v_exp_f32 v122,v122\n"
"v_exp_f32 v123,v123\n"
"v_exp_f32 v124,v124\n"
"v_exp_f32 v125,v125\n"
"v_exp_f32 v126,v126\n"
"v_exp_f32 v127,v127\n"
"v_cvt_pk_bf16_f32 v40,v96,v97\n"
"v_cvt_pk_bf16_f32 v41,v98,v99\n"
"v_cvt_pk_bf16_f32 v42,v100,v101\n"
"v_cvt_pk_bf16_f32 v43,v102,v103\n"
"v_cvt_pk_bf16_f32 v44,v104,v105\n"
"v_cvt_pk_bf16_f32 v45,v106,v107\n"
"v_cvt_pk_bf16_f32 v46,v108,v109\n"
"v_cvt_pk_bf16_f32 v47,v110,v111\n"
"v_add_f32 v13,v13,v117\n"
"v_add_f32 v13,v13,v118\n"
"v_add_f32 v13,v13,v119\n"
"v_add_f32 v13,v13,v120\n"
"v_add_f32 v13,v13,v121\n"
"v_add_f32 v13,v13,v122\n"
"v_add_f32 v13,v13,v123\n"
"v_add_f32 v13,v13,v124\n"
"v_add_f32 v13,v13,v125\n"
"v_add_f32 v13,v13,v126\n"
"v_add_f32 v13,v13,v127\n"
"v_cvt_pk_bf16_f32 v48,v112,v113\n"
"v_cvt_pk_bf16_f32 v49,v114,v115\n"
"v_cvt_pk_bf16_f32 v50,v116,v117\n"
"v_cvt_pk_bf16_f32 v51,v118,v119\n"
"v_cvt_pk_bf16_f32 v52,v120,v121\n"
"v_cvt_pk_bf16_f32 v53,v122,v123\n"
"v_cvt_pk_bf16_f32 v54,v124,v125\n"
"v_cvt_pk_bf16_f32 v55,v126,v127\n"
"s_waitcnt vmcnt(0)\n"
"v_cmp_ne_u64 vcc,s[34:35],v[2:3]\n"
"s_nop 0\n"
"s_cbranch_vccnz L_pt_%=\n"
"L_pt_ret_%=:\n"
"s_lshl_b32 s96,s11,10\n"
"s_add_u32 s59,s96,0x10000\n"
"buffer_load_dwordx4 v[56:59],v8,s[64:67],s59 offen sc1\n"
"s_lshl_b32 s96,s11,14\n"
"v_mfma_f32_16x16x32_bf16 v[128:131],a[192:195],v[40:43],v[128:131]\n"
"ds_read_b128 a[220:223],v8 offset:44032\n"
"v_mfma_f32_16x16x32_bf16 v[128:131],a[196:199],v[44:47],v[128:131]\n"
"v_mfma_f32_16x16x32_bf16 v[192:195],a[192:195],v[48:51],v[192:195]\n"
"s_add_u32 s59,s96,0x0\n"
"ds_read_b128 a[224:227],v8 offset:36864\n"
"v_mfma_f32_16x16x32_bf16 v[192:195],a[196:199],v[52:55],v[192:195]\n"
"s_waitcnt lgkmcnt(5)\n"
"buffer_load_dwordx4 v[64:67],v8,s[64:67],s59 offen offset:0 sc1\n"
"v_mfma_f32_16x16x32_bf16 v[132:135],a[200:203],v[40:43],v[132:135]\n"
"ds_read_b128 a[228:231],v8 offset:45056\n"
"v_mfma_f32_16x16x32_bf16 v[132:135],a[204:207],v[44:47],v[132:135]\n"
"v_mfma_f32_16x16x32_bf16 v[196:199],a[200:203],v[48:51],v[196:199]\n"
"ds_read_b128 a[232:235],v8 offset:37888\n"
"v_mfma_f32_16x16x32_bf16 v[196:199],a[204:207],v[52:55],v[196:199]\n"
"s_waitcnt lgkmcnt(5)\n"
"buffer_load_dwordx4 v[68:71],v8,s[64:67],s59 offen offset:1024 sc1\n"
"v_mfma_f32_16x16x32_bf16 v[136:139],a[208:211],v[40:43],v[136:139]\n"
"ds_read_b128 a[236:239],v8 offset:46080\n"
"v_mfma_f32_16x16x32_bf16 v[136:139],a[212:215],v[44:47],v[136:139]\n"
"v_mfma_f32_16x16x32_bf16 v[200:203],a[208:211],v[48:51],v[200:203]\n"
"ds_read_b128 a[240:243],v8 offset:38912\n"
"v_mfma_f32_16x16x32_bf16 v[200:203],a[212:215],v[52:55],v[200:203]\n"
"s_waitcnt lgkmcnt(5)\n"
"buffer_load_dwordx4 v[72:75],v8,s[64:67],s59 offen offset:2048 sc1\n"
"v_mfma_f32_16x16x32_bf16 v[140:143],a[216:219],v[40:43],v[140:143]\n"
"ds_read_b128 a[244:247],v8 offset:47104\n"
"v_mfma_f32_16x16x32_bf16 v[140:143],a[220:223],v[44:47],v[140:143]\n"
"v_mfma_f32_16x16x32_bf16 v[204:207],a[216:219],v[48:51],v[204:207]\n"
"ds_read_b128 a[248:251],v8 offset:39936\n"
"v_mfma_f32_16x16x32_bf16 v[204:207],a[220:223],v[52:55],v[204:207]\n"
"s_waitcnt lgkmcnt(5)\n"
"buffer_load_dwordx4 v[76:79],v8,s[64:67],s59 offen offset:3072 sc1\n"
"v_mfma_f32_16x16x32_bf16 v[144:147],a[224:227],v[40:43],v[144:147]\n"
"ds_read_b128 a[252:255],v8 offset:48128\n"
"v_mfma_f32_16x16x32_bf16 v[144:147],a[228:231],v[44:47],v[144:147]\n"
"v_mfma_f32_16x16x32_bf16 v[208:211],a[224:227],v[48:51],v[208:211]\n"
"s_add_u32 s59,s96,0x1000\n"
"ds_read_b128 a[192:195],v8 offset:49152\n"
"v_mfma_f32_16x16x32_bf16 v[208:211],a[228:231],v[52:55],v[208:211]\n"
"s_waitcnt lgkmcnt(5)\n"
"buffer_load_dwordx4 v[80:83],v8,s[64:67],s59 offen offset:0 sc1\n"
"v_mfma_f32_16x16x32_bf16 v[148:151],a[232:235],v[40:43],v[148:151]\n"
"ds_read_b128 a[196:199],v8 offset:57344\n"
"v_mfma_f32_16x16x32_bf16 v[148:151],a[236:239],v[44:47],v[148:151]\n"
"v_mfma_f32_16x16x32_bf16 v[212:215],a[232:235],v[48:51],v[212:215]\n"
"ds_read_b128 a[200:203],v8 offset:50176\n"
"v_mfma_f32_16x16x32_bf16 v[212:215],a[236:239],v[52:55],v[212:215]\n"
"s_waitcnt lgkmcnt(5)\n"
"buffer_load_dwordx4 v[84:87],v8,s[64:67],s59 offen offset:1024 sc1\n"
"v_mfma_f32_16x16x32_bf16 v[152:155],a[240:243],v[40:43],v[152:155]\n"
"ds_read_b128 a[204:207],v8 offset:58368\n"
"v_mfma_f32_16x16x32_bf16 v[152:155],a[244:247],v[44:47],v[152:155]\n"
"v_mfma_f32_16x16x32_bf16 v[216:219],a[240:243],v[48:51],v[216:219]\n"
"ds_read_b128 a[208:211],v8 offset:51200\n"
"v_mfma_f32_16x16x32_bf16 v[216:219],a[244:247],v[52:55],v[216:219]\n"
"s_waitcnt lgkmcnt(5)\n"
"buffer_load_dwordx4 v[88:91],v8,s[64:67],s59 offen offset:2048 sc1\n"
"v_mfma_f32_16x16x32_bf16 v[156:159],a[248:251],v[40:43],v[156:159]\n"
"ds_read_b128 a[212:215],v8 offset:59392\n"
"v_mfma_f32_16x16x32_bf16 v[156:159],a[252:255],v[44:47],v[156:159]\n"
"v_mfma_f32_16x16x32_bf16 v[220:223],a[248:251],v[48:51],v[220:223]\n"
"ds_read_b128 a[216:219],v8 offset:52224\n"
"v_mfma_f32_16x16x32_bf16 v[220:223],a[252:255],v[52:55],v[220:223]\n"
"s_waitcnt lgkmcnt(5)\n"
"buffer_load_dwordx4 v[92:95],v8,s[64:67],s59 offen offset:3072 sc1\n"
"v_mfma_f32_16x16x32_bf16 v[160:163],a[192:195],v[40:43],v[160:163]\n"
"ds_read_b128 a[220:223],v8 offset:60416\n"
"v_mfma_f32_16x16x32_bf16 v[160:163],a[196:199],v[44:47],v[160:163]\n"
"v_mfma_f32_16x16x32_bf16 v[224:227],a[192:195],v[48:51],v[224:227]\n"
"s_add_u32 s59,s96,0x2000\n"
"ds_read_b128 a[224:227],v8 offset:53248\n"
"v_mfma_f32_16x16x32_bf16 v[224:227],a[196:199],v[52:55],v[224:227]\n"
"s_waitcnt lgkmcnt(5)\n"
"buffer_load_dwordx4 v[96:99],v8,s[64:67],s59 offen offset:0 sc1\n"
"v_mfma_f32_16x16x32_bf16 v[164:167],a[200:203],v[40:43],v[164:167]\n"
"ds_read_b128 a[228:231],v8 offset:61440\n"
"v_mfma_f32_16x16x32_bf16 v[164:167],a[204:207],v[44:47],v[164:167]\n"
"v_mfma_f32_16x16x32_bf16 v[228:231],a[200:203],v[48:51],v[228:231]\n"
"ds_read_b128 a[232:235],v8 offset:54272\n"
"v_mfma_f32_16x16x32_bf16 v[228:231],a[204:207],v[52:55],v[228:231]\n"
"s_waitcnt lgkmcnt(5)\n"
"buffer_load_dwordx4 v[100:103],v8,s[64:67],s59 offen offset:1024 sc1\n"
"v_mfma_f32_16x16x32_bf16 v[168:171],a[208:211],v[40:43],v[168:171]\n"
"ds_read_b128 a[236:239],v8 offset:62464\n"
"v_mfma_f32_16x16x32_bf16 v[168:171],a[212:215],v[44:47],v[168:171]\n"
"v_mfma_f32_16x16x32_bf16 v[232:235],a[208:211],v[48:51],v[232:235]\n"
"ds_read_b128 a[240:243],v8 offset:55296\n"
"v_mfma_f32_16x16x32_bf16 v[232:235],a[212:215],v[52:55],v[232:235]\n"
"s_waitcnt lgkmcnt(5)\n"
"buffer_load_dwordx4 v[104:107],v8,s[64:67],s59 offen offset:2048 sc1\n"
"v_mfma_f32_16x16x32_bf16 v[172:175],a[216:219],v[40:43],v[172:175]\n"
"ds_read_b128 a[244:247],v8 offset:63488\n"
"v_mfma_f32_16x16x32_bf16 v[172:175],a[220:223],v[44:47],v[172:175]\n"
"v_mfma_f32_16x16x32_bf16 v[236:239],a[216:219],v[48:51],v[236:239]\n"
"ds_read_b128 a[248:251],v8 offset:56320\n"
"v_mfma_f32_16x16x32_bf16 v[236:239],a[220:223],v[52:55],v[236:239]\n"
"s_waitcnt lgkmcnt(5)\n"
"buffer_load_dwordx4 v[108:111],v8,s[64:67],s59 offen offset:3072 sc1\n"
"v_mfma_f32_16x16x32_bf16 v[176:179],a[224:227],v[40:43],v[176:179]\n"
"ds_read_b128 a[252:255],v8 offset:64512\n"
"v_mfma_f32_16x16x32_bf16 v[176:179],a[228:231],v[44:47],v[176:179]\n"
"v_mfma_f32_16x16x32_bf16 v[240:243],a[224:227],v[48:51],v[240:243]\n"
"s_add_u32 s59,s96,0x3000\n"
"v_mfma_f32_16x16x32_bf16 v[240:243],a[228:231],v[52:55],v[240:243]\n"
"s_waitcnt lgkmcnt(4)\n"
"buffer_load_dwordx4 v[112:115],v8,s[64:67],s59 offen offset:0 sc1\n"
"v_mfma_f32_16x16x32_bf16 v[180:183],a[232:235],v[40:43],v[180:183]\n"
"v_mfma_f32_16x16x32_bf16 v[180:183],a[236:239],v[44:47],v[180:183]\n"
"v_mfma_f32_16x16x32_bf16 v[244:247],a[232:235],v[48:51],v[244:247]\n"
"v_mfma_f32_16x16x32_bf16 v[244:247],a[236:239],v[52:55],v[244:247]\n"
"s_waitcnt lgkmcnt(2)\n"
"buffer_load_dwordx4 v[116:119],v8,s[64:67],s59 offen offset:1024 sc1\n"
"v_mfma_f32_16x16x32_bf16 v[184:187],a[240:243],v[40:43],v[184:187]\n"
"v_mfma_f32_16x16x32_bf16 v[184:187],a[244:247],v[44:47],v[184:187]\n"
"v_mfma_f32_16x16x32_bf16 v[248:251],a[240:243],v[48:51],v[248:251]\n"
"v_mfma_f32_16x16x32_bf16 v[248:251],a[244:247],v[52:55],v[248:251]\n"
"s_waitcnt lgkmcnt(0)\n"
"buffer_load_dwordx4 v[120:123],v8,s[64:67],s59 offen offset:2048 sc1\n"
"v_mfma_f32_16x16x32_bf16 v[188:191],a[248:251],v[40:43],v[188:191]\n"
"v_mfma_f32_16x16x32_bf16 v[188:191],a[252:255],v[44:47],v[188:191]\n"
"v_mfma_f32_16x16x32_bf16 v[252:255],a[248:251],v[48:51],v[252:255]\n"
"v_mfma_f32_16x16x32_bf16 v[252:255],a[252:255],v[52:55],v[252:255]\n"
"buffer_load_dwordx4 v[124:127],v8,s[64:67],s59 offen offset:3072 sc1\n"
"v_mov_b32 v2,v12\n"
"v_mov_b32 v3,v13\n"
"s_nop 0\n"
"v_permlane32_swap_b32 v12,v2\n"
"v_permlane32_swap_b32 v13,v3\n"
"v_add_f32 v12,v12,v2\n"
"v_add_f32 v13,v13,v3\n"
"v_mov_b32 v2,v12\n"
"v_mov_b32 v3,v13\n"
"s_nop 0\n"
"v_permlane16_swap_b32 v12,v2\n"
"v_permlane16_swap_b32 v13,v3\n"
"v_add_f32 v12,v12,v2\n"
"v_add_f32 v13,v13,v3\n"
"s_waitcnt vmcnt(16)\n"
"v_max_f32 v1,v14,v56\n"
"v_sub_f32 v2,v14,v1\n"
"v_sub_f32 v3,v56,v1\n"
"v_exp_f32 v2,v2\n"
"v_exp_f32 v3,v3\n"
"s_nop 0\n"
"v_mul_f32 v4,v3,v57\n"
"v_fmac_f32 v4,v2,v12\n"
"v_rcp_f32 v4,v4\n"
"s_nop 0\n"
"v_mul_f32 v40,v2,v4\n"
"v_mul_f32 v42,v3,v4\n"
"v_mov_b32 v41,v40\n"
"v_mov_b32 v43,v42\n"
"v_max_f32 v1,v15,v58\n"
"v_sub_f32 v2,v15,v1\n"
"v_sub_f32 v3,v58,v1\n"
"v_exp_f32 v2,v2\n"
"v_exp_f32 v3,v3\n"
"s_nop 0\n"
"v_mul_f32 v4,v3,v59\n"
"v_fmac_f32 v4,v2,v13\n"
"v_rcp_f32 v4,v4\n"
"s_nop 0\n"
"v_mul_f32 v44,v2,v4\n"
"v_mul_f32 v46,v3,v4\n"
"v_mov_b32 v45,v44\n"
"v_mov_b32 v47,v46\n"
"buffer_load_dwordx4 v38,s[40:43],s77 offen offset:2048 lds\n"
"s_waitcnt vmcnt(16)\n"
"v_lshlrev_b32 v48,16,v64\n"
"v_and_b32 v49,0xffff0000,v64\n"
"v_lshlrev_b32 v50,16,v65\n"
"v_and_b32 v51,0xffff0000,v65\n"
"v_lshlrev_b32 v52,16,v66\n"
"v_and_b32 v53,0xffff0000,v66\n"
"v_lshlrev_b32 v54,16,v67\n"
"v_and_b32 v55,0xffff0000,v67\n"
"v_pk_mul_f32 v[48:49],v[48:49],v[42:43]\n"
"v_pk_mul_f32 v[50:51],v[50:51],v[42:43]\n"
"v_pk_mul_f32 v[52:53],v[52:53],v[42:43]\n"
"v_pk_mul_f32 v[54:55],v[54:55],v[42:43]\n"
"v_pk_fma_f32 v[48:49],v[128:129],v[40:41],v[48:49]\n"
"v_pk_fma_f32 v[50:51],v[130:131],v[40:41],v[50:51]\n"
"v_pk_fma_f32 v[52:53],v[132:133],v[40:41],v[52:53]\n"
"v_pk_fma_f32 v[54:55],v[134:135],v[40:41],v[54:55]\n"
"buffer_load_dwordx4 v39,s[40:43],s77 offen offset:3072 lds\n"
"v_cvt_pk_bf16_f32 v128,v48,v49\n"
"v_cvt_pk_bf16_f32 v129,v50,v51\n"
"v_cvt_pk_bf16_f32 v130,v52,v53\n"
"v_cvt_pk_bf16_f32 v131,v54,v55\n"
"s_waitcnt vmcnt(16)\n"
"v_lshlrev_b32 v48,16,v68\n"
"v_and_b32 v49,0xffff0000,v68\n"
"v_lshlrev_b32 v50,16,v69\n"
"v_and_b32 v51,0xffff0000,v69\n"
"v_lshlrev_b32 v52,16,v70\n"
"v_and_b32 v53,0xffff0000,v70\n"
"v_lshlrev_b32 v54,16,v71\n"
"v_and_b32 v55,0xffff0000,v71\n"
"v_pk_mul_f32 v[48:49],v[48:49],v[42:43]\n"
"v_pk_mul_f32 v[50:51],v[50:51],v[42:43]\n"
"v_pk_mul_f32 v[52:53],v[52:53],v[42:43]\n"
"v_pk_mul_f32 v[54:55],v[54:55],v[42:43]\n"
"s_mov_b32 s77,128\n"
"s_add_u32 m0,s57,0x10000\n"
"s_nop 0\n"
"buffer_load_dwordx4 v32,s[40:43],s77 offen offset:0 lds\n"
"v_pk_fma_f32 v[48:49],v[136:137],v[40:41],v[48:49]\n"
"v_pk_fma_f32 v[50:51],v[138:139],v[40:41],v[50:51]\n"
"v_pk_fma_f32 v[52:53],v[140:141],v[40:41],v[52:53]\n"
"v_pk_fma_f32 v[54:55],v[142:143],v[40:41],v[54:55]\n"
"v_cvt_pk_bf16_f32 v136,v48,v49\n"
"v_cvt_pk_bf16_f32 v137,v50,v51\n"
"v_cvt_pk_bf16_f32 v138,v52,v53\n"
"v_cvt_pk_bf16_f32 v139,v54,v55\n"
"s_waitcnt vmcnt(16)\n"
"v_lshlrev_b32 v48,16,v72\n"
"v_and_b32 v49,0xffff0000,v72\n"
"v_lshlrev_b32 v50,16,v73\n"
"v_and_b32 v51,0xffff0000,v73\n"
"v_lshlrev_b32 v52,16,v74\n"
"v_and_b32 v53,0xffff0000,v74\n"
"v_lshlrev_b32 v54,16,v75\n"
"v_and_b32 v55,0xffff0000,v75\n"
"buffer_load_dwordx4 v33,s[40:43],s77 offen offset:1024 lds\n"
"v_pk_mul_f32 v[48:49],v[48:49],v[42:43]\n"
"v_pk_mul_f32 v[50:51],v[50:51],v[42:43]\n"
"v_pk_mul_f32 v[52:53],v[52:53],v[42:43]\n"
"v_pk_mul_f32 v[54:55],v[54:55],v[42:43]\n"
"v_pk_fma_f32 v[48:49],v[144:145],v[40:41],v[48:49]\n"
"v_pk_fma_f32 v[50:51],v[146:147],v[40:41],v[50:51]\n"
"v_pk_fma_f32 v[52:53],v[148:149],v[40:41],v[52:53]\n"
"v_pk_fma_f32 v[54:55],v[150:151],v[40:41],v[54:55]\n"
"v_cvt_pk_bf16_f32 v144,v48,v49\n"
"v_cvt_pk_bf16_f32 v145,v50,v51\n"
"v_cvt_pk_bf16_f32 v146,v52,v53\n"
"v_cvt_pk_bf16_f32 v147,v54,v55\n"
"s_waitcnt vmcnt(16)\n"
"v_lshlrev_b32 v48,16,v76\n"
"v_and_b32 v49,0xffff0000,v76\n"
"v_lshlrev_b32 v50,16,v77\n"
"v_and_b32 v51,0xffff0000,v77\n"
"buffer_load_dwordx4 v34,s[40:43],s77 offen offset:2048 lds\n"
"v_lshlrev_b32 v52,16,v78\n"
"v_and_b32 v53,0xffff0000,v78\n"
"v_lshlrev_b32 v54,16,v79\n"
"v_and_b32 v55,0xffff0000,v79\n"
"v_pk_mul_f32 v[48:49],v[48:49],v[42:43]\n"
"v_pk_mul_f32 v[50:51],v[50:51],v[42:43]\n"
"v_pk_mul_f32 v[52:53],v[52:53],v[42:43]\n"
"v_pk_mul_f32 v[54:55],v[54:55],v[42:43]\n"
"v_pk_fma_f32 v[48:49],v[152:153],v[40:41],v[48:49]\n"
"v_pk_fma_f32 v[50:51],v[154:155],v[40:41],v[50:51]\n"
"v_pk_fma_f32 v[52:53],v[156:157],v[40:41],v[52:53]\n"
"v_pk_fma_f32 v[54:55],v[158:159],v[40:41],v[54:55]\n"
"v_cvt_pk_bf16_f32 v152,v48,v49\n"
"v_cvt_pk_bf16_f32 v153,v50,v51\n"
"v_cvt_pk_bf16_f32 v154,v52,v53\n"
"v_cvt_pk_bf16_f32 v155,v54,v55\n"
"buffer_load_dwordx4 v35,s[40:43],s77 offen offset:3072 lds\n"
"s_waitcnt vmcnt(17)\n"
"v_lshlrev_b32 v48,16,v80\n"
"v_and_b32 v49,0xffff0000,v80\n"
"v_lshlrev_b32 v50,16,v81\n"
"v_and_b32 v51,0xffff0000,v81\n"
"v_lshlrev_b32 v52,16,v82\n"
"v_and_b32 v53,0xffff0000,v82\n"
"v_lshlrev_b32 v54,16,v83\n"
"v_and_b32 v55,0xffff0000,v83\n"
"v_pk_mul_f32 v[48:49],v[48:49],v[42:43]\n"
"v_pk_mul_f32 v[50:51],v[50:51],v[42:43]\n"
"v_pk_mul_f32 v[52:53],v[52:53],v[42:43]\n"
"v_pk_mul_f32 v[54:55],v[54:55],v[42:43]\n"
"v_pk_fma_f32 v[48:49],v[160:161],v[40:41],v[48:49]\n"
"v_pk_fma_f32 v[50:51],v[162:163],v[40:41],v[50:51]\n"
"v_pk_fma_f32 v[52:53],v[164:165],v[40:41],v[52:53]\n"
"v_pk_fma_f32 v[54:55],v[166:167],v[40:41],v[54:55]\n"
"s_add_u32 m0,m0,0x1000\n"
"s_nop 0\n"
"buffer_load_dwordx4 v36,s[40:43],s77 offen offset:0 lds\n"
"v_cvt_pk_bf16_f32 v160,v48,v49\n"
"v_cvt_pk_bf16_f32 v161,v50,v51\n"
"v_cvt_pk_bf16_f32 v162,v52,v53\n"
"v_cvt_pk_bf16_f32 v163,v54,v55\n"
"s_waitcnt vmcnt(17)\n"
"v_lshlrev_b32 v48,16,v84\n"
"v_and_b32 v49,0xffff0000,v84\n"
"v_lshlrev_b32 v50,16,v85\n"
"v_and_b32 v51,0xffff0000,v85\n"
"v_lshlrev_b32 v52,16,v86\n"
"v_and_b32 v53,0xffff0000,v86\n"
"v_lshlrev_b32 v54,16,v87\n"
"v_and_b32 v55,0xffff0000,v87\n"
"v_pk_mul_f32 v[48:49],v[48:49],v[42:43]\n"
"v_pk_mul_f32 v[50:51],v[50:51],v[42:43]\n"
"v_pk_mul_f32 v[52:53],v[52:53],v[42:43]\n"
"v_pk_mul_f32 v[54:55],v[54:55],v[42:43]\n"
"buffer_load_dwordx4 v37,s[40:43],s77 offen offset:1024 lds\n"
"v_pk_fma_f32 v[48:49],v[168:169],v[40:41],v[48:49]\n"
"v_pk_fma_f32 v[50:51],v[170:171],v[40:41],v[50:51]\n"
"v_pk_fma_f32 v[52:53],v[172:173],v[40:41],v[52:53]\n"
"v_pk_fma_f32 v[54:55],v[174:175],v[40:41],v[54:55]\n"
"v_cvt_pk_bf16_f32 v168,v48,v49\n"
"v_cvt_pk_bf16_f32 v169,v50,v51\n"
"v_cvt_pk_bf16_f32 v170,v52,v53\n"
"v_cvt_pk_bf16_f32 v171,v54,v55\n"
"s_waitcnt vmcnt(17)\n"
"v_lshlrev_b32 v48,16,v88\n"
"v_and_b32 v49,0xffff0000,v88\n"
"v_lshlrev_b32 v50,16,v89\n"
"v_and_b32 v51,0xffff0000,v89\n"
"v_lshlrev_b32 v52,16,v90\n"
"v_and_b32 v53,0xffff0000,v90\n"
"v_lshlrev_b32 v54,16,v91\n"
"v_and_b32 v55,0xffff0000,v91\n"
"buffer_load_dwordx4 v38,s[40:43],s77 offen offset:2048 lds\n"
"v_pk_mul_f32 v[48:49],v[48:49],v[42:43]\n"
"v_pk_mul_f32 v[50:51],v[50:51],v[42:43]\n"
"v_pk_mul_f32 v[52:53],v[52:53],v[42:43]\n"
"v_pk_mul_f32 v[54:55],v[54:55],v[42:43]\n"
"v_pk_fma_f32 v[48:49],v[176:177],v[40:41],v[48:49]\n"
"v_pk_fma_f32 v[50:51],v[178:179],v[40:41],v[50:51]\n"
"v_pk_fma_f32 v[52:53],v[180:181],v[40:41],v[52:53]\n"
"v_pk_fma_f32 v[54:55],v[182:183],v[40:41],v[54:55]\n"
"v_cvt_pk_bf16_f32 v176,v48,v49\n"
"v_cvt_pk_bf16_f32 v177,v50,v51\n"
"v_cvt_pk_bf16_f32 v178,v52,v53\n"
"v_cvt_pk_bf16_f32 v179,v54,v55\n"
"s_waitcnt vmcnt(17)\n"
"v_lshlrev_b32 v48,16,v92\n"
"v_and_b32 v49,0xffff0000,v92\n"
"v_lshlrev_b32 v50,16,v93\n"
"v_and_b32 v51,0xffff0000,v93\n"
"buffer_load_dwordx4 v39,s[40:43],s77 offen offset:3072 lds\n"
"v_lshlrev_b32 v52,16,v94\n"
"v_and_b32 v53,0xffff0000,v94\n"
"v_lshlrev_b32 v54,16,v95\n"
"v_and_b32 v55,0xffff0000,v95\n"
"v_pk_mul_f32 v[48:49],v[48:49],v[42:43]\n"
"v_pk_mul_f32 v[50:51],v[50:51],v[42:43]\n"
"v_pk_mul_f32 v[52:53],v[52:53],v[42:43]\n"
"v_pk_mul_f32 v[54:55],v[54:55],v[42:43]\n"
"v_pk_fma_f32 v[48:49],v[184:185],v[40:41],v[48:49]\n"
"v_pk_fma_f32 v[50:51],v[186:187],v[40:41],v[50:51]\n"
"v_pk_fma_f32 v[52:53],v[188:189],v[40:41],v[52:53]\n"
"v_pk_fma_f32 v[54:55],v[190:191],v[40:41],v[54:55]\n"
"v_cvt_pk_bf16_f32 v184,v48,v49\n"
"v_cvt_pk_bf16_f32 v185,v50,v51\n"
"v_cvt_pk_bf16_f32 v186,v52,v53\n"
"v_cvt_pk_bf16_f32 v187,v54,v55\n"
"s_mov_b32 s77,256\n"
"s_add_u32 m0,s57,0x18000\n"
"s_nop 0\n"
"buffer_load_dwordx4 v32,s[40:43],s77 offen offset:0 lds\n"
"s_waitcnt vmcnt(18)\n"
"v_lshlrev_b32 v48,16,v96\n"
"v_and_b32 v49,0xffff0000,v96\n"
"v_lshlrev_b32 v50,16,v97\n"
"v_and_b32 v51,0xffff0000,v97\n"
"v_lshlrev_b32 v52,16,v98\n"
"v_and_b32 v53,0xffff0000,v98\n"
"v_lshlrev_b32 v54,16,v99\n"
"v_and_b32 v55,0xffff0000,v99\n"
"v_pk_mul_f32 v[48:49],v[48:49],v[46:47]\n"
"v_pk_mul_f32 v[50:51],v[50:51],v[46:47]\n"
"v_pk_mul_f32 v[52:53],v[52:53],v[46:47]\n"
"v_pk_mul_f32 v[54:55],v[54:55],v[46:47]\n"
"v_pk_fma_f32 v[48:49],v[192:193],v[44:45],v[48:49]\n"
"v_pk_fma_f32 v[50:51],v[194:195],v[44:45],v[50:51]\n"
"v_pk_fma_f32 v[52:53],v[196:197],v[44:45],v[52:53]\n"
"v_pk_fma_f32 v[54:55],v[198:199],v[44:45],v[54:55]\n"
"buffer_load_dwordx4 v33,s[40:43],s77 offen offset:1024 lds\n"
"v_cvt_pk_bf16_f32 v192,v48,v49\n"
"v_cvt_pk_bf16_f32 v193,v50,v51\n"
"v_cvt_pk_bf16_f32 v194,v52,v53\n"
"v_cvt_pk_bf16_f32 v195,v54,v55\n"
"s_waitcnt vmcnt(18)\n"
"v_lshlrev_b32 v48,16,v100\n"
"v_and_b32 v49,0xffff0000,v100\n"
"v_lshlrev_b32 v50,16,v101\n"
"v_and_b32 v51,0xffff0000,v101\n"
"v_lshlrev_b32 v52,16,v102\n"
"v_and_b32 v53,0xffff0000,v102\n"
"v_lshlrev_b32 v54,16,v103\n"
"v_and_b32 v55,0xffff0000,v103\n"
"v_pk_mul_f32 v[48:49],v[48:49],v[46:47]\n"
"v_pk_mul_f32 v[50:51],v[50:51],v[46:47]\n"
"v_pk_mul_f32 v[52:53],v[52:53],v[46:47]\n"
"v_pk_mul_f32 v[54:55],v[54:55],v[46:47]\n"
"buffer_load_dwordx4 v34,s[40:43],s77 offen offset:2048 lds\n"
"v_pk_fma_f32 v[48:49],v[200:201],v[44:45],v[48:49]\n"
"v_pk_fma_f32 v[50:51],v[202:203],v[44:45],v[50:51]\n"
"v_pk_fma_f32 v[52:53],v[204:205],v[44:45],v[52:53]\n"
"v_pk_fma_f32 v[54:55],v[206:207],v[44:45],v[54:55]\n"
"v_cvt_pk_bf16_f32 v200,v48,v49\n"
"v_cvt_pk_bf16_f32 v201,v50,v51\n"
"v_cvt_pk_bf16_f32 v202,v52,v53\n"
"v_cvt_pk_bf16_f32 v203,v54,v55\n"
"s_waitcnt vmcnt(18)\n"
"v_lshlrev_b32 v48,16,v104\n"
"v_and_b32 v49,0xffff0000,v104\n"
"v_lshlrev_b32 v50,16,v105\n"
"v_and_b32 v51,0xffff0000,v105\n"
"v_lshlrev_b32 v52,16,v106\n"
"v_and_b32 v53,0xffff0000,v106\n"
"v_lshlrev_b32 v54,16,v107\n"
"v_and_b32 v55,0xffff0000,v107\n"
"buffer_load_dwordx4 v35,s[40:43],s77 offen offset:3072 lds\n"
"v_pk_mul_f32 v[48:49],v[48:49],v[46:47]\n"
"v_pk_mul_f32 v[50:51],v[50:51],v[46:47]\n"
"v_pk_mul_f32 v[52:53],v[52:53],v[46:47]\n"
"v_pk_mul_f32 v[54:55],v[54:55],v[46:47]\n"
"v_pk_fma_f32 v[48:49],v[208:209],v[44:45],v[48:49]\n"
"v_pk_fma_f32 v[50:51],v[210:211],v[44:45],v[50:51]\n"
"v_pk_fma_f32 v[52:53],v[212:213],v[44:45],v[52:53]\n"
"v_pk_fma_f32 v[54:55],v[214:215],v[44:45],v[54:55]\n"
"v_cvt_pk_bf16_f32 v208,v48,v49\n"
"v_cvt_pk_bf16_f32 v209,v50,v51\n"
"v_cvt_pk_bf16_f32 v210,v52,v53\n"
"v_cvt_pk_bf16_f32 v211,v54,v55\n"
"s_waitcnt vmcnt(18)\n"
"v_lshlrev_b32 v48,16,v108\n"
"v_and_b32 v49,0xffff0000,v108\n"
"v_lshlrev_b32 v50,16,v109\n"
"v_and_b32 v51,0xffff0000,v109\n"
"s_add_u32 m0,m0,0x1000\n"
"s_nop 0\n"
"buffer_load_dwordx4 v36,s[40:43],s77 offen offset:0 lds\n"
"v_lshlrev_b32 v52,16,v110\n"
"v_and_b32 v53,0xffff0000,v110\n"
"v_lshlrev_b32 v54,16,v111\n"
"v_and_b32 v55,0xffff0000,v111\n"
"v_pk_mul_f32 v[48:49],v[48:49],v[46:47]\n"
"v_pk_mul_f32 v[50:51],v[50:51],v[46:47]\n"
"v_pk_mul_f32 v[52:53],v[52:53],v[46:47]\n"
"v_pk_mul_f32 v[54:55],v[54:55],v[46:47]\n"
"v_pk_fma_f32 v[48:49],v[216:217],v[44:45],v[48:49]\n"
"v_pk_fma_f32 v[50:51],v[218:219],v[44:45],v[50:51]\n"
"v_pk_fma_f32 v[52:53],v[220:221],v[44:45],v[52:53]\n"
"v_pk_fma_f32 v[54:55],v[222:223],v[44:45],v[54:55]\n"
"v_cvt_pk_bf16_f32 v216,v48,v49\n"
"v_cvt_pk_bf16_f32 v217,v50,v51\n"
"v_cvt_pk_bf16_f32 v218,v52,v53\n"
"v_cvt_pk_bf16_f32 v219,v54,v55\n"
"buffer_load_dwordx4 v37,s[40:43],s77 offen offset:1024 lds\n"
"s_waitcnt vmcnt(19)\n"
"v_lshlrev_b32 v48,16,v112\n"
"v_and_b32 v49,0xffff0000,v112\n"
"v_lshlrev_b32 v50,16,v113\n"
"v_and_b32 v51,0xffff0000,v113\n"
"v_lshlrev_b32 v52,16,v114\n"
"v_and_b32 v53,0xffff0000,v114\n"
"v_lshlrev_b32 v54,16,v115\n"
"v_and_b32 v55,0xffff0000,v115\n"
"v_pk_mul_f32 v[48:49],v[48:49],v[46:47]\n"
"v_pk_mul_f32 v[50:51],v[50:51],v[46:47]\n"
"v_pk_mul_f32 v[52:53],v[52:53],v[46:47]\n"
"v_pk_mul_f32 v[54:55],v[54:55],v[46:47]\n"
"v_pk_fma_f32 v[48:49],v[224:225],v[44:45],v[48:49]\n"
"v_pk_fma_f32 v[50:51],v[226:227],v[44:45],v[50:51]\n"
"v_pk_fma_f32 v[52:53],v[228:229],v[44:45],v[52:53]\n"
"v_pk_fma_f32 v[54:55],v[230:231],v[44:45],v[54:55]\n"
"buffer_load_dwordx4 v38,s[40:43],s77 offen offset:2048 lds\n"
"v_cvt_pk_bf16_f32 v224,v48,v49\n"
"v_cvt_pk_bf16_f32 v225,v50,v51\n"
"v_cvt_pk_bf16_f32 v226,v52,v53\n"
"v_cvt_pk_bf16_f32 v227,v54,v55\n"
"s_waitcnt vmcnt(19)\n"
"v_lshlrev_b32 v48,16,v116\n"
"v_and_b32 v49,0xffff0000,v116\n"
"v_lshlrev_b32 v50,16,v117\n"
"v_and_b32 v51,0xffff0000,v117\n"
"v_lshlrev_b32 v52,16,v118\n"
"v_and_b32 v53,0xffff0000,v118\n"
"v_lshlrev_b32 v54,16,v119\n"
"v_and_b32 v55,0xffff0000,v119\n"
"v_pk_mul_f32 v[48:49],v[48:49],v[46:47]\n"
"v_pk_mul_f32 v[50:51],v[50:51],v[46:47]\n"
"v_pk_mul_f32 v[52:53],v[52:53],v[46:47]\n"
"v_pk_mul_f32 v[54:55],v[54:55],v[46:47]\n"
"buffer_load_dwordx4 v39,s[40:43],s77 offen offset:3072 lds\n"
"v_pk_fma_f32 v[48:49],v[232:233],v[44:45],v[48:49]\n"
"v_pk_fma_f32 v[50:51],v[234:235],v[44:45],v[50:51]\n"
"v_pk_fma_f32 v[52:53],v[236:237],v[44:45],v[52:53]\n"
"v_pk_fma_f32 v[54:55],v[238:239],v[44:45],v[54:55]\n"
"v_cvt_pk_bf16_f32 v232,v48,v49\n"
"v_cvt_pk_bf16_f32 v233,v50,v51\n"
"v_cvt_pk_bf16_f32 v234,v52,v53\n"
"v_cvt_pk_bf16_f32 v235,v54,v55\n"
"s_waitcnt vmcnt(19)\n"
"v_lshlrev_b32 v48,16,v120\n"
"v_and_b32 v49,0xffff0000,v120\n"
"v_lshlrev_b32 v50,16,v121\n"
"v_and_b32 v51,0xffff0000,v121\n"
"v_lshlrev_b32 v52,16,v122\n"
"v_and_b32 v53,0xffff0000,v122\n"
"v_lshlrev_b32 v54,16,v123\n"
"v_and_b32 v55,0xffff0000,v123\n"
"v_pk_mul_f32 v[48:49],v[48:49],v[46:47]\n"
"v_pk_mul_f32 v[50:51],v[50:51],v[46:47]\n"
"v_pk_mul_f32 v[52:53],v[52:53],v[46:47]\n"
"v_pk_mul_f32 v[54:55],v[54:55],v[46:47]\n"
"v_pk_fma_f32 v[48:49],v[240:241],v[44:45],v[48:49]\n"
"v_pk_fma_f32 v[50:51],v[242:243],v[44:45],v[50:51]\n"
"v_pk_fma_f32 v[52:53],v[244:245],v[44:45],v[52:53]\n"
"v_pk_fma_f32 v[54:55],v[246:247],v[44:45],v[54:55]\n"
"v_cvt_pk_bf16_f32 v240,v48,v49\n"
"v_cvt_pk_bf16_f32 v241,v50,v51\n"
"v_cvt_pk_bf16_f32 v242,v52,v53\n"
"v_cvt_pk_bf16_f32 v243,v54,v55\n"
"s_waitcnt vmcnt(18)\n"
"v_lshlrev_b32 v48,16,v124\n"
"v_and_b32 v49,0xffff0000,v124\n"
"v_lshlrev_b32 v50,16,v125\n"
"v_and_b32 v51,0xffff0000,v125\n"
"v_lshlrev_b32 v52,16,v126\n"
"v_and_b32 v53,0xffff0000,v126\n"
"v_lshlrev_b32 v54,16,v127\n"
"v_and_b32 v55,0xffff0000,v127\n"
"v_pk_mul_f32 v[48:49],v[48:49],v[46:47]\n"
"v_pk_mul_f32 v[50:51],v[50:51],v[46:47]\n"
"v_pk_mul_f32 v[52:53],v[52:53],v[46:47]\n"
"v_pk_mul_f32 v[54:55],v[54:55],v[46:47]\n"
"v_pk_fma_f32 v[48:49],v[248:249],v[44:45],v[48:49]\n"
"v_pk_fma_f32 v[50:51],v[250:251],v[44:45],v[50:51]\n"
"v_pk_fma_f32 v[52:53],v[252:253],v[44:45],v[52:53]\n"
"v_pk_fma_f32 v[54:55],v[254:255],v[44:45],v[54:55]\n"
"v_cvt_pk_bf16_f32 v248,v48,v49\n"
"v_cvt_pk_bf16_f32 v249,v50,v51\n"
"v_cvt_pk_bf16_f32 v250,v52,v53\n"
"v_cvt_pk_bf16_f32 v251,v54,v55\n"
"s_branch L_oproj_%=\n"
"L_notA_%=:\n"
"s_cmp_eq_u32 s56,2\n"
"s_cbranch_scc0 L_finp_%=\n"
"v_exp_f32 v118,v118\n"
"v_exp_f32 v119,v119\n"
"v_exp_f32 v120,v120\n"
"v_exp_f32 v121,v121\n"
"v_exp_f32 v122,v122\n"
"v_exp_f32 v123,v123\n"
"v_exp_f32 v124,v124\n"
"v_exp_f32 v125,v125\n"
"v_exp_f32 v126,v126\n"
"v_exp_f32 v127,v127\n"
"v_cvt_pk_bf16_f32 v40,v96,v97\n"
"v_cvt_pk_bf16_f32 v41,v98,v99\n"
"v_cvt_pk_bf16_f32 v42,v100,v101\n"
"v_cvt_pk_bf16_f32 v43,v102,v103\n"
"v_cvt_pk_bf16_f32 v44,v104,v105\n"
"v_cvt_pk_bf16_f32 v45,v106,v107\n"
"v_cvt_pk_bf16_f32 v46,v108,v109\n"
"v_cvt_pk_bf16_f32 v47,v110,v111\n"
"v_add_f32 v13,v13,v117\n"
"v_add_f32 v13,v13,v118\n"
"v_add_f32 v13,v13,v119\n"
"v_add_f32 v13,v13,v120\n"
"v_add_f32 v13,v13,v121\n"
"v_add_f32 v13,v13,v122\n"
"v_add_f32 v13,v13,v123\n"
"v_add_f32 v13,v13,v124\n"
"v_add_f32 v13,v13,v125\n"
"v_add_f32 v13,v13,v126\n"
"v_add_f32 v13,v13,v127\n"
"v_cvt_pk_bf16_f32 v48,v112,v113\n"
"v_cvt_pk_bf16_f32 v49,v114,v115\n"
"v_cvt_pk_bf16_f32 v50,v116,v117\n"
"v_cvt_pk_bf16_f32 v51,v118,v119\n"
"v_cvt_pk_bf16_f32 v52,v120,v121\n"
"v_cvt_pk_bf16_f32 v53,v122,v123\n"
"v_cvt_pk_bf16_f32 v54,v124,v125\n"
"v_cvt_pk_bf16_f32 v55,v126,v127\n"
"v_mov_b32 v56,v12\n"
"v_mov_b32 v57,v13\n"
"s_nop 0\n"
"v_permlane32_swap_b32 v12,v56\n"
"v_permlane32_swap_b32 v13,v57\n"
"v_add_f32 v12,v12,v56\n"
"v_add_f32 v13,v13,v57\n"
"v_mov_b32 v56,v12\n"
"v_mov_b32 v57,v13\n"
"s_nop 0\n"
"v_permlane16_swap_b32 v12,v56\n"
"v_permlane16_swap_b32 v13,v57\n"
"v_add_f32 v12,v12,v56\n"
"v_add_f32 v13,v13,v57\n"
"v_rcp_f32 v24,v12\n"
"v_rcp_f32 v25,v13\n"
"v_mfma_f32_16x16x32_bf16 v[128:131],a[192:195],v[40:43],v[128:131]\n"
"ds_read_b128 a[220:223],v8 offset:44032\n"
"v_mfma_f32_16x16x32_bf16 v[128:131],a[196:199],v[44:47],v[128:131]\n"
"buffer_load_dwordx4 v38,s[40:43],s77 offen offset:2048 lds\n"
"v_mfma_f32_16x16x32_bf16 v[192:195],a[192:195],v[48:51],v[192:195]\n"
"ds_read_b128 a[224:227],v8 offset:36864\n"
"v_mfma_f32_16x16x32_bf16 v[192:195],a[196:199],v[52:55],v[192:195]\n"
"s_waitcnt lgkmcnt(5)\n"
"v_mfma_f32_16x16x32_bf16 v[132:135],a[200:203],v[40:43],v[132:135]\n"
"ds_read_b128 a[228:231],v8 offset:45056\n"
"v_mfma_f32_16x16x32_bf16 v[132:135],a[204:207],v[44:47],v[132:135]\n"
"buffer_load_dwordx4 v39,s[40:43],s77 offen offset:3072 lds\n"
"v_mfma_f32_16x16x32_bf16 v[196:199],a[200:203],v[48:51],v[196:199]\n"
"s_mov_b32 s77,128\n"
"ds_read_b128 a[232:235],v8 offset:37888\n"
"v_mfma_f32_16x16x32_bf16 v[196:199],a[204:207],v[52:55],v[196:199]\n"
"s_waitcnt lgkmcnt(5)\n"
"v_mfma_f32_16x16x32_bf16 v[136:139],a[208:211],v[40:43],v[136:139]\n"
"s_add_u32 m0,s57,0x10000\n"
"ds_read_b128 a[236:239],v8 offset:46080\n"
"v_mfma_f32_16x16x32_bf16 v[136:139],a[212:215],v[44:47],v[136:139]\n"
"buffer_load_dwordx4 v32,s[40:43],s77 offen offset:0 lds\n"
"v_mfma_f32_16x16x32_bf16 v[200:203],a[208:211],v[48:51],v[200:203]\n"
"ds_read_b128 a[240:243],v8 offset:38912\n"
"v_mul_f32 v128,v128,v24\n"
"v_mfma_f32_16x16x32_bf16 v[200:203],a[212:215],v[52:55],v[200:203]\n"
"s_waitcnt lgkmcnt(5)\n"
"v_mul_f32 v129,v129,v24\n"
"v_mul_f32 v130,v130,v24\n"
"v_mfma_f32_16x16x32_bf16 v[140:143],a[216:219],v[40:43],v[140:143]\n"
"ds_read_b128 a[244:247],v8 offset:47104\n"
"v_mul_f32 v131,v131,v24\n"
"v_mfma_f32_16x16x32_bf16 v[140:143],a[220:223],v[44:47],v[140:143]\n"
"buffer_load_dwordx4 v33,s[40:43],s77 offen offset:1024 lds\n"
"v_mfma_f32_16x16x32_bf16 v[204:207],a[216:219],v[48:51],v[204:207]\n"
"ds_read_b128 a[248:251],v8 offset:39936\n"
"v_mul_f32 v132,v132,v24\n"
"v_mfma_f32_16x16x32_bf16 v[204:207],a[220:223],v[52:55],v[204:207]\n"
"s_waitcnt lgkmcnt(5)\n"
"v_mul_f32 v133,v133,v24\n"
"v_mul_f32 v134,v134,v24\n"
"v_mfma_f32_16x16x32_bf16 v[144:147],a[224:227],v[40:43],v[144:147]\n"
"ds_read_b128 a[252:255],v8 offset:48128\n"
"v_mul_f32 v135,v135,v24\n"
"v_mfma_f32_16x16x32_bf16 v[144:147],a[228:231],v[44:47],v[144:147]\n"
"buffer_load_dwordx4 v34,s[40:43],s77 offen offset:2048 lds\n"
"v_mfma_f32_16x16x32_bf16 v[208:211],a[224:227],v[48:51],v[208:211]\n"
"ds_read_b128 a[192:195],v8 offset:49152\n"
"v_cvt_pk_bf16_f32 v128,v128,v129\n"
"v_mfma_f32_16x16x32_bf16 v[208:211],a[228:231],v[52:55],v[208:211]\n"
"s_waitcnt lgkmcnt(5)\n"
"v_cvt_pk_bf16_f32 v129,v130,v131\n"
"v_cvt_pk_bf16_f32 v130,v132,v133\n"
"v_mfma_f32_16x16x32_bf16 v[148:151],a[232:235],v[40:43],v[148:151]\n"
"ds_read_b128 a[196:199],v8 offset:57344\n"
"v_cvt_pk_bf16_f32 v131,v134,v135\n"
"v_mfma_f32_16x16x32_bf16 v[148:151],a[236:239],v[44:47],v[148:151]\n"
"buffer_load_dwordx4 v35,s[40:43],s77 offen offset:3072 lds\n"
"v_mfma_f32_16x16x32_bf16 v[212:215],a[232:235],v[48:51],v[212:215]\n"
"ds_read_b128 a[200:203],v8 offset:50176\n"
"v_mul_f32 v192,v192,v25\n"
"v_mfma_f32_16x16x32_bf16 v[212:215],a[236:239],v[52:55],v[212:215]\n"
"s_waitcnt lgkmcnt(5)\n"
"v_mul_f32 v193,v193,v25\n"
"v_mul_f32 v194,v194,v25\n"
"v_mfma_f32_16x16x32_bf16 v[152:155],a[240:243],v[40:43],v[152:155]\n"
"s_add_u32 m0,m0,0x1000\n"
"ds_read_b128 a[204:207],v8 offset:58368\n"
"v_mul_f32 v195,v195,v25\n"
"v_mfma_f32_16x16x32_bf16 v[152:155],a[244:247],v[44:47],v[152:155]\n"
"buffer_load_dwordx4 v36,s[40:43],s77 offen offset:0 lds\n"
"v_mfma_f32_16x16x32_bf16 v[216:219],a[240:243],v[48:51],v[216:219]\n"
"ds_read_b128 a[208:211],v8 offset:51200\n"
"v_mul_f32 v196,v196,v25\n"
"v_mfma_f32_16x16x32_bf16 v[216:219],a[244:247],v[52:55],v[216:219]\n"
"s_waitcnt lgkmcnt(5)\n"
"v_mul_f32 v197,v197,v25\n"
"v_mul_f32 v198,v198,v25\n"
"v_mfma_f32_16x16x32_bf16 v[156:159],a[248:251],v[40:43],v[156:159]\n"
"ds_read_b128 a[212:215],v8 offset:59392\n"
"v_mul_f32 v199,v199,v25\n"
"v_mfma_f32_16x16x32_bf16 v[156:159],a[252:255],v[44:47],v[156:159]\n"
"buffer_load_dwordx4 v37,s[40:43],s77 offen offset:1024 lds\n"
"v_mfma_f32_16x16x32_bf16 v[220:223],a[248:251],v[48:51],v[220:223]\n"
"ds_read_b128 a[216:219],v8 offset:52224\n"
"v_cvt_pk_bf16_f32 v192,v192,v193\n"
"v_mfma_f32_16x16x32_bf16 v[220:223],a[252:255],v[52:55],v[220:223]\n"
"s_waitcnt lgkmcnt(5)\n"
"v_cvt_pk_bf16_f32 v193,v194,v195\n"
"v_cvt_pk_bf16_f32 v194,v196,v197\n"
"v_mfma_f32_16x16x32_bf16 v[160:163],a[192:195],v[40:43],v[160:163]\n"
"ds_read_b128 a[220:223],v8 offset:60416\n"
"v_cvt_pk_bf16_f32 v195,v198,v199\n"
"v_mfma_f32_16x16x32_bf16 v[160:163],a[196:199],v[44:47],v[160:163]\n"
"buffer_load_dwordx4 v38,s[40:43],s77 offen offset:2048 lds\n"
"v_mfma_f32_16x16x32_bf16 v[224:227],a[192:195],v[48:51],v[224:227]\n"
"ds_read_b128 a[224:227],v8 offset:53248\n"
"v_mul_f32 v136,v136,v24\n"
"v_mfma_f32_16x16x32_bf16 v[224:227],a[196:199],v[52:55],v[224:227]\n"
"s_waitcnt lgkmcnt(5)\n"
"v_mul_f32 v137,v137,v24\n"
"v_mul_f32 v138,v138,v24\n"
"v_mfma_f32_16x16x32_bf16 v[164:167],a[200:203],v[40:43],v[164:167]\n"
"ds_read_b128 a[228:231],v8 offset:61440\n"
"v_mul_f32 v139,v139,v24\n"
"v_mfma_f32_16x16x32_bf16 v[164:167],a[204:207],v[44:47],v[164:167]\n"
"buffer_load_dwordx4 v39,s[40:43],s77 offen offset:3072 lds\n"
"v_mfma_f32_16x16x32_bf16 v[228:231],a[200:203],v[48:51],v[228:231]\n"
"s_mov_b32 s77,256\n"
"ds_read_b128 a[232:235],v8 offset:54272\n"
"v_mul_f32 v140,v140,v24\n"
"v_mfma_f32_16x16x32_bf16 v[228:231],a[204:207],v[52:55],v[228:231]\n"
"s_waitcnt lgkmcnt(5)\n"
"v_mul_f32 v141,v141,v24\n"
"v_mul_f32 v142,v142,v24\n"
"v_mfma_f32_16x16x32_bf16 v[168:171],a[208:211],v[40:43],v[168:171]\n"
"s_add_u32 m0,s57,0x18000\n"
"ds_read_b128 a[236:239],v8 offset:62464\n"
"v_mul_f32 v143,v143,v24\n"
"v_mfma_f32_16x16x32_bf16 v[168:171],a[212:215],v[44:47],v[168:171]\n"
"buffer_load_dwordx4 v32,s[40:43],s77 offen offset:0 lds\n"
"v_mfma_f32_16x16x32_bf16 v[232:235],a[208:211],v[48:51],v[232:235]\n"
"ds_read_b128 a[240:243],v8 offset:55296\n"
"v_cvt_pk_bf16_f32 v136,v136,v137\n"
"v_mfma_f32_16x16x32_bf16 v[232:235],a[212:215],v[52:55],v[232:235]\n"
"s_waitcnt lgkmcnt(5)\n"
"v_cvt_pk_bf16_f32 v137,v138,v139\n"
"v_cvt_pk_bf16_f32 v138,v140,v141\n"
"v_mfma_f32_16x16x32_bf16 v[172:175],a[216:219],v[40:43],v[172:175]\n"
"ds_read_b128 a[244:247],v8 offset:63488\n"
"v_cvt_pk_bf16_f32 v139,v142,v143\n"
"v_mfma_f32_16x16x32_bf16 v[172:175],a[220:223],v[44:47],v[172:175]\n"
"buffer_load_dwordx4 v33,s[40:43],s77 offen offset:1024 lds\n"
"v_mfma_f32_16x16x32_bf16 v[236:239],a[216:219],v[48:51],v[236:239]\n"
"ds_read_b128 a[248:251],v8 offset:56320\n"
"v_mul_f32 v200,v200,v25\n"
"v_mfma_f32_16x16x32_bf16 v[236:239],a[220:223],v[52:55],v[236:239]\n"
"s_waitcnt lgkmcnt(5)\n"
"v_mul_f32 v201,v201,v25\n"
"v_mul_f32 v202,v202,v25\n"
"v_mfma_f32_16x16x32_bf16 v[176:179],a[224:227],v[40:43],v[176:179]\n"
"ds_read_b128 a[252:255],v8 offset:64512\n"
"v_mul_f32 v203,v203,v25\n"
"v_mfma_f32_16x16x32_bf16 v[176:179],a[228:231],v[44:47],v[176:179]\n"
"buffer_load_dwordx4 v34,s[40:43],s77 offen offset:2048 lds\n"
"v_mfma_f32_16x16x32_bf16 v[240:243],a[224:227],v[48:51],v[240:243]\n"
"v_mul_f32 v204,v204,v25\n"
"v_mul_f32 v205,v205,v25\n"
"v_mfma_f32_16x16x32_bf16 v[240:243],a[228:231],v[52:55],v[240:243]\n"
"s_waitcnt lgkmcnt(4)\n"
"v_mul_f32 v206,v206,v25\n"
"v_mul_f32 v207,v207,v25\n"
"v_mfma_f32_16x16x32_bf16 v[180:183],a[232:235],v[40:43],v[180:183]\n"
"v_cvt_pk_bf16_f32 v200,v200,v201\n"
"v_cvt_pk_bf16_f32 v201,v202,v203\n"
"v_mfma_f32_16x16x32_bf16 v[180:183],a[236:239],v[44:47],v[180:183]\n"
"buffer_load_dwordx4 v35,s[40:43],s77 offen offset:3072 lds\n"
"v_mfma_f32_16x16x32_bf16 v[244:247],a[232:235],v[48:51],v[244:247]\n"
"v_cvt_pk_bf16_f32 v202,v204,v205\n"
"v_cvt_pk_bf16_f32 v203,v206,v207\n"
"v_mfma_f32_16x16x32_bf16 v[244:247],a[236:239],v[52:55],v[244:247]\n"
"s_waitcnt lgkmcnt(2)\n"
"v_mul_f32 v144,v144,v24\n"
"v_mul_f32 v145,v145,v24\n"
"v_mfma_f32_16x16x32_bf16 v[184:187],a[240:243],v[40:43],v[184:187]\n"
"s_add_u32 m0,m0,0x1000\n"
"v_mul_f32 v146,v146,v24\n"
"v_mul_f32 v147,v147,v24\n"
"v_mfma_f32_16x16x32_bf16 v[184:187],a[244:247],v[44:47],v[184:187]\n"
"buffer_load_dwordx4 v36,s[40:43],s77 offen offset:0 lds\n"
"v_mfma_f32_16x16x32_bf16 v[248:251],a[240:243],v[48:51],v[248:251]\n"
"v_mul_f32 v148,v148,v24\n"
"v_mul_f32 v149,v149,v24\n"
"v_mfma_f32_16x16x32_bf16 v[248:251],a[244:247],v[52:55],v[248:251]\n"
"s_waitcnt lgkmcnt(0)\n"
"v_mul_f32 v150,v150,v24\n"
"v_mul_f32 v151,v151,v24\n"
"v_mfma_f32_16x16x32_bf16 v[188:191],a[248:251],v[40:43],v[188:191]\n"
"v_cvt_pk_bf16_f32 v144,v144,v145\n"
"v_cvt_pk_bf16_f32 v145,v146,v147\n"
"v_mfma_f32_16x16x32_bf16 v[188:191],a[252:255],v[44:47],v[188:191]\n"
"buffer_load_dwordx4 v37,s[40:43],s77 offen offset:1024 lds\n"
"v_mfma_f32_16x16x32_bf16 v[252:255],a[248:251],v[48:51],v[252:255]\n"
"v_cvt_pk_bf16_f32 v146,v148,v149\n"
"v_cvt_pk_bf16_f32 v147,v150,v151\n"
"v_mfma_f32_16x16x32_bf16 v[252:255],a[252:255],v[52:55],v[252:255]\n"
"v_mul_f32 v208,v208,v25\n"
"v_mul_f32 v209,v209,v25\n"
"buffer_load_dwordx4 v38,s[40:43],s77 offen offset:2048 lds\n"
"v_mul_f32 v210,v210,v25\n"
"v_mul_f32 v211,v211,v25\n"
"v_mul_f32 v212,v212,v25\n"
"v_mul_f32 v213,v213,v25\n"
"v_mul_f32 v214,v214,v25\n"
"v_mul_f32 v215,v215,v25\n"
"v_cvt_pk_bf16_f32 v208,v208,v209\n"
"v_cvt_pk_bf16_f32 v209,v210,v211\n"
"v_cvt_pk_bf16_f32 v210,v212,v213\n"
"v_cvt_pk_bf16_f32 v211,v214,v215\n"
"v_mul_f32 v152,v152,v24\n"
"v_mul_f32 v153,v153,v24\n"
"v_mul_f32 v154,v154,v24\n"
"v_mul_f32 v155,v155,v24\n"
"v_mul_f32 v156,v156,v24\n"
"v_mul_f32 v157,v157,v24\n"
"buffer_load_dwordx4 v39,s[40:43],s77 offen offset:3072 lds\n"
"v_mul_f32 v158,v158,v24\n"
"v_mul_f32 v159,v159,v24\n"
"v_cvt_pk_bf16_f32 v152,v152,v153\n"
"v_cvt_pk_bf16_f32 v153,v154,v155\n"
"v_cvt_pk_bf16_f32 v154,v156,v157\n"
"v_cvt_pk_bf16_f32 v155,v158,v159\n"
"v_mul_f32 v216,v216,v25\n"
"v_mul_f32 v217,v217,v25\n"
"v_mul_f32 v218,v218,v25\n"
"v_mul_f32 v219,v219,v25\n"
"v_mul_f32 v220,v220,v25\n"
"v_mul_f32 v221,v221,v25\n"
"v_mul_f32 v222,v222,v25\n"
"v_mul_f32 v223,v223,v25\n"
"v_cvt_pk_bf16_f32 v216,v216,v217\n"
"v_cvt_pk_bf16_f32 v217,v218,v219\n"
"v_cvt_pk_bf16_f32 v218,v220,v221\n"
"v_cvt_pk_bf16_f32 v219,v222,v223\n"
"v_mul_f32 v160,v160,v24\n"
"v_mul_f32 v161,v161,v24\n"
"v_mul_f32 v162,v162,v24\n"
"v_mul_f32 v163,v163,v24\n"
"v_mul_f32 v164,v164,v24\n"
"v_mul_f32 v165,v165,v24\n"
"v_mul_f32 v166,v166,v24\n"
"v_mul_f32 v167,v167,v24\n"
"v_cvt_pk_bf16_f32 v160,v160,v161\n"
"v_cvt_pk_bf16_f32 v161,v162,v163\n"
"v_cvt_pk_bf16_f32 v162,v164,v165\n"
"v_cvt_pk_bf16_f32 v163,v166,v167\n"
"v_mul_f32 v224,v224,v25\n"
"v_mul_f32 v225,v225,v25\n"
"v_mul_f32 v226,v226,v25\n"
"v_mul_f32 v227,v227,v25\n"
"v_mul_f32 v228,v228,v25\n"
"v_mul_f32 v229,v229,v25\n"
"v_mul_f32 v230,v230,v25\n"
"v_mul_f32 v231,v231,v25\n"
"v_cvt_pk_bf16_f32 v224,v224,v225\n"
"v_cvt_pk_bf16_f32 v225,v226,v227\n"
"v_cvt_pk_bf16_f32 v226,v228,v229\n"
"v_cvt_pk_bf16_f32 v227,v230,v231\n"
"v_mul_f32 v168,v168,v24\n"
"v_mul_f32 v169,v169,v24\n"
"v_mul_f32 v170,v170,v24\n"
"v_mul_f32 v171,v171,v24\n"
"v_mul_f32 v172,v172,v24\n"
"v_mul_f32 v173,v173,v24\n"
"v_mul_f32 v174,v174,v24\n"
"v_mul_f32 v175,v175,v24\n"
"v_cvt_pk_bf16_f32 v168,v168,v169\n"
"v_cvt_pk_bf16_f32 v169,v170,v171\n"
"v_cvt_pk_bf16_f32 v170,v172,v173\n"
"v_cvt_pk_bf16_f32 v171,v174,v175\n"
"v_mul_f32 v232,v232,v25\n"
"v_mul_f32 v233,v233,v25\n"
"v_mul_f32 v234,v234,v25\n"
"v_mul_f32 v235,v235,v25\n"
"v_mul_f32 v236,v236,v25\n"
"v_mul_f32 v237,v237,v25\n"
"v_mul_f32 v238,v238,v25\n"
"v_mul_f32 v239,v239,v25\n"
"v_cvt_pk_bf16_f32 v232,v232,v233\n"
"v_cvt_pk_bf16_f32 v233,v234,v235\n"
"v_cvt_pk_bf16_f32 v234,v236,v237\n"
"v_cvt_pk_bf16_f32 v235,v238,v239\n"
"v_mul_f32 v176,v176,v24\n"
"v_mul_f32 v177,v177,v24\n"
"v_mul_f32 v178,v178,v24\n"
"v_mul_f32 v179,v179,v24\n"
"v_mul_f32 v180,v180,v24\n"
"v_mul_f32 v181,v181,v24\n"
"v_mul_f32 v182,v182,v24\n"
"v_mul_f32 v183,v183,v24\n"
"v_cvt_pk_bf16_f32 v176,v176,v177\n"
"v_cvt_pk_bf16_f32 v177,v178,v179\n"
"v_cvt_pk_bf16_f32 v178,v180,v181\n"
"v_cvt_pk_bf16_f32 v179,v182,v183\n"
"v_mul_f32 v240,v240,v25\n"
"v_mul_f32 v241,v241,v25\n"
"v_mul_f32 v242,v242,v25\n"
"v_mul_f32 v243,v243,v25\n"
"v_mul_f32 v244,v244,v25\n"
"v_mul_f32 v245,v245,v25\n"
"v_mul_f32 v246,v246,v25\n"
"v_mul_f32 v247,v247,v25\n"
"v_cvt_pk_bf16_f32 v240,v240,v241\n"
"v_cvt_pk_bf16_f32 v241,v242,v243\n"
"v_cvt_pk_bf16_f32 v242,v244,v245\n"
"v_cvt_pk_bf16_f32 v243,v246,v247\n"
"v_mul_f32 v184,v184,v24\n"
"v_mul_f32 v185,v185,v24\n"
"v_mul_f32 v186,v186,v24\n"
"v_mul_f32 v187,v187,v24\n"
"v_mul_f32 v188,v188,v24\n"
"v_mul_f32 v189,v189,v24\n"
"v_mul_f32 v190,v190,v24\n"
"v_mul_f32 v191,v191,v24\n"
"v_cvt_pk_bf16_f32 v184,v184,v185\n"
"v_cvt_pk_bf16_f32 v185,v186,v187\n"
"v_cvt_pk_bf16_f32 v186,v188,v189\n"
"v_cvt_pk_bf16_f32 v187,v190,v191\n"
"v_mul_f32 v248,v248,v25\n"
"v_mul_f32 v249,v249,v25\n"
"v_mul_f32 v250,v250,v25\n"
"v_mul_f32 v251,v251,v25\n"
"v_mul_f32 v252,v252,v25\n"
"v_mul_f32 v253,v253,v25\n"
"v_mul_f32 v254,v254,v25\n"
"v_mul_f32 v255,v255,v25\n"
"v_cvt_pk_bf16_f32 v248,v248,v249\n"
"v_cvt_pk_bf16_f32 v249,v250,v251\n"
"v_cvt_pk_bf16_f32 v250,v252,v253\n"
"v_cvt_pk_bf16_f32 v251,v254,v255\n"
"s_branch L_oproj_%=\n"
"L_finp_%=:\n"
"v_exp_f32 v118,v118\n"
"v_exp_f32 v119,v119\n"
"v_exp_f32 v120,v120\n"
"v_exp_f32 v121,v121\n"
"v_exp_f32 v122,v122\n"
"v_exp_f32 v123,v123\n"
"v_exp_f32 v124,v124\n"
"v_exp_f32 v125,v125\n"
"v_exp_f32 v126,v126\n"
"v_exp_f32 v127,v127\n"
"v_cvt_pk_bf16_f32 v40,v96,v97\n"
"v_cvt_pk_bf16_f32 v41,v98,v99\n"
"v_cvt_pk_bf16_f32 v42,v100,v101\n"
"v_cvt_pk_bf16_f32 v43,v102,v103\n"
"v_cvt_pk_bf16_f32 v44,v104,v105\n"
"v_cvt_pk_bf16_f32 v45,v106,v107\n"
"v_cvt_pk_bf16_f32 v46,v108,v109\n"
"v_cvt_pk_bf16_f32 v47,v110,v111\n"
"v_add_f32 v13,v13,v117\n"
"v_add_f32 v13,v13,v118\n"
"v_add_f32 v13,v13,v119\n"
"v_add_f32 v13,v13,v120\n"
"v_add_f32 v13,v13,v121\n"
"v_add_f32 v13,v13,v122\n"
"v_add_f32 v13,v13,v123\n"
"v_add_f32 v13,v13,v124\n"
"v_add_f32 v13,v13,v125\n"
"v_add_f32 v13,v13,v126\n"
"v_add_f32 v13,v13,v127\n"
"v_cvt_pk_bf16_f32 v48,v112,v113\n"
"v_cvt_pk_bf16_f32 v49,v114,v115\n"
"v_cvt_pk_bf16_f32 v50,v116,v117\n"
"v_cvt_pk_bf16_f32 v51,v118,v119\n"
"v_cvt_pk_bf16_f32 v52,v120,v121\n"
"v_cvt_pk_bf16_f32 v53,v122,v123\n"
"v_cvt_pk_bf16_f32 v54,v124,v125\n"
"v_cvt_pk_bf16_f32 v55,v126,v127\n"
"v_mov_b32 v56,v12\n"
"v_mov_b32 v57,v13\n"
"s_nop 0\n"
"v_permlane32_swap_b32 v12,v56\n"
"v_permlane32_swap_b32 v13,v57\n"
"v_add_f32 v12,v12,v56\n"
"v_add_f32 v13,v13,v57\n"
"v_mov_b32 v56,v12\n"
"v_mov_b32 v57,v13\n"
"s_nop 0\n"
"v_permlane16_swap_b32 v12,v56\n"
"v_permlane16_swap_b32 v13,v57\n"
"v_add_f32 v12,v12,v56\n"
"v_add_f32 v13,v13,v57\n"
"s_lshl_b32 s86,s11,14\n"
"s_add_u32 s87,s86,0x1000\n"
"s_add_u32 s88,s86,0x2000\n"
"s_add_u32 s89,s86,0x3000\n"
"v_mov_b32 v56,v14\n"
"v_mov_b32 v57,v12\n"
"v_mov_b32 v58,v15\n"
"v_mov_b32 v59,v13\n"
"s_lshl_b32 s96,s11,10\n"
"s_add_u32 s59,s96,0x10000\n"
"buffer_store_dwordx4 v[56:59],v8,s[64:67],s59 offen sc1\n"
"v_mfma_f32_16x16x32_bf16 v[128:131],a[192:195],v[40:43],v[128:131]\n"
"ds_read_b128 a[220:223],v8 offset:44032\n"
"v_mfma_f32_16x16x32_bf16 v[128:131],a[196:199],v[44:47],v[128:131]\n"
"buffer_load_dwordx4 v38,s[40:43],s77 offen offset:2048 lds\n"
"v_mfma_f32_16x16x32_bf16 v[192:195],a[192:195],v[48:51],v[192:195]\n"
"ds_read_b128 a[224:227],v8 offset:36864\n"
"v_mfma_f32_16x16x32_bf16 v[192:195],a[196:199],v[52:55],v[192:195]\n"
"s_waitcnt lgkmcnt(5)\n"
"v_mfma_f32_16x16x32_bf16 v[132:135],a[200:203],v[40:43],v[132:135]\n"
"ds_read_b128 a[228:231],v8 offset:45056\n"
"v_mfma_f32_16x16x32_bf16 v[132:135],a[204:207],v[44:47],v[132:135]\n"
"buffer_load_dwordx4 v39,s[40:43],s77 offen offset:3072 lds\n"
"v_mfma_f32_16x16x32_bf16 v[196:199],a[200:203],v[48:51],v[196:199]\n"
"s_mov_b32 s77,128\n"
"ds_read_b128 a[232:235],v8 offset:37888\n"
"v_mfma_f32_16x16x32_bf16 v[196:199],a[204:207],v[52:55],v[196:199]\n"
"s_waitcnt lgkmcnt(5)\n"
"v_mfma_f32_16x16x32_bf16 v[136:139],a[208:211],v[40:43],v[136:139]\n"
"s_add_u32 m0,s57,0x10000\n"
"ds_read_b128 a[236:239],v8 offset:46080\n"
"v_mfma_f32_16x16x32_bf16 v[136:139],a[212:215],v[44:47],v[136:139]\n"
"buffer_load_dwordx4 v32,s[40:43],s77 offen offset:0 lds\n"
"v_mfma_f32_16x16x32_bf16 v[200:203],a[208:211],v[48:51],v[200:203]\n"
"ds_read_b128 a[240:243],v8 offset:38912\n"
"v_cvt_pk_bf16_f32 v128,v128,v129\n"
"v_mfma_f32_16x16x32_bf16 v[200:203],a[212:215],v[52:55],v[200:203]\n"
"s_waitcnt lgkmcnt(5)\n"
"v_cvt_pk_bf16_f32 v129,v130,v131\n"
"v_cvt_pk_bf16_f32 v130,v132,v133\n"
"v_mfma_f32_16x16x32_bf16 v[140:143],a[216:219],v[40:43],v[140:143]\n"
"ds_read_b128 a[244:247],v8 offset:47104\n"
"v_cvt_pk_bf16_f32 v131,v134,v135\n"
"v_mfma_f32_16x16x32_bf16 v[140:143],a[220:223],v[44:47],v[140:143]\n"
"buffer_store_dwordx4 v[128:131],v8,s[64:67],s86 offen offset:0 sc1\n"
"v_mfma_f32_16x16x32_bf16 v[204:207],a[216:219],v[48:51],v[204:207]\n"
"ds_read_b128 a[248:251],v8 offset:39936\n"
"v_cvt_pk_bf16_f32 v192,v192,v193\n"
"v_mfma_f32_16x16x32_bf16 v[204:207],a[220:223],v[52:55],v[204:207]\n"
"s_waitcnt lgkmcnt(5)\n"
"v_cvt_pk_bf16_f32 v193,v194,v195\n"
"v_cvt_pk_bf16_f32 v194,v196,v197\n"
"v_mfma_f32_16x16x32_bf16 v[144:147],a[224:227],v[40:43],v[144:147]\n"
"ds_read_b128 a[252:255],v8 offset:48128\n"
"v_cvt_pk_bf16_f32 v195,v198,v199\n"
"v_mfma_f32_16x16x32_bf16 v[144:147],a[228:231],v[44:47],v[144:147]\n"
"buffer_store_dwordx4 v[192:195],v8,s[64:67],s88 offen offset:0 sc1\n"
"v_mfma_f32_16x16x32_bf16 v[208:211],a[224:227],v[48:51],v[208:211]\n"
"ds_read_b128 a[192:195],v8 offset:49152\n"
"v_cvt_pk_bf16_f32 v136,v136,v137\n"
"v_mfma_f32_16x16x32_bf16 v[208:211],a[228:231],v[52:55],v[208:211]\n"
"s_waitcnt lgkmcnt(5)\n"
"v_cvt_pk_bf16_f32 v137,v138,v139\n"
"v_cvt_pk_bf16_f32 v138,v140,v141\n"
"v_mfma_f32_16x16x32_bf16 v[148:151],a[232:235],v[40:43],v[148:151]\n"
"ds_read_b128 a[196:199],v8 offset:57344\n"
"v_cvt_pk_bf16_f32 v139,v142,v143\n"
"v_mfma_f32_16x16x32_bf16 v[148:151],a[236:239],v[44:47],v[148:151]\n"
"buffer_store_dwordx4 v[136:139],v8,s[64:67],s86 offen offset:1024 sc1\n"
"v_mfma_f32_16x16x32_bf16 v[212:215],a[232:235],v[48:51],v[212:215]\n"
"ds_read_b128 a[200:203],v8 offset:50176\n"
"v_cvt_pk_bf16_f32 v200,v200,v201\n"
"v_mfma_f32_16x16x32_bf16 v[212:215],a[236:239],v[52:55],v[212:215]\n"
"s_waitcnt lgkmcnt(5)\n"
"v_cvt_pk_bf16_f32 v201,v202,v203\n"
"v_cvt_pk_bf16_f32 v202,v204,v205\n"
"v_mfma_f32_16x16x32_bf16 v[152:155],a[240:243],v[40:43],v[152:155]\n"
"ds_read_b128 a[204:207],v8 offset:58368\n"
"v_cvt_pk_bf16_f32 v203,v206,v207\n"
"v_mfma_f32_16x16x32_bf16 v[152:155],a[244:247],v[44:47],v[152:155]\n"
"buffer_store_dwordx4 v[200:203],v8,s[64:67],s88 offen offset:1024 sc1\n"
"v_mfma_f32_16x16x32_bf16 v[216:219],a[240:243],v[48:51],v[216:219]\n"
"ds_read_b128 a[208:211],v8 offset:51200\n"
"v_cvt_pk_bf16_f32 v144,v144,v145\n"
"v_mfma_f32_16x16x32_bf16 v[216:219],a[244:247],v[52:55],v[216:219]\n"
"s_waitcnt lgkmcnt(5)\n"
"v_cvt_pk_bf16_f32 v145,v146,v147\n"
"v_cvt_pk_bf16_f32 v146,v148,v149\n"
"v_mfma_f32_16x16x32_bf16 v[156:159],a[248:251],v[40:43],v[156:159]\n"
"ds_read_b128 a[212:215],v8 offset:59392\n"
"v_cvt_pk_bf16_f32 v147,v150,v151\n"
"v_mfma_f32_16x16x32_bf16 v[156:159],a[252:255],v[44:47],v[156:159]\n"
"buffer_store_dwordx4 v[144:147],v8,s[64:67],s86 offen offset:2048 sc1\n"
"v_mfma_f32_16x16x32_bf16 v[220:223],a[248:251],v[48:51],v[220:223]\n"
"ds_read_b128 a[216:219],v8 offset:52224\n"
"v_cvt_pk_bf16_f32 v208,v208,v209\n"
"v_mfma_f32_16x16x32_bf16 v[220:223],a[252:255],v[52:55],v[220:223]\n"
"s_waitcnt lgkmcnt(5)\n"
"v_cvt_pk_bf16_f32 v209,v210,v211\n"
"v_cvt_pk_bf16_f32 v210,v212,v213\n"
"v_mfma_f32_16x16x32_bf16 v[160:163],a[192:195],v[40:43],v[160:163]\n"
"ds_read_b128 a[220:223],v8 offset:60416\n"
"v_cvt_pk_bf16_f32 v211,v214,v215\n"
"v_mfma_f32_16x16x32_bf16 v[160:163],a[196:199],v[44:47],v[160:163]\n"
"buffer_store_dwordx4 v[208:211],v8,s[64:67],s88 offen offset:2048 sc1\n"
"v_mfma_f32_16x16x32_bf16 v[224:227],a[192:195],v[48:51],v[224:227]\n"
"ds_read_b128 a[224:227],v8 offset:53248\n"
"v_cvt_pk_bf16_f32 v152,v152,v153\n"
"v_mfma_f32_16x16x32_bf16 v[224:227],a[196:199],v[52:55],v[224:227]\n"
"s_waitcnt lgkmcnt(5)\n"
"v_cvt_pk_bf16_f32 v153,v154,v155\n"
"v_cvt_pk_bf16_f32 v154,v156,v157\n"
"v_mfma_f32_16x16x32_bf16 v[164:167],a[200:203],v[40:43],v[164:167]\n"
"ds_read_b128 a[228:231],v8 offset:61440\n"
"v_cvt_pk_bf16_f32 v155,v158,v159\n"
"v_mfma_f32_16x16x32_bf16 v[164:167],a[204:207],v[44:47],v[164:167]\n"
"buffer_store_dwordx4 v[152:155],v8,s[64:67],s86 offen offset:3072 sc1\n"
"v_mfma_f32_16x16x32_bf16 v[228:231],a[200:203],v[48:51],v[228:231]\n"
"ds_read_b128 a[232:235],v8 offset:54272\n"
"v_cvt_pk_bf16_f32 v216,v216,v217\n"
"v_mfma_f32_16x16x32_bf16 v[228:231],a[204:207],v[52:55],v[228:231]\n"
"s_waitcnt lgkmcnt(5)\n"
"v_cvt_pk_bf16_f32 v217,v218,v219\n"
"v_cvt_pk_bf16_f32 v218,v220,v221\n"
"v_mfma_f32_16x16x32_bf16 v[168:171],a[208:211],v[40:43],v[168:171]\n"
"ds_read_b128 a[236:239],v8 offset:62464\n"
"v_cvt_pk_bf16_f32 v219,v222,v223\n"
"v_mfma_f32_16x16x32_bf16 v[168:171],a[212:215],v[44:47],v[168:171]\n"
"buffer_store_dwordx4 v[216:219],v8,s[64:67],s88 offen offset:3072 sc1\n"
"v_mfma_f32_16x16x32_bf16 v[232:235],a[208:211],v[48:51],v[232:235]\n"
"ds_read_b128 a[240:243],v8 offset:55296\n"
"v_cvt_pk_bf16_f32 v160,v160,v161\n"
"v_mfma_f32_16x16x32_bf16 v[232:235],a[212:215],v[52:55],v[232:235]\n"
"s_waitcnt lgkmcnt(5)\n"
"v_cvt_pk_bf16_f32 v161,v162,v163\n"
"v_cvt_pk_bf16_f32 v162,v164,v165\n"
"v_mfma_f32_16x16x32_bf16 v[172:175],a[216:219],v[40:43],v[172:175]\n"
"ds_read_b128 a[244:247],v8 offset:63488\n"
"v_cvt_pk_bf16_f32 v163,v166,v167\n"
"v_mfma_f32_16x16x32_bf16 v[172:175],a[220:223],v[44:47],v[172:175]\n"
"buffer_store_dwordx4 v[160:163],v8,s[64:67],s87 offen offset:0 sc1\n"
"v_mfma_f32_16x16x32_bf16 v[236:239],a[216:219],v[48:51],v[236:239]\n"
"ds_read_b128 a[248:251],v8 offset:56320\n"
"v_cvt_pk_bf16_f32 v224,v224,v225\n"
"v_mfma_f32_16x16x32_bf16 v[236:239],a[220:223],v[52:55],v[236:239]\n"
"s_waitcnt lgkmcnt(5)\n"
"v_cvt_pk_bf16_f32 v225,v226,v227\n"
"v_cvt_pk_bf16_f32 v226,v228,v229\n"
"v_mfma_f32_16x16x32_bf16 v[176:179],a[224:227],v[40:43],v[176:179]\n"
"ds_read_b128 a[252:255],v8 offset:64512\n"
"v_cvt_pk_bf16_f32 v227,v230,v231\n"
"v_mfma_f32_16x16x32_bf16 v[176:179],a[228:231],v[44:47],v[176:179]\n"
"buffer_store_dwordx4 v[224:227],v8,s[64:67],s89 offen offset:0 sc1\n"
"v_mfma_f32_16x16x32_bf16 v[240:243],a[224:227],v[48:51],v[240:243]\n"
"v_cvt_pk_bf16_f32 v168,v168,v169\n"
"v_cvt_pk_bf16_f32 v169,v170,v171\n"
"v_mfma_f32_16x16x32_bf16 v[240:243],a[228:231],v[52:55],v[240:243]\n"
"s_waitcnt lgkmcnt(4)\n"
"v_cvt_pk_bf16_f32 v170,v172,v173\n"
"v_cvt_pk_bf16_f32 v171,v174,v175\n"
"v_mfma_f32_16x16x32_bf16 v[180:183],a[232:235],v[40:43],v[180:183]\n"
"buffer_store_dwordx4 v[168:171],v8,s[64:67],s87 offen offset:1024 sc1\n"
"v_mfma_f32_16x16x32_bf16 v[180:183],a[236:239],v[44:47],v[180:183]\n"
"v_cvt_pk_bf16_f32 v232,v232,v233\n"
"v_cvt_pk_bf16_f32 v233,v234,v235\n"
"v_mfma_f32_16x16x32_bf16 v[244:247],a[232:235],v[48:51],v[244:247]\n"
"v_cvt_pk_bf16_f32 v234,v236,v237\n"
"v_cvt_pk_bf16_f32 v235,v238,v239\n"
"v_mfma_f32_16x16x32_bf16 v[244:247],a[236:239],v[52:55],v[244:247]\n"
"s_waitcnt lgkmcnt(2)\n"
"buffer_store_dwordx4 v[232:235],v8,s[64:67],s89 offen offset:1024 sc1\n"
"v_mfma_f32_16x16x32_bf16 v[184:187],a[240:243],v[40:43],v[184:187]\n"
"v_mfma_f32_16x16x32_bf16 v[184:187],a[244:247],v[44:47],v[184:187]\n"
"v_mfma_f32_16x16x32_bf16 v[248:251],a[240:243],v[48:51],v[248:251]\n"
"v_cvt_pk_bf16_f32 v176,v176,v177\n"
"v_cvt_pk_bf16_f32 v177,v178,v179\n"
"v_mfma_f32_16x16x32_bf16 v[248:251],a[244:247],v[52:55],v[248:251]\n"
"s_waitcnt lgkmcnt(0)\n"
"v_cvt_pk_bf16_f32 v178,v180,v181\n"
"v_cvt_pk_bf16_f32 v179,v182,v183\n"
"v_mfma_f32_16x16x32_bf16 v[188:191],a[248:251],v[40:43],v[188:191]\n"
"buffer_store_dwordx4 v[176:179],v8,s[64:67],s87 offen offset:2048 sc1\n"
"v_mfma_f32_16x16x32_bf16 v[188:191],a[252:255],v[44:47],v[188:191]\n"
"v_cvt_pk_bf16_f32 v240,v240,v241\n"
"v_cvt_pk_bf16_f32 v241,v242,v243\n"
"v_mfma_f32_16x16x32_bf16 v[252:255],a[248:251],v[48:51],v[252:255]\n"
"v_cvt_pk_bf16_f32 v242,v244,v245\n"
"v_cvt_pk_bf16_f32 v243,v246,v247\n"
"v_mfma_f32_16x16x32_bf16 v[252:255],a[252:255],v[52:55],v[252:255]\n"
"buffer_store_dwordx4 v[240:243],v8,s[64:67],s89 offen offset:2048 sc1\n"
"v_cvt_pk_bf16_f32 v184,v184,v185\n"
"v_cvt_pk_bf16_f32 v185,v186,v187\n"
"v_cvt_pk_bf16_f32 v186,v188,v189\n"
"v_cvt_pk_bf16_f32 v187,v190,v191\n"
"buffer_store_dwordx4 v[184:187],v8,s[64:67],s87 offen offset:3072 sc1\n"
"v_cvt_pk_bf16_f32 v248,v248,v249\n"
"v_cvt_pk_bf16_f32 v249,v250,v251\n"
"v_cvt_pk_bf16_f32 v250,v252,v253\n"
"v_cvt_pk_bf16_f32 v251,v254,v255\n"
"buffer_store_dwordx4 v[248:251],v8,s[64:67],s89 offen offset:3072 sc1\n"
"v_rcp_f32 v40,v60\n"
"v_rcp_f32 v42,v61\n"
"v_mov_b32 v41,v40\n"
"v_mov_b32 v43,v42\n"
"s_waitcnt vmcnt(0)\n"
"s_barrier\n"
"s_cmp_eq_u32 s11,0\n"
"s_cbranch_scc0 L_np1_%=\n"
"v_mov_b32 v2,s34\n"
"v_mov_b32 v3,s35\n"
"v_mov_b32 v4,0\n"
"s_mov_b64 exec,1\n"
"global_store_dwordx2 v4,v[2:3],s[84:85] sc1\n"
"s_mov_b64 exec,-1\n"
"L_np1_%=:\n"
"buffer_load_dwordx4 v33,s[40:43],s77 offen offset:1024 lds\n"
"v_accvgpr_read_b32 v128,a0\n"
"v_accvgpr_read_b32 v129,a1\n"
"v_accvgpr_read_b32 v130,a2\n"
"v_accvgpr_read_b32 v131,a3\n"
"v_accvgpr_read_b32 v132,a4\n"
"v_accvgpr_read_b32 v133,a5\n"
"v_accvgpr_read_b32 v134,a6\n"
"v_accvgpr_read_b32 v135,a7\n"
"v_pk_mul_f32 v[128:129],v[128:129],v[40:41]\n"
"v_pk_mul_f32 v[130:131],v[130:131],v[40:41]\n"
"v_pk_mul_f32 v[132:133],v[132:133],v[40:41]\n"
"v_pk_mul_f32 v[134:135],v[134:135],v[40:41]\n"
"v_cvt_pk_bf16_f32 v128,v128,v129\n"
"v_cvt_pk_bf16_f32 v129,v130,v131\n"
"buffer_load_dwordx4 v34,s[40:43],s77 offen offset:2048 lds\n"
"v_cvt_pk_bf16_f32 v130,v132,v133\n"
"v_cvt_pk_bf16_f32 v131,v134,v135\n"
"v_accvgpr_read_b32 v136,a8\n"
"v_accvgpr_read_b32 v137,a9\n"
"v_accvgpr_read_b32 v138,a10\n"
"v_accvgpr_read_b32 v139,a11\n"
"v_accvgpr_read_b32 v140,a12\n"
"v_accvgpr_read_b32 v141,a13\n"
"v_accvgpr_read_b32 v142,a14\n"
"v_accvgpr_read_b32 v143,a15\n"
"v_pk_mul_f32 v[136:137],v[136:137],v[40:41]\n"
"v_pk_mul_f32 v[138:139],v[138:139],v[40:41]\n"
"v_pk_mul_f32 v[140:141],v[140:141],v[40:41]\n"
"v_pk_mul_f32 v[142:143],v[142:143],v[40:41]\n"
"buffer_load_dwordx4 v35,s[40:43],s77 offen offset:3072 lds\n"
"v_cvt_pk_bf16_f32 v136,v136,v137\n"
"v_cvt_pk_bf16_f32 v137,v138,v139\n"
"v_cvt_pk_bf16_f32 v138,v140,v141\n"
"v_cvt_pk_bf16_f32 v139,v142,v143\n"
"v_accvgpr_read_b32 v144,a16\n"
"v_accvgpr_read_b32 v145,a17\n"
"v_accvgpr_read_b32 v146,a18\n"
"v_accvgpr_read_b32 v147,a19\n"
"v_accvgpr_read_b32 v148,a20\n"
"v_accvgpr_read_b32 v149,a21\n"
"v_accvgpr_read_b32 v150,a22\n"
"v_accvgpr_read_b32 v151,a23\n"
"v_pk_mul_f32 v[144:145],v[144:145],v[40:41]\n"
"v_pk_mul_f32 v[146:147],v[146:147],v[40:41]\n"
"s_add_u32 m0,m0,0x1000\n"
"s_nop 0\n"
"buffer_load_dwordx4 v36,s[40:43],s77 offen offset:0 lds\n"
"v_pk_mul_f32 v[148:149],v[148:149],v[40:41]\n"
"v_pk_mul_f32 v[150:151],v[150:151],v[40:41]\n"
"v_cvt_pk_bf16_f32 v144,v144,v145\n"
"v_cvt_pk_bf16_f32 v145,v146,v147\n"
"v_cvt_pk_bf16_f32 v146,v148,v149\n"
"v_cvt_pk_bf16_f32 v147,v150,v151\n"
"v_accvgpr_read_b32 v152,a24\n"
"v_accvgpr_read_b32 v153,a25\n"
"v_accvgpr_read_b32 v154,a26\n"
"v_accvgpr_read_b32 v155,a27\n"
"v_accvgpr_read_b32 v156,a28\n"
"v_accvgpr_read_b32 v157,a29\n"
"v_accvgpr_read_b32 v158,a30\n"
"v_accvgpr_read_b32 v159,a31\n"
"buffer_load_dwordx4 v37,s[40:43],s77 offen offset:1024 lds\n"
"v_pk_mul_f32 v[152:153],v[152:153],v[40:41]\n"
"v_pk_mul_f32 v[154:155],v[154:155],v[40:41]\n"
"v_pk_mul_f32 v[156:157],v[156:157],v[40:41]\n"
"v_pk_mul_f32 v[158:159],v[158:159],v[40:41]\n"
"v_cvt_pk_bf16_f32 v152,v152,v153\n"
"v_cvt_pk_bf16_f32 v153,v154,v155\n"
"v_cvt_pk_bf16_f32 v154,v156,v157\n"
"v_cvt_pk_bf16_f32 v155,v158,v159\n"
"v_accvgpr_read_b32 v160,a32\n"
"v_accvgpr_read_b32 v161,a33\n"
"v_accvgpr_read_b32 v162,a34\n"
"v_accvgpr_read_b32 v163,a35\n"
"v_accvgpr_read_b32 v164,a36\n"
"v_accvgpr_read_b32 v165,a37\n"
"buffer_load_dwordx4 v38,s[40:43],s77 offen offset:2048 lds\n"
"v_accvgpr_read_b32 v166,a38\n"
"v_accvgpr_read_b32 v167,a39\n"
"v_pk_mul_f32 v[160:161],v[160:161],v[40:41]\n"
"v_pk_mul_f32 v[162:163],v[162:163],v[40:41]\n"
"v_pk_mul_f32 v[164:165],v[164:165],v[40:41]\n"
"v_pk_mul_f32 v[166:167],v[166:167],v[40:41]\n"
"v_cvt_pk_bf16_f32 v160,v160,v161\n"
"v_cvt_pk_bf16_f32 v161,v162,v163\n"
"v_cvt_pk_bf16_f32 v162,v164,v165\n"
"v_cvt_pk_bf16_f32 v163,v166,v167\n"
"v_accvgpr_read_b32 v168,a40\n"
"v_accvgpr_read_b32 v169,a41\n"
"v_accvgpr_read_b32 v170,a42\n"
"v_accvgpr_read_b32 v171,a43\n"
"buffer_load_dwordx4 v39,s[40:43],s77 offen offset:3072 lds\n"
"v_accvgpr_read_b32 v172,a44\n"
"v_accvgpr_read_b32 v173,a45\n"
"v_accvgpr_read_b32 v174,a46\n"
"v_accvgpr_read_b32 v175,a47\n"
"v_pk_mul_f32 v[168:169],v[168:169],v[40:41]\n"
"v_pk_mul_f32 v[170:171],v[170:171],v[40:41]\n"
"v_pk_mul_f32 v[172:173],v[172:173],v[40:41]\n"
"v_pk_mul_f32 v[174:175],v[174:175],v[40:41]\n"
"v_cvt_pk_bf16_f32 v168,v168,v169\n"
"v_cvt_pk_bf16_f32 v169,v170,v171\n"
"v_cvt_pk_bf16_f32 v170,v172,v173\n"
"v_cvt_pk_bf16_f32 v171,v174,v175\n"
"v_accvgpr_read_b32 v176,a48\n"
"v_accvgpr_read_b32 v177,a49\n"
"s_mov_b32 s77,256\n"
"s_add_u32 m0,s57,0x18000\n"
"s_nop 0\n"
"buffer_load_dwordx4 v32,s[40:43],s77 offen offset:0 lds\n"
"v_accvgpr_read_b32 v178,a50\n"
"v_accvgpr_read_b32 v179,a51\n"
"v_accvgpr_read_b32 v180,a52\n"
"v_accvgpr_read_b32 v181,a53\n"
"v_accvgpr_read_b32 v182,a54\n"
"v_accvgpr_read_b32 v183,a55\n"
"v_pk_mul_f32 v[176:177],v[176:177],v[40:41]\n"
"v_pk_mul_f32 v[178:179],v[178:179],v[40:41]\n"
"v_pk_mul_f32 v[180:181],v[180:181],v[40:41]\n"
"v_pk_mul_f32 v[182:183],v[182:183],v[40:41]\n"
"v_cvt_pk_bf16_f32 v176,v176,v177\n"
"v_cvt_pk_bf16_f32 v177,v178,v179\n"
"v_cvt_pk_bf16_f32 v178,v180,v181\n"
"v_cvt_pk_bf16_f32 v179,v182,v183\n"
"buffer_load_dwordx4 v33,s[40:43],s77 offen offset:1024 lds\n"
"v_accvgpr_read_b32 v184,a56\n"
"v_accvgpr_read_b32 v185,a57\n"
"v_accvgpr_read_b32 v186,a58\n"
"v_accvgpr_read_b32 v187,a59\n"
"v_accvgpr_read_b32 v188,a60\n"
"v_accvgpr_read_b32 v189,a61\n"
"v_accvgpr_read_b32 v190,a62\n"
"v_accvgpr_read_b32 v191,a63\n"
"v_pk_mul_f32 v[184:185],v[184:185],v[40:41]\n"
"v_pk_mul_f32 v[186:187],v[186:187],v[40:41]\n"
"v_pk_mul_f32 v[188:189],v[188:189],v[40:41]\n"
"v_pk_mul_f32 v[190:191],v[190:191],v[40:41]\n"
"v_cvt_pk_bf16_f32 v184,v184,v185\n"
"v_cvt_pk_bf16_f32 v185,v186,v187\n"
"buffer_load_dwordx4 v34,s[40:43],s77 offen offset:2048 lds\n"
"v_cvt_pk_bf16_f32 v186,v188,v189\n"
"v_cvt_pk_bf16_f32 v187,v190,v191\n"
"v_accvgpr_read_b32 v192,a64\n"
"v_accvgpr_read_b32 v193,a65\n"
"v_accvgpr_read_b32 v194,a66\n"
"v_accvgpr_read_b32 v195,a67\n"
"v_accvgpr_read_b32 v196,a68\n"
"v_accvgpr_read_b32 v197,a69\n"
"v_accvgpr_read_b32 v198,a70\n"
"v_accvgpr_read_b32 v199,a71\n"
"v_pk_mul_f32 v[192:193],v[192:193],v[42:43]\n"
"v_pk_mul_f32 v[194:195],v[194:195],v[42:43]\n"
"v_pk_mul_f32 v[196:197],v[196:197],v[42:43]\n"
"v_pk_mul_f32 v[198:199],v[198:199],v[42:43]\n"
"buffer_load_dwordx4 v35,s[40:43],s77 offen offset:3072 lds\n"
"v_cvt_pk_bf16_f32 v192,v192,v193\n"
"v_cvt_pk_bf16_f32 v193,v194,v195\n"
"v_cvt_pk_bf16_f32 v194,v196,v197\n"
"v_cvt_pk_bf16_f32 v195,v198,v199\n"
"v_accvgpr_read_b32 v200,a72\n"
"v_accvgpr_read_b32 v201,a73\n"
"v_accvgpr_read_b32 v202,a74\n"
"v_accvgpr_read_b32 v203,a75\n"
"v_accvgpr_read_b32 v204,a76\n"
"v_accvgpr_read_b32 v205,a77\n"
"v_accvgpr_read_b32 v206,a78\n"
"v_accvgpr_read_b32 v207,a79\n"
"v_pk_mul_f32 v[200:201],v[200:201],v[42:43]\n"
"v_pk_mul_f32 v[202:203],v[202:203],v[42:43]\n"
"s_add_u32 m0,m0,0x1000\n"
"s_nop 0\n"
"buffer_load_dwordx4 v36,s[40:43],s77 offen offset:0 lds\n"
"v_pk_mul_f32 v[204:205],v[204:205],v[42:43]\n"
"v_pk_mul_f32 v[206:207],v[206:207],v[42:43]\n"
"v_cvt_pk_bf16_f32 v200,v200,v201\n"
"v_cvt_pk_bf16_f32 v201,v202,v203\n"
"v_cvt_pk_bf16_f32 v202,v204,v205\n"
"v_cvt_pk_bf16_f32 v203,v206,v207\n"
"v_accvgpr_read_b32 v208,a80\n"
"v_accvgpr_read_b32 v209,a81\n"
"v_accvgpr_read_b32 v210,a82\n"
"v_accvgpr_read_b32 v211,a83\n"
"v_accvgpr_read_b32 v212,a84\n"
"v_accvgpr_read_b32 v213,a85\n"
"v_accvgpr_read_b32 v214,a86\n"
"v_accvgpr_read_b32 v215,a87\n"
"buffer_load_dwordx4 v37,s[40:43],s77 offen offset:1024 lds\n"
"v_pk_mul_f32 v[208:209],v[208:209],v[42:43]\n"
"v_pk_mul_f32 v[210:211],v[210:211],v[42:43]\n"
"v_pk_mul_f32 v[212:213],v[212:213],v[42:43]\n"
"v_pk_mul_f32 v[214:215],v[214:215],v[42:43]\n"
"v_cvt_pk_bf16_f32 v208,v208,v209\n"
"v_cvt_pk_bf16_f32 v209,v210,v211\n"
"v_cvt_pk_bf16_f32 v210,v212,v213\n"
"v_cvt_pk_bf16_f32 v211,v214,v215\n"
"v_accvgpr_read_b32 v216,a88\n"
"v_accvgpr_read_b32 v217,a89\n"
"v_accvgpr_read_b32 v218,a90\n"
"v_accvgpr_read_b32 v219,a91\n"
"v_accvgpr_read_b32 v220,a92\n"
"v_accvgpr_read_b32 v221,a93\n"
"buffer_load_dwordx4 v38,s[40:43],s77 offen offset:2048 lds\n"
"v_accvgpr_read_b32 v222,a94\n"
"v_accvgpr_read_b32 v223,a95\n"
"v_pk_mul_f32 v[216:217],v[216:217],v[42:43]\n"
"v_pk_mul_f32 v[218:219],v[218:219],v[42:43]\n"
"v_pk_mul_f32 v[220:221],v[220:221],v[42:43]\n"
"v_pk_mul_f32 v[222:223],v[222:223],v[42:43]\n"
"v_cvt_pk_bf16_f32 v216,v216,v217\n"
"v_cvt_pk_bf16_f32 v217,v218,v219\n"
"v_cvt_pk_bf16_f32 v218,v220,v221\n"
"v_cvt_pk_bf16_f32 v219,v222,v223\n"
"v_accvgpr_read_b32 v224,a96\n"
"v_accvgpr_read_b32 v225,a97\n"
"v_accvgpr_read_b32 v226,a98\n"
"v_accvgpr_read_b32 v227,a99\n"
"buffer_load_dwordx4 v39,s[40:43],s77 offen offset:3072 lds\n"
"v_accvgpr_read_b32 v228,a100\n"
"v_accvgpr_read_b32 v229,a101\n"
"v_accvgpr_read_b32 v230,a102\n"
"v_accvgpr_read_b32 v231,a103\n"
"v_pk_mul_f32 v[224:225],v[224:225],v[42:43]\n"
"v_pk_mul_f32 v[226:227],v[226:227],v[42:43]\n"
"v_pk_mul_f32 v[228:229],v[228:229],v[42:43]\n"
"v_pk_mul_f32 v[230:231],v[230:231],v[42:43]\n"
"v_cvt_pk_bf16_f32 v224,v224,v225\n"
"v_cvt_pk_bf16_f32 v225,v226,v227\n"
"v_cvt_pk_bf16_f32 v226,v228,v229\n"
"v_cvt_pk_bf16_f32 v227,v230,v231\n"
"v_accvgpr_read_b32 v232,a104\n"
"v_accvgpr_read_b32 v233,a105\n"
"v_accvgpr_read_b32 v234,a106\n"
"v_accvgpr_read_b32 v235,a107\n"
"v_accvgpr_read_b32 v236,a108\n"
"v_accvgpr_read_b32 v237,a109\n"
"v_accvgpr_read_b32 v238,a110\n"
"v_accvgpr_read_b32 v239,a111\n"
"v_pk_mul_f32 v[232:233],v[232:233],v[42:43]\n"
"v_pk_mul_f32 v[234:235],v[234:235],v[42:43]\n"
"v_pk_mul_f32 v[236:237],v[236:237],v[42:43]\n"
"v_pk_mul_f32 v[238:239],v[238:239],v[42:43]\n"
"v_cvt_pk_bf16_f32 v232,v232,v233\n"
"v_cvt_pk_bf16_f32 v233,v234,v235\n"
"v_cvt_pk_bf16_f32 v234,v236,v237\n"
"v_cvt_pk_bf16_f32 v235,v238,v239\n"
"v_accvgpr_read_b32 v240,a112\n"
"v_accvgpr_read_b32 v241,a113\n"
"v_accvgpr_read_b32 v242,a114\n"
"v_accvgpr_read_b32 v243,a115\n"
"v_accvgpr_read_b32 v244,a116\n"
"v_accvgpr_read_b32 v245,a117\n"
"v_accvgpr_read_b32 v246,a118\n"
"v_accvgpr_read_b32 v247,a119\n"
"v_pk_mul_f32 v[240:241],v[240:241],v[42:43]\n"
"v_pk_mul_f32 v[242:243],v[242:243],v[42:43]\n"
"v_pk_mul_f32 v[244:245],v[244:245],v[42:43]\n"
"v_pk_mul_f32 v[246:247],v[246:247],v[42:43]\n"
"v_cvt_pk_bf16_f32 v240,v240,v241\n"
"v_cvt_pk_bf16_f32 v241,v242,v243\n"
"v_cvt_pk_bf16_f32 v242,v244,v245\n"
"v_cvt_pk_bf16_f32 v243,v246,v247\n"
"v_accvgpr_read_b32 v248,a120\n"
"v_accvgpr_read_b32 v249,a121\n"
"v_accvgpr_read_b32 v250,a122\n"
"v_accvgpr_read_b32 v251,a123\n"
"v_accvgpr_read_b32 v252,a124\n"
"v_accvgpr_read_b32 v253,a125\n"
"v_accvgpr_read_b32 v254,a126\n"
"v_accvgpr_read_b32 v255,a127\n"
"v_pk_mul_f32 v[248:249],v[248:249],v[42:43]\n"
"v_pk_mul_f32 v[250:251],v[250:251],v[42:43]\n"
"v_pk_mul_f32 v[252:253],v[252:253],v[42:43]\n"
"v_pk_mul_f32 v[254:255],v[254:255],v[42:43]\n"
"v_cvt_pk_bf16_f32 v248,v248,v249\n"
"v_cvt_pk_bf16_f32 v249,v250,v251\n"
"v_cvt_pk_bf16_f32 v250,v252,v253\n"
"v_cvt_pk_bf16_f32 v251,v254,v255\n"
"L_oproj_%=:\n"
"s_mul_i32 s96,s92,1835008\n"
"s_lshl_b32 s97,s54,3\n"
"s_lshl_b32 s59,s11,1\n"
"s_add_u32 s97,s97,s59\n"
"s_mul_i32 s97,s97,14336\n"
"s_add_u32 s96,s96,s97\n"
"s_add_u32 s44,s32,s96\n"
"s_addc_u32 s45,s33,0\n"
"s_add_u32 s44,s44,0x3400000\n"
"s_addc_u32 s45,s45,0\n"
"s_and_b32 s45,s45,0xffff\n"
"s_mov_b32 s46,0x7000\n"
"s_mov_b32 s47,0x20000\n"
"s_mov_b32 s86,14336\n"
"s_mov_b32 s87,7168\n"
"s_mov_b32 s88,21504\n"
"v_lshrrev_b32 v1,3,v0\n"
"s_waitcnt vmcnt(18)\n"
"v_lshrrev_b32 v2,4,v0\n"
"v_bfe_u32 v3,v0,1,2\n"
"v_xor_b32 v3,v3,v2\n"
"v_and_b32 v4,1,v0\n"
"v_lshlrev_b32 v3,5,v3\n"
"v_lshl_add_u32 v3,v4,4,v3\n"
"s_lshl_b32 s96,s11,4\n"
"v_add_u32 v4,s96,v1\n"
"v_mul_u32_u24 v4,3584,v4\n"
"v_add_u32 v60,v4,v3\n"
"v_add_u32 v61,0x6c00,v60\n"
"s_lshl_b32 s96,s11,11\n"
"s_waitcnt vmcnt(16)\n"
"s_barrier\n"
"ds_read_b64_tr_b16 a[192:193],v28 offset:0\n"
"ds_read_b64_tr_b16 a[194:195],v28 offset:2048\n"
"ds_read_b64_tr_b16 a[196:197],v28 offset:4096\n"
"ds_read_b64_tr_b16 a[198:199],v28 offset:6144\n"
"ds_read_b64_tr_b16 a[200:201],v28 offset:8192\n"
"ds_read_b64_tr_b16 a[202:203],v28 offset:10240\n"
"ds_read_b64_tr_b16 a[204:205],v28 offset:12288\n"
"ds_read_b64_tr_b16 a[206:207],v28 offset:14336\n"
"ds_read_b64_tr_b16 a[208:209],v28 offset:16384\n"
"ds_read_b64_tr_b16 a[210:211],v28 offset:18432\n"
"ds_read_b64_tr_b16 a[212:213],v28 offset:20480\n"
"ds_read_b64_tr_b16 a[214:215],v28 offset:22528\n"
"ds_read_b64_tr_b16 a[216:217],v28 offset:24576\n"
"ds_read_b64_tr_b16 a[218:219],v28 offset:26624\n"
"ds_read_b64_tr_b16 a[220:221],v28 offset:28672\n"
"ds_read_b64_tr_b16 a[222:223],v28 offset:30720\n"
"ds_read_b64_tr_b16 a[224:225],v29 offset:0\n"
"ds_read_b64_tr_b16 a[226:227],v29 offset:2048\n"
"ds_read_b64_tr_b16 a[228:229],v29 offset:4096\n"
"ds_read_b64_tr_b16 a[230:231],v29 offset:6144\n"
"s_waitcnt lgkmcnt(12)\n"
"v_mfma_f32_16x16x32_bf16 v[64:67],a[192:195],v[128:131],0\n"
"ds_read_b64_tr_b16 a[232:233],v29 offset:8192\n"
"v_mfma_f32_16x16x32_bf16 v[64:67],a[196:199],v[136:139],v[64:67]\n"
"s_mov_b32 s77,384\n"
"ds_read_b64_tr_b16 a[234:235],v29 offset:10240\n"
"v_mfma_f32_16x16x32_bf16 v[64:67],a[200:203],v[144:147],v[64:67]\n"
"s_add_u32 m0,s57,0x0\n"
"ds_read_b64_tr_b16 a[236:237],v29 offset:12288\n"
"v_mfma_f32_16x16x32_bf16 v[64:67],a[204:207],v[152:155],v[64:67]\n"
"s_waitcnt lgkmcnt(7)\n"
"ds_read_b64_tr_b16 a[238:239],v29 offset:14336\n"
"buffer_load_dwordx4 v32,s[40:43],s77 offen offset:0 lds\n"
"v_mfma_f32_16x16x32_bf16 v[64:67],a[208:211],v[160:163],v[64:67]\n"
"ds_read_b64_tr_b16 a[240:241],v29 offset:16384\n"
"v_mfma_f32_16x16x32_bf16 v[64:67],a[212:215],v[168:171],v[64:67]\n"
"ds_read_b64_tr_b16 a[242:243],v29 offset:18432\n"
"v_mfma_f32_16x16x32_bf16 v[64:67],a[216:219],v[176:179],v[64:67]\n"
"ds_read_b64_tr_b16 a[244:245],v29 offset:20480\n"
"v_mfma_f32_16x16x32_bf16 v[64:67],a[220:223],v[184:187],v[64:67]\n"
"ds_read_b64_tr_b16 a[246:247],v29 offset:22528\n"
"buffer_load_dwordx4 v33,s[40:43],s77 offen offset:1024 lds\n"
"v_mfma_f32_16x16x32_bf16 v[80:83],a[192:195],v[192:195],0\n"
"ds_read_b64_tr_b16 a[248:249],v29 offset:24576\n"
"v_mfma_f32_16x16x32_bf16 v[80:83],a[196:199],v[200:203],v[80:83]\n"
"ds_read_b64_tr_b16 a[250:251],v29 offset:26624\n"
"v_mfma_f32_16x16x32_bf16 v[80:83],a[200:203],v[208:211],v[80:83]\n"
"ds_read_b64_tr_b16 a[252:253],v29 offset:28672\n"
"v_mfma_f32_16x16x32_bf16 v[80:83],a[204:207],v[216:219],v[80:83]\n"
"ds_read_b64_tr_b16 a[254:255],v29 offset:30720\n"
"buffer_load_dwordx4 v34,s[40:43],s77 offen offset:2048 lds\n"
"v_mfma_f32_16x16x32_bf16 v[80:83],a[208:211],v[224:227],v[80:83]\n"
"ds_read_b64_tr_b16 a[192:193],v30 offset:0\n"
"v_mfma_f32_16x16x32_bf16 v[80:83],a[212:215],v[232:235],v[80:83]\n"
"ds_read_b64_tr_b16 a[194:195],v30 offset:2048\n"
"v_mfma_f32_16x16x32_bf16 v[80:83],a[216:219],v[240:243],v[80:83]\n"
"ds_read_b64_tr_b16 a[196:197],v30 offset:4096\n"
"v_mfma_f32_16x16x32_bf16 v[80:83],a[220:223],v[248:251],v[80:83]\n"
"s_waitcnt lgkmcnt(11)\n"
"ds_read_b64_tr_b16 a[198:199],v30 offset:6144\n"
"buffer_load_dwordx4 v35,s[40:43],s77 offen offset:3072 lds\n"
"v_mfma_f32_16x16x32_bf16 v[68:71],a[224:227],v[128:131],0\n"
"ds_read_b64_tr_b16 a[200:201],v30 offset:8192\n"
"v_mfma_f32_16x16x32_bf16 v[68:71],a[228:231],v[136:139],v[68:71]\n"
"ds_read_b64_tr_b16 a[202:203],v30 offset:10240\n"
"v_mfma_f32_16x16x32_bf16 v[68:71],a[232:235],v[144:147],v[68:71]\n"
"s_add_u32 m0,m0,0x1000\n"
"ds_read_b64_tr_b16 a[204:205],v30 offset:12288\n"
"v_mfma_f32_16x16x32_bf16 v[68:71],a[236:239],v[152:155],v[68:71]\n"
"s_waitcnt lgkmcnt(7)\n"
"ds_read_b64_tr_b16 a[206:207],v30 offset:14336\n"
"buffer_load_dwordx4 v36,s[40:43],s77 offen offset:0 lds\n"
"v_mfma_f32_16x16x32_bf16 v[68:71],a[240:243],v[160:163],v[68:71]\n"
"ds_read_b64_tr_b16 a[208:209],v30 offset:16384\n"
"v_mfma_f32_16x16x32_bf16 v[68:71],a[244:247],v[168:171],v[68:71]\n"
"ds_read_b64_tr_b16 a[210:211],v30 offset:18432\n"
"v_mfma_f32_16x16x32_bf16 v[68:71],a[248:251],v[176:179],v[68:71]\n"
"ds_read_b64_tr_b16 a[212:213],v30 offset:20480\n"
"v_mfma_f32_16x16x32_bf16 v[68:71],a[252:255],v[184:187],v[68:71]\n"
"ds_read_b64_tr_b16 a[214:215],v30 offset:22528\n"
"buffer_load_dwordx4 v37,s[40:43],s77 offen offset:1024 lds\n"
"v_mfma_f32_16x16x32_bf16 v[84:87],a[224:227],v[192:195],0\n"
"ds_read_b64_tr_b16 a[216:217],v30 offset:24576\n"
"v_mfma_f32_16x16x32_bf16 v[84:87],a[228:231],v[200:203],v[84:87]\n"
"ds_read_b64_tr_b16 a[218:219],v30 offset:26624\n"
"v_mfma_f32_16x16x32_bf16 v[84:87],a[232:235],v[208:211],v[84:87]\n"
"ds_read_b64_tr_b16 a[220:221],v30 offset:28672\n"
"v_mfma_f32_16x16x32_bf16 v[84:87],a[236:239],v[216:219],v[84:87]\n"
"ds_read_b64_tr_b16 a[222:223],v30 offset:30720\n"
"buffer_load_dwordx4 v38,s[40:43],s77 offen offset:2048 lds\n"
"v_mfma_f32_16x16x32_bf16 v[84:87],a[240:243],v[224:227],v[84:87]\n"
"ds_read_b64_tr_b16 a[224:225],v31 offset:0\n"
"v_mfma_f32_16x16x32_bf16 v[84:87],a[244:247],v[232:235],v[84:87]\n"
"ds_read_b64_tr_b16 a[226:227],v31 offset:2048\n"
"v_mfma_f32_16x16x32_bf16 v[84:87],a[248:251],v[240:243],v[84:87]\n"
"ds_read_b64_tr_b16 a[228:229],v31 offset:4096\n"
"v_add_u32 v28,0xffff0000,v28\n"
"v_mfma_f32_16x16x32_bf16 v[84:87],a[252:255],v[248:251],v[84:87]\n"
"s_waitcnt lgkmcnt(11)\n"
"ds_read_b64_tr_b16 a[230:231],v31 offset:6144\n"
"buffer_load_dwordx4 v39,s[40:43],s77 offen offset:3072 lds\n"
"v_mfma_f32_16x16x32_bf16 v[72:75],a[192:195],v[128:131],0\n"
"ds_read_b64_tr_b16 a[232:233],v31 offset:8192\n"
"v_mfma_f32_16x16x32_bf16 v[72:75],a[196:199],v[136:139],v[72:75]\n"
"s_mov_b32 s77,512\n"
"ds_read_b64_tr_b16 a[234:235],v31 offset:10240\n"
"v_mfma_f32_16x16x32_bf16 v[72:75],a[200:203],v[144:147],v[72:75]\n"
"s_add_u32 m0,s57,0x8000\n"
"ds_read_b64_tr_b16 a[236:237],v31 offset:12288\n"
"v_mfma_f32_16x16x32_bf16 v[72:75],a[204:207],v[152:155],v[72:75]\n"
"s_waitcnt lgkmcnt(7)\n"
"ds_read_b64_tr_b16 a[238:239],v31 offset:14336\n"
"buffer_load_dwordx4 v32,s[40:43],s77 offen offset:0 lds\n"
"v_mfma_f32_16x16x32_bf16 v[72:75],a[208:211],v[160:163],v[72:75]\n"
"ds_read_b64_tr_b16 a[240:241],v31 offset:16384\n"
"v_mfma_f32_16x16x32_bf16 v[72:75],a[212:215],v[168:171],v[72:75]\n"
"ds_read_b64_tr_b16 a[242:243],v31 offset:18432\n"
"v_mfma_f32_16x16x32_bf16 v[72:75],a[216:219],v[176:179],v[72:75]\n"
"ds_read_b64_tr_b16 a[244:245],v31 offset:20480\n"
"v_mfma_f32_16x16x32_bf16 v[72:75],a[220:223],v[184:187],v[72:75]\n"
"ds_read_b64_tr_b16 a[246:247],v31 offset:22528\n"
"buffer_load_dwordx4 v33,s[40:43],s77 offen offset:1024 lds\n"
"v_mfma_f32_16x16x32_bf16 v[88:91],a[192:195],v[192:195],0\n"
"ds_read_b64_tr_b16 a[248:249],v31 offset:24576\n"
"v_mfma_f32_16x16x32_bf16 v[88:91],a[196:199],v[200:203],v[88:91]\n"
"ds_read_b64_tr_b16 a[250:251],v31 offset:26624\n"
"v_mfma_f32_16x16x32_bf16 v[88:91],a[200:203],v[208:211],v[88:91]\n"
"ds_read_b64_tr_b16 a[252:253],v31 offset:28672\n"
"v_mfma_f32_16x16x32_bf16 v[88:91],a[204:207],v[216:219],v[88:91]\n"
"s_waitcnt vmcnt(18)\n"
"s_barrier\n"
"ds_read_b64_tr_b16 a[254:255],v31 offset:30720\n"
"v_add_u32 v29,0xffff0000,v29\n"
"v_mfma_f32_16x16x32_bf16 v[88:91],a[208:211],v[224:227],v[88:91]\n"
"ds_read_b64_tr_b16 a[192:193],v28 offset:0\n"
"v_add_u32 v30,0xffff0000,v30\n"
"v_mfma_f32_16x16x32_bf16 v[88:91],a[212:215],v[232:235],v[88:91]\n"
"ds_read_b64_tr_b16 a[194:195],v28 offset:2048\n"
"v_add_u32 v31,0xffff0000,v31\n"
"v_mfma_f32_16x16x32_bf16 v[88:91],a[216:219],v[240:243],v[88:91]\n"
"ds_read_b64_tr_b16 a[196:197],v28 offset:4096\n"
"buffer_load_dwordx4 v34,s[40:43],s77 offen offset:2048 lds\n"
"v_mfma_f32_16x16x32_bf16 v[88:91],a[220:223],v[248:251],v[88:91]\n"
"s_waitcnt lgkmcnt(11)\n"
"ds_read_b64_tr_b16 a[198:199],v28 offset:6144\n"
"v_mfma_f32_16x16x32_bf16 v[76:79],a[224:227],v[128:131],0\n"
"ds_read_b64_tr_b16 a[200:201],v28 offset:8192\n"
"v_mfma_f32_16x16x32_bf16 v[76:79],a[228:231],v[136:139],v[76:79]\n"
"ds_read_b64_tr_b16 a[202:203],v28 offset:10240\n"
"v_mfma_f32_16x16x32_bf16 v[76:79],a[232:235],v[144:147],v[76:79]\n"
"ds_read_b64_tr_b16 a[204:205],v28 offset:12288\n"
"buffer_load_dwordx4 v35,s[40:43],s77 offen offset:3072 lds\n"
"v_mfma_f32_16x16x32_bf16 v[76:79],a[236:239],v[152:155],v[76:79]\n"
"s_waitcnt lgkmcnt(7)\n"
"ds_read_b64_tr_b16 a[206:207],v28 offset:14336\n"
"v_mfma_f32_16x16x32_bf16 v[76:79],a[240:243],v[160:163],v[76:79]\n"
"ds_read_b64_tr_b16 a[208:209],v28 offset:16384\n"
"v_mfma_f32_16x16x32_bf16 v[76:79],a[244:247],v[168:171],v[76:79]\n"
"s_add_u32 m0,m0,0x1000\n"
"ds_read_b64_tr_b16 a[210:211],v28 offset:18432\n"
"v_mfma_f32_16x16x32_bf16 v[76:79],a[248:251],v[176:179],v[76:79]\n"
"ds_read_b64_tr_b16 a[212:213],v28 offset:20480\n"
"buffer_load_dwordx4 v36,s[40:43],s77 offen offset:0 lds\n"
"v_mfma_f32_16x16x32_bf16 v[76:79],a[252:255],v[184:187],v[76:79]\n"
"ds_read_b64_tr_b16 a[214:215],v28 offset:22528\n"
"v_mfma_f32_16x16x32_bf16 v[92:95],a[224:227],v[192:195],0\n"
"ds_read_b64_tr_b16 a[216:217],v28 offset:24576\n"
"v_mfma_f32_16x16x32_bf16 v[92:95],a[228:231],v[200:203],v[92:95]\n"
"ds_read_b64_tr_b16 a[218:219],v28 offset:26624\n"
"v_mfma_f32_16x16x32_bf16 v[92:95],a[232:235],v[208:211],v[92:95]\n"
"ds_read_b64_tr_b16 a[220:221],v28 offset:28672\n"
"buffer_load_dwordx4 v37,s[40:43],s77 offen offset:1024 lds\n"
"v_mfma_f32_16x16x32_bf16 v[92:95],a[236:239],v[216:219],v[92:95]\n"
"ds_read_b64_tr_b16 a[222:223],v28 offset:30720\n"
"v_mfma_f32_16x16x32_bf16 v[92:95],a[240:243],v[224:227],v[92:95]\n"
"ds_read_b64_tr_b16 a[224:225],v29 offset:0\n"
"v_mfma_f32_16x16x32_bf16 v[92:95],a[244:247],v[232:235],v[92:95]\n"
"ds_read_b64_tr_b16 a[226:227],v29 offset:2048\n"
"v_mfma_f32_16x16x32_bf16 v[92:95],a[248:251],v[240:243],v[92:95]\n"
"ds_read_b64_tr_b16 a[228:229],v29 offset:4096\n"
"buffer_load_dwordx4 v38,s[40:43],s77 offen offset:2048 lds\n"
"v_mfma_f32_16x16x32_bf16 v[92:95],a[252:255],v[248:251],v[92:95]\n"
"s_waitcnt lgkmcnt(11)\n"
"ds_read_b64_tr_b16 a[230:231],v29 offset:6144\n"
"v_mfma_f32_16x16x32_bf16 v[96:99],a[192:195],v[128:131],0\n"
"ds_read_b64_tr_b16 a[232:233],v29 offset:8192\n"
"v_mfma_f32_16x16x32_bf16 v[96:99],a[196:199],v[136:139],v[96:99]\n"
"ds_read_b64_tr_b16 a[234:235],v29 offset:10240\n"
"v_cvt_pk_bf16_f32 v64,v64,v65\n"
"v_mfma_f32_16x16x32_bf16 v[96:99],a[200:203],v[144:147],v[96:99]\n"
"ds_read_b64_tr_b16 a[236:237],v29 offset:12288\n"
"buffer_load_dwordx4 v39,s[40:43],s77 offen offset:3072 lds\n"
"v_mfma_f32_16x16x32_bf16 v[96:99],a[204:207],v[152:155],v[96:99]\n"
"s_waitcnt lgkmcnt(7)\n"
"ds_read_b64_tr_b16 a[238:239],v29 offset:14336\n"
"v_cvt_pk_bf16_f32 v65,v66,v67\n"
"v_mfma_f32_16x16x32_bf16 v[96:99],a[208:211],v[160:163],v[96:99]\n"
"ds_read_b64_tr_b16 a[240:241],v29 offset:16384\n"
"v_cvt_pk_bf16_f32 v66,v68,v69\n"
"v_mfma_f32_16x16x32_bf16 v[96:99],a[212:215],v[168:171],v[96:99]\n"
"ds_read_b64_tr_b16 a[242:243],v29 offset:18432\n"
"v_cvt_pk_bf16_f32 v67,v70,v71\n"
"v_mfma_f32_16x16x32_bf16 v[96:99],a[216:219],v[176:179],v[96:99]\n"
"ds_read_b64_tr_b16 a[244:245],v29 offset:20480\n"
"v_cvt_pk_bf16_f32 v72,v72,v73\n"
"v_mfma_f32_16x16x32_bf16 v[96:99],a[220:223],v[184:187],v[96:99]\n"
"ds_read_b64_tr_b16 a[246:247],v29 offset:22528\n"
"v_cvt_pk_bf16_f32 v73,v74,v75\n"
"v_mfma_f32_16x16x32_bf16 v[112:115],a[192:195],v[192:195],0\n"
"ds_read_b64_tr_b16 a[248:249],v29 offset:24576\n"
"v_cvt_pk_bf16_f32 v74,v76,v77\n"
"v_mfma_f32_16x16x32_bf16 v[112:115],a[196:199],v[200:203],v[112:115]\n"
"ds_read_b64_tr_b16 a[250:251],v29 offset:26624\n"
"v_cvt_pk_bf16_f32 v75,v78,v79\n"
"v_mfma_f32_16x16x32_bf16 v[112:115],a[200:203],v[208:211],v[112:115]\n"
"ds_read_b64_tr_b16 a[252:253],v29 offset:28672\n"
"v_mov_b32 v68,v64\n"
"v_mfma_f32_16x16x32_bf16 v[112:115],a[204:207],v[216:219],v[112:115]\n"
"ds_read_b64_tr_b16 a[254:255],v29 offset:30720\n"
"v_mov_b32 v69,v65\n"
"v_mfma_f32_16x16x32_bf16 v[112:115],a[208:211],v[224:227],v[112:115]\n"
"ds_read_b64_tr_b16 a[192:193],v30 offset:0\n"
"v_mov_b32 v70,v66\n"
"v_mfma_f32_16x16x32_bf16 v[112:115],a[212:215],v[232:235],v[112:115]\n"
"ds_read_b64_tr_b16 a[194:195],v30 offset:2048\n"
"v_mov_b32 v71,v67\n"
"v_mfma_f32_16x16x32_bf16 v[112:115],a[216:219],v[240:243],v[112:115]\n"
"ds_read_b64_tr_b16 a[196:197],v30 offset:4096\n"
"v_mov_b32_dpp v64,v72 row_ror:8 row_mask:0xf bank_mask:0xc\n"
"v_mfma_f32_16x16x32_bf16 v[112:115],a[220:223],v[248:251],v[112:115]\n"
"s_waitcnt lgkmcnt(11)\n"
"ds_read_b64_tr_b16 a[198:199],v30 offset:6144\n"
"v_mov_b32_dpp v65,v73 row_ror:8 row_mask:0xf bank_mask:0xc\n"
"v_mfma_f32_16x16x32_bf16 v[100:103],a[224:227],v[128:131],0\n"
"ds_read_b64_tr_b16 a[200:201],v30 offset:8192\n"
"v_mov_b32_dpp v66,v74 row_ror:8 row_mask:0xf bank_mask:0xc\n"
"v_mfma_f32_16x16x32_bf16 v[100:103],a[228:231],v[136:139],v[100:103]\n"
"ds_read_b64_tr_b16 a[202:203],v30 offset:10240\n"
"v_mov_b32_dpp v67,v75 row_ror:8 row_mask:0xf bank_mask:0xc\n"
"v_mfma_f32_16x16x32_bf16 v[100:103],a[232:235],v[144:147],v[100:103]\n"
"ds_read_b64_tr_b16 a[204:205],v30 offset:12288\n"
"v_mov_b32_dpp v72,v68 row_ror:8 row_mask:0xf bank_mask:0x3\n"
"v_mfma_f32_16x16x32_bf16 v[100:103],a[236:239],v[152:155],v[100:103]\n"
"s_waitcnt lgkmcnt(7)\n"
"ds_read_b64_tr_b16 a[206:207],v30 offset:14336\n"
"v_mov_b32_dpp v73,v69 row_ror:8 row_mask:0xf bank_mask:0x3\n"
"v_mfma_f32_16x16x32_bf16 v[100:103],a[240:243],v[160:163],v[100:103]\n"
"ds_read_b64_tr_b16 a[208:209],v30 offset:16384\n"
"v_mov_b32_dpp v74,v70 row_ror:8 row_mask:0xf bank_mask:0x3\n"
"v_mfma_f32_16x16x32_bf16 v[100:103],a[244:247],v[168:171],v[100:103]\n"
"ds_read_b64_tr_b16 a[210:211],v30 offset:18432\n"
"buffer_store_dwordx4 v[64:67],v6,s[44:47],0 offen offset:0 sc1\n"
"v_mfma_f32_16x16x32_bf16 v[100:103],a[248:251],v[176:179],v[100:103]\n"
"ds_read_b64_tr_b16 a[212:213],v30 offset:20480\n"
"v_mov_b32_dpp v75,v71 row_ror:8 row_mask:0xf bank_mask:0x3\n"
"v_mfma_f32_16x16x32_bf16 v[100:103],a[252:255],v[184:187],v[100:103]\n"
"ds_read_b64_tr_b16 a[214:215],v30 offset:22528\n"
"v_mfma_f32_16x16x32_bf16 v[116:119],a[224:227],v[192:195],0\n"
"ds_read_b64_tr_b16 a[216:217],v30 offset:24576\n"
"v_mfma_f32_16x16x32_bf16 v[116:119],a[228:231],v[200:203],v[116:119]\n"
"ds_read_b64_tr_b16 a[218:219],v30 offset:26624\n"
"v_mfma_f32_16x16x32_bf16 v[116:119],a[232:235],v[208:211],v[116:119]\n"
"ds_read_b64_tr_b16 a[220:221],v30 offset:28672\n"
"v_mfma_f32_16x16x32_bf16 v[116:119],a[236:239],v[216:219],v[116:119]\n"
"ds_read_b64_tr_b16 a[222:223],v30 offset:30720\n"
"v_mfma_f32_16x16x32_bf16 v[116:119],a[240:243],v[224:227],v[116:119]\n"
"ds_read_b64_tr_b16 a[224:225],v31 offset:0\n"
"v_mfma_f32_16x16x32_bf16 v[116:119],a[244:247],v[232:235],v[116:119]\n"
"ds_read_b64_tr_b16 a[226:227],v31 offset:2048\n"
"buffer_store_dwordx4 v[72:75],v6,s[44:47],s87 offen offset:0 sc1\n"
"v_mfma_f32_16x16x32_bf16 v[116:119],a[248:251],v[240:243],v[116:119]\n"
"ds_read_b64_tr_b16 a[228:229],v31 offset:4096\n"
"v_cvt_pk_bf16_f32 v80,v80,v81\n"
"v_mfma_f32_16x16x32_bf16 v[116:119],a[252:255],v[248:251],v[116:119]\n"
"s_waitcnt lgkmcnt(11)\n"
"ds_read_b64_tr_b16 a[230:231],v31 offset:6144\n"
"v_cvt_pk_bf16_f32 v81,v82,v83\n"
"v_mfma_f32_16x16x32_bf16 v[104:107],a[192:195],v[128:131],0\n"
"ds_read_b64_tr_b16 a[232:233],v31 offset:8192\n"
"v_cvt_pk_bf16_f32 v82,v84,v85\n"
"v_mfma_f32_16x16x32_bf16 v[104:107],a[196:199],v[136:139],v[104:107]\n"
"ds_read_b64_tr_b16 a[234:235],v31 offset:10240\n"
"v_cvt_pk_bf16_f32 v83,v86,v87\n"
"v_mfma_f32_16x16x32_bf16 v[104:107],a[200:203],v[144:147],v[104:107]\n"
"ds_read_b64_tr_b16 a[236:237],v31 offset:12288\n"
"v_cvt_pk_bf16_f32 v88,v88,v89\n"
"v_mfma_f32_16x16x32_bf16 v[104:107],a[204:207],v[152:155],v[104:107]\n"
"s_waitcnt lgkmcnt(7)\n"
"ds_read_b64_tr_b16 a[238:239],v31 offset:14336\n"
"v_cvt_pk_bf16_f32 v89,v90,v91\n"
"v_mfma_f32_16x16x32_bf16 v[104:107],a[208:211],v[160:163],v[104:107]\n"
"ds_read_b64_tr_b16 a[240:241],v31 offset:16384\n"
"v_cvt_pk_bf16_f32 v90,v92,v93\n"
"v_mfma_f32_16x16x32_bf16 v[104:107],a[212:215],v[168:171],v[104:107]\n"
"ds_read_b64_tr_b16 a[242:243],v31 offset:18432\n"
"v_cvt_pk_bf16_f32 v91,v94,v95\n"
"v_mfma_f32_16x16x32_bf16 v[104:107],a[216:219],v[176:179],v[104:107]\n"
"ds_read_b64_tr_b16 a[244:245],v31 offset:20480\n"
"v_mov_b32 v84,v80\n"
"v_mfma_f32_16x16x32_bf16 v[104:107],a[220:223],v[184:187],v[104:107]\n"
"ds_read_b64_tr_b16 a[246:247],v31 offset:22528\n"
"v_mov_b32 v85,v81\n"
"v_mfma_f32_16x16x32_bf16 v[120:123],a[192:195],v[192:195],0\n"
"ds_read_b64_tr_b16 a[248:249],v31 offset:24576\n"
"v_mov_b32 v86,v82\n"
"v_mfma_f32_16x16x32_bf16 v[120:123],a[196:199],v[200:203],v[120:123]\n"
"ds_read_b64_tr_b16 a[250:251],v31 offset:26624\n"
"v_mov_b32 v87,v83\n"
"v_mfma_f32_16x16x32_bf16 v[120:123],a[200:203],v[208:211],v[120:123]\n"
"ds_read_b64_tr_b16 a[252:253],v31 offset:28672\n"
"v_mov_b32_dpp v80,v88 row_ror:8 row_mask:0xf bank_mask:0xc\n"
"v_mfma_f32_16x16x32_bf16 v[120:123],a[204:207],v[216:219],v[120:123]\n"
"s_waitcnt vmcnt(18)\n"
"s_barrier\n"
"ds_read_b64_tr_b16 a[254:255],v31 offset:30720\n"
"v_mov_b32_dpp v81,v89 row_ror:8 row_mask:0xf bank_mask:0xc\n"
"v_mfma_f32_16x16x32_bf16 v[120:123],a[208:211],v[224:227],v[120:123]\n"
"ds_read_b64_tr_b16 a[192:193],v28 offset:32768\n"
"v_mov_b32_dpp v82,v90 row_ror:8 row_mask:0xf bank_mask:0xc\n"
"v_mfma_f32_16x16x32_bf16 v[120:123],a[212:215],v[232:235],v[120:123]\n"
"s_mov_b32 s77,640\n"
"ds_read_b64_tr_b16 a[194:195],v28 offset:34816\n"
"v_mov_b32_dpp v83,v91 row_ror:8 row_mask:0xf bank_mask:0xc\n"
"v_mfma_f32_16x16x32_bf16 v[120:123],a[216:219],v[240:243],v[120:123]\n"
"s_add_u32 m0,s57,0x20000\n"
"ds_read_b64_tr_b16 a[196:197],v28 offset:36864\n"
"v_mov_b32_dpp v88,v84 row_ror:8 row_mask:0xf bank_mask:0x3\n"
"v_mfma_f32_16x16x32_bf16 v[120:123],a[220:223],v[248:251],v[120:123]\n"
"s_waitcnt lgkmcnt(11)\n"
"ds_read_b64_tr_b16 a[198:199],v28 offset:38912\n"
"buffer_load_dwordx4 v32,s[40:43],s77 offen offset:0 lds\n"
"v_mfma_f32_16x16x32_bf16 v[108:111],a[224:227],v[128:131],0\n"
"ds_read_b64_tr_b16 a[200:201],v28 offset:40960\n"
"v_mov_b32_dpp v89,v85 row_ror:8 row_mask:0xf bank_mask:0x3\n"
"v_mfma_f32_16x16x32_bf16 v[108:111],a[228:231],v[136:139],v[108:111]\n"
"ds_read_b64_tr_b16 a[202:203],v28 offset:43008\n"
"v_mov_b32_dpp v90,v86 row_ror:8 row_mask:0xf bank_mask:0x3\n"
"v_mfma_f32_16x16x32_bf16 v[108:111],a[232:235],v[144:147],v[108:111]\n"
"ds_read_b64_tr_b16 a[204:205],v28 offset:45056\n"
"v_mov_b32_dpp v91,v87 row_ror:8 row_mask:0xf bank_mask:0x3\n"
"v_mfma_f32_16x16x32_bf16 v[108:111],a[236:239],v[152:155],v[108:111]\n"
"s_waitcnt lgkmcnt(7)\n"
"ds_read_b64_tr_b16 a[206:207],v28 offset:47104\n"
"v_mfma_f32_16x16x32_bf16 v[108:111],a[240:243],v[160:163],v[108:111]\n"
"ds_read_b64_tr_b16 a[208:209],v28 offset:49152\n"
"v_mfma_f32_16x16x32_bf16 v[108:111],a[244:247],v[168:171],v[108:111]\n"
"ds_read_b64_tr_b16 a[210:211],v28 offset:51200\n"
"buffer_store_dwordx4 v[80:83],v6,s[44:47],s86 offen offset:0 sc1\n"
"v_mfma_f32_16x16x32_bf16 v[108:111],a[248:251],v[176:179],v[108:111]\n"
"ds_read_b64_tr_b16 a[212:213],v28 offset:53248\n"
"v_mfma_f32_16x16x32_bf16 v[108:111],a[252:255],v[184:187],v[108:111]\n"
"ds_read_b64_tr_b16 a[214:215],v28 offset:55296\n"
"v_mfma_f32_16x16x32_bf16 v[124:127],a[224:227],v[192:195],0\n"
"ds_read_b64_tr_b16 a[216:217],v28 offset:57344\n"
"v_mfma_f32_16x16x32_bf16 v[124:127],a[228:231],v[200:203],v[124:127]\n"
"ds_read_b64_tr_b16 a[218:219],v28 offset:59392\n"
"buffer_load_dwordx4 v33,s[40:43],s77 offen offset:1024 lds\n"
"v_mfma_f32_16x16x32_bf16 v[124:127],a[232:235],v[208:211],v[124:127]\n"
"ds_read_b64_tr_b16 a[220:221],v28 offset:61440\n"
"v_mfma_f32_16x16x32_bf16 v[124:127],a[236:239],v[216:219],v[124:127]\n"
"ds_read_b64_tr_b16 a[222:223],v28 offset:63488\n"
"v_mfma_f32_16x16x32_bf16 v[124:127],a[240:243],v[224:227],v[124:127]\n"
"ds_read_b64_tr_b16 a[224:225],v29 offset:32768\n"
"v_mfma_f32_16x16x32_bf16 v[124:127],a[244:247],v[232:235],v[124:127]\n"
"ds_read_b64_tr_b16 a[226:227],v29 offset:34816\n"
"buffer_store_dwordx4 v[88:91],v6,s[44:47],s88 offen offset:0 sc1\n"
"v_mfma_f32_16x16x32_bf16 v[124:127],a[248:251],v[240:243],v[124:127]\n"
"ds_read_b64_tr_b16 a[228:229],v29 offset:36864\n"
"v_mfma_f32_16x16x32_bf16 v[124:127],a[252:255],v[248:251],v[124:127]\n"
"s_waitcnt lgkmcnt(11)\n"
"ds_read_b64_tr_b16 a[230:231],v29 offset:38912\n"
"v_mfma_f32_16x16x32_bf16 v[64:67],a[192:195],v[128:131],0\n"
"ds_read_b64_tr_b16 a[232:233],v29 offset:40960\n"
"v_mfma_f32_16x16x32_bf16 v[64:67],a[196:199],v[136:139],v[64:67]\n"
"ds_read_b64_tr_b16 a[234:235],v29 offset:43008\n"
"buffer_load_dwordx4 v34,s[40:43],s77 offen offset:2048 lds\n"
"v_mfma_f32_16x16x32_bf16 v[64:67],a[200:203],v[144:147],v[64:67]\n"
"ds_read_b64_tr_b16 a[236:237],v29 offset:45056\n"
"v_cvt_pk_bf16_f32 v96,v96,v97\n"
"v_cvt_pk_bf16_f32 v97,v98,v99\n"
"v_mfma_f32_16x16x32_bf16 v[64:67],a[204:207],v[152:155],v[64:67]\n"
"s_waitcnt lgkmcnt(7)\n"
"ds_read_b64_tr_b16 a[238:239],v29 offset:47104\n"
"v_cvt_pk_bf16_f32 v98,v100,v101\n"
"v_mfma_f32_16x16x32_bf16 v[64:67],a[208:211],v[160:163],v[64:67]\n"
"ds_read_b64_tr_b16 a[240:241],v29 offset:49152\n"
"v_cvt_pk_bf16_f32 v99,v102,v103\n"
"v_mfma_f32_16x16x32_bf16 v[64:67],a[212:215],v[168:171],v[64:67]\n"
"ds_read_b64_tr_b16 a[242:243],v29 offset:51200\n"
"buffer_load_dwordx4 v35,s[40:43],s77 offen offset:3072 lds\n"
"v_mfma_f32_16x16x32_bf16 v[64:67],a[216:219],v[176:179],v[64:67]\n"
"ds_read_b64_tr_b16 a[244:245],v29 offset:53248\n"
"v_cvt_pk_bf16_f32 v104,v104,v105\n"
"v_mfma_f32_16x16x32_bf16 v[64:67],a[220:223],v[184:187],v[64:67]\n"
"ds_read_b64_tr_b16 a[246:247],v29 offset:55296\n"
"v_cvt_pk_bf16_f32 v105,v106,v107\n"
"v_mfma_f32_16x16x32_bf16 v[80:83],a[192:195],v[192:195],0\n"
"s_add_u32 m0,m0,0x1000\n"
"ds_read_b64_tr_b16 a[248:249],v29 offset:57344\n"
"v_cvt_pk_bf16_f32 v106,v108,v109\n"
"v_mfma_f32_16x16x32_bf16 v[80:83],a[196:199],v[200:203],v[80:83]\n"
"ds_read_b64_tr_b16 a[250:251],v29 offset:59392\n"
"buffer_load_dwordx4 v36,s[40:43],s77 offen offset:0 lds\n"
"v_mfma_f32_16x16x32_bf16 v[80:83],a[200:203],v[208:211],v[80:83]\n"
"ds_read_b64_tr_b16 a[252:253],v29 offset:61440\n"
"v_cvt_pk_bf16_f32 v107,v110,v111\n"
"v_mfma_f32_16x16x32_bf16 v[80:83],a[204:207],v[216:219],v[80:83]\n"
"ds_read_b64_tr_b16 a[254:255],v29 offset:63488\n"
"v_mov_b32 v100,v96\n"
"v_mfma_f32_16x16x32_bf16 v[80:83],a[208:211],v[224:227],v[80:83]\n"
"ds_read_b64_tr_b16 a[192:193],v30 offset:32768\n"
"v_mov_b32 v101,v97\n"
"v_mfma_f32_16x16x32_bf16 v[80:83],a[212:215],v[232:235],v[80:83]\n"
"ds_read_b64_tr_b16 a[194:195],v30 offset:34816\n"
"buffer_load_dwordx4 v37,s[40:43],s77 offen offset:1024 lds\n"
"v_mfma_f32_16x16x32_bf16 v[80:83],a[216:219],v[240:243],v[80:83]\n"
"ds_read_b64_tr_b16 a[196:197],v30 offset:36864\n"
"v_mov_b32 v102,v98\n"
"v_mfma_f32_16x16x32_bf16 v[80:83],a[220:223],v[248:251],v[80:83]\n"
"s_waitcnt lgkmcnt(11)\n"
"ds_read_b64_tr_b16 a[198:199],v30 offset:38912\n"
"v_mov_b32 v103,v99\n"
"v_mfma_f32_16x16x32_bf16 v[68:71],a[224:227],v[128:131],0\n"
"ds_read_b64_tr_b16 a[200:201],v30 offset:40960\n"
"v_mov_b32_dpp v96,v104 row_ror:8 row_mask:0xf bank_mask:0xc\n"
"v_mfma_f32_16x16x32_bf16 v[68:71],a[228:231],v[136:139],v[68:71]\n"
"ds_read_b64_tr_b16 a[202:203],v30 offset:43008\n"
"buffer_load_dwordx4 v38,s[40:43],s77 offen offset:2048 lds\n"
"v_mfma_f32_16x16x32_bf16 v[68:71],a[232:235],v[144:147],v[68:71]\n"
"ds_read_b64_tr_b16 a[204:205],v30 offset:45056\n"
"v_mov_b32_dpp v97,v105 row_ror:8 row_mask:0xf bank_mask:0xc\n"
"v_mfma_f32_16x16x32_bf16 v[68:71],a[236:239],v[152:155],v[68:71]\n"
"s_waitcnt lgkmcnt(7)\n"
"ds_read_b64_tr_b16 a[206:207],v30 offset:47104\n"
"v_mov_b32_dpp v98,v106 row_ror:8 row_mask:0xf bank_mask:0xc\n"
"v_mfma_f32_16x16x32_bf16 v[68:71],a[240:243],v[160:163],v[68:71]\n"
"ds_read_b64_tr_b16 a[208:209],v30 offset:49152\n"
"v_mov_b32_dpp v99,v107 row_ror:8 row_mask:0xf bank_mask:0xc\n"
"v_mfma_f32_16x16x32_bf16 v[68:71],a[244:247],v[168:171],v[68:71]\n"
"ds_read_b64_tr_b16 a[210:211],v30 offset:51200\n"
"buffer_store_dwordx4 v[96:99],v6,s[44:47],0 offen offset:128 sc1\n"
"v_mfma_f32_16x16x32_bf16 v[68:71],a[248:251],v[176:179],v[68:71]\n"
"ds_read_b64_tr_b16 a[212:213],v30 offset:53248\n"
"v_mov_b32_dpp v104,v100 row_ror:8 row_mask:0xf bank_mask:0x3\n"
"v_mfma_f32_16x16x32_bf16 v[68:71],a[252:255],v[184:187],v[68:71]\n"
"ds_read_b64_tr_b16 a[214:215],v30 offset:55296\n"
"v_mov_b32_dpp v105,v101 row_ror:8 row_mask:0xf bank_mask:0x3\n"
"v_mfma_f32_16x16x32_bf16 v[84:87],a[224:227],v[192:195],0\n"
"ds_read_b64_tr_b16 a[216:217],v30 offset:57344\n"
"v_mov_b32_dpp v106,v102 row_ror:8 row_mask:0xf bank_mask:0x3\n"
"v_mfma_f32_16x16x32_bf16 v[84:87],a[228:231],v[200:203],v[84:87]\n"
"ds_read_b64_tr_b16 a[218:219],v30 offset:59392\n"
"buffer_load_dwordx4 v39,s[40:43],s77 offen offset:3072 lds\n"
"v_mfma_f32_16x16x32_bf16 v[84:87],a[232:235],v[208:211],v[84:87]\n"
"ds_read_b64_tr_b16 a[220:221],v30 offset:61440\n"
"v_mov_b32_dpp v107,v103 row_ror:8 row_mask:0xf bank_mask:0x3\n"
"v_mfma_f32_16x16x32_bf16 v[84:87],a[236:239],v[216:219],v[84:87]\n"
"ds_read_b64_tr_b16 a[222:223],v30 offset:63488\n"
"v_mfma_f32_16x16x32_bf16 v[84:87],a[240:243],v[224:227],v[84:87]\n"
"ds_read_b64_tr_b16 a[224:225],v31 offset:32768\n"
"v_mfma_f32_16x16x32_bf16 v[84:87],a[244:247],v[232:235],v[84:87]\n"
"ds_read_b64_tr_b16 a[226:227],v31 offset:34816\n"
"buffer_store_dwordx4 v[104:107],v6,s[44:47],s87 offen offset:128 sc1\n"
"v_mfma_f32_16x16x32_bf16 v[84:87],a[248:251],v[240:243],v[84:87]\n"
"ds_read_b64_tr_b16 a[228:229],v31 offset:36864\n"
"v_add_u32 v28,0xffff0000,v28\n"
"v_mfma_f32_16x16x32_bf16 v[84:87],a[252:255],v[248:251],v[84:87]\n"
"s_waitcnt lgkmcnt(11)\n"
"ds_read_b64_tr_b16 a[230:231],v31 offset:38912\n"
"v_cvt_pk_bf16_f32 v112,v112,v113\n"
"v_mfma_f32_16x16x32_bf16 v[72:75],a[192:195],v[128:131],0\n"
"ds_read_b64_tr_b16 a[232:233],v31 offset:40960\n"
"v_cvt_pk_bf16_f32 v113,v114,v115\n"
"v_mfma_f32_16x16x32_bf16 v[72:75],a[196:199],v[136:139],v[72:75]\n"
"ds_read_b64_tr_b16 a[234:235],v31 offset:43008\n"
"v_cvt_pk_bf16_f32 v114,v116,v117\n"
"v_mfma_f32_16x16x32_bf16 v[72:75],a[200:203],v[144:147],v[72:75]\n"
"ds_read_b64_tr_b16 a[236:237],v31 offset:45056\n"
"v_cvt_pk_bf16_f32 v115,v118,v119\n"
"v_mfma_f32_16x16x32_bf16 v[72:75],a[204:207],v[152:155],v[72:75]\n"
"s_waitcnt lgkmcnt(7)\n"
"ds_read_b64_tr_b16 a[238:239],v31 offset:47104\n"
"v_cvt_pk_bf16_f32 v120,v120,v121\n"
"v_mfma_f32_16x16x32_bf16 v[72:75],a[208:211],v[160:163],v[72:75]\n"
"ds_read_b64_tr_b16 a[240:241],v31 offset:49152\n"
"v_cvt_pk_bf16_f32 v121,v122,v123\n"
"v_mfma_f32_16x16x32_bf16 v[72:75],a[212:215],v[168:171],v[72:75]\n"
"ds_read_b64_tr_b16 a[242:243],v31 offset:51200\n"
"v_cvt_pk_bf16_f32 v122,v124,v125\n"
"v_mfma_f32_16x16x32_bf16 v[72:75],a[216:219],v[176:179],v[72:75]\n"
"ds_read_b64_tr_b16 a[244:245],v31 offset:53248\n"
"v_cvt_pk_bf16_f32 v123,v126,v127\n"
"v_mfma_f32_16x16x32_bf16 v[72:75],a[220:223],v[184:187],v[72:75]\n"
"ds_read_b64_tr_b16 a[246:247],v31 offset:55296\n"
"v_mov_b32 v116,v112\n"
"v_mfma_f32_16x16x32_bf16 v[88:91],a[192:195],v[192:195],0\n"
"ds_read_b64_tr_b16 a[248:249],v31 offset:57344\n"
"v_mov_b32 v117,v113\n"
"v_mfma_f32_16x16x32_bf16 v[88:91],a[196:199],v[200:203],v[88:91]\n"
"ds_read_b64_tr_b16 a[250:251],v31 offset:59392\n"
"v_mov_b32 v118,v114\n"
"v_mfma_f32_16x16x32_bf16 v[88:91],a[200:203],v[208:211],v[88:91]\n"
"ds_read_b64_tr_b16 a[252:253],v31 offset:61440\n"
"v_mov_b32 v119,v115\n"
"v_mfma_f32_16x16x32_bf16 v[88:91],a[204:207],v[216:219],v[88:91]\n"
"s_waitcnt vmcnt(22)\n"
"s_barrier\n"
"ds_read_b64_tr_b16 a[254:255],v31 offset:63488\n"
"v_add_u32 v29,0xffff0000,v29\n"
"v_mfma_f32_16x16x32_bf16 v[88:91],a[208:211],v[224:227],v[88:91]\n"
"ds_read_b64_tr_b16 a[192:193],v28 offset:0\n"
"v_add_u32 v30,0xffff0000,v30\n"
"v_mfma_f32_16x16x32_bf16 v[88:91],a[212:215],v[232:235],v[88:91]\n"
"s_mov_b32 s77,768\n"
"ds_read_b64_tr_b16 a[194:195],v28 offset:2048\n"
"v_add_u32 v31,0xffff0000,v31\n"
"v_mfma_f32_16x16x32_bf16 v[88:91],a[216:219],v[240:243],v[88:91]\n"
"s_add_u32 m0,s57,0x10000\n"
"ds_read_b64_tr_b16 a[196:197],v28 offset:4096\n"
"v_mov_b32_dpp v112,v120 row_ror:8 row_mask:0xf bank_mask:0xc\n"
"v_mfma_f32_16x16x32_bf16 v[88:91],a[220:223],v[248:251],v[88:91]\n"
"s_waitcnt lgkmcnt(11)\n"
"ds_read_b64_tr_b16 a[198:199],v28 offset:6144\n"
"buffer_load_dwordx4 v32,s[40:43],s77 offen offset:0 lds\n"
"v_mfma_f32_16x16x32_bf16 v[76:79],a[224:227],v[128:131],0\n"
"ds_read_b64_tr_b16 a[200:201],v28 offset:8192\n"
"v_mov_b32_dpp v113,v121 row_ror:8 row_mask:0xf bank_mask:0xc\n"
"v_mfma_f32_16x16x32_bf16 v[76:79],a[228:231],v[136:139],v[76:79]\n"
"ds_read_b64_tr_b16 a[202:203],v28 offset:10240\n"
"v_mov_b32_dpp v114,v122 row_ror:8 row_mask:0xf bank_mask:0xc\n"
"v_mfma_f32_16x16x32_bf16 v[76:79],a[232:235],v[144:147],v[76:79]\n"
"ds_read_b64_tr_b16 a[204:205],v28 offset:12288\n"
"v_mov_b32_dpp v115,v123 row_ror:8 row_mask:0xf bank_mask:0xc\n"
"v_mfma_f32_16x16x32_bf16 v[76:79],a[236:239],v[152:155],v[76:79]\n"
"s_waitcnt lgkmcnt(7)\n"
"ds_read_b64_tr_b16 a[206:207],v28 offset:14336\n"
"v_mov_b32_dpp v120,v116 row_ror:8 row_mask:0xf bank_mask:0x3\n"
"v_mfma_f32_16x16x32_bf16 v[76:79],a[240:243],v[160:163],v[76:79]\n"
"ds_read_b64_tr_b16 a[208:209],v28 offset:16384\n"
"v_mov_b32_dpp v121,v117 row_ror:8 row_mask:0xf bank_mask:0x3\n"
"v_mfma_f32_16x16x32_bf16 v[76:79],a[244:247],v[168:171],v[76:79]\n"
"ds_read_b64_tr_b16 a[210:211],v28 offset:18432\n"
"buffer_store_dwordx4 v[112:115],v6,s[44:47],s86 offen offset:128 sc1\n"
"v_mfma_f32_16x16x32_bf16 v[76:79],a[248:251],v[176:179],v[76:79]\n"
"ds_read_b64_tr_b16 a[212:213],v28 offset:20480\n"
"v_mov_b32_dpp v122,v118 row_ror:8 row_mask:0xf bank_mask:0x3\n"
"v_mfma_f32_16x16x32_bf16 v[76:79],a[252:255],v[184:187],v[76:79]\n"
"ds_read_b64_tr_b16 a[214:215],v28 offset:22528\n"
"v_mov_b32_dpp v123,v119 row_ror:8 row_mask:0xf bank_mask:0x3\n"
"v_mfma_f32_16x16x32_bf16 v[92:95],a[224:227],v[192:195],0\n"
"ds_read_b64_tr_b16 a[216:217],v28 offset:24576\n"
"v_mfma_f32_16x16x32_bf16 v[92:95],a[228:231],v[200:203],v[92:95]\n"
"ds_read_b64_tr_b16 a[218:219],v28 offset:26624\n"
"buffer_load_dwordx4 v33,s[40:43],s77 offen offset:1024 lds\n"
"v_mfma_f32_16x16x32_bf16 v[92:95],a[232:235],v[208:211],v[92:95]\n"
"ds_read_b64_tr_b16 a[220:221],v28 offset:28672\n"
"v_mfma_f32_16x16x32_bf16 v[92:95],a[236:239],v[216:219],v[92:95]\n"
"ds_read_b64_tr_b16 a[222:223],v28 offset:30720\n"
"v_mfma_f32_16x16x32_bf16 v[92:95],a[240:243],v[224:227],v[92:95]\n"
"ds_read_b64_tr_b16 a[224:225],v29 offset:0\n"
"v_mfma_f32_16x16x32_bf16 v[92:95],a[244:247],v[232:235],v[92:95]\n"
"ds_read_b64_tr_b16 a[226:227],v29 offset:2048\n"
"buffer_store_dwordx4 v[120:123],v6,s[44:47],s88 offen offset:128 sc1\n"
"v_mfma_f32_16x16x32_bf16 v[92:95],a[248:251],v[240:243],v[92:95]\n"
"ds_read_b64_tr_b16 a[228:229],v29 offset:4096\n"
"v_mfma_f32_16x16x32_bf16 v[92:95],a[252:255],v[248:251],v[92:95]\n"
"s_waitcnt lgkmcnt(11)\n"
"ds_read_b64_tr_b16 a[230:231],v29 offset:6144\n"
"v_mfma_f32_16x16x32_bf16 v[96:99],a[192:195],v[128:131],0\n"
"ds_read_b64_tr_b16 a[232:233],v29 offset:8192\n"
"v_mfma_f32_16x16x32_bf16 v[96:99],a[196:199],v[136:139],v[96:99]\n"
"ds_read_b64_tr_b16 a[234:235],v29 offset:10240\n"
"buffer_load_dwordx4 v34,s[40:43],s77 offen offset:2048 lds\n"
"v_mfma_f32_16x16x32_bf16 v[96:99],a[200:203],v[144:147],v[96:99]\n"
"ds_read_b64_tr_b16 a[236:237],v29 offset:12288\n"
"v_cvt_pk_bf16_f32 v64,v64,v65\n"
"v_cvt_pk_bf16_f32 v65,v66,v67\n"
"v_mfma_f32_16x16x32_bf16 v[96:99],a[204:207],v[152:155],v[96:99]\n"
"s_waitcnt lgkmcnt(7)\n"
"ds_read_b64_tr_b16 a[238:239],v29 offset:14336\n"
"v_cvt_pk_bf16_f32 v66,v68,v69\n"
"v_mfma_f32_16x16x32_bf16 v[96:99],a[208:211],v[160:163],v[96:99]\n"
"ds_read_b64_tr_b16 a[240:241],v29 offset:16384\n"
"v_cvt_pk_bf16_f32 v67,v70,v71\n"
"v_mfma_f32_16x16x32_bf16 v[96:99],a[212:215],v[168:171],v[96:99]\n"
"ds_read_b64_tr_b16 a[242:243],v29 offset:18432\n"
"buffer_load_dwordx4 v35,s[40:43],s77 offen offset:3072 lds\n"
"v_mfma_f32_16x16x32_bf16 v[96:99],a[216:219],v[176:179],v[96:99]\n"
"ds_read_b64_tr_b16 a[244:245],v29 offset:20480\n"
"v_cvt_pk_bf16_f32 v72,v72,v73\n"
"v_mfma_f32_16x16x32_bf16 v[96:99],a[220:223],v[184:187],v[96:99]\n"
"ds_read_b64_tr_b16 a[246:247],v29 offset:22528\n"
"v_cvt_pk_bf16_f32 v73,v74,v75\n"
"v_mfma_f32_16x16x32_bf16 v[112:115],a[192:195],v[192:195],0\n"
"s_add_u32 m0,m0,0x1000\n"
"ds_read_b64_tr_b16 a[248:249],v29 offset:24576\n"
"v_cvt_pk_bf16_f32 v74,v76,v77\n"
"v_mfma_f32_16x16x32_bf16 v[112:115],a[196:199],v[200:203],v[112:115]\n"
"ds_read_b64_tr_b16 a[250:251],v29 offset:26624\n"
"buffer_load_dwordx4 v36,s[40:43],s77 offen offset:0 lds\n"
"v_mfma_f32_16x16x32_bf16 v[112:115],a[200:203],v[208:211],v[112:115]\n"
"ds_read_b64_tr_b16 a[252:253],v29 offset:28672\n"
"v_cvt_pk_bf16_f32 v75,v78,v79\n"
"v_mfma_f32_16x16x32_bf16 v[112:115],a[204:207],v[216:219],v[112:115]\n"
"ds_read_b64_tr_b16 a[254:255],v29 offset:30720\n"
"v_mov_b32 v68,v64\n"
"v_mfma_f32_16x16x32_bf16 v[112:115],a[208:211],v[224:227],v[112:115]\n"
"ds_read_b64_tr_b16 a[192:193],v30 offset:0\n"
"v_mov_b32 v69,v65\n"
"v_mfma_f32_16x16x32_bf16 v[112:115],a[212:215],v[232:235],v[112:115]\n"
"ds_read_b64_tr_b16 a[194:195],v30 offset:2048\n"
"buffer_load_dwordx4 v37,s[40:43],s77 offen offset:1024 lds\n"
"v_mfma_f32_16x16x32_bf16 v[112:115],a[216:219],v[240:243],v[112:115]\n"
"ds_read_b64_tr_b16 a[196:197],v30 offset:4096\n"
"v_mov_b32 v70,v66\n"
"v_mfma_f32_16x16x32_bf16 v[112:115],a[220:223],v[248:251],v[112:115]\n"
"s_waitcnt lgkmcnt(11)\n"
"ds_read_b64_tr_b16 a[198:199],v30 offset:6144\n"
"v_mov_b32 v71,v67\n"
"v_mfma_f32_16x16x32_bf16 v[100:103],a[224:227],v[128:131],0\n"
"ds_read_b64_tr_b16 a[200:201],v30 offset:8192\n"
"v_mov_b32_dpp v64,v72 row_ror:8 row_mask:0xf bank_mask:0xc\n"
"v_mfma_f32_16x16x32_bf16 v[100:103],a[228:231],v[136:139],v[100:103]\n"
"ds_read_b64_tr_b16 a[202:203],v30 offset:10240\n"
"buffer_load_dwordx4 v38,s[40:43],s77 offen offset:2048 lds\n"
"v_mfma_f32_16x16x32_bf16 v[100:103],a[232:235],v[144:147],v[100:103]\n"
"ds_read_b64_tr_b16 a[204:205],v30 offset:12288\n"
"v_mov_b32_dpp v65,v73 row_ror:8 row_mask:0xf bank_mask:0xc\n"
"v_mfma_f32_16x16x32_bf16 v[100:103],a[236:239],v[152:155],v[100:103]\n"
"s_waitcnt lgkmcnt(7)\n"
"ds_read_b64_tr_b16 a[206:207],v30 offset:14336\n"
"v_mov_b32_dpp v66,v74 row_ror:8 row_mask:0xf bank_mask:0xc\n"
"v_mfma_f32_16x16x32_bf16 v[100:103],a[240:243],v[160:163],v[100:103]\n"
"ds_read_b64_tr_b16 a[208:209],v30 offset:16384\n"
"v_mov_b32_dpp v67,v75 row_ror:8 row_mask:0xf bank_mask:0xc\n"
"v_mfma_f32_16x16x32_bf16 v[100:103],a[244:247],v[168:171],v[100:103]\n"
"ds_read_b64_tr_b16 a[210:211],v30 offset:18432\n"
"buffer_store_dwordx4 v[64:67],v6,s[44:47],0 offen offset:256 sc1\n"
"v_mfma_f32_16x16x32_bf16 v[100:103],a[248:251],v[176:179],v[100:103]\n"
"ds_read_b64_tr_b16 a[212:213],v30 offset:20480\n"
"v_mov_b32_dpp v72,v68 row_ror:8 row_mask:0xf bank_mask:0x3\n"
"v_mfma_f32_16x16x32_bf16 v[100:103],a[252:255],v[184:187],v[100:103]\n"
"ds_read_b64_tr_b16 a[214:215],v30 offset:22528\n"
"v_mov_b32_dpp v73,v69 row_ror:8 row_mask:0xf bank_mask:0x3\n"
"v_mfma_f32_16x16x32_bf16 v[116:119],a[224:227],v[192:195],0\n"
"ds_read_b64_tr_b16 a[216:217],v30 offset:24576\n"
"v_mov_b32_dpp v74,v70 row_ror:8 row_mask:0xf bank_mask:0x3\n"
"v_mfma_f32_16x16x32_bf16 v[116:119],a[228:231],v[200:203],v[116:119]\n"
"ds_read_b64_tr_b16 a[218:219],v30 offset:26624\n"
"buffer_load_dwordx4 v39,s[40:43],s77 offen offset:3072 lds\n"
"v_mfma_f32_16x16x32_bf16 v[116:119],a[232:235],v[208:211],v[116:119]\n"
"ds_read_b64_tr_b16 a[220:221],v30 offset:28672\n"
"v_mov_b32_dpp v75,v71 row_ror:8 row_mask:0xf bank_mask:0x3\n"
"v_mfma_f32_16x16x32_bf16 v[116:119],a[236:239],v[216:219],v[116:119]\n"
"ds_read_b64_tr_b16 a[222:223],v30 offset:30720\n"
"v_mfma_f32_16x16x32_bf16 v[116:119],a[240:243],v[224:227],v[116:119]\n"
"ds_read_b64_tr_b16 a[224:225],v31 offset:0\n"
"v_mfma_f32_16x16x32_bf16 v[116:119],a[244:247],v[232:235],v[116:119]\n"
"ds_read_b64_tr_b16 a[226:227],v31 offset:2048\n"
"buffer_store_dwordx4 v[72:75],v6,s[44:47],s87 offen offset:256 sc1\n"
"v_mfma_f32_16x16x32_bf16 v[116:119],a[248:251],v[240:243],v[116:119]\n"
"ds_read_b64_tr_b16 a[228:229],v31 offset:4096\n"
"v_cvt_pk_bf16_f32 v80,v80,v81\n"
"v_mfma_f32_16x16x32_bf16 v[116:119],a[252:255],v[248:251],v[116:119]\n"
"s_waitcnt lgkmcnt(11)\n"
"ds_read_b64_tr_b16 a[230:231],v31 offset:6144\n"
"v_cvt_pk_bf16_f32 v81,v82,v83\n"
"v_mfma_f32_16x16x32_bf16 v[104:107],a[192:195],v[128:131],0\n"
"ds_read_b64_tr_b16 a[232:233],v31 offset:8192\n"
"v_cvt_pk_bf16_f32 v82,v84,v85\n"
"v_mfma_f32_16x16x32_bf16 v[104:107],a[196:199],v[136:139],v[104:107]\n"
"ds_read_b64_tr_b16 a[234:235],v31 offset:10240\n"
"v_cvt_pk_bf16_f32 v83,v86,v87\n"
"v_mfma_f32_16x16x32_bf16 v[104:107],a[200:203],v[144:147],v[104:107]\n"
"ds_read_b64_tr_b16 a[236:237],v31 offset:12288\n"
"v_cvt_pk_bf16_f32 v88,v88,v89\n"
"v_mfma_f32_16x16x32_bf16 v[104:107],a[204:207],v[152:155],v[104:107]\n"
"s_waitcnt lgkmcnt(7)\n"
"ds_read_b64_tr_b16 a[238:239],v31 offset:14336\n"
"v_cvt_pk_bf16_f32 v89,v90,v91\n"
"v_mfma_f32_16x16x32_bf16 v[104:107],a[208:211],v[160:163],v[104:107]\n"
"ds_read_b64_tr_b16 a[240:241],v31 offset:16384\n"
"v_cvt_pk_bf16_f32 v90,v92,v93\n"
"v_mfma_f32_16x16x32_bf16 v[104:107],a[212:215],v[168:171],v[104:107]\n"
"ds_read_b64_tr_b16 a[242:243],v31 offset:18432\n"
"v_cvt_pk_bf16_f32 v91,v94,v95\n"
"v_mfma_f32_16x16x32_bf16 v[104:107],a[216:219],v[176:179],v[104:107]\n"
"ds_read_b64_tr_b16 a[244:245],v31 offset:20480\n"
"v_mov_b32 v84,v80\n"
"v_mfma_f32_16x16x32_bf16 v[104:107],a[220:223],v[184:187],v[104:107]\n"
"ds_read_b64_tr_b16 a[246:247],v31 offset:22528\n"
"v_mov_b32 v85,v81\n"
"v_mfma_f32_16x16x32_bf16 v[120:123],a[192:195],v[192:195],0\n"
"ds_read_b64_tr_b16 a[248:249],v31 offset:24576\n"
"v_mov_b32 v86,v82\n"
"v_mfma_f32_16x16x32_bf16 v[120:123],a[196:199],v[200:203],v[120:123]\n"
"ds_read_b64_tr_b16 a[250:251],v31 offset:26624\n"
"v_mov_b32 v87,v83\n"
"v_mfma_f32_16x16x32_bf16 v[120:123],a[200:203],v[208:211],v[120:123]\n"
"ds_read_b64_tr_b16 a[252:253],v31 offset:28672\n"
"v_mov_b32_dpp v80,v88 row_ror:8 row_mask:0xf bank_mask:0xc\n"
"v_mfma_f32_16x16x32_bf16 v[120:123],a[204:207],v[216:219],v[120:123]\n"
"s_waitcnt vmcnt(26)\n"
"s_barrier\n"
"ds_read_b64_tr_b16 a[254:255],v31 offset:30720\n"
"v_mov_b32_dpp v81,v89 row_ror:8 row_mask:0xf bank_mask:0xc\n"
"v_mfma_f32_16x16x32_bf16 v[120:123],a[208:211],v[224:227],v[120:123]\n"
"ds_read_b64_tr_b16 a[192:193],v28 offset:32768\n"
"v_mov_b32_dpp v82,v90 row_ror:8 row_mask:0xf bank_mask:0xc\n"
"v_mfma_f32_16x16x32_bf16 v[120:123],a[212:215],v[232:235],v[120:123]\n"
"ds_read_b64_tr_b16 a[194:195],v28 offset:34816\n"
"v_mov_b32_dpp v83,v91 row_ror:8 row_mask:0xf bank_mask:0xc\n"
"v_mfma_f32_16x16x32_bf16 v[120:123],a[216:219],v[240:243],v[120:123]\n"
"ds_read_b64_tr_b16 a[196:197],v28 offset:36864\n"
"v_mov_b32_dpp v88,v84 row_ror:8 row_mask:0xf bank_mask:0x3\n"
"v_mfma_f32_16x16x32_bf16 v[120:123],a[220:223],v[248:251],v[120:123]\n"
"s_waitcnt lgkmcnt(11)\n"
"ds_read_b64_tr_b16 a[198:199],v28 offset:38912\n"
"v_mov_b32_dpp v89,v85 row_ror:8 row_mask:0xf bank_mask:0x3\n"
"v_mfma_f32_16x16x32_bf16 v[108:111],a[224:227],v[128:131],0\n"
"ds_read_b64_tr_b16 a[200:201],v28 offset:40960\n"
"v_mov_b32_dpp v90,v86 row_ror:8 row_mask:0xf bank_mask:0x3\n"
"v_mfma_f32_16x16x32_bf16 v[108:111],a[228:231],v[136:139],v[108:111]\n"
"ds_read_b64_tr_b16 a[202:203],v28 offset:43008\n"
"v_mov_b32_dpp v91,v87 row_ror:8 row_mask:0xf bank_mask:0x3\n"
"v_mfma_f32_16x16x32_bf16 v[108:111],a[232:235],v[144:147],v[108:111]\n"
"ds_read_b64_tr_b16 a[204:205],v28 offset:45056\n"
"v_mfma_f32_16x16x32_bf16 v[108:111],a[236:239],v[152:155],v[108:111]\n"
"s_waitcnt lgkmcnt(7)\n"
"ds_read_b64_tr_b16 a[206:207],v28 offset:47104\n"
"v_mfma_f32_16x16x32_bf16 v[108:111],a[240:243],v[160:163],v[108:111]\n"
"ds_read_b64_tr_b16 a[208:209],v28 offset:49152\n"
"v_mfma_f32_16x16x32_bf16 v[108:111],a[244:247],v[168:171],v[108:111]\n"
"ds_read_b64_tr_b16 a[210:211],v28 offset:51200\n"
"buffer_store_dwordx4 v[80:83],v6,s[44:47],s86 offen offset:256 sc1\n"
"v_mfma_f32_16x16x32_bf16 v[108:111],a[248:251],v[176:179],v[108:111]\n"
"ds_read_b64_tr_b16 a[212:213],v28 offset:53248\n"
"v_mfma_f32_16x16x32_bf16 v[108:111],a[252:255],v[184:187],v[108:111]\n"
"ds_read_b64_tr_b16 a[214:215],v28 offset:55296\n"
"v_mfma_f32_16x16x32_bf16 v[124:127],a[224:227],v[192:195],0\n"
"ds_read_b64_tr_b16 a[216:217],v28 offset:57344\n"
"v_mfma_f32_16x16x32_bf16 v[124:127],a[228:231],v[200:203],v[124:127]\n"
"ds_read_b64_tr_b16 a[218:219],v28 offset:59392\n"
"v_mfma_f32_16x16x32_bf16 v[124:127],a[232:235],v[208:211],v[124:127]\n"
"ds_read_b64_tr_b16 a[220:221],v28 offset:61440\n"
"v_mfma_f32_16x16x32_bf16 v[124:127],a[236:239],v[216:219],v[124:127]\n"
"ds_read_b64_tr_b16 a[222:223],v28 offset:63488\n"
"v_mfma_f32_16x16x32_bf16 v[124:127],a[240:243],v[224:227],v[124:127]\n"
"ds_read_b64_tr_b16 a[224:225],v29 offset:32768\n"
"v_mfma_f32_16x16x32_bf16 v[124:127],a[244:247],v[232:235],v[124:127]\n"
"ds_read_b64_tr_b16 a[226:227],v29 offset:34816\n"
"buffer_store_dwordx4 v[88:91],v6,s[44:47],s88 offen offset:256 sc1\n"
"v_mfma_f32_16x16x32_bf16 v[124:127],a[248:251],v[240:243],v[124:127]\n"
"ds_read_b64_tr_b16 a[228:229],v29 offset:36864\n"
"v_mfma_f32_16x16x32_bf16 v[124:127],a[252:255],v[248:251],v[124:127]\n"
"s_waitcnt lgkmcnt(11)\n"
"ds_read_b64_tr_b16 a[230:231],v29 offset:38912\n"
"v_mfma_f32_16x16x32_bf16 v[64:67],a[192:195],v[128:131],0\n"
"ds_read_b64_tr_b16 a[232:233],v29 offset:40960\n"
"v_mfma_f32_16x16x32_bf16 v[64:67],a[196:199],v[136:139],v[64:67]\n"
"ds_read_b64_tr_b16 a[234:235],v29 offset:43008\n"
"v_cvt_pk_bf16_f32 v96,v96,v97\n"
"v_mfma_f32_16x16x32_bf16 v[64:67],a[200:203],v[144:147],v[64:67]\n"
"ds_read_b64_tr_b16 a[236:237],v29 offset:45056\n"
"v_cvt_pk_bf16_f32 v97,v98,v99\n"
"v_mfma_f32_16x16x32_bf16 v[64:67],a[204:207],v[152:155],v[64:67]\n"
"s_waitcnt lgkmcnt(7)\n"
"ds_read_b64_tr_b16 a[238:239],v29 offset:47104\n"
"v_cvt_pk_bf16_f32 v98,v100,v101\n"
"v_mfma_f32_16x16x32_bf16 v[64:67],a[208:211],v[160:163],v[64:67]\n"
"ds_read_b64_tr_b16 a[240:241],v29 offset:49152\n"
"v_cvt_pk_bf16_f32 v99,v102,v103\n"
"v_mfma_f32_16x16x32_bf16 v[64:67],a[212:215],v[168:171],v[64:67]\n"
"ds_read_b64_tr_b16 a[242:243],v29 offset:51200\n"
"v_cvt_pk_bf16_f32 v104,v104,v105\n"
"v_mfma_f32_16x16x32_bf16 v[64:67],a[216:219],v[176:179],v[64:67]\n"
"ds_read_b64_tr_b16 a[244:245],v29 offset:53248\n"
"v_cvt_pk_bf16_f32 v105,v106,v107\n"
"v_mfma_f32_16x16x32_bf16 v[64:67],a[220:223],v[184:187],v[64:67]\n"
"ds_read_b64_tr_b16 a[246:247],v29 offset:55296\n"
"v_cvt_pk_bf16_f32 v106,v108,v109\n"
"v_mfma_f32_16x16x32_bf16 v[80:83],a[192:195],v[192:195],0\n"
"ds_read_b64_tr_b16 a[248:249],v29 offset:57344\n"
"v_cvt_pk_bf16_f32 v107,v110,v111\n"
"v_mfma_f32_16x16x32_bf16 v[80:83],a[196:199],v[200:203],v[80:83]\n"
"ds_read_b64_tr_b16 a[250:251],v29 offset:59392\n"
"v_mov_b32 v100,v96\n"
"v_mfma_f32_16x16x32_bf16 v[80:83],a[200:203],v[208:211],v[80:83]\n"
"ds_read_b64_tr_b16 a[252:253],v29 offset:61440\n"
"v_mov_b32 v101,v97\n"
"v_mfma_f32_16x16x32_bf16 v[80:83],a[204:207],v[216:219],v[80:83]\n"
"ds_read_b64_tr_b16 a[254:255],v29 offset:63488\n"
"v_mov_b32 v102,v98\n"
"v_mfma_f32_16x16x32_bf16 v[80:83],a[208:211],v[224:227],v[80:83]\n"
"ds_read_b64_tr_b16 a[192:193],v30 offset:32768\n"
"v_mov_b32 v103,v99\n"
"v_mfma_f32_16x16x32_bf16 v[80:83],a[212:215],v[232:235],v[80:83]\n"
"ds_read_b64_tr_b16 a[194:195],v30 offset:34816\n"
"v_mov_b32_dpp v96,v104 row_ror:8 row_mask:0xf bank_mask:0xc\n"
"v_mfma_f32_16x16x32_bf16 v[80:83],a[216:219],v[240:243],v[80:83]\n"
"ds_read_b64_tr_b16 a[196:197],v30 offset:36864\n"
"v_mov_b32_dpp v97,v105 row_ror:8 row_mask:0xf bank_mask:0xc\n"
"v_mfma_f32_16x16x32_bf16 v[80:83],a[220:223],v[248:251],v[80:83]\n"
"s_waitcnt lgkmcnt(11)\n"
"ds_read_b64_tr_b16 a[198:199],v30 offset:38912\n"
"v_mov_b32_dpp v98,v106 row_ror:8 row_mask:0xf bank_mask:0xc\n"
"v_mfma_f32_16x16x32_bf16 v[68:71],a[224:227],v[128:131],0\n"
"ds_read_b64_tr_b16 a[200:201],v30 offset:40960\n"
"v_mov_b32_dpp v99,v107 row_ror:8 row_mask:0xf bank_mask:0xc\n"
"v_mfma_f32_16x16x32_bf16 v[68:71],a[228:231],v[136:139],v[68:71]\n"
"ds_read_b64_tr_b16 a[202:203],v30 offset:43008\n"
"v_mov_b32_dpp v104,v100 row_ror:8 row_mask:0xf bank_mask:0x3\n"
"v_mfma_f32_16x16x32_bf16 v[68:71],a[232:235],v[144:147],v[68:71]\n"
"ds_read_b64_tr_b16 a[204:205],v30 offset:45056\n"
"v_mov_b32_dpp v105,v101 row_ror:8 row_mask:0xf bank_mask:0x3\n"
"v_mfma_f32_16x16x32_bf16 v[68:71],a[236:239],v[152:155],v[68:71]\n"
"s_waitcnt lgkmcnt(7)\n"
"ds_read_b64_tr_b16 a[206:207],v30 offset:47104\n"
"v_mov_b32_dpp v106,v102 row_ror:8 row_mask:0xf bank_mask:0x3\n"
"v_mfma_f32_16x16x32_bf16 v[68:71],a[240:243],v[160:163],v[68:71]\n"
"ds_read_b64_tr_b16 a[208:209],v30 offset:49152\n"
"v_mov_b32_dpp v107,v103 row_ror:8 row_mask:0xf bank_mask:0x3\n"
"v_mfma_f32_16x16x32_bf16 v[68:71],a[244:247],v[168:171],v[68:71]\n"
"ds_read_b64_tr_b16 a[210:211],v30 offset:51200\n"
"buffer_store_dwordx4 v[96:99],v6,s[44:47],0 offen offset:384 sc1\n"
"v_mfma_f32_16x16x32_bf16 v[68:71],a[248:251],v[176:179],v[68:71]\n"
"ds_read_b64_tr_b16 a[212:213],v30 offset:53248\n"
"v_mfma_f32_16x16x32_bf16 v[68:71],a[252:255],v[184:187],v[68:71]\n"
"ds_read_b64_tr_b16 a[214:215],v30 offset:55296\n"
"v_mfma_f32_16x16x32_bf16 v[84:87],a[224:227],v[192:195],0\n"
"ds_read_b64_tr_b16 a[216:217],v30 offset:57344\n"
"v_mfma_f32_16x16x32_bf16 v[84:87],a[228:231],v[200:203],v[84:87]\n"
"ds_read_b64_tr_b16 a[218:219],v30 offset:59392\n"
"v_mfma_f32_16x16x32_bf16 v[84:87],a[232:235],v[208:211],v[84:87]\n"
"ds_read_b64_tr_b16 a[220:221],v30 offset:61440\n"
"v_mfma_f32_16x16x32_bf16 v[84:87],a[236:239],v[216:219],v[84:87]\n"
"ds_read_b64_tr_b16 a[222:223],v30 offset:63488\n"
"v_mfma_f32_16x16x32_bf16 v[84:87],a[240:243],v[224:227],v[84:87]\n"
"ds_read_b64_tr_b16 a[224:225],v31 offset:32768\n"
"v_mfma_f32_16x16x32_bf16 v[84:87],a[244:247],v[232:235],v[84:87]\n"
"ds_read_b64_tr_b16 a[226:227],v31 offset:34816\n"
"buffer_store_dwordx4 v[104:107],v6,s[44:47],s87 offen offset:384 sc1\n"
"v_mfma_f32_16x16x32_bf16 v[84:87],a[248:251],v[240:243],v[84:87]\n"
"ds_read_b64_tr_b16 a[228:229],v31 offset:36864\n"
"v_add_u32 v28,0x20000,v28\n"
"v_mfma_f32_16x16x32_bf16 v[84:87],a[252:255],v[248:251],v[84:87]\n"
"s_waitcnt lgkmcnt(11)\n"
"ds_read_b64_tr_b16 a[230:231],v31 offset:38912\n"
"v_cvt_pk_bf16_f32 v112,v112,v113\n"
"v_mfma_f32_16x16x32_bf16 v[72:75],a[192:195],v[128:131],0\n"
"ds_read_b64_tr_b16 a[232:233],v31 offset:40960\n"
"v_cvt_pk_bf16_f32 v113,v114,v115\n"
"v_mfma_f32_16x16x32_bf16 v[72:75],a[196:199],v[136:139],v[72:75]\n"
"ds_read_b64_tr_b16 a[234:235],v31 offset:43008\n"
"v_cvt_pk_bf16_f32 v114,v116,v117\n"
"v_mfma_f32_16x16x32_bf16 v[72:75],a[200:203],v[144:147],v[72:75]\n"
"ds_read_b64_tr_b16 a[236:237],v31 offset:45056\n"
"v_cvt_pk_bf16_f32 v115,v118,v119\n"
"v_mfma_f32_16x16x32_bf16 v[72:75],a[204:207],v[152:155],v[72:75]\n"
"s_waitcnt lgkmcnt(7)\n"
"ds_read_b64_tr_b16 a[238:239],v31 offset:47104\n"
"v_cvt_pk_bf16_f32 v120,v120,v121\n"
"v_mfma_f32_16x16x32_bf16 v[72:75],a[208:211],v[160:163],v[72:75]\n"
"ds_read_b64_tr_b16 a[240:241],v31 offset:49152\n"
"v_cvt_pk_bf16_f32 v121,v122,v123\n"
"v_mfma_f32_16x16x32_bf16 v[72:75],a[212:215],v[168:171],v[72:75]\n"
"ds_read_b64_tr_b16 a[242:243],v31 offset:51200\n"
"v_cvt_pk_bf16_f32 v122,v124,v125\n"
"v_mfma_f32_16x16x32_bf16 v[72:75],a[216:219],v[176:179],v[72:75]\n"
"ds_read_b64_tr_b16 a[244:245],v31 offset:53248\n"
"v_cvt_pk_bf16_f32 v123,v126,v127\n"
"v_mfma_f32_16x16x32_bf16 v[72:75],a[220:223],v[184:187],v[72:75]\n"
"ds_read_b64_tr_b16 a[246:247],v31 offset:55296\n"
"v_mov_b32 v116,v112\n"
"v_mfma_f32_16x16x32_bf16 v[88:91],a[192:195],v[192:195],0\n"
"ds_read_b64_tr_b16 a[248:249],v31 offset:57344\n"
"v_mov_b32 v117,v113\n"
"v_mfma_f32_16x16x32_bf16 v[88:91],a[196:199],v[200:203],v[88:91]\n"
"ds_read_b64_tr_b16 a[250:251],v31 offset:59392\n"
"v_mov_b32 v118,v114\n"
"v_mfma_f32_16x16x32_bf16 v[88:91],a[200:203],v[208:211],v[88:91]\n"
"ds_read_b64_tr_b16 a[252:253],v31 offset:61440\n"
"v_mov_b32 v119,v115\n"
"v_mfma_f32_16x16x32_bf16 v[88:91],a[204:207],v[216:219],v[88:91]\n"
"s_waitcnt vmcnt(17)\n"
"s_barrier\n"
"ds_read_b64_tr_b16 a[254:255],v31 offset:63488\n"
"v_add_u32 v29,0x20000,v29\n"
"v_mfma_f32_16x16x32_bf16 v[88:91],a[208:211],v[224:227],v[88:91]\n"
"ds_read_b64_tr_b16 a[192:193],v28 offset:0\n"
"v_add_u32 v30,0x20000,v30\n"
"v_mfma_f32_16x16x32_bf16 v[88:91],a[212:215],v[232:235],v[88:91]\n"
"ds_read_b64_tr_b16 a[194:195],v28 offset:2048\n"
"v_add_u32 v31,0x20000,v31\n"
"v_mfma_f32_16x16x32_bf16 v[88:91],a[216:219],v[240:243],v[88:91]\n"
"ds_read_b64_tr_b16 a[196:197],v28 offset:4096\n"
"v_mov_b32_dpp v112,v120 row_ror:8 row_mask:0xf bank_mask:0xc\n"
"v_mfma_f32_16x16x32_bf16 v[88:91],a[220:223],v[248:251],v[88:91]\n"
"s_waitcnt lgkmcnt(11)\n"
"ds_read_b64_tr_b16 a[198:199],v28 offset:6144\n"
"v_mov_b32_dpp v113,v121 row_ror:8 row_mask:0xf bank_mask:0xc\n"
"v_mfma_f32_16x16x32_bf16 v[76:79],a[224:227],v[128:131],0\n"
"ds_read_b64_tr_b16 a[200:201],v28 offset:8192\n"
"v_mov_b32_dpp v114,v122 row_ror:8 row_mask:0xf bank_mask:0xc\n"
"v_mfma_f32_16x16x32_bf16 v[76:79],a[228:231],v[136:139],v[76:79]\n"
"ds_read_b64_tr_b16 a[202:203],v28 offset:10240\n"
"v_mov_b32_dpp v115,v123 row_ror:8 row_mask:0xf bank_mask:0xc\n"
"v_mfma_f32_16x16x32_bf16 v[76:79],a[232:235],v[144:147],v[76:79]\n"
"ds_read_b64_tr_b16 a[204:205],v28 offset:12288\n"
"v_mov_b32_dpp v120,v116 row_ror:8 row_mask:0xf bank_mask:0x3\n"
"v_mfma_f32_16x16x32_bf16 v[76:79],a[236:239],v[152:155],v[76:79]\n"
"s_waitcnt lgkmcnt(7)\n"
"ds_read_b64_tr_b16 a[206:207],v28 offset:14336\n"
"v_mov_b32_dpp v121,v117 row_ror:8 row_mask:0xf bank_mask:0x3\n"
"v_mfma_f32_16x16x32_bf16 v[76:79],a[240:243],v[160:163],v[76:79]\n"
"ds_read_b64_tr_b16 a[208:209],v28 offset:16384\n"
"v_mov_b32_dpp v122,v118 row_ror:8 row_mask:0xf bank_mask:0x3\n"
"v_mfma_f32_16x16x32_bf16 v[76:79],a[244:247],v[168:171],v[76:79]\n"
"ds_read_b64_tr_b16 a[210:211],v28 offset:18432\n"
"buffer_store_dwordx4 v[112:115],v6,s[44:47],s86 offen offset:384 sc1\n"
"v_mfma_f32_16x16x32_bf16 v[76:79],a[248:251],v[176:179],v[76:79]\n"
"ds_read_b64_tr_b16 a[212:213],v28 offset:20480\n"
"v_mov_b32_dpp v123,v119 row_ror:8 row_mask:0xf bank_mask:0x3\n"
"v_mfma_f32_16x16x32_bf16 v[76:79],a[252:255],v[184:187],v[76:79]\n"
"ds_read_b64_tr_b16 a[214:215],v28 offset:22528\n"
"v_mfma_f32_16x16x32_bf16 v[92:95],a[224:227],v[192:195],0\n"
"ds_read_b64_tr_b16 a[216:217],v28 offset:24576\n"
"v_mfma_f32_16x16x32_bf16 v[92:95],a[228:231],v[200:203],v[92:95]\n"
"ds_read_b64_tr_b16 a[218:219],v28 offset:26624\n"
"v_mfma_f32_16x16x32_bf16 v[92:95],a[232:235],v[208:211],v[92:95]\n"
"ds_read_b64_tr_b16 a[220:221],v28 offset:28672\n"
"v_mfma_f32_16x16x32_bf16 v[92:95],a[236:239],v[216:219],v[92:95]\n"
"ds_read_b64_tr_b16 a[222:223],v28 offset:30720\n"
"v_mfma_f32_16x16x32_bf16 v[92:95],a[240:243],v[224:227],v[92:95]\n"
"ds_read_b64_tr_b16 a[224:225],v29 offset:0\n"
"v_mfma_f32_16x16x32_bf16 v[92:95],a[244:247],v[232:235],v[92:95]\n"
"ds_read_b64_tr_b16 a[226:227],v29 offset:2048\n"
"buffer_store_dwordx4 v[120:123],v6,s[44:47],s88 offen offset:384 sc1\n"
"v_mfma_f32_16x16x32_bf16 v[92:95],a[248:251],v[240:243],v[92:95]\n"
"ds_read_b64_tr_b16 a[228:229],v29 offset:4096\n"
"v_mfma_f32_16x16x32_bf16 v[92:95],a[252:255],v[248:251],v[92:95]\n"
"s_waitcnt lgkmcnt(11)\n"
"ds_read_b64_tr_b16 a[230:231],v29 offset:6144\n"
"v_mfma_f32_16x16x32_bf16 v[96:99],a[192:195],v[128:131],0\n"
"ds_read_b64_tr_b16 a[232:233],v29 offset:8192\n"
"v_mfma_f32_16x16x32_bf16 v[96:99],a[196:199],v[136:139],v[96:99]\n"
"ds_read_b64_tr_b16 a[234:235],v29 offset:10240\n"
"v_cvt_pk_bf16_f32 v64,v64,v65\n"
"v_mfma_f32_16x16x32_bf16 v[96:99],a[200:203],v[144:147],v[96:99]\n"
"ds_read_b64_tr_b16 a[236:237],v29 offset:12288\n"
"v_cvt_pk_bf16_f32 v65,v66,v67\n"
"v_mfma_f32_16x16x32_bf16 v[96:99],a[204:207],v[152:155],v[96:99]\n"
"s_waitcnt lgkmcnt(7)\n"
"ds_read_b64_tr_b16 a[238:239],v29 offset:14336\n"
"v_cvt_pk_bf16_f32 v66,v68,v69\n"
"v_mfma_f32_16x16x32_bf16 v[96:99],a[208:211],v[160:163],v[96:99]\n"
"ds_read_b64_tr_b16 a[240:241],v29 offset:16384\n"
"v_cvt_pk_bf16_f32 v67,v70,v71\n"
"v_mfma_f32_16x16x32_bf16 v[96:99],a[212:215],v[168:171],v[96:99]\n"
"ds_read_b64_tr_b16 a[242:243],v29 offset:18432\n"
"v_cvt_pk_bf16_f32 v72,v72,v73\n"
"v_mfma_f32_16x16x32_bf16 v[96:99],a[216:219],v[176:179],v[96:99]\n"
"ds_read_b64_tr_b16 a[244:245],v29 offset:20480\n"
"v_cvt_pk_bf16_f32 v73,v74,v75\n"
"v_mfma_f32_16x16x32_bf16 v[96:99],a[220:223],v[184:187],v[96:99]\n"
"ds_read_b64_tr_b16 a[246:247],v29 offset:22528\n"
"v_cvt_pk_bf16_f32 v74,v76,v77\n"
"v_mfma_f32_16x16x32_bf16 v[112:115],a[192:195],v[192:195],0\n"
"ds_read_b64_tr_b16 a[248:249],v29 offset:24576\n"
"v_cvt_pk_bf16_f32 v75,v78,v79\n"
"v_mfma_f32_16x16x32_bf16 v[112:115],a[196:199],v[200:203],v[112:115]\n"
"ds_read_b64_tr_b16 a[250:251],v29 offset:26624\n"
"v_mov_b32 v68,v64\n"
"v_mfma_f32_16x16x32_bf16 v[112:115],a[200:203],v[208:211],v[112:115]\n"
"ds_read_b64_tr_b16 a[252:253],v29 offset:28672\n"
"v_mov_b32 v69,v65\n"
"v_mfma_f32_16x16x32_bf16 v[112:115],a[204:207],v[216:219],v[112:115]\n"
"ds_read_b64_tr_b16 a[254:255],v29 offset:30720\n"
"v_mov_b32 v70,v66\n"
"v_mfma_f32_16x16x32_bf16 v[112:115],a[208:211],v[224:227],v[112:115]\n"
"ds_read_b64_tr_b16 a[192:193],v30 offset:0\n"
"v_mov_b32 v71,v67\n"
"v_mfma_f32_16x16x32_bf16 v[112:115],a[212:215],v[232:235],v[112:115]\n"
"ds_read_b64_tr_b16 a[194:195],v30 offset:2048\n"
"v_mov_b32_dpp v64,v72 row_ror:8 row_mask:0xf bank_mask:0xc\n"
"v_mfma_f32_16x16x32_bf16 v[112:115],a[216:219],v[240:243],v[112:115]\n"
"ds_read_b64_tr_b16 a[196:197],v30 offset:4096\n"
"v_mov_b32_dpp v65,v73 row_ror:8 row_mask:0xf bank_mask:0xc\n"
"v_mfma_f32_16x16x32_bf16 v[112:115],a[220:223],v[248:251],v[112:115]\n"
"s_waitcnt lgkmcnt(11)\n"
"ds_read_b64_tr_b16 a[198:199],v30 offset:6144\n"
"v_mov_b32_dpp v66,v74 row_ror:8 row_mask:0xf bank_mask:0xc\n"
"v_mfma_f32_16x16x32_bf16 v[100:103],a[224:227],v[128:131],0\n"
"ds_read_b64_tr_b16 a[200:201],v30 offset:8192\n"
"v_mov_b32_dpp v67,v75 row_ror:8 row_mask:0xf bank_mask:0xc\n"
"v_mfma_f32_16x16x32_bf16 v[100:103],a[228:231],v[136:139],v[100:103]\n"
"ds_read_b64_tr_b16 a[202:203],v30 offset:10240\n"
"v_mov_b32_dpp v72,v68 row_ror:8 row_mask:0xf bank_mask:0x3\n"
"v_mfma_f32_16x16x32_bf16 v[100:103],a[232:235],v[144:147],v[100:103]\n"
"ds_read_b64_tr_b16 a[204:205],v30 offset:12288\n"
"v_mov_b32_dpp v73,v69 row_ror:8 row_mask:0xf bank_mask:0x3\n"
"v_mfma_f32_16x16x32_bf16 v[100:103],a[236:239],v[152:155],v[100:103]\n"
"s_waitcnt lgkmcnt(7)\n"
"ds_read_b64_tr_b16 a[206:207],v30 offset:14336\n"
"v_mov_b32_dpp v74,v70 row_ror:8 row_mask:0xf bank_mask:0x3\n"
"v_mfma_f32_16x16x32_bf16 v[100:103],a[240:243],v[160:163],v[100:103]\n"
"ds_read_b64_tr_b16 a[208:209],v30 offset:16384\n"
"v_mov_b32_dpp v75,v71 row_ror:8 row_mask:0xf bank_mask:0x3\n"
"v_mfma_f32_16x16x32_bf16 v[100:103],a[244:247],v[168:171],v[100:103]\n"
"ds_read_b64_tr_b16 a[210:211],v30 offset:18432\n"
"buffer_store_dwordx4 v[64:67],v6,s[44:47],0 offen offset:512 sc1\n"
"v_mfma_f32_16x16x32_bf16 v[100:103],a[248:251],v[176:179],v[100:103]\n"
"ds_read_b64_tr_b16 a[212:213],v30 offset:20480\n"
"v_mfma_f32_16x16x32_bf16 v[100:103],a[252:255],v[184:187],v[100:103]\n"
"ds_read_b64_tr_b16 a[214:215],v30 offset:22528\n"
"v_mfma_f32_16x16x32_bf16 v[116:119],a[224:227],v[192:195],0\n"
"ds_read_b64_tr_b16 a[216:217],v30 offset:24576\n"
"v_mfma_f32_16x16x32_bf16 v[116:119],a[228:231],v[200:203],v[116:119]\n"
"ds_read_b64_tr_b16 a[218:219],v30 offset:26624\n"
"v_mfma_f32_16x16x32_bf16 v[116:119],a[232:235],v[208:211],v[116:119]\n"
"ds_read_b64_tr_b16 a[220:221],v30 offset:28672\n"
"v_mfma_f32_16x16x32_bf16 v[116:119],a[236:239],v[216:219],v[116:119]\n"
"ds_read_b64_tr_b16 a[222:223],v30 offset:30720\n"
"v_mfma_f32_16x16x32_bf16 v[116:119],a[240:243],v[224:227],v[116:119]\n"
"ds_read_b64_tr_b16 a[224:225],v31 offset:0\n"
"v_mfma_f32_16x16x32_bf16 v[116:119],a[244:247],v[232:235],v[116:119]\n"
"ds_read_b64_tr_b16 a[226:227],v31 offset:2048\n"
"buffer_store_dwordx4 v[72:75],v6,s[44:47],s87 offen offset:512 sc1\n"
"v_mfma_f32_16x16x32_bf16 v[116:119],a[248:251],v[240:243],v[116:119]\n"
"ds_read_b64_tr_b16 a[228:229],v31 offset:4096\n"
"v_add_u32 v28,0xffff0000,v28\n"
"v_mfma_f32_16x16x32_bf16 v[116:119],a[252:255],v[248:251],v[116:119]\n"
"s_waitcnt lgkmcnt(11)\n"
"ds_read_b64_tr_b16 a[230:231],v31 offset:6144\n"
"v_cvt_pk_bf16_f32 v80,v80,v81\n"
"v_mfma_f32_16x16x32_bf16 v[104:107],a[192:195],v[128:131],0\n"
"ds_read_b64_tr_b16 a[232:233],v31 offset:8192\n"
"v_cvt_pk_bf16_f32 v81,v82,v83\n"
"v_mfma_f32_16x16x32_bf16 v[104:107],a[196:199],v[136:139],v[104:107]\n"
"ds_read_b64_tr_b16 a[234:235],v31 offset:10240\n"
"v_cvt_pk_bf16_f32 v82,v84,v85\n"
"v_mfma_f32_16x16x32_bf16 v[104:107],a[200:203],v[144:147],v[104:107]\n"
"ds_read_b64_tr_b16 a[236:237],v31 offset:12288\n"
"v_cvt_pk_bf16_f32 v83,v86,v87\n"
"v_mfma_f32_16x16x32_bf16 v[104:107],a[204:207],v[152:155],v[104:107]\n"
"s_waitcnt lgkmcnt(7)\n"
"ds_read_b64_tr_b16 a[238:239],v31 offset:14336\n"
"v_cvt_pk_bf16_f32 v88,v88,v89\n"
"v_mfma_f32_16x16x32_bf16 v[104:107],a[208:211],v[160:163],v[104:107]\n"
"ds_read_b64_tr_b16 a[240:241],v31 offset:16384\n"
"v_cvt_pk_bf16_f32 v89,v90,v91\n"
"v_mfma_f32_16x16x32_bf16 v[104:107],a[212:215],v[168:171],v[104:107]\n"
"ds_read_b64_tr_b16 a[242:243],v31 offset:18432\n"
"v_cvt_pk_bf16_f32 v90,v92,v93\n"
"v_mfma_f32_16x16x32_bf16 v[104:107],a[216:219],v[176:179],v[104:107]\n"
"ds_read_b64_tr_b16 a[244:245],v31 offset:20480\n"
"v_cvt_pk_bf16_f32 v91,v94,v95\n"
"v_mfma_f32_16x16x32_bf16 v[104:107],a[220:223],v[184:187],v[104:107]\n"
"ds_read_b64_tr_b16 a[246:247],v31 offset:22528\n"
"v_mov_b32 v84,v80\n"
"v_mfma_f32_16x16x32_bf16 v[120:123],a[192:195],v[192:195],0\n"
"ds_read_b64_tr_b16 a[248:249],v31 offset:24576\n"
"v_mov_b32 v85,v81\n"
"v_mfma_f32_16x16x32_bf16 v[120:123],a[196:199],v[200:203],v[120:123]\n"
"ds_read_b64_tr_b16 a[250:251],v31 offset:26624\n"
"v_mov_b32 v86,v82\n"
"v_mfma_f32_16x16x32_bf16 v[120:123],a[200:203],v[208:211],v[120:123]\n"
"ds_read_b64_tr_b16 a[252:253],v31 offset:28672\n"
"v_mov_b32 v87,v83\n"
"v_mfma_f32_16x16x32_bf16 v[120:123],a[204:207],v[216:219],v[120:123]\n"
"s_waitcnt vmcnt(9)\n"
"s_barrier\n"
"ds_read_b64_tr_b16 a[254:255],v31 offset:30720\n"
"v_add_u32 v29,0xffff0000,v29\n"
"v_mfma_f32_16x16x32_bf16 v[120:123],a[208:211],v[224:227],v[120:123]\n"
"ds_read_b64_tr_b16 a[192:193],v28 offset:0\n"
"v_add_u32 v30,0xffff0000,v30\n"
"v_mfma_f32_16x16x32_bf16 v[120:123],a[212:215],v[232:235],v[120:123]\n"
"ds_read_b64_tr_b16 a[194:195],v28 offset:2048\n"
"v_add_u32 v31,0xffff0000,v31\n"
"v_mfma_f32_16x16x32_bf16 v[120:123],a[216:219],v[240:243],v[120:123]\n"
"ds_read_b64_tr_b16 a[196:197],v28 offset:4096\n"
"v_mov_b32_dpp v80,v88 row_ror:8 row_mask:0xf bank_mask:0xc\n"
"v_mfma_f32_16x16x32_bf16 v[120:123],a[220:223],v[248:251],v[120:123]\n"
"s_waitcnt lgkmcnt(11)\n"
"ds_read_b64_tr_b16 a[198:199],v28 offset:6144\n"
"v_mov_b32_dpp v81,v89 row_ror:8 row_mask:0xf bank_mask:0xc\n"
"v_mfma_f32_16x16x32_bf16 v[108:111],a[224:227],v[128:131],0\n"
"ds_read_b64_tr_b16 a[200:201],v28 offset:8192\n"
"v_mov_b32_dpp v82,v90 row_ror:8 row_mask:0xf bank_mask:0xc\n"
"v_mfma_f32_16x16x32_bf16 v[108:111],a[228:231],v[136:139],v[108:111]\n"
"ds_read_b64_tr_b16 a[202:203],v28 offset:10240\n"
"v_mov_b32_dpp v83,v91 row_ror:8 row_mask:0xf bank_mask:0xc\n"
"v_mfma_f32_16x16x32_bf16 v[108:111],a[232:235],v[144:147],v[108:111]\n"
"ds_read_b64_tr_b16 a[204:205],v28 offset:12288\n"
"v_mov_b32_dpp v88,v84 row_ror:8 row_mask:0xf bank_mask:0x3\n"
"v_mfma_f32_16x16x32_bf16 v[108:111],a[236:239],v[152:155],v[108:111]\n"
"s_waitcnt lgkmcnt(7)\n"
"ds_read_b64_tr_b16 a[206:207],v28 offset:14336\n"
"v_mov_b32_dpp v89,v85 row_ror:8 row_mask:0xf bank_mask:0x3\n"
"v_mfma_f32_16x16x32_bf16 v[108:111],a[240:243],v[160:163],v[108:111]\n"
"ds_read_b64_tr_b16 a[208:209],v28 offset:16384\n"
"v_mov_b32_dpp v90,v86 row_ror:8 row_mask:0xf bank_mask:0x3\n"
"v_mfma_f32_16x16x32_bf16 v[108:111],a[244:247],v[168:171],v[108:111]\n"
"ds_read_b64_tr_b16 a[210:211],v28 offset:18432\n"
"buffer_store_dwordx4 v[80:83],v6,s[44:47],s86 offen offset:512 sc1\n"
"v_mfma_f32_16x16x32_bf16 v[108:111],a[248:251],v[176:179],v[108:111]\n"
"ds_read_b64_tr_b16 a[212:213],v28 offset:20480\n"
"v_mov_b32_dpp v91,v87 row_ror:8 row_mask:0xf bank_mask:0x3\n"
"v_mfma_f32_16x16x32_bf16 v[108:111],a[252:255],v[184:187],v[108:111]\n"
"ds_read_b64_tr_b16 a[214:215],v28 offset:22528\n"
"v_mfma_f32_16x16x32_bf16 v[124:127],a[224:227],v[192:195],0\n"
"ds_read_b64_tr_b16 a[216:217],v28 offset:24576\n"
"v_mfma_f32_16x16x32_bf16 v[124:127],a[228:231],v[200:203],v[124:127]\n"
"ds_read_b64_tr_b16 a[218:219],v28 offset:26624\n"
"v_mfma_f32_16x16x32_bf16 v[124:127],a[232:235],v[208:211],v[124:127]\n"
"ds_read_b64_tr_b16 a[220:221],v28 offset:28672\n"
"v_mfma_f32_16x16x32_bf16 v[124:127],a[236:239],v[216:219],v[124:127]\n"
"ds_read_b64_tr_b16 a[222:223],v28 offset:30720\n"
"v_mfma_f32_16x16x32_bf16 v[124:127],a[240:243],v[224:227],v[124:127]\n"
"ds_read_b64_tr_b16 a[224:225],v29 offset:0\n"
"v_mfma_f32_16x16x32_bf16 v[124:127],a[244:247],v[232:235],v[124:127]\n"
"ds_read_b64_tr_b16 a[226:227],v29 offset:2048\n"
"buffer_store_dwordx4 v[88:91],v6,s[44:47],s88 offen offset:512 sc1\n"
"v_mfma_f32_16x16x32_bf16 v[124:127],a[248:251],v[240:243],v[124:127]\n"
"ds_read_b64_tr_b16 a[228:229],v29 offset:4096\n"
"v_mfma_f32_16x16x32_bf16 v[124:127],a[252:255],v[248:251],v[124:127]\n"
"s_waitcnt lgkmcnt(11)\n"
"ds_read_b64_tr_b16 a[230:231],v29 offset:6144\n"
"v_mfma_f32_16x16x32_bf16 v[64:67],a[192:195],v[128:131],0\n"
"ds_read_b64_tr_b16 a[232:233],v29 offset:8192\n"
"v_mfma_f32_16x16x32_bf16 v[64:67],a[196:199],v[136:139],v[64:67]\n"
"ds_read_b64_tr_b16 a[234:235],v29 offset:10240\n"
"v_cvt_pk_bf16_f32 v96,v96,v97\n"
"v_mfma_f32_16x16x32_bf16 v[64:67],a[200:203],v[144:147],v[64:67]\n"
"ds_read_b64_tr_b16 a[236:237],v29 offset:12288\n"
"v_cvt_pk_bf16_f32 v97,v98,v99\n"
"v_mfma_f32_16x16x32_bf16 v[64:67],a[204:207],v[152:155],v[64:67]\n"
"s_waitcnt lgkmcnt(7)\n"
"ds_read_b64_tr_b16 a[238:239],v29 offset:14336\n"
"v_cvt_pk_bf16_f32 v98,v100,v101\n"
"v_mfma_f32_16x16x32_bf16 v[64:67],a[208:211],v[160:163],v[64:67]\n"
"ds_read_b64_tr_b16 a[240:241],v29 offset:16384\n"
"v_cvt_pk_bf16_f32 v99,v102,v103\n"
"v_mfma_f32_16x16x32_bf16 v[64:67],a[212:215],v[168:171],v[64:67]\n"
"ds_read_b64_tr_b16 a[242:243],v29 offset:18432\n"
"v_cvt_pk_bf16_f32 v104,v104,v105\n"
"v_mfma_f32_16x16x32_bf16 v[64:67],a[216:219],v[176:179],v[64:67]\n"
"ds_read_b64_tr_b16 a[244:245],v29 offset:20480\n"
"v_cvt_pk_bf16_f32 v105,v106,v107\n"
"v_mfma_f32_16x16x32_bf16 v[64:67],a[220:223],v[184:187],v[64:67]\n"
"ds_read_b64_tr_b16 a[246:247],v29 offset:22528\n"
"v_cvt_pk_bf16_f32 v106,v108,v109\n"
"v_mfma_f32_16x16x32_bf16 v[80:83],a[192:195],v[192:195],0\n"
"ds_read_b64_tr_b16 a[248:249],v29 offset:24576\n"
"v_cvt_pk_bf16_f32 v107,v110,v111\n"
"v_mfma_f32_16x16x32_bf16 v[80:83],a[196:199],v[200:203],v[80:83]\n"
"ds_read_b64_tr_b16 a[250:251],v29 offset:26624\n"
"v_mov_b32 v100,v96\n"
"v_mfma_f32_16x16x32_bf16 v[80:83],a[200:203],v[208:211],v[80:83]\n"
"ds_read_b64_tr_b16 a[252:253],v29 offset:28672\n"
"v_mov_b32 v101,v97\n"
"v_mfma_f32_16x16x32_bf16 v[80:83],a[204:207],v[216:219],v[80:83]\n"
"ds_read_b64_tr_b16 a[254:255],v29 offset:30720\n"
"v_mov_b32 v102,v98\n"
"v_mfma_f32_16x16x32_bf16 v[80:83],a[208:211],v[224:227],v[80:83]\n"
"ds_read_b64_tr_b16 a[192:193],v30 offset:0\n"
"v_mov_b32 v103,v99\n"
"v_mfma_f32_16x16x32_bf16 v[80:83],a[212:215],v[232:235],v[80:83]\n"
"ds_read_b64_tr_b16 a[194:195],v30 offset:2048\n"
"v_mov_b32_dpp v96,v104 row_ror:8 row_mask:0xf bank_mask:0xc\n"
"v_mfma_f32_16x16x32_bf16 v[80:83],a[216:219],v[240:243],v[80:83]\n"
"ds_read_b64_tr_b16 a[196:197],v30 offset:4096\n"
"v_mov_b32_dpp v97,v105 row_ror:8 row_mask:0xf bank_mask:0xc\n"
"v_mfma_f32_16x16x32_bf16 v[80:83],a[220:223],v[248:251],v[80:83]\n"
"s_waitcnt lgkmcnt(11)\n"
"ds_read_b64_tr_b16 a[198:199],v30 offset:6144\n"
"v_mov_b32_dpp v98,v106 row_ror:8 row_mask:0xf bank_mask:0xc\n"
"v_mfma_f32_16x16x32_bf16 v[68:71],a[224:227],v[128:131],0\n"
"ds_read_b64_tr_b16 a[200:201],v30 offset:8192\n"
"v_mov_b32_dpp v99,v107 row_ror:8 row_mask:0xf bank_mask:0xc\n"
"v_mfma_f32_16x16x32_bf16 v[68:71],a[228:231],v[136:139],v[68:71]\n"
"ds_read_b64_tr_b16 a[202:203],v30 offset:10240\n"
"v_mov_b32_dpp v104,v100 row_ror:8 row_mask:0xf bank_mask:0x3\n"
"v_mfma_f32_16x16x32_bf16 v[68:71],a[232:235],v[144:147],v[68:71]\n"
"ds_read_b64_tr_b16 a[204:205],v30 offset:12288\n"
"v_mov_b32_dpp v105,v101 row_ror:8 row_mask:0xf bank_mask:0x3\n"
"v_mfma_f32_16x16x32_bf16 v[68:71],a[236:239],v[152:155],v[68:71]\n"
"s_waitcnt lgkmcnt(7)\n"
"ds_read_b64_tr_b16 a[206:207],v30 offset:14336\n"
"v_mov_b32_dpp v106,v102 row_ror:8 row_mask:0xf bank_mask:0x3\n"
"v_mfma_f32_16x16x32_bf16 v[68:71],a[240:243],v[160:163],v[68:71]\n"
"ds_read_b64_tr_b16 a[208:209],v30 offset:16384\n"
"v_mov_b32_dpp v107,v103 row_ror:8 row_mask:0xf bank_mask:0x3\n"
"v_mfma_f32_16x16x32_bf16 v[68:71],a[244:247],v[168:171],v[68:71]\n"
"ds_read_b64_tr_b16 a[210:211],v30 offset:18432\n"
"buffer_store_dwordx4 v[96:99],v6,s[44:47],0 offen offset:640 sc1\n"
"v_mfma_f32_16x16x32_bf16 v[68:71],a[248:251],v[176:179],v[68:71]\n"
"ds_read_b64_tr_b16 a[212:213],v30 offset:20480\n"
"v_mfma_f32_16x16x32_bf16 v[68:71],a[252:255],v[184:187],v[68:71]\n"
"ds_read_b64_tr_b16 a[214:215],v30 offset:22528\n"
"v_mfma_f32_16x16x32_bf16 v[84:87],a[224:227],v[192:195],0\n"
"ds_read_b64_tr_b16 a[216:217],v30 offset:24576\n"
"v_mfma_f32_16x16x32_bf16 v[84:87],a[228:231],v[200:203],v[84:87]\n"
"ds_read_b64_tr_b16 a[218:219],v30 offset:26624\n"
"v_mfma_f32_16x16x32_bf16 v[84:87],a[232:235],v[208:211],v[84:87]\n"
"ds_read_b64_tr_b16 a[220:221],v30 offset:28672\n"
"v_mfma_f32_16x16x32_bf16 v[84:87],a[236:239],v[216:219],v[84:87]\n"
"ds_read_b64_tr_b16 a[222:223],v30 offset:30720\n"
"v_mfma_f32_16x16x32_bf16 v[84:87],a[240:243],v[224:227],v[84:87]\n"
"ds_read_b64_tr_b16 a[224:225],v31 offset:0\n"
"v_mfma_f32_16x16x32_bf16 v[84:87],a[244:247],v[232:235],v[84:87]\n"
"ds_read_b64_tr_b16 a[226:227],v31 offset:2048\n"
"buffer_store_dwordx4 v[104:107],v6,s[44:47],s87 offen offset:640 sc1\n"
"v_mfma_f32_16x16x32_bf16 v[84:87],a[248:251],v[240:243],v[84:87]\n"
"ds_read_b64_tr_b16 a[228:229],v31 offset:4096\n"
"v_cvt_pk_bf16_f32 v112,v112,v113\n"
"v_mfma_f32_16x16x32_bf16 v[84:87],a[252:255],v[248:251],v[84:87]\n"
"s_waitcnt lgkmcnt(11)\n"
"ds_read_b64_tr_b16 a[230:231],v31 offset:6144\n"
"v_cvt_pk_bf16_f32 v113,v114,v115\n"
"v_mfma_f32_16x16x32_bf16 v[72:75],a[192:195],v[128:131],0\n"
"ds_read_b64_tr_b16 a[232:233],v31 offset:8192\n"
"v_cvt_pk_bf16_f32 v114,v116,v117\n"
"v_mfma_f32_16x16x32_bf16 v[72:75],a[196:199],v[136:139],v[72:75]\n"
"ds_read_b64_tr_b16 a[234:235],v31 offset:10240\n"
"v_cvt_pk_bf16_f32 v115,v118,v119\n"
"v_mfma_f32_16x16x32_bf16 v[72:75],a[200:203],v[144:147],v[72:75]\n"
"ds_read_b64_tr_b16 a[236:237],v31 offset:12288\n"
"v_cvt_pk_bf16_f32 v120,v120,v121\n"
"v_mfma_f32_16x16x32_bf16 v[72:75],a[204:207],v[152:155],v[72:75]\n"
"s_waitcnt lgkmcnt(7)\n"
"ds_read_b64_tr_b16 a[238:239],v31 offset:14336\n"
"v_cvt_pk_bf16_f32 v121,v122,v123\n"
"v_mfma_f32_16x16x32_bf16 v[72:75],a[208:211],v[160:163],v[72:75]\n"
"ds_read_b64_tr_b16 a[240:241],v31 offset:16384\n"
"v_cvt_pk_bf16_f32 v122,v124,v125\n"
"v_mfma_f32_16x16x32_bf16 v[72:75],a[212:215],v[168:171],v[72:75]\n"
"ds_read_b64_tr_b16 a[242:243],v31 offset:18432\n"
"v_cvt_pk_bf16_f32 v123,v126,v127\n"
"v_mfma_f32_16x16x32_bf16 v[72:75],a[216:219],v[176:179],v[72:75]\n"
"ds_read_b64_tr_b16 a[244:245],v31 offset:20480\n"
"v_mov_b32 v116,v112\n"
"v_mfma_f32_16x16x32_bf16 v[72:75],a[220:223],v[184:187],v[72:75]\n"
"ds_read_b64_tr_b16 a[246:247],v31 offset:22528\n"
"v_mov_b32 v117,v113\n"
"v_mfma_f32_16x16x32_bf16 v[88:91],a[192:195],v[192:195],0\n"
"ds_read_b64_tr_b16 a[248:249],v31 offset:24576\n"
"v_mov_b32 v118,v114\n"
"v_mfma_f32_16x16x32_bf16 v[88:91],a[196:199],v[200:203],v[88:91]\n"
"ds_read_b64_tr_b16 a[250:251],v31 offset:26624\n"
"v_mov_b32 v119,v115\n"
"v_mfma_f32_16x16x32_bf16 v[88:91],a[200:203],v[208:211],v[88:91]\n"
"ds_read_b64_tr_b16 a[252:253],v31 offset:28672\n"
"v_mov_b32_dpp v112,v120 row_ror:8 row_mask:0xf bank_mask:0xc\n"
"v_mfma_f32_16x16x32_bf16 v[88:91],a[204:207],v[216:219],v[88:91]\n"
"ds_read_b64_tr_b16 a[254:255],v31 offset:30720\n"
"v_mov_b32_dpp v113,v121 row_ror:8 row_mask:0xf bank_mask:0xc\n"
"v_mfma_f32_16x16x32_bf16 v[88:91],a[208:211],v[224:227],v[88:91]\n"
"v_mov_b32_dpp v114,v122 row_ror:8 row_mask:0xf bank_mask:0xc\n"
"v_mov_b32_dpp v115,v123 row_ror:8 row_mask:0xf bank_mask:0xc\n"
"v_mfma_f32_16x16x32_bf16 v[88:91],a[212:215],v[232:235],v[88:91]\n"
"v_mov_b32_dpp v120,v116 row_ror:8 row_mask:0xf bank_mask:0x3\n"
"v_mov_b32_dpp v121,v117 row_ror:8 row_mask:0xf bank_mask:0x3\n"
"v_mfma_f32_16x16x32_bf16 v[88:91],a[216:219],v[240:243],v[88:91]\n"
"v_mov_b32_dpp v122,v118 row_ror:8 row_mask:0xf bank_mask:0x3\n"
"v_mov_b32_dpp v123,v119 row_ror:8 row_mask:0xf bank_mask:0x3\n"
"v_mfma_f32_16x16x32_bf16 v[88:91],a[220:223],v[248:251],v[88:91]\n"
"s_waitcnt lgkmcnt(8)\n"
"v_mfma_f32_16x16x32_bf16 v[76:79],a[224:227],v[128:131],0\n"
"v_mfma_f32_16x16x32_bf16 v[76:79],a[228:231],v[136:139],v[76:79]\n"
"v_mfma_f32_16x16x32_bf16 v[76:79],a[232:235],v[144:147],v[76:79]\n"
"v_mfma_f32_16x16x32_bf16 v[76:79],a[236:239],v[152:155],v[76:79]\n"
"s_waitcnt lgkmcnt(0)\n"
"v_mfma_f32_16x16x32_bf16 v[76:79],a[240:243],v[160:163],v[76:79]\n"
"v_mfma_f32_16x16x32_bf16 v[76:79],a[244:247],v[168:171],v[76:79]\n"
"buffer_store_dwordx4 v[112:115],v6,s[44:47],s86 offen offset:640 sc1\n"
"v_mfma_f32_16x16x32_bf16 v[76:79],a[248:251],v[176:179],v[76:79]\n"
"v_mfma_f32_16x16x32_bf16 v[76:79],a[252:255],v[184:187],v[76:79]\n"
"v_mfma_f32_16x16x32_bf16 v[92:95],a[224:227],v[192:195],0\n"
"v_mfma_f32_16x16x32_bf16 v[92:95],a[228:231],v[200:203],v[92:95]\n"
"v_mfma_f32_16x16x32_bf16 v[92:95],a[232:235],v[208:211],v[92:95]\n"
"v_mfma_f32_16x16x32_bf16 v[92:95],a[236:239],v[216:219],v[92:95]\n"
"v_mfma_f32_16x16x32_bf16 v[92:95],a[240:243],v[224:227],v[92:95]\n"
"v_mfma_f32_16x16x32_bf16 v[92:95],a[244:247],v[232:235],v[92:95]\n"
"buffer_store_dwordx4 v[120:123],v6,s[44:47],s88 offen offset:640 sc1\n"
"v_mfma_f32_16x16x32_bf16 v[92:95],a[248:251],v[240:243],v[92:95]\n"
"v_mfma_f32_16x16x32_bf16 v[92:95],a[252:255],v[248:251],v[92:95]\n"
"v_cvt_pk_bf16_f32 v64,v64,v65\n"
"v_cvt_pk_bf16_f32 v65,v66,v67\n"
"v_cvt_pk_bf16_f32 v66,v68,v69\n"
"v_cvt_pk_bf16_f32 v67,v70,v71\n"
"v_cvt_pk_bf16_f32 v72,v72,v73\n"
"v_cvt_pk_bf16_f32 v73,v74,v75\n"
"v_cvt_pk_bf16_f32 v74,v76,v77\n"
"v_cvt_pk_bf16_f32 v75,v78,v79\n"
"v_mov_b32 v68,v64\n"
"v_mov_b32 v69,v65\n"
"v_mov_b32 v70,v66\n"
"v_mov_b32 v71,v67\n"
"v_mov_b32_dpp v64,v72 row_ror:8 row_mask:0xf bank_mask:0xc\n"
"v_mov_b32_dpp v65,v73 row_ror:8 row_mask:0xf bank_mask:0xc\n"
"v_mov_b32_dpp v66,v74 row_ror:8 row_mask:0xf bank_mask:0xc\n"
"v_mov_b32_dpp v67,v75 row_ror:8 row_mask:0xf bank_mask:0xc\n"
"buffer_store_dwordx4 v[64:67],v6,s[44:47],0 offen offset:768 sc1\n"
"v_mov_b32_dpp v72,v68 row_ror:8 row_mask:0xf bank_mask:0x3\n"
"v_mov_b32_dpp v73,v69 row_ror:8 row_mask:0xf bank_mask:0x3\n"
"v_mov_b32_dpp v74,v70 row_ror:8 row_mask:0xf bank_mask:0x3\n"
"v_mov_b32_dpp v75,v71 row_ror:8 row_mask:0xf bank_mask:0x3\n"
"buffer_store_dwordx4 v[72:75],v6,s[44:47],s87 offen offset:768 sc1\n"
"v_cvt_pk_bf16_f32 v80,v80,v81\n"
"v_cvt_pk_bf16_f32 v81,v82,v83\n"
"v_cvt_pk_bf16_f32 v82,v84,v85\n"
"v_cvt_pk_bf16_f32 v83,v86,v87\n"
"v_cvt_pk_bf16_f32 v88,v88,v89\n"
"v_cvt_pk_bf16_f32 v89,v90,v91\n"
"v_cvt_pk_bf16_f32 v90,v92,v93\n"
"v_cvt_pk_bf16_f32 v91,v94,v95\n"
"v_mov_b32 v84,v80\n"
"v_mov_b32 v85,v81\n"
"v_mov_b32 v86,v82\n"
"v_mov_b32 v87,v83\n"
"v_mov_b32_dpp v80,v88 row_ror:8 row_mask:0xf bank_mask:0xc\n"
"v_mov_b32_dpp v81,v89 row_ror:8 row_mask:0xf bank_mask:0xc\n"
"v_mov_b32_dpp v82,v90 row_ror:8 row_mask:0xf bank_mask:0xc\n"
"v_mov_b32_dpp v83,v91 row_ror:8 row_mask:0xf bank_mask:0xc\n"
"buffer_store_dwordx4 v[80:83],v6,s[44:47],s86 offen offset:768 sc1\n"
"v_mov_b32_dpp v88,v84 row_ror:8 row_mask:0xf bank_mask:0x3\n"
"v_mov_b32_dpp v89,v85 row_ror:8 row_mask:0xf bank_mask:0x3\n"
"v_mov_b32_dpp v90,v86 row_ror:8 row_mask:0xf bank_mask:0x3\n"
"v_mov_b32_dpp v91,v87 row_ror:8 row_mask:0xf bank_mask:0x3\n"
"buffer_store_dwordx4 v[88:91],v6,s[44:47],s88 offen offset:768 sc1\n"
"s_lshl_b32 s76,s54,4\n"
"s_add_u32 s77,s76,s92\n"
"s_lshl_b32 s77,s77,3\n"
"s_add_u32 s84,s77,0x3000\n"
"s_add_u32 s84,s84,s32\n"
"s_addc_u32 s85,s33,0\n"
"s_lshl_b32 s76,s76,3\n"
"s_add_u32 s82,s76,0x3000\n"
"s_add_u32 s82,s82,s32\n"
"s_addc_u32 s83,s33,0\n"
"s_lshl_b32 s48,s54,7\n"
"s_lshl_b32 s76,s92,3\n"
"s_add_u32 s48,s48,s76\n"
"s_lshl_b32 s76,s11,1\n"
"s_add_u32 s49,s48,s76\n"
"s_mul_i32 s76,s49,896\n"
"s_add_u32 s40,s12,s76\n"
"s_addc_u32 s41,s13,0\n"
"s_and_b32 s41,s41,0xffff\n"
"s_mov_b32 s42,0x700\n"
"s_mov_b32 s43,0x20000\n"
"s_sub_u32 s77,0x1c00000,s76\n"
"s_waitcnt vmcnt(0)\n"
"s_barrier\n"
"s_cmp_eq_u32 s11,0\n"
"s_cbranch_scc0 L_np2_%=\n"
"v_mov_b32 v2,s34\n"
"v_mov_b32 v3,s35\n"
"v_mov_b32 v4,0\n"
"s_mov_b64 exec,1\n"
"global_store_dwordx2 v4,v[2:3],s[84:85] sc1\n"
"s_mov_b64 exec,-1\n"
"L_np2_%=:\n"
"s_add_u32 s36,s32,s76\n"
"s_addc_u32 s37,s33,0\n"
"s_add_u32 s36,s36,0x3400000\n"
"s_addc_u32 s37,s37,0\n"
"s_and_b32 s37,s37,0xffff\n"
"s_mov_b32 s38,s77\n"
"s_mov_b32 s39,0x20000\n"
"v_lshlrev_b32 v1,4,v0\n"
"v_lshrrev_b32 v2,2,v0\n"
"v_and_b32 v3,3,v0\n"
"v_lshlrev_b32 v4,6,v2\n"
"v_lshl_add_u32 v4,v3,3,v4\n"
"s_mov_b32 exec_hi,0xffffff\n"
"buffer_load_dwordx2 v[32:33],v4,s[40:43],0 offen offset:0\n"
"buffer_load_dwordx2 v[34:35],v4,s[40:43],0 offen offset:32\n"
"buffer_load_dwordx2 v[36:37],v4,s[40:43],0 offen offset:896\n"
"buffer_load_dwordx2 v[38:39],v4,s[40:43],0 offen offset:928\n"
"s_mov_b64 exec,-1\n"
"s_lshl_b32 s76,s76,1\n"
"s_add_u32 s44,s32,s76\n"
"s_addc_u32 s45,s33,0\n"
"s_add_u32 s44,s44,0x5c00000\n"
"s_addc_u32 s45,s45,0\n"
"s_and_b32 s45,s45,0xffff\n"
"s_mov_b32 s46,0xe00\n"
"s_mov_b32 s47,0x20000\n"
"s_lshr_b32 s76,s48,4\n"
"s_mul_i32 s76,s76,14336\n"
"s_and_b32 s77,s48,8\n"
"s_lshl_b32 s77,s77,4\n"
"s_add_u32 s76,s76,s77\n"
"s_add_u32 s60,s32,s76\n"
"s_addc_u32 s61,s33,0\n"
"s_add_u32 s60,s60,0x6000000\n"
"s_addc_u32 s61,s61,0\n"
"s_and_b32 s61,s61,0xffff\n"
"s_mov_b32 s62,0x3800\n"
"s_mov_b32 s63,0x20000\n"
"s_mov_b32 s50,0x3f80\n"
"s_mov_b32 s51,0x3f800000\n"
"s_lshl_b32 s76,s11,5\n"
"s_add_u32 s76,s76,0x24000\n"
"v_lshl_add_u32 v5,v0,7,s76\n"
"s_lshl_b32 s76,s11,10\n"
"s_add_u32 s76,s76,0x24000\n"
"v_add_u32 v6,s76,v1\n"
"v_lshrrev_b32 v7,5,v0\n"
"v_bfe_u32 v8,v0,3,2\n"
"v_and_b32 v9,7,v0\n"
"v_lshlrev_b32 v7,10,v7\n"
"v_lshl_add_u32 v7,v8,8,v7\n"
"v_lshl_add_u32 v7,v9,4,v7\n"
"s_lshl_b32 s52,s11,11\n"
"s_add_u32 s53,s52,8192\n"
"v_mov_b32 v16,0\n"
"v_mov_b32 v17,0\n"
"v_mov_b32 v18,0\n"
"v_mov_b32 v19,0\n"
"v_mov_b32 v20,0\n"
"v_mov_b32 v21,0\n"
"v_mov_b32 v22,0\n"
"v_mov_b32 v23,0\n"
"v_mov_b32 v24,0\n"
"v_mov_b32 v25,0\n"
"v_mov_b32 v26,0\n"
"v_mov_b32 v27,0\n"
"v_mov_b32 v28,0\n"
"v_mov_b32 v29,0\n"
"v_mov_b32 v30,0\n"
"v_mov_b32 v31,0\n"
"v_and_b32 v10,15,v0\n"
"v_lshlrev_b32 v10,3,v10\n"
"s_mov_b32 s76,0\n"
"L_sl_lp_%=:\n"
"global_load_dwordx2 v[12:13],v10,s[82:83] sc1\n"
"s_add_u32 s76,s76,1\n"
"s_waitcnt vmcnt(0)\n"
"v_cmp_ne_u64 vcc,s[34:35],v[12:13]\n"
"s_nop 0\n"
"s_cbranch_vccz L_sl_ok_%=\n"
"s_sleep 1\n"
"s_cmp_lt_u32 s76,0x20000\n"
"s_cbranch_scc1 L_sl_lp_%=\n"
"L_sl_ok_%=:\n"
"s_mov_b32 exec_hi,0xffffff\n"
"s_mov_b32 s76,0x0\n"
"buffer_load_dwordx4 v[64:67],v1,s[36:39],s76 offen offset:0 sc1\n"
"buffer_load_dwordx4 v[68:71],v1,s[36:39],s76 offen offset:896 sc1\n"
"s_mov_b32 s76,0x1c0000\n"
"buffer_load_dwordx4 v[72:75],v1,s[36:39],s76 offen offset:0 sc1\n"
"buffer_load_dwordx4 v[76:79],v1,s[36:39],s76 offen offset:896 sc1\n"
"s_mov_b32 s76,0x380000\n"
"buffer_load_dwordx4 v[80:83],v1,s[36:39],s76 offen offset:0 sc1\n"
"buffer_load_dwordx4 v[84:87],v1,s[36:39],s76 offen offset:896 sc1\n"
"s_mov_b32 s76,0x540000\n"
"buffer_load_dwordx4 v[88:91],v1,s[36:39],s76 offen offset:0 sc1\n"
"buffer_load_dwordx4 v[92:95],v1,s[36:39],s76 offen offset:896 sc1\n"
"s_mov_b32 s76,0x700000\n"
"buffer_load_dwordx4 v[96:99],v1,s[36:39],s76 offen offset:0 sc1\n"
"buffer_load_dwordx4 v[100:103],v1,s[36:39],s76 offen offset:896 sc1\n"
"s_mov_b32 s76,0x8c0000\n"
"buffer_load_dwordx4 v[104:107],v1,s[36:39],s76 offen offset:0 sc1\n"
"buffer_load_dwordx4 v[108:111],v1,s[36:39],s76 offen offset:896 sc1\n"
"s_mov_b32 s76,0xa80000\n"
"buffer_load_dwordx4 v[112:115],v1,s[36:39],s76 offen offset:0 sc1\n"
"buffer_load_dwordx4 v[116:119],v1,s[36:39],s76 offen offset:896 sc1\n"
"s_mov_b32 s76,0xc40000\n"
"buffer_load_dwordx4 v[120:123],v1,s[36:39],s76 offen offset:0 sc1\n"
"buffer_load_dwordx4 v[124:127],v1,s[36:39],s76 offen offset:896 sc1\n"
"s_mov_b32 s76,0xe00000\n"
"buffer_load_dwordx4 v[128:131],v1,s[36:39],s76 offen offset:0 sc1\n"
"buffer_load_dwordx4 v[132:135],v1,s[36:39],s76 offen offset:896 sc1\n"
"s_mov_b32 s76,0xfc0000\n"
"buffer_load_dwordx4 v[136:139],v1,s[36:39],s76 offen offset:0 sc1\n"
"buffer_load_dwordx4 v[140:143],v1,s[36:39],s76 offen offset:896 sc1\n"
"s_mov_b32 s76,0x1180000\n"
"buffer_load_dwordx4 v[144:147],v1,s[36:39],s76 offen offset:0 sc1\n"
"buffer_load_dwordx4 v[148:151],v1,s[36:39],s76 offen offset:896 sc1\n"
"s_mov_b32 s76,0x1340000\n"
"buffer_load_dwordx4 v[152:155],v1,s[36:39],s76 offen offset:0 sc1\n"
"buffer_load_dwordx4 v[156:159],v1,s[36:39],s76 offen offset:896 sc1\n"
"s_mov_b32 s76,0x1500000\n"
"buffer_load_dwordx4 v[160:163],v1,s[36:39],s76 offen offset:0 sc1\n"
"buffer_load_dwordx4 v[164:167],v1,s[36:39],s76 offen offset:896 sc1\n"
"s_mov_b32 s76,0x16c0000\n"
"buffer_load_dwordx4 v[168:171],v1,s[36:39],s76 offen offset:0 sc1\n"
"buffer_load_dwordx4 v[172:175],v1,s[36:39],s76 offen offset:896 sc1\n"
"s_mov_b32 s76,0x1880000\n"
"buffer_load_dwordx4 v[176:179],v1,s[36:39],s76 offen offset:0 sc1\n"
"buffer_load_dwordx4 v[180:183],v1,s[36:39],s76 offen offset:896 sc1\n"
"s_mov_b32 s76,0x1a40000\n"
"buffer_load_dwordx4 v[184:187],v1,s[36:39],s76 offen offset:0 sc1\n"
"buffer_load_dwordx4 v[188:191],v1,s[36:39],s76 offen offset:896 sc1\n"
"v_dot2_f32_bf16 v16,v32,s50,v16\n"
"v_dot2_f32_bf16 v17,v32,s51,v17\n"
"v_dot2_f32_bf16 v18,v33,s50,v18\n"
"v_dot2_f32_bf16 v19,v33,s51,v19\n"
"v_dot2_f32_bf16 v20,v34,s50,v20\n"
"v_dot2_f32_bf16 v21,v34,s51,v21\n"
"v_dot2_f32_bf16 v22,v35,s50,v22\n"
"v_dot2_f32_bf16 v23,v35,s51,v23\n"
"v_dot2_f32_bf16 v24,v36,s50,v24\n"
"v_dot2_f32_bf16 v25,v36,s51,v25\n"
"v_dot2_f32_bf16 v26,v37,s50,v26\n"
"v_dot2_f32_bf16 v27,v37,s51,v27\n"
"v_dot2_f32_bf16 v28,v38,s50,v28\n"
"v_dot2_f32_bf16 v29,v38,s51,v29\n"
"v_dot2_f32_bf16 v30,v39,s50,v30\n"
"v_dot2_f32_bf16 v31,v39,s51,v31\n"
"s_waitcnt vmcnt(31)\n"
"v_dot2_f32_bf16 v16,v64,s50,v16\n"
"v_dot2_f32_bf16 v17,v64,s51,v17\n"
"v_dot2_f32_bf16 v18,v65,s50,v18\n"
"v_dot2_f32_bf16 v19,v65,s51,v19\n"
"v_dot2_f32_bf16 v20,v66,s50,v20\n"
"v_dot2_f32_bf16 v21,v66,s51,v21\n"
"v_dot2_f32_bf16 v22,v67,s50,v22\n"
"v_dot2_f32_bf16 v23,v67,s51,v23\n"
"s_waitcnt vmcnt(30)\n"
"v_dot2_f32_bf16 v24,v68,s50,v24\n"
"v_dot2_f32_bf16 v25,v68,s51,v25\n"
"v_dot2_f32_bf16 v26,v69,s50,v26\n"
"v_dot2_f32_bf16 v27,v69,s51,v27\n"
"v_dot2_f32_bf16 v28,v70,s50,v28\n"
"v_dot2_f32_bf16 v29,v70,s51,v29\n"
"v_dot2_f32_bf16 v30,v71,s50,v30\n"
"v_dot2_f32_bf16 v31,v71,s51,v31\n"
"s_waitcnt vmcnt(29)\n"
"v_dot2_f32_bf16 v16,v72,s50,v16\n"
"v_dot2_f32_bf16 v17,v72,s51,v17\n"
"v_dot2_f32_bf16 v18,v73,s50,v18\n"
"v_dot2_f32_bf16 v19,v73,s51,v19\n"
"v_dot2_f32_bf16 v20,v74,s50,v20\n"
"v_dot2_f32_bf16 v21,v74,s51,v21\n"
"v_dot2_f32_bf16 v22,v75,s50,v22\n"
"v_dot2_f32_bf16 v23,v75,s51,v23\n"
"s_waitcnt vmcnt(28)\n"
"v_dot2_f32_bf16 v24,v76,s50,v24\n"
"v_dot2_f32_bf16 v25,v76,s51,v25\n"
"v_dot2_f32_bf16 v26,v77,s50,v26\n"
"v_dot2_f32_bf16 v27,v77,s51,v27\n"
"v_dot2_f32_bf16 v28,v78,s50,v28\n"
"v_dot2_f32_bf16 v29,v78,s51,v29\n"
"v_dot2_f32_bf16 v30,v79,s50,v30\n"
"v_dot2_f32_bf16 v31,v79,s51,v31\n"
"s_waitcnt vmcnt(27)\n"
"v_dot2_f32_bf16 v16,v80,s50,v16\n"
"v_dot2_f32_bf16 v17,v80,s51,v17\n"
"v_dot2_f32_bf16 v18,v81,s50,v18\n"
"v_dot2_f32_bf16 v19,v81,s51,v19\n"
"v_dot2_f32_bf16 v20,v82,s50,v20\n"
"v_dot2_f32_bf16 v21,v82,s51,v21\n"
"v_dot2_f32_bf16 v22,v83,s50,v22\n"
"v_dot2_f32_bf16 v23,v83,s51,v23\n"
"s_waitcnt vmcnt(26)\n"
"v_dot2_f32_bf16 v24,v84,s50,v24\n"
"v_dot2_f32_bf16 v25,v84,s51,v25\n"
"v_dot2_f32_bf16 v26,v85,s50,v26\n"
"v_dot2_f32_bf16 v27,v85,s51,v27\n"
"v_dot2_f32_bf16 v28,v86,s50,v28\n"
"v_dot2_f32_bf16 v29,v86,s51,v29\n"
"v_dot2_f32_bf16 v30,v87,s50,v30\n"
"v_dot2_f32_bf16 v31,v87,s51,v31\n"
"s_waitcnt vmcnt(25)\n"
"v_dot2_f32_bf16 v16,v88,s50,v16\n"
"v_dot2_f32_bf16 v17,v88,s51,v17\n"
"v_dot2_f32_bf16 v18,v89,s50,v18\n"
"v_dot2_f32_bf16 v19,v89,s51,v19\n"
"v_dot2_f32_bf16 v20,v90,s50,v20\n"
"v_dot2_f32_bf16 v21,v90,s51,v21\n"
"v_dot2_f32_bf16 v22,v91,s50,v22\n"
"v_dot2_f32_bf16 v23,v91,s51,v23\n"
"s_waitcnt vmcnt(24)\n"
"v_dot2_f32_bf16 v24,v92,s50,v24\n"
"v_dot2_f32_bf16 v25,v92,s51,v25\n"
"v_dot2_f32_bf16 v26,v93,s50,v26\n"
"v_dot2_f32_bf16 v27,v93,s51,v27\n"
"v_dot2_f32_bf16 v28,v94,s50,v28\n"
"v_dot2_f32_bf16 v29,v94,s51,v29\n"
"v_dot2_f32_bf16 v30,v95,s50,v30\n"
"v_dot2_f32_bf16 v31,v95,s51,v31\n"
"s_waitcnt vmcnt(23)\n"
"v_dot2_f32_bf16 v16,v96,s50,v16\n"
"v_dot2_f32_bf16 v17,v96,s51,v17\n"
"v_dot2_f32_bf16 v18,v97,s50,v18\n"
"v_dot2_f32_bf16 v19,v97,s51,v19\n"
"v_dot2_f32_bf16 v20,v98,s50,v20\n"
"v_dot2_f32_bf16 v21,v98,s51,v21\n"
"v_dot2_f32_bf16 v22,v99,s50,v22\n"
"v_dot2_f32_bf16 v23,v99,s51,v23\n"
"s_waitcnt vmcnt(22)\n"
"v_dot2_f32_bf16 v24,v100,s50,v24\n"
"v_dot2_f32_bf16 v25,v100,s51,v25\n"
"v_dot2_f32_bf16 v26,v101,s50,v26\n"
"v_dot2_f32_bf16 v27,v101,s51,v27\n"
"v_dot2_f32_bf16 v28,v102,s50,v28\n"
"v_dot2_f32_bf16 v29,v102,s51,v29\n"
"v_dot2_f32_bf16 v30,v103,s50,v30\n"
"v_dot2_f32_bf16 v31,v103,s51,v31\n"
"s_waitcnt vmcnt(21)\n"
"v_dot2_f32_bf16 v16,v104,s50,v16\n"
"v_dot2_f32_bf16 v17,v104,s51,v17\n"
"v_dot2_f32_bf16 v18,v105,s50,v18\n"
"v_dot2_f32_bf16 v19,v105,s51,v19\n"
"v_dot2_f32_bf16 v20,v106,s50,v20\n"
"v_dot2_f32_bf16 v21,v106,s51,v21\n"
"v_dot2_f32_bf16 v22,v107,s50,v22\n"
"v_dot2_f32_bf16 v23,v107,s51,v23\n"
"s_waitcnt vmcnt(20)\n"
"v_dot2_f32_bf16 v24,v108,s50,v24\n"
"v_dot2_f32_bf16 v25,v108,s51,v25\n"
"v_dot2_f32_bf16 v26,v109,s50,v26\n"
"v_dot2_f32_bf16 v27,v109,s51,v27\n"
"v_dot2_f32_bf16 v28,v110,s50,v28\n"
"v_dot2_f32_bf16 v29,v110,s51,v29\n"
"v_dot2_f32_bf16 v30,v111,s50,v30\n"
"v_dot2_f32_bf16 v31,v111,s51,v31\n"
"s_waitcnt vmcnt(19)\n"
"v_dot2_f32_bf16 v16,v112,s50,v16\n"
"v_dot2_f32_bf16 v17,v112,s51,v17\n"
"v_dot2_f32_bf16 v18,v113,s50,v18\n"
"v_dot2_f32_bf16 v19,v113,s51,v19\n"
"v_dot2_f32_bf16 v20,v114,s50,v20\n"
"v_dot2_f32_bf16 v21,v114,s51,v21\n"
"v_dot2_f32_bf16 v22,v115,s50,v22\n"
"v_dot2_f32_bf16 v23,v115,s51,v23\n"
"s_waitcnt vmcnt(18)\n"
"v_dot2_f32_bf16 v24,v116,s50,v24\n"
"v_dot2_f32_bf16 v25,v116,s51,v25\n"
"v_dot2_f32_bf16 v26,v117,s50,v26\n"
"v_dot2_f32_bf16 v27,v117,s51,v27\n"
"v_dot2_f32_bf16 v28,v118,s50,v28\n"
"v_dot2_f32_bf16 v29,v118,s51,v29\n"
"v_dot2_f32_bf16 v30,v119,s50,v30\n"
"v_dot2_f32_bf16 v31,v119,s51,v31\n"
"s_waitcnt vmcnt(17)\n"
"v_dot2_f32_bf16 v16,v120,s50,v16\n"
"v_dot2_f32_bf16 v17,v120,s51,v17\n"
"v_dot2_f32_bf16 v18,v121,s50,v18\n"
"v_dot2_f32_bf16 v19,v121,s51,v19\n"
"v_dot2_f32_bf16 v20,v122,s50,v20\n"
"v_dot2_f32_bf16 v21,v122,s51,v21\n"
"v_dot2_f32_bf16 v22,v123,s50,v22\n"
"v_dot2_f32_bf16 v23,v123,s51,v23\n"
"s_waitcnt vmcnt(16)\n"
"v_dot2_f32_bf16 v24,v124,s50,v24\n"
"v_dot2_f32_bf16 v25,v124,s51,v25\n"
"v_dot2_f32_bf16 v26,v125,s50,v26\n"
"v_dot2_f32_bf16 v27,v125,s51,v27\n"
"v_dot2_f32_bf16 v28,v126,s50,v28\n"
"v_dot2_f32_bf16 v29,v126,s51,v29\n"
"v_dot2_f32_bf16 v30,v127,s50,v30\n"
"v_dot2_f32_bf16 v31,v127,s51,v31\n"
"s_waitcnt vmcnt(15)\n"
"v_dot2_f32_bf16 v16,v128,s50,v16\n"
"v_dot2_f32_bf16 v17,v128,s51,v17\n"
"v_dot2_f32_bf16 v18,v129,s50,v18\n"
"v_dot2_f32_bf16 v19,v129,s51,v19\n"
"v_dot2_f32_bf16 v20,v130,s50,v20\n"
"v_dot2_f32_bf16 v21,v130,s51,v21\n"
"v_dot2_f32_bf16 v22,v131,s50,v22\n"
"v_dot2_f32_bf16 v23,v131,s51,v23\n"
"s_waitcnt vmcnt(14)\n"
"v_dot2_f32_bf16 v24,v132,s50,v24\n"
"v_dot2_f32_bf16 v25,v132,s51,v25\n"
"v_dot2_f32_bf16 v26,v133,s50,v26\n"
"v_dot2_f32_bf16 v27,v133,s51,v27\n"
"v_dot2_f32_bf16 v28,v134,s50,v28\n"
"v_dot2_f32_bf16 v29,v134,s51,v29\n"
"v_dot2_f32_bf16 v30,v135,s50,v30\n"
"v_dot2_f32_bf16 v31,v135,s51,v31\n"
"s_waitcnt vmcnt(13)\n"
"v_dot2_f32_bf16 v16,v136,s50,v16\n"
"v_dot2_f32_bf16 v17,v136,s51,v17\n"
"v_dot2_f32_bf16 v18,v137,s50,v18\n"
"v_dot2_f32_bf16 v19,v137,s51,v19\n"
"v_dot2_f32_bf16 v20,v138,s50,v20\n"
"v_dot2_f32_bf16 v21,v138,s51,v21\n"
"v_dot2_f32_bf16 v22,v139,s50,v22\n"
"v_dot2_f32_bf16 v23,v139,s51,v23\n"
"s_waitcnt vmcnt(12)\n"
"v_dot2_f32_bf16 v24,v140,s50,v24\n"
"v_dot2_f32_bf16 v25,v140,s51,v25\n"
"v_dot2_f32_bf16 v26,v141,s50,v26\n"
"v_dot2_f32_bf16 v27,v141,s51,v27\n"
"v_dot2_f32_bf16 v28,v142,s50,v28\n"
"v_dot2_f32_bf16 v29,v142,s51,v29\n"
"v_dot2_f32_bf16 v30,v143,s50,v30\n"
"v_dot2_f32_bf16 v31,v143,s51,v31\n"
"s_waitcnt vmcnt(11)\n"
"v_dot2_f32_bf16 v16,v144,s50,v16\n"
"v_dot2_f32_bf16 v17,v144,s51,v17\n"
"v_dot2_f32_bf16 v18,v145,s50,v18\n"
"v_dot2_f32_bf16 v19,v145,s51,v19\n"
"v_dot2_f32_bf16 v20,v146,s50,v20\n"
"v_dot2_f32_bf16 v21,v146,s51,v21\n"
"v_dot2_f32_bf16 v22,v147,s50,v22\n"
"v_dot2_f32_bf16 v23,v147,s51,v23\n"
"s_waitcnt vmcnt(10)\n"
"v_dot2_f32_bf16 v24,v148,s50,v24\n"
"v_dot2_f32_bf16 v25,v148,s51,v25\n"
"v_dot2_f32_bf16 v26,v149,s50,v26\n"
"v_dot2_f32_bf16 v27,v149,s51,v27\n"
"v_dot2_f32_bf16 v28,v150,s50,v28\n"
"v_dot2_f32_bf16 v29,v150,s51,v29\n"
"v_dot2_f32_bf16 v30,v151,s50,v30\n"
"v_dot2_f32_bf16 v31,v151,s51,v31\n"
"s_waitcnt vmcnt(9)\n"
"v_dot2_f32_bf16 v16,v152,s50,v16\n"
"v_dot2_f32_bf16 v17,v152,s51,v17\n"
"v_dot2_f32_bf16 v18,v153,s50,v18\n"
"v_dot2_f32_bf16 v19,v153,s51,v19\n"
"v_dot2_f32_bf16 v20,v154,s50,v20\n"
"v_dot2_f32_bf16 v21,v154,s51,v21\n"
"v_dot2_f32_bf16 v22,v155,s50,v22\n"
"v_dot2_f32_bf16 v23,v155,s51,v23\n"
"s_waitcnt vmcnt(8)\n"
"v_dot2_f32_bf16 v24,v156,s50,v24\n"
"v_dot2_f32_bf16 v25,v156,s51,v25\n"
"v_dot2_f32_bf16 v26,v157,s50,v26\n"
"v_dot2_f32_bf16 v27,v157,s51,v27\n"
"v_dot2_f32_bf16 v28,v158,s50,v28\n"
"v_dot2_f32_bf16 v29,v158,s51,v29\n"
"v_dot2_f32_bf16 v30,v159,s50,v30\n"
"v_dot2_f32_bf16 v31,v159,s51,v31\n"
"s_waitcnt vmcnt(7)\n"
"v_dot2_f32_bf16 v16,v160,s50,v16\n"
"v_dot2_f32_bf16 v17,v160,s51,v17\n"
"v_dot2_f32_bf16 v18,v161,s50,v18\n"
"v_dot2_f32_bf16 v19,v161,s51,v19\n"
"v_dot2_f32_bf16 v20,v162,s50,v20\n"
"v_dot2_f32_bf16 v21,v162,s51,v21\n"
"v_dot2_f32_bf16 v22,v163,s50,v22\n"
"v_dot2_f32_bf16 v23,v163,s51,v23\n"
"s_waitcnt vmcnt(6)\n"
"v_dot2_f32_bf16 v24,v164,s50,v24\n"
"v_dot2_f32_bf16 v25,v164,s51,v25\n"
"v_dot2_f32_bf16 v26,v165,s50,v26\n"
"v_dot2_f32_bf16 v27,v165,s51,v27\n"
"v_dot2_f32_bf16 v28,v166,s50,v28\n"
"v_dot2_f32_bf16 v29,v166,s51,v29\n"
"v_dot2_f32_bf16 v30,v167,s50,v30\n"
"v_dot2_f32_bf16 v31,v167,s51,v31\n"
"s_waitcnt vmcnt(5)\n"
"v_dot2_f32_bf16 v16,v168,s50,v16\n"
"v_dot2_f32_bf16 v17,v168,s51,v17\n"
"v_dot2_f32_bf16 v18,v169,s50,v18\n"
"v_dot2_f32_bf16 v19,v169,s51,v19\n"
"v_dot2_f32_bf16 v20,v170,s50,v20\n"
"v_dot2_f32_bf16 v21,v170,s51,v21\n"
"v_dot2_f32_bf16 v22,v171,s50,v22\n"
"v_dot2_f32_bf16 v23,v171,s51,v23\n"
"s_waitcnt vmcnt(4)\n"
"v_dot2_f32_bf16 v24,v172,s50,v24\n"
"v_dot2_f32_bf16 v25,v172,s51,v25\n"
"v_dot2_f32_bf16 v26,v173,s50,v26\n"
"v_dot2_f32_bf16 v27,v173,s51,v27\n"
"v_dot2_f32_bf16 v28,v174,s50,v28\n"
"v_dot2_f32_bf16 v29,v174,s51,v29\n"
"v_dot2_f32_bf16 v30,v175,s50,v30\n"
"v_dot2_f32_bf16 v31,v175,s51,v31\n"
"s_waitcnt vmcnt(3)\n"
"v_dot2_f32_bf16 v16,v176,s50,v16\n"
"v_dot2_f32_bf16 v17,v176,s51,v17\n"
"v_dot2_f32_bf16 v18,v177,s50,v18\n"
"v_dot2_f32_bf16 v19,v177,s51,v19\n"
"v_dot2_f32_bf16 v20,v178,s50,v20\n"
"v_dot2_f32_bf16 v21,v178,s51,v21\n"
"v_dot2_f32_bf16 v22,v179,s50,v22\n"
"v_dot2_f32_bf16 v23,v179,s51,v23\n"
"s_waitcnt vmcnt(2)\n"
"v_dot2_f32_bf16 v24,v180,s50,v24\n"
"v_dot2_f32_bf16 v25,v180,s51,v25\n"
"v_dot2_f32_bf16 v26,v181,s50,v26\n"
"v_dot2_f32_bf16 v27,v181,s51,v27\n"
"v_dot2_f32_bf16 v28,v182,s50,v28\n"
"v_dot2_f32_bf16 v29,v182,s51,v29\n"
"v_dot2_f32_bf16 v30,v183,s50,v30\n"
"v_dot2_f32_bf16 v31,v183,s51,v31\n"
"s_waitcnt vmcnt(1)\n"
"v_dot2_f32_bf16 v16,v184,s50,v16\n"
"v_dot2_f32_bf16 v17,v184,s51,v17\n"
"v_dot2_f32_bf16 v18,v185,s50,v18\n"
"v_dot2_f32_bf16 v19,v185,s51,v19\n"
"v_dot2_f32_bf16 v20,v186,s50,v20\n"
"v_dot2_f32_bf16 v21,v186,s51,v21\n"
"v_dot2_f32_bf16 v22,v187,s50,v22\n"
"v_dot2_f32_bf16 v23,v187,s51,v23\n"
"s_waitcnt vmcnt(0)\n"
"v_dot2_f32_bf16 v24,v188,s50,v24\n"
"v_dot2_f32_bf16 v25,v188,s51,v25\n"
"v_dot2_f32_bf16 v26,v189,s50,v26\n"
"v_dot2_f32_bf16 v27,v189,s51,v27\n"
"v_dot2_f32_bf16 v28,v190,s50,v28\n"
"v_dot2_f32_bf16 v29,v190,s51,v29\n"
"v_dot2_f32_bf16 v30,v191,s50,v30\n"
"v_dot2_f32_bf16 v31,v191,s51,v31\n"
"buffer_store_dwordx4 v[16:19],v1,s[44:47],0 offen offset:0 sc1\n"
"buffer_store_dwordx4 v[20:23],v1,s[44:47],0 offen offset:896 sc1\n"
"buffer_store_dwordx4 v[24:27],v1,s[44:47],0 offen offset:1792 sc1\n"
"buffer_store_dwordx4 v[28:31],v1,s[44:47],0 offen offset:2688 sc1\n"
"s_mov_b64 exec,-1\n"
"v_add_f32 v80,v16,v17\n"
"v_add_f32 v81,v24,v25\n"
"v_add_f32 v80,v80,v18\n"
"v_add_f32 v81,v81,v26\n"
"v_add_f32 v80,v80,v19\n"
"v_add_f32 v81,v81,v27\n"
"v_add_f32 v80,v80,v20\n"
"v_add_f32 v81,v81,v28\n"
"v_add_f32 v80,v80,v21\n"
"v_add_f32 v81,v81,v29\n"
"v_add_f32 v80,v80,v22\n"
"v_add_f32 v81,v81,v30\n"
"v_add_f32 v80,v80,v23\n"
"v_add_f32 v81,v81,v31\n"
"s_nop 0\n"
"v_add_f32_dpp v80,v80,v80 row_ror:1 row_mask:0xf bank_mask:0xf\n"
"v_add_f32_dpp v81,v81,v81 row_ror:1 row_mask:0xf bank_mask:0xf\n"
"s_nop 0\n"
"v_add_f32_dpp v80,v80,v80 row_ror:2 row_mask:0xf bank_mask:0xf\n"
"v_add_f32_dpp v81,v81,v81 row_ror:2 row_mask:0xf bank_mask:0xf\n"
"s_nop 0\n"
"v_add_f32_dpp v80,v80,v80 row_ror:4 row_mask:0xf bank_mask:0xf\n"
"v_add_f32_dpp v81,v81,v81 row_ror:4 row_mask:0xf bank_mask:0xf\n"
"s_nop 0\n"
"v_add_f32_dpp v80,v80,v80 row_ror:8 row_mask:0xf bank_mask:0xf\n"
"v_add_f32_dpp v81,v81,v81 row_ror:8 row_mask:0xf bank_mask:0xf\n"
"v_mov_b32 v84,v80\n"
"v_mov_b32 v85,v81\n"
"s_nop 0\n"
"v_permlane32_swap_b32 v80,v84\n"
"v_permlane32_swap_b32 v81,v85\n"
"v_add_f32 v80,v80,v84\n"
"v_add_f32 v81,v81,v85\n"
"v_mov_b32 v84,v80\n"
"v_mov_b32 v85,v81\n"
"s_nop 0\n"
"v_permlane16_swap_b32 v80,v84\n"
"v_permlane16_swap_b32 v81,v85\n"
"v_add_f32 v80,v80,v84\n"
"v_add_f32 v81,v81,v85\n"
"v_mul_f32 v80,0x3b124925,v80\n"
"v_mul_f32 v81,0x3b124925,v81\n"
"v_mov_b32 v82,0\n"
"v_mov_b32 v83,0\n"
"s_mov_b32 exec_hi,0xffffff\n"
"v_sub_f32 v64,v16,v80\n"
"v_sub_f32 v72,v24,v81\n"
"v_sub_f32 v65,v17,v80\n"
"v_sub_f32 v73,v25,v81\n"
"v_sub_f32 v66,v18,v80\n"
"v_sub_f32 v74,v26,v81\n"
"v_sub_f32 v67,v19,v80\n"
"v_sub_f32 v75,v27,v81\n"
"v_sub_f32 v68,v20,v80\n"
"v_sub_f32 v76,v28,v81\n"
"v_sub_f32 v69,v21,v80\n"
"v_sub_f32 v77,v29,v81\n"
"v_sub_f32 v70,v22,v80\n"
"v_sub_f32 v78,v30,v81\n"
"v_sub_f32 v71,v23,v80\n"
"v_sub_f32 v79,v31,v81\n"
"v_fmac_f32 v82,v64,v64\n"
"v_fmac_f32 v83,v72,v72\n"
"v_fmac_f32 v82,v65,v65\n"
"v_fmac_f32 v83,v73,v73\n"
"v_fmac_f32 v82,v66,v66\n"
"v_fmac_f32 v83,v74,v74\n"
"v_fmac_f32 v82,v67,v67\n"
"v_fmac_f32 v83,v75,v75\n"
"v_fmac_f32 v82,v68,v68\n"
"v_fmac_f32 v83,v76,v76\n"
"v_fmac_f32 v82,v69,v69\n"
"v_fmac_f32 v83,v77,v77\n"
"v_fmac_f32 v82,v70,v70\n"
"v_fmac_f32 v83,v78,v78\n"
"v_fmac_f32 v82,v71,v71\n"
"v_fmac_f32 v83,v79,v79\n"
"s_mov_b64 exec,-1\n"
"v_add_f32_dpp v82,v82,v82 row_ror:1 row_mask:0xf bank_mask:0xf\n"
"v_add_f32_dpp v83,v83,v83 row_ror:1 row_mask:0xf bank_mask:0xf\n"
"s_nop 0\n"
"v_add_f32_dpp v82,v82,v82 row_ror:2 row_mask:0xf bank_mask:0xf\n"
"v_add_f32_dpp v83,v83,v83 row_ror:2 row_mask:0xf bank_mask:0xf\n"
"s_nop 0\n"
"v_add_f32_dpp v82,v82,v82 row_ror:4 row_mask:0xf bank_mask:0xf\n"
"v_add_f32_dpp v83,v83,v83 row_ror:4 row_mask:0xf bank_mask:0xf\n"
"s_nop 0\n"
"v_add_f32_dpp v82,v82,v82 row_ror:8 row_mask:0xf bank_mask:0xf\n"
"v_add_f32_dpp v83,v83,v83 row_ror:8 row_mask:0xf bank_mask:0xf\n"
"v_mov_b32 v84,v82\n"
"v_mov_b32 v85,v83\n"
"s_nop 0\n"
"v_permlane32_swap_b32 v82,v84\n"
"v_permlane32_swap_b32 v83,v85\n"
"v_add_f32 v82,v82,v84\n"
"v_add_f32 v83,v83,v85\n"
"v_mov_b32 v84,v82\n"
"v_mov_b32 v85,v83\n"
"s_nop 0\n"
"v_permlane16_swap_b32 v82,v84\n"
"v_permlane16_swap_b32 v83,v85\n"
"v_add_f32 v82,v82,v84\n"
"v_add_f32 v83,v83,v85\n"
"v_mov_b32 v80,0x3727c5ac\n"
"v_mov_b32 v81,0x3727c5ac\n"
"v_fmac_f32 v80,0x3b124925,v82\n"
"v_fmac_f32 v81,0x3b124925,v83\n"
"v_rsq_f32 v80,v80\n"
"v_rsq_f32 v81,v81\n"
"v_mul_f32 v64,v64,v80\n"
"v_mul_f32 v72,v72,v81\n"
"v_mul_f32 v65,v65,v80\n"
"v_mul_f32 v73,v73,v81\n"
"v_mul_f32 v66,v66,v80\n"
"v_mul_f32 v74,v74,v81\n"
"v_mul_f32 v67,v67,v80\n"
"v_mul_f32 v75,v75,v81\n"
"v_mul_f32 v68,v68,v80\n"
"v_mul_f32 v76,v76,v81\n"
"v_mul_f32 v69,v69,v80\n"
"v_mul_f32 v77,v77,v81\n"
"v_mul_f32 v70,v70,v80\n"
"v_mul_f32 v78,v78,v81\n"
"v_mul_f32 v71,v71,v80\n"
"v_mul_f32 v79,v79,v81\n"
"v_cvt_pk_bf16_f32 v64,v64,v65\n"
"v_cvt_pk_bf16_f32 v72,v72,v73\n"
"v_cvt_pk_bf16_f32 v65,v66,v67\n"
"v_cvt_pk_bf16_f32 v73,v74,v75\n"
"v_cvt_pk_bf16_f32 v66,v68,v69\n"
"v_cvt_pk_bf16_f32 v74,v76,v77\n"
"v_cvt_pk_bf16_f32 v67,v70,v71\n"
"v_cvt_pk_bf16_f32 v75,v78,v79\n"
"s_mov_b32 exec_hi,0xffffff\n"
"ds_write_b128 v5,v[64:67] offset:0\n"
"ds_write_b128 v5,v[72:75] offset:16\n"
"s_mov_b64 exec,-1\n"
"s_waitcnt lgkmcnt(0)\n"
"s_barrier\n"
"ds_read_b128 v[64:67],v6\n"
"ds_read_b128 v[68:71],v6 offset:4096\n"
"s_waitcnt lgkmcnt(1)\n"
"buffer_store_dwordx4 v[64:67],v7,s[60:63],s52 offen sc1\n"
"s_cmp_eq_u32 s11,3\n"
"s_cbranch_scc1 L_r1s_%=\n"
"s_waitcnt lgkmcnt(0)\n"
"buffer_store_dwordx4 v[68:71],v7,s[60:63],s53 offen sc1\n"
"L_r1s_%=:\n"
"s_mov_b32 s76,s48\n"
"s_add_u32 s84,s76,0x4000\n"
"s_add_u32 s84,s84,s32\n"
"s_addc_u32 s85,s33,0\n"
"s_lshr_b32 s48,s92,2\n"
"s_xor_b32 s76,s94,1\n"
"s_lshl_b32 s76,s76,2\n"
"s_add_u32 s48,s48,s76\n"
"s_bfe_u32 s49,s92,0x10001\n"
"s_mul_i32 s49,s49,14\n"
"s_add_u32 s49,s49,s95\n"
"s_lshl_b32 s76,s48,8\n"
"s_add_u32 s82,s76,0x4000\n"
"s_add_u32 s82,s82,s32\n"
"s_addc_u32 s83,s33,0\n"
"s_lshl_b32 s78,s48,2\n"
"s_add_u32 s78,s78,s11\n"
"s_mul_i32 s76,s78,57344\n"
"s_add_u32 s44,s32,s76\n"
"s_addc_u32 s45,s33,0\n"
"s_add_u32 s44,s44,0x6000000\n"
"s_addc_u32 s45,s45,0\n"
"s_and_b32 s45,s45,0xffff\n"
"s_mov_b32 s46,0xe000\n"
"s_mov_b32 s47,0x20000\n"
"s_waitcnt vmcnt(0)\n"
"s_barrier\n"
"s_cmp_eq_u32 s11,0\n"
"s_cbranch_scc0 L_np3_%=\n"
"v_mov_b32 v2,s34\n"
"v_mov_b32 v3,s35\n"
"v_mov_b32 v4,0\n"
"s_mov_b64 exec,1\n"
"global_store_dwordx2 v4,v[2:3],s[84:85] sc1\n"
"s_mov_b64 exec,-1\n"
"L_np3_%=:\n"
"s_cmp_lt_u32 s95,14\n"
"s_cbranch_scc0 L_done_%=\n"
"s_mov_b32 s59,0x0\n"
"s_add_u32 m0,s96,0x0\n"
"s_nop 0\n"
"buffer_load_dwordx4 v60,s[68:71],s59 offen offset:0 lds\n"
"buffer_load_dwordx4 v61,s[68:71],s59 offen offset:1024 lds\n"
"s_add_u32 m0,s96,0x2000\n"
"s_nop 0\n"
"buffer_load_dwordx4 v60,s[72:75],s59 offen offset:0 lds\n"
"buffer_load_dwordx4 v61,s[72:75],s59 offen offset:1024 lds\n"
"s_mov_b32 s59,0x38000\n"
"s_add_u32 m0,s96,0x4000\n"
"s_nop 0\n"
"buffer_load_dwordx4 v60,s[68:71],s59 offen offset:0 lds\n"
"buffer_load_dwordx4 v61,s[68:71],s59 offen offset:1024 lds\n"
"s_add_u32 m0,s96,0x6000\n"
"s_nop 0\n"
"buffer_load_dwordx4 v60,s[72:75],s59 offen offset:0 lds\n"
"buffer_load_dwordx4 v61,s[72:75],s59 offen offset:1024 lds\n"
"s_mov_b32 s59,0x70000\n"
"s_add_u32 m0,s96,0x8000\n"
"s_nop 0\n"
"buffer_load_dwordx4 v60,s[68:71],s59 offen offset:0 lds\n"
"buffer_load_dwordx4 v61,s[68:71],s59 offen offset:1024 lds\n"
"s_add_u32 m0,s96,0xa000\n"
"s_nop 0\n"
"buffer_load_dwordx4 v60,s[72:75],s59 offen offset:0 lds\n"
"buffer_load_dwordx4 v61,s[72:75],s59 offen offset:1024 lds\n"
"s_mov_b32 s59,0xa8000\n"
"s_add_u32 m0,s96,0xc000\n"
"s_nop 0\n"
"buffer_load_dwordx4 v60,s[68:71],s59 offen offset:0 lds\n"
"buffer_load_dwordx4 v61,s[68:71],s59 offen offset:1024 lds\n"
"s_add_u32 m0,s96,0xe000\n"
"s_nop 0\n"
"buffer_load_dwordx4 v60,s[72:75],s59 offen offset:0 lds\n"
"buffer_load_dwordx4 v61,s[72:75],s59 offen offset:1024 lds\n"
"s_mov_b32 s59,0xe0000\n"
"s_add_u32 m0,s96,0x18000\n"
"s_nop 0\n"
"buffer_load_dwordx4 v60,s[68:71],s59 offen offset:0 lds\n"
"buffer_load_dwordx4 v61,s[68:71],s59 offen offset:1024 lds\n"
"s_add_u32 m0,s96,0x1a000\n"
"s_nop 0\n"
"buffer_load_dwordx4 v60,s[72:75],s59 offen offset:0 lds\n"
"buffer_load_dwordx4 v61,s[72:75],s59 offen offset:1024 lds\n"
"s_mov_b32 s59,0x118000\n"
"s_add_u32 m0,s96,0x1c000\n"
"s_nop 0\n"
"buffer_load_dwordx4 v60,s[68:71],s59 offen offset:0 lds\n"
"buffer_load_dwordx4 v61,s[68:71],s59 offen offset:1024 lds\n"
"s_add_u32 m0,s96,0x1e000\n"
"s_nop 0\n"
"buffer_load_dwordx4 v60,s[72:75],s59 offen offset:0 lds\n"
"buffer_load_dwordx4 v61,s[72:75],s59 offen offset:1024 lds\n"
"s_mov_b32 s59,0x150000\n"
"s_add_u32 m0,s96,0x20000\n"
"s_nop 0\n"
"buffer_load_dwordx4 v60,s[68:71],s59 offen offset:0 lds\n"
"buffer_load_dwordx4 v61,s[68:71],s59 offen offset:1024 lds\n"
"s_add_u32 m0,s96,0x22000\n"
"s_nop 0\n"
"buffer_load_dwordx4 v60,s[72:75],s59 offen offset:0 lds\n"
"buffer_load_dwordx4 v61,s[72:75],s59 offen offset:1024 lds\n"
"v_lshlrev_b32 v8,4,v0\n"
"v_bfe_u32 v1,v0,2,4\n"
"v_bfe_u32 v3,v0,3,2\n"
"v_and_b32 v4,3,v0\n"
"v_lshlrev_b32 v1,7,v1\n"
"v_lshl_add_u32 v1,v4,3,v1\n"
"v_xor_b32 v4,0,v3\n"
"v_lshl_add_u32 v12,v4,5,v1\n"
"v_xor_b32 v4,1,v3\n"
"v_lshl_add_u32 v13,v4,5,v1\n"
"v_xor_b32 v4,2,v3\n"
"v_lshl_add_u32 v14,v4,5,v1\n"
"v_xor_b32 v4,3,v3\n"
"v_lshl_add_u32 v15,v4,5,v1\n"
"v_add_u32 v16,0x10000,v12\n"
"v_add_u32 v20,0x20000,v12\n"
"v_add_u32 v17,0x10000,v13\n"
"v_add_u32 v21,0x20000,v13\n"
"v_add_u32 v18,0x10000,v14\n"
"v_add_u32 v22,0x20000,v14\n"
"v_add_u32 v19,0x10000,v15\n"
"v_add_u32 v23,0x20000,v15\n"
"s_mul_i32 s76,s78,229376\n"
"s_lshl_b32 s77,s49,11\n"
"s_add_u32 s76,s76,s77\n"
"s_add_u32 s60,s32,s76\n"
"s_addc_u32 s61,s33,0\n"
"s_add_u32 s60,s60,0x6200000\n"
"s_addc_u32 s61,s61,0\n"
"s_and_b32 s61,s61,0xffff\n"
"s_mov_b32 s62,0x2a800\n"
"s_mov_b32 s63,0x20000\n"
"s_mov_b32 s86,0x0\n"
"s_mov_b32 s87,0x3800\n"
"s_mov_b32 s88,0x7000\n"
"s_mov_b32 s89,0xa800\n"
"s_cmp_ge_u32 s95,7\n"
"s_cselect_b32 s76,1,0\n"
"s_cselect_b32 s77,7,0\n"
"s_sub_u32 s51,s95,s77\n"
"s_and_b32 s50,s92,14\n"
"s_add_u32 s50,s50,s76\n"
"s_xor_b32 s76,s94,1\n"
"s_lshl_b32 s76,s76,4\n"
"s_add_u32 s50,s50,s76\n"
"s_mul_i32 s76,s11,401408\n"
"s_lshl_b32 s77,s51,7\n"
"s_add_u32 s76,s76,s77\n"
"s_sub_u32 s77,401408,s77\n"
"s_add_u32 s36,s28,s76\n"
"s_addc_u32 s37,s29,0\n"
"s_and_b32 s37,s37,0xffff\n"
"s_mov_b32 s38,s77\n"
"s_mov_b32 s39,0x20000\n"
"s_mul_i32 s76,s50,229376\n"
"s_mul_i32 s77,s11,14336\n"
"s_add_u32 s76,s76,s77\n"
"s_add_u32 s40,s32,s76\n"
"s_addc_u32 s41,s33,0\n"
"s_add_u32 s40,s40,0x6200000\n"
"s_addc_u32 s41,s41,0\n"
"s_and_b32 s41,s41,0xffff\n"
"s_mov_b32 s42,0x2d800\n"
"s_mov_b32 s43,0x20000\n"
"v_lshrrev_b32 v1,3,v0\n"
"v_lshrrev_b32 v2,4,v0\n"
"v_bfe_u32 v3,v0,1,2\n"
"v_xor_b32 v3,v3,v2\n"
"v_and_b32 v4,1,v0\n"
"v_mul_u32_u24 v1,896,v1\n"
"v_lshl_add_u32 v1,v3,5,v1\n"
"v_lshl_add_u32 v24,v4,4,v1\n"
"v_add_u32 v25,0x1800,v24\n"
"v_add_u32 v26,0x3000,v24\n"
"v_add_u32 v27,0x4800,v24\n"
"v_add_u32 v28,0x7000,v24\n"
"v_add_u32 v29,0x8800,v24\n"
"v_add_u32 v30,0xa000,v24\n"
"v_add_u32 v31,0xb800,v24\n"
"s_lshl_b32 s57,s11,13\n"
"v_bfe_u32 v1,v0,2,4\n"
"v_bfe_u32 v3,v0,3,2\n"
"v_and_b32 v4,3,v0\n"
"v_lshlrev_b32 v1,7,v1\n"
"v_lshl_add_u32 v1,v4,3,v1\n"
"v_add_u32 v1,s57,v1\n"
"v_xor_b32 v4,0,v3\n"
"v_lshl_add_u32 v32,v4,5,v1\n"
"v_xor_b32 v4,1,v3\n"
"v_lshl_add_u32 v33,v4,5,v1\n"
"v_xor_b32 v4,2,v3\n"
"v_lshl_add_u32 v34,v4,5,v1\n"
"v_xor_b32 v4,3,v3\n"
"v_lshl_add_u32 v35,v4,5,v1\n"
"v_add_u32 v40,0x10000,v32\n"
"v_add_u32 v44,0x20000,v32\n"
"v_add_u32 v41,0x10000,v33\n"
"v_add_u32 v45,0x20000,v33\n"
"v_add_u32 v42,0x10000,v34\n"
"v_add_u32 v46,0x20000,v34\n"
"v_add_u32 v43,0x10000,v35\n"
"v_add_u32 v47,0x20000,v35\n"
"s_lshl_b32 s76,s50,2\n"
"s_add_u32 s55,s76,s11\n"
"s_mul_i32 s76,s55,28672\n"
"s_lshl_b32 s77,s51,7\n"
"s_add_u32 s76,s76,s77\n"
"s_add_u32 s64,s32,s76\n"
"s_addc_u32 s65,s33,0\n"
"s_add_u32 s64,s64,0x5c00000\n"
"s_addc_u32 s65,s65,0\n"
"s_and_b32 s65,s65,0xffff\n"
"s_mov_b32 s66,0x6d00\n"
"s_mov_b32 s67,0x20000\n"
"s_mul_i32 s76,s55,14336\n"
"s_add_u32 s76,s76,s77\n"
"s_add_u32 s68,s30,s76\n"
"s_addc_u32 s69,s31,0\n"
"s_and_b32 s69,s69,0xffff\n"
"s_mov_b32 s70,0x3500\n"
"s_mov_b32 s71,0x20000\n"
"v_and_b32 v1,15,v0\n"
"v_lshrrev_b32 v2,4,v0\n"
"v_mul_u32_u24 v5,1792,v1\n"
"v_lshl_add_u32 v36,v2,4,v5\n"
"v_mul_u32_u24 v5,896,v1\n"
"v_lshl_add_u32 v37,v2,3,v5\n"
"v_and_b32 v10,31,v0\n"
"v_lshlrev_b32 v10,3,v10\n"
"s_mov_b32 s76,0\n"
"L_hn_lp_%=:\n"
"global_load_dwordx2 v[2:3],v10,s[82:83] sc1\n"
"s_add_u32 s76,s76,1\n"
"s_waitcnt vmcnt(0)\n"
"v_cmp_ne_u64 vcc,s[34:35],v[2:3]\n"
"s_nop 0\n"
"s_cbranch_vccz L_hn_ok_%=\n"
"s_sleep 1\n"
"s_cmp_lt_u32 s76,0x20000\n"
"s_cbranch_scc1 L_hn_lp_%=\n"
"L_hn_ok_%=:\n"
"buffer_load_dwordx4 a[0:3],v8,s[44:47],s86 offen offset:0 sc1\n"
"buffer_load_dwordx4 a[16:19],v8,s[44:47],s86 offen offset:1024 sc1\n"
"buffer_load_dwordx4 a[4:7],v8,s[44:47],s87 offen offset:0 sc1\n"
"buffer_load_dwordx4 a[20:23],v8,s[44:47],s87 offen offset:1024 sc1\n"
"buffer_load_dwordx4 a[8:11],v8,s[44:47],s88 offen offset:0 sc1\n"
"buffer_load_dwordx4 a[24:27],v8,s[44:47],s88 offen offset:1024 sc1\n"
"buffer_load_dwordx4 a[12:15],v8,s[44:47],s89 offen offset:0 sc1\n"
"buffer_load_dwordx4 a[28:31],v8,s[44:47],s89 offen offset:1024 sc1\n"
"buffer_load_dwordx4 a[32:35],v8,s[44:47],s86 offen offset:2048 sc1\n"
"buffer_load_dwordx4 a[48:51],v8,s[44:47],s86 offen offset:3072 sc1\n"
"buffer_load_dwordx4 a[36:39],v8,s[44:47],s87 offen offset:2048 sc1\n"
"buffer_load_dwordx4 a[52:55],v8,s[44:47],s87 offen offset:3072 sc1\n"
"s_barrier\n"
"ds_read_b64_tr_b16 a[192:193],v12 offset:0\n"
"ds_read_b64_tr_b16 a[194:195],v12 offset:2048\n"
"ds_read_b64_tr_b16 a[196:197],v12 offset:4096\n"
"ds_read_b64_tr_b16 a[198:199],v12 offset:6144\n"
"ds_read_b64_tr_b16 a[200:201],v12 offset:8192\n"
"ds_read_b64_tr_b16 a[202:203],v12 offset:10240\n"
"ds_read_b64_tr_b16 a[204:205],v12 offset:12288\n"
"ds_read_b64_tr_b16 a[206:207],v12 offset:14336\n"
"ds_read_b64_tr_b16 a[208:209],v13 offset:0\n"
"ds_read_b64_tr_b16 a[210:211],v13 offset:2048\n"
"ds_read_b64_tr_b16 a[212:213],v13 offset:4096\n"
"ds_read_b64_tr_b16 a[214:215],v13 offset:6144\n"
"s_waitcnt vmcnt(11) lgkmcnt(10)\n"
"v_mfma_f32_16x16x32_bf16 v[128:131],a[192:195],a[0:3],0\n"
"ds_read_b64_tr_b16 a[216:217],v13 offset:8192\n"
"s_waitcnt vmcnt(10) lgkmcnt(9)\n"
"v_mfma_f32_16x16x32_bf16 v[128:131],a[196:199],a[16:19],v[128:131]\n"
"buffer_load_dwordx4 a[40:43],v8,s[44:47],s88 offen offset:2048 sc1\n"
"s_waitcnt vmcnt(10)\n"
"v_mfma_f32_16x16x32_bf16 v[160:163],a[192:195],a[4:7],0\n"
"ds_read_b64_tr_b16 a[218:219],v13 offset:10240\n"
"s_waitcnt vmcnt(9)\n"
"v_mfma_f32_16x16x32_bf16 v[160:163],a[196:199],a[20:23],v[160:163]\n"
"s_waitcnt vmcnt(8)\n"
"v_mfma_f32_16x16x32_bf16 v[192:195],a[192:195],a[8:11],0\n"
"ds_read_b64_tr_b16 a[220:221],v13 offset:12288\n"
"s_waitcnt vmcnt(7)\n"
"v_mfma_f32_16x16x32_bf16 v[192:195],a[196:199],a[24:27],v[192:195]\n"
"buffer_load_dwordx4 a[56:59],v8,s[44:47],s88 offen offset:3072 sc1\n"
"s_waitcnt vmcnt(7)\n"
"v_mfma_f32_16x16x32_bf16 v[224:227],a[192:195],a[12:15],0\n"
"ds_read_b64_tr_b16 a[222:223],v13 offset:14336\n"
"s_waitcnt vmcnt(6)\n"
"v_mfma_f32_16x16x32_bf16 v[224:227],a[196:199],a[28:31],v[224:227]\n"
"s_waitcnt lgkmcnt(8)\n"
"v_mfma_f32_16x16x32_bf16 v[136:139],a[200:203],a[0:3],0\n"
"ds_read_b64_tr_b16 a[224:225],v14 offset:0\n"
"v_mfma_f32_16x16x32_bf16 v[136:139],a[204:207],a[16:19],v[136:139]\n"
"buffer_load_dwordx4 a[44:47],v8,s[44:47],s89 offen offset:2048 sc1\n"
"v_mfma_f32_16x16x32_bf16 v[168:171],a[200:203],a[4:7],0\n"
"ds_read_b64_tr_b16 a[226:227],v14 offset:2048\n"
"v_mfma_f32_16x16x32_bf16 v[168:171],a[204:207],a[20:23],v[168:171]\n"
"v_mfma_f32_16x16x32_bf16 v[200:203],a[200:203],a[8:11],0\n"
"ds_read_b64_tr_b16 a[228:229],v14 offset:4096\n"
"v_mfma_f32_16x16x32_bf16 v[200:203],a[204:207],a[24:27],v[200:203]\n"
"buffer_load_dwordx4 a[60:63],v8,s[44:47],s89 offen offset:3072 sc1\n"
"v_mfma_f32_16x16x32_bf16 v[232:235],a[200:203],a[12:15],0\n"
"ds_read_b64_tr_b16 a[230:231],v14 offset:6144\n"
"v_mfma_f32_16x16x32_bf16 v[232:235],a[204:207],a[28:31],v[232:235]\n"
"s_waitcnt lgkmcnt(8)\n"
"v_mfma_f32_16x16x32_bf16 v[132:135],a[208:211],a[0:3],0\n"
"s_add_u32 s86,s86,0x1000\n"
"ds_read_b64_tr_b16 a[232:233],v14 offset:8192\n"
"v_mfma_f32_16x16x32_bf16 v[132:135],a[212:215],a[16:19],v[132:135]\n"
"buffer_load_dwordx4 a[64:67],v8,s[44:47],s86 offen offset:0 sc1\n"
"v_mfma_f32_16x16x32_bf16 v[164:167],a[208:211],a[4:7],0\n"
"ds_read_b64_tr_b16 a[234:235],v14 offset:10240\n"
"v_mfma_f32_16x16x32_bf16 v[164:167],a[212:215],a[20:23],v[164:167]\n"
"v_mfma_f32_16x16x32_bf16 v[196:199],a[208:211],a[8:11],0\n"
"ds_read_b64_tr_b16 a[236:237],v14 offset:12288\n"
"v_mfma_f32_16x16x32_bf16 v[196:199],a[212:215],a[24:27],v[196:199]\n"
"buffer_load_dwordx4 a[80:83],v8,s[44:47],s86 offen offset:1024 sc1\n"
"v_mfma_f32_16x16x32_bf16 v[228:231],a[208:211],a[12:15],0\n"
"ds_read_b64_tr_b16 a[238:239],v14 offset:14336\n"
"v_mfma_f32_16x16x32_bf16 v[228:231],a[212:215],a[28:31],v[228:231]\n"
"s_waitcnt lgkmcnt(8)\n"
"v_mfma_f32_16x16x32_bf16 v[140:143],a[216:219],a[0:3],0\n"
"s_add_u32 s87,s87,0x1000\n"
"ds_read_b64_tr_b16 a[240:241],v15 offset:0\n"
"v_mfma_f32_16x16x32_bf16 v[140:143],a[220:223],a[16:19],v[140:143]\n"
"buffer_load_dwordx4 a[68:71],v8,s[44:47],s87 offen offset:0 sc1\n"
"v_mfma_f32_16x16x32_bf16 v[172:175],a[216:219],a[4:7],0\n"
"ds_read_b64_tr_b16 a[242:243],v15 offset:2048\n"
"v_mfma_f32_16x16x32_bf16 v[172:175],a[220:223],a[20:23],v[172:175]\n"
"v_mfma_f32_16x16x32_bf16 v[204:207],a[216:219],a[8:11],0\n"
"ds_read_b64_tr_b16 a[244:245],v15 offset:4096\n"
"v_mfma_f32_16x16x32_bf16 v[204:207],a[220:223],a[24:27],v[204:207]\n"
"buffer_load_dwordx4 a[84:87],v8,s[44:47],s87 offen offset:1024 sc1\n"
"v_mfma_f32_16x16x32_bf16 v[236:239],a[216:219],a[12:15],0\n"
"ds_read_b64_tr_b16 a[246:247],v15 offset:6144\n"
"v_mfma_f32_16x16x32_bf16 v[236:239],a[220:223],a[28:31],v[236:239]\n"
"s_waitcnt lgkmcnt(8)\n"
"v_mfma_f32_16x16x32_bf16 v[144:147],a[224:227],a[0:3],0\n"
"s_add_u32 s88,s88,0x1000\n"
"ds_read_b64_tr_b16 a[248:249],v15 offset:8192\n"
"v_mfma_f32_16x16x32_bf16 v[144:147],a[228:231],a[16:19],v[144:147]\n"
"buffer_load_dwordx4 a[72:75],v8,s[44:47],s88 offen offset:0 sc1\n"
"v_mfma_f32_16x16x32_bf16 v[176:179],a[224:227],a[4:7],0\n"
"ds_read_b64_tr_b16 a[250:251],v15 offset:10240\n"
"v_mfma_f32_16x16x32_bf16 v[176:179],a[228:231],a[20:23],v[176:179]\n"
"v_mfma_f32_16x16x32_bf16 v[208:211],a[224:227],a[8:11],0\n"
"ds_read_b64_tr_b16 a[252:253],v15 offset:12288\n"
"v_mfma_f32_16x16x32_bf16 v[208:211],a[228:231],a[24:27],v[208:211]\n"
"buffer_load_dwordx4 a[88:91],v8,s[44:47],s88 offen offset:1024 sc1\n"
"v_mfma_f32_16x16x32_bf16 v[240:243],a[224:227],a[12:15],0\n"
"ds_read_b64_tr_b16 a[254:255],v15 offset:14336\n"
"v_mfma_f32_16x16x32_bf16 v[240:243],a[228:231],a[28:31],v[240:243]\n"
"s_waitcnt lgkmcnt(8)\n"
"v_mfma_f32_16x16x32_bf16 v[152:155],a[232:235],a[0:3],0\n"
"s_add_u32 s89,s89,0x1000\n"
"ds_read_b64_tr_b16 a[192:193],v12 offset:16384\n"
"v_mfma_f32_16x16x32_bf16 v[152:155],a[236:239],a[16:19],v[152:155]\n"
"buffer_load_dwordx4 a[76:79],v8,s[44:47],s89 offen offset:0 sc1\n"
"v_mfma_f32_16x16x32_bf16 v[184:187],a[232:235],a[4:7],0\n"
"ds_read_b64_tr_b16 a[194:195],v12 offset:18432\n"
"v_mfma_f32_16x16x32_bf16 v[184:187],a[236:239],a[20:23],v[184:187]\n"
"v_mfma_f32_16x16x32_bf16 v[216:219],a[232:235],a[8:11],0\n"
"ds_read_b64_tr_b16 a[196:197],v12 offset:20480\n"
"v_mfma_f32_16x16x32_bf16 v[216:219],a[236:239],a[24:27],v[216:219]\n"
"buffer_load_dwordx4 a[92:95],v8,s[44:47],s89 offen offset:1024 sc1\n"
"v_mfma_f32_16x16x32_bf16 v[248:251],a[232:235],a[12:15],0\n"
"ds_read_b64_tr_b16 a[198:199],v12 offset:22528\n"
"v_mfma_f32_16x16x32_bf16 v[248:251],a[236:239],a[28:31],v[248:251]\n"
"s_waitcnt lgkmcnt(8)\n"
"v_mfma_f32_16x16x32_bf16 v[148:151],a[240:243],a[0:3],0\n"
"ds_read_b64_tr_b16 a[200:201],v12 offset:24576\n"
"v_mfma_f32_16x16x32_bf16 v[148:151],a[244:247],a[16:19],v[148:151]\n"
"buffer_load_dwordx4 a[96:99],v8,s[44:47],s86 offen offset:2048 sc1\n"
"v_mfma_f32_16x16x32_bf16 v[180:183],a[240:243],a[4:7],0\n"
"ds_read_b64_tr_b16 a[202:203],v12 offset:26624\n"
"v_mfma_f32_16x16x32_bf16 v[180:183],a[244:247],a[20:23],v[180:183]\n"
"v_mfma_f32_16x16x32_bf16 v[212:215],a[240:243],a[8:11],0\n"
"ds_read_b64_tr_b16 a[204:205],v12 offset:28672\n"
"v_mfma_f32_16x16x32_bf16 v[212:215],a[244:247],a[24:27],v[212:215]\n"
"buffer_load_dwordx4 a[112:115],v8,s[44:47],s86 offen offset:3072 sc1\n"
"v_mfma_f32_16x16x32_bf16 v[244:247],a[240:243],a[12:15],0\n"
"ds_read_b64_tr_b16 a[206:207],v12 offset:30720\n"
"v_mfma_f32_16x16x32_bf16 v[244:247],a[244:247],a[28:31],v[244:247]\n"
"s_waitcnt lgkmcnt(8)\n"
"v_mfma_f32_16x16x32_bf16 v[156:159],a[248:251],a[0:3],0\n"
"ds_read_b64_tr_b16 a[208:209],v13 offset:16384\n"
"v_mfma_f32_16x16x32_bf16 v[156:159],a[252:255],a[16:19],v[156:159]\n"
"buffer_load_dwordx4 a[100:103],v8,s[44:47],s87 offen offset:2048 sc1\n"
"v_mfma_f32_16x16x32_bf16 v[188:191],a[248:251],a[4:7],0\n"
"ds_read_b64_tr_b16 a[210:211],v13 offset:18432\n"
"v_mfma_f32_16x16x32_bf16 v[188:191],a[252:255],a[20:23],v[188:191]\n"
"v_mfma_f32_16x16x32_bf16 v[220:223],a[248:251],a[8:11],0\n"
"ds_read_b64_tr_b16 a[212:213],v13 offset:20480\n"
"v_mfma_f32_16x16x32_bf16 v[220:223],a[252:255],a[24:27],v[220:223]\n"
"buffer_load_dwordx4 a[116:119],v8,s[44:47],s87 offen offset:3072 sc1\n"
"v_mfma_f32_16x16x32_bf16 v[252:255],a[248:251],a[12:15],0\n"
"s_waitcnt vmcnt(12)\n"
"ds_read_b64_tr_b16 a[214:215],v13 offset:22528\n"
"v_mfma_f32_16x16x32_bf16 v[252:255],a[252:255],a[28:31],v[252:255]\n"
"s_waitcnt lgkmcnt(8)\n"
"v_mfma_f32_16x16x32_bf16 v[128:131],a[192:195],a[32:35],v[128:131]\n"
"ds_read_b64_tr_b16 a[216:217],v13 offset:24576\n"
"v_mfma_f32_16x16x32_bf16 v[128:131],a[196:199],a[48:51],v[128:131]\n"
"buffer_load_dwordx4 a[104:107],v8,s[44:47],s88 offen offset:2048 sc1\n"
"v_mfma_f32_16x16x32_bf16 v[160:163],a[192:195],a[36:39],v[160:163]\n"
"ds_read_b64_tr_b16 a[218:219],v13 offset:26624\n"
"v_mfma_f32_16x16x32_bf16 v[160:163],a[196:199],a[52:55],v[160:163]\n"
"v_mfma_f32_16x16x32_bf16 v[192:195],a[192:195],a[40:43],v[192:195]\n"
"ds_read_b64_tr_b16 a[220:221],v13 offset:28672\n"
"v_mfma_f32_16x16x32_bf16 v[192:195],a[196:199],a[56:59],v[192:195]\n"
"buffer_load_dwordx4 a[120:123],v8,s[44:47],s88 offen offset:3072 sc1\n"
"v_mfma_f32_16x16x32_bf16 v[224:227],a[192:195],a[44:47],v[224:227]\n"
"ds_read_b64_tr_b16 a[222:223],v13 offset:30720\n"
"v_mfma_f32_16x16x32_bf16 v[224:227],a[196:199],a[60:63],v[224:227]\n"
"s_waitcnt lgkmcnt(8)\n"
"v_mfma_f32_16x16x32_bf16 v[136:139],a[200:203],a[32:35],v[136:139]\n"
"ds_read_b64_tr_b16 a[224:225],v14 offset:16384\n"
"v_mfma_f32_16x16x32_bf16 v[136:139],a[204:207],a[48:51],v[136:139]\n"
"buffer_load_dwordx4 a[108:111],v8,s[44:47],s89 offen offset:2048 sc1\n"
"v_mfma_f32_16x16x32_bf16 v[168:171],a[200:203],a[36:39],v[168:171]\n"
"ds_read_b64_tr_b16 a[226:227],v14 offset:18432\n"
"v_mfma_f32_16x16x32_bf16 v[168:171],a[204:207],a[52:55],v[168:171]\n"
"v_mfma_f32_16x16x32_bf16 v[200:203],a[200:203],a[40:43],v[200:203]\n"
"ds_read_b64_tr_b16 a[228:229],v14 offset:20480\n"
"v_mfma_f32_16x16x32_bf16 v[200:203],a[204:207],a[56:59],v[200:203]\n"
"buffer_load_dwordx4 a[124:127],v8,s[44:47],s89 offen offset:3072 sc1\n"
"v_mfma_f32_16x16x32_bf16 v[232:235],a[200:203],a[44:47],v[232:235]\n"
"ds_read_b64_tr_b16 a[230:231],v14 offset:22528\n"
"v_mfma_f32_16x16x32_bf16 v[232:235],a[204:207],a[60:63],v[232:235]\n"
"s_waitcnt lgkmcnt(8)\n"
"v_mfma_f32_16x16x32_bf16 v[132:135],a[208:211],a[32:35],v[132:135]\n"
"s_add_u32 s86,s86,0x1000\n"
"ds_read_b64_tr_b16 a[232:233],v14 offset:24576\n"
"v_mfma_f32_16x16x32_bf16 v[132:135],a[212:215],a[48:51],v[132:135]\n"
"buffer_load_dwordx4 a[128:131],v8,s[44:47],s86 offen offset:0 sc1\n"
"v_mfma_f32_16x16x32_bf16 v[164:167],a[208:211],a[36:39],v[164:167]\n"
"ds_read_b64_tr_b16 a[234:235],v14 offset:26624\n"
"v_mfma_f32_16x16x32_bf16 v[164:167],a[212:215],a[52:55],v[164:167]\n"
"v_mfma_f32_16x16x32_bf16 v[196:199],a[208:211],a[40:43],v[196:199]\n"
"ds_read_b64_tr_b16 a[236:237],v14 offset:28672\n"
"v_mfma_f32_16x16x32_bf16 v[196:199],a[212:215],a[56:59],v[196:199]\n"
"buffer_load_dwordx4 a[144:147],v8,s[44:47],s86 offen offset:1024 sc1\n"
"v_mfma_f32_16x16x32_bf16 v[228:231],a[208:211],a[44:47],v[228:231]\n"
"ds_read_b64_tr_b16 a[238:239],v14 offset:30720\n"
"v_mfma_f32_16x16x32_bf16 v[228:231],a[212:215],a[60:63],v[228:231]\n"
"s_waitcnt lgkmcnt(8)\n"
"v_mfma_f32_16x16x32_bf16 v[140:143],a[216:219],a[32:35],v[140:143]\n"
"s_add_u32 s87,s87,0x1000\n"
"ds_read_b64_tr_b16 a[240:241],v15 offset:16384\n"
"v_mfma_f32_16x16x32_bf16 v[140:143],a[220:223],a[48:51],v[140:143]\n"
"buffer_load_dwordx4 a[132:135],v8,s[44:47],s87 offen offset:0 sc1\n"
"v_mfma_f32_16x16x32_bf16 v[172:175],a[216:219],a[36:39],v[172:175]\n"
"ds_read_b64_tr_b16 a[242:243],v15 offset:18432\n"
"v_mfma_f32_16x16x32_bf16 v[172:175],a[220:223],a[52:55],v[172:175]\n"
"v_mfma_f32_16x16x32_bf16 v[204:207],a[216:219],a[40:43],v[204:207]\n"
"ds_read_b64_tr_b16 a[244:245],v15 offset:20480\n"
"v_mfma_f32_16x16x32_bf16 v[204:207],a[220:223],a[56:59],v[204:207]\n"
"buffer_load_dwordx4 a[148:151],v8,s[44:47],s87 offen offset:1024 sc1\n"
"v_mfma_f32_16x16x32_bf16 v[236:239],a[216:219],a[44:47],v[236:239]\n"
"ds_read_b64_tr_b16 a[246:247],v15 offset:22528\n"
"v_mfma_f32_16x16x32_bf16 v[236:239],a[220:223],a[60:63],v[236:239]\n"
"s_waitcnt lgkmcnt(8)\n"
"v_mfma_f32_16x16x32_bf16 v[144:147],a[224:227],a[32:35],v[144:147]\n"
"s_add_u32 s88,s88,0x1000\n"
"ds_read_b64_tr_b16 a[248:249],v15 offset:24576\n"
"v_mfma_f32_16x16x32_bf16 v[144:147],a[228:231],a[48:51],v[144:147]\n"
"buffer_load_dwordx4 a[136:139],v8,s[44:47],s88 offen offset:0 sc1\n"
"v_mfma_f32_16x16x32_bf16 v[176:179],a[224:227],a[36:39],v[176:179]\n"
"ds_read_b64_tr_b16 a[250:251],v15 offset:26624\n"
"v_mfma_f32_16x16x32_bf16 v[176:179],a[228:231],a[52:55],v[176:179]\n"
"v_mfma_f32_16x16x32_bf16 v[208:211],a[224:227],a[40:43],v[208:211]\n"
"ds_read_b64_tr_b16 a[252:253],v15 offset:28672\n"
"v_mfma_f32_16x16x32_bf16 v[208:211],a[228:231],a[56:59],v[208:211]\n"
"buffer_load_dwordx4 a[152:155],v8,s[44:47],s88 offen offset:1024 sc1\n"
"v_mfma_f32_16x16x32_bf16 v[240:243],a[224:227],a[44:47],v[240:243]\n"
"ds_read_b64_tr_b16 a[254:255],v15 offset:30720\n"
"v_mfma_f32_16x16x32_bf16 v[240:243],a[228:231],a[60:63],v[240:243]\n"
"s_waitcnt lgkmcnt(8)\n"
"v_mfma_f32_16x16x32_bf16 v[152:155],a[232:235],a[32:35],v[152:155]\n"
"s_add_u32 s89,s89,0x1000\n"
"ds_read_b64_tr_b16 a[192:193],v12 offset:32768\n"
"v_mfma_f32_16x16x32_bf16 v[152:155],a[236:239],a[48:51],v[152:155]\n"
"buffer_load_dwordx4 a[140:143],v8,s[44:47],s89 offen offset:0 sc1\n"
"v_mfma_f32_16x16x32_bf16 v[184:187],a[232:235],a[36:39],v[184:187]\n"
"ds_read_b64_tr_b16 a[194:195],v12 offset:34816\n"
"v_mfma_f32_16x16x32_bf16 v[184:187],a[236:239],a[52:55],v[184:187]\n"
"v_mfma_f32_16x16x32_bf16 v[216:219],a[232:235],a[40:43],v[216:219]\n"
"ds_read_b64_tr_b16 a[196:197],v12 offset:36864\n"
"v_mfma_f32_16x16x32_bf16 v[216:219],a[236:239],a[56:59],v[216:219]\n"
"buffer_load_dwordx4 a[156:159],v8,s[44:47],s89 offen offset:1024 sc1\n"
"v_mfma_f32_16x16x32_bf16 v[248:251],a[232:235],a[44:47],v[248:251]\n"
"ds_read_b64_tr_b16 a[198:199],v12 offset:38912\n"
"v_mfma_f32_16x16x32_bf16 v[248:251],a[236:239],a[60:63],v[248:251]\n"
"s_waitcnt lgkmcnt(8)\n"
"v_mfma_f32_16x16x32_bf16 v[148:151],a[240:243],a[32:35],v[148:151]\n"
"ds_read_b64_tr_b16 a[200:201],v12 offset:40960\n"
"v_mfma_f32_16x16x32_bf16 v[148:151],a[244:247],a[48:51],v[148:151]\n"
"buffer_load_dwordx4 a[160:163],v8,s[44:47],s86 offen offset:2048 sc1\n"
"v_mfma_f32_16x16x32_bf16 v[180:183],a[240:243],a[36:39],v[180:183]\n"
"ds_read_b64_tr_b16 a[202:203],v12 offset:43008\n"
"v_mfma_f32_16x16x32_bf16 v[180:183],a[244:247],a[52:55],v[180:183]\n"
"v_mfma_f32_16x16x32_bf16 v[212:215],a[240:243],a[40:43],v[212:215]\n"
"ds_read_b64_tr_b16 a[204:205],v12 offset:45056\n"
"v_mfma_f32_16x16x32_bf16 v[212:215],a[244:247],a[56:59],v[212:215]\n"
"buffer_load_dwordx4 a[176:179],v8,s[44:47],s86 offen offset:3072 sc1\n"
"v_mfma_f32_16x16x32_bf16 v[244:247],a[240:243],a[44:47],v[244:247]\n"
"ds_read_b64_tr_b16 a[206:207],v12 offset:47104\n"
"v_mfma_f32_16x16x32_bf16 v[244:247],a[244:247],a[60:63],v[244:247]\n"
"s_waitcnt lgkmcnt(8)\n"
"v_mfma_f32_16x16x32_bf16 v[156:159],a[248:251],a[32:35],v[156:159]\n"
"ds_read_b64_tr_b16 a[208:209],v13 offset:32768\n"
"v_mfma_f32_16x16x32_bf16 v[156:159],a[252:255],a[48:51],v[156:159]\n"
"buffer_load_dwordx4 a[164:167],v8,s[44:47],s87 offen offset:2048 sc1\n"
"v_mfma_f32_16x16x32_bf16 v[188:191],a[248:251],a[36:39],v[188:191]\n"
"ds_read_b64_tr_b16 a[210:211],v13 offset:34816\n"
"v_mfma_f32_16x16x32_bf16 v[188:191],a[252:255],a[52:55],v[188:191]\n"
"v_mfma_f32_16x16x32_bf16 v[220:223],a[248:251],a[40:43],v[220:223]\n"
"ds_read_b64_tr_b16 a[212:213],v13 offset:36864\n"
"v_mfma_f32_16x16x32_bf16 v[220:223],a[252:255],a[56:59],v[220:223]\n"
"buffer_load_dwordx4 a[180:183],v8,s[44:47],s87 offen offset:3072 sc1\n"
"v_mfma_f32_16x16x32_bf16 v[252:255],a[248:251],a[44:47],v[252:255]\n"
"s_waitcnt vmcnt(20)\n"
"ds_read_b64_tr_b16 a[214:215],v13 offset:38912\n"
"v_mfma_f32_16x16x32_bf16 v[252:255],a[252:255],a[60:63],v[252:255]\n"
"s_waitcnt lgkmcnt(8)\n"
"v_mfma_f32_16x16x32_bf16 v[128:131],a[192:195],a[64:67],v[128:131]\n"
"ds_read_b64_tr_b16 a[216:217],v13 offset:40960\n"
"v_mfma_f32_16x16x32_bf16 v[128:131],a[196:199],a[80:83],v[128:131]\n"
"buffer_load_dwordx4 a[168:171],v8,s[44:47],s88 offen offset:2048 sc1\n"
"v_mfma_f32_16x16x32_bf16 v[160:163],a[192:195],a[68:71],v[160:163]\n"
"ds_read_b64_tr_b16 a[218:219],v13 offset:43008\n"
"v_mfma_f32_16x16x32_bf16 v[160:163],a[196:199],a[84:87],v[160:163]\n"
"v_mfma_f32_16x16x32_bf16 v[192:195],a[192:195],a[72:75],v[192:195]\n"
"ds_read_b64_tr_b16 a[220:221],v13 offset:45056\n"
"v_mfma_f32_16x16x32_bf16 v[192:195],a[196:199],a[88:91],v[192:195]\n"
"s_barrier\n"
"buffer_load_dwordx4 a[184:187],v8,s[44:47],s88 offen offset:3072 sc1\n"
"v_mfma_f32_16x16x32_bf16 v[224:227],a[192:195],a[76:79],v[224:227]\n"
"ds_read_b64_tr_b16 a[222:223],v13 offset:47104\n"
"v_mfma_f32_16x16x32_bf16 v[224:227],a[196:199],a[92:95],v[224:227]\n"
"s_waitcnt lgkmcnt(8)\n"
"v_mfma_f32_16x16x32_bf16 v[136:139],a[200:203],a[64:67],v[136:139]\n"
"ds_read_b64_tr_b16 a[224:225],v14 offset:32768\n"
"v_mfma_f32_16x16x32_bf16 v[136:139],a[204:207],a[80:83],v[136:139]\n"
"buffer_load_dwordx4 a[172:175],v8,s[44:47],s89 offen offset:2048 sc1\n"
"v_mfma_f32_16x16x32_bf16 v[168:171],a[200:203],a[68:71],v[168:171]\n"
"ds_read_b64_tr_b16 a[226:227],v14 offset:34816\n"
"v_mfma_f32_16x16x32_bf16 v[168:171],a[204:207],a[84:87],v[168:171]\n"
"v_mfma_f32_16x16x32_bf16 v[200:203],a[200:203],a[72:75],v[200:203]\n"
"ds_read_b64_tr_b16 a[228:229],v14 offset:36864\n"
"v_mfma_f32_16x16x32_bf16 v[200:203],a[204:207],a[88:91],v[200:203]\n"
"buffer_load_dwordx4 a[188:191],v8,s[44:47],s89 offen offset:3072 sc1\n"
"v_mfma_f32_16x16x32_bf16 v[232:235],a[200:203],a[76:79],v[232:235]\n"
"ds_read_b64_tr_b16 a[230:231],v14 offset:38912\n"
"v_mfma_f32_16x16x32_bf16 v[232:235],a[204:207],a[92:95],v[232:235]\n"
"s_waitcnt lgkmcnt(8)\n"
"v_mfma_f32_16x16x32_bf16 v[132:135],a[208:211],a[64:67],v[132:135]\n"
"s_add_u32 s86,s86,0x1000\n"
"ds_read_b64_tr_b16 a[232:233],v14 offset:40960\n"
"v_mfma_f32_16x16x32_bf16 v[132:135],a[212:215],a[80:83],v[132:135]\n"
"buffer_load_dwordx4 v[64:67],v8,s[44:47],s86 offen offset:0 sc1\n"
"v_mfma_f32_16x16x32_bf16 v[164:167],a[208:211],a[68:71],v[164:167]\n"
"ds_read_b64_tr_b16 a[234:235],v14 offset:43008\n"
"v_mfma_f32_16x16x32_bf16 v[164:167],a[212:215],a[84:87],v[164:167]\n"
"v_mfma_f32_16x16x32_bf16 v[196:199],a[208:211],a[72:75],v[196:199]\n"
"ds_read_b64_tr_b16 a[236:237],v14 offset:45056\n"
"v_mfma_f32_16x16x32_bf16 v[196:199],a[212:215],a[88:91],v[196:199]\n"
"buffer_load_dwordx4 v[80:83],v8,s[44:47],s86 offen offset:1024 sc1\n"
"v_mfma_f32_16x16x32_bf16 v[228:231],a[208:211],a[76:79],v[228:231]\n"
"ds_read_b64_tr_b16 a[238:239],v14 offset:47104\n"
"v_mfma_f32_16x16x32_bf16 v[228:231],a[212:215],a[92:95],v[228:231]\n"
"s_waitcnt lgkmcnt(8)\n"
"v_mfma_f32_16x16x32_bf16 v[140:143],a[216:219],a[64:67],v[140:143]\n"
"s_add_u32 s87,s87,0x1000\n"
"ds_read_b64_tr_b16 a[240:241],v15 offset:32768\n"
"v_mfma_f32_16x16x32_bf16 v[140:143],a[220:223],a[80:83],v[140:143]\n"
"buffer_load_dwordx4 v[68:71],v8,s[44:47],s87 offen offset:0 sc1\n"
"v_mfma_f32_16x16x32_bf16 v[172:175],a[216:219],a[68:71],v[172:175]\n"
"ds_read_b64_tr_b16 a[242:243],v15 offset:34816\n"
"v_mfma_f32_16x16x32_bf16 v[172:175],a[220:223],a[84:87],v[172:175]\n"
"v_mfma_f32_16x16x32_bf16 v[204:207],a[216:219],a[72:75],v[204:207]\n"
"ds_read_b64_tr_b16 a[244:245],v15 offset:36864\n"
"v_mfma_f32_16x16x32_bf16 v[204:207],a[220:223],a[88:91],v[204:207]\n"
"buffer_load_dwordx4 v[84:87],v8,s[44:47],s87 offen offset:1024 sc1\n"
"v_mfma_f32_16x16x32_bf16 v[236:239],a[216:219],a[76:79],v[236:239]\n"
"ds_read_b64_tr_b16 a[246:247],v15 offset:38912\n"
"v_mfma_f32_16x16x32_bf16 v[236:239],a[220:223],a[92:95],v[236:239]\n"
"s_waitcnt lgkmcnt(8)\n"
"v_mfma_f32_16x16x32_bf16 v[144:147],a[224:227],a[64:67],v[144:147]\n"
"s_add_u32 s88,s88,0x1000\n"
"ds_read_b64_tr_b16 a[248:249],v15 offset:40960\n"
"v_mfma_f32_16x16x32_bf16 v[144:147],a[228:231],a[80:83],v[144:147]\n"
"buffer_load_dwordx4 v[72:75],v8,s[44:47],s88 offen offset:0 sc1\n"
"v_mfma_f32_16x16x32_bf16 v[176:179],a[224:227],a[68:71],v[176:179]\n"
"ds_read_b64_tr_b16 a[250:251],v15 offset:43008\n"
"v_mfma_f32_16x16x32_bf16 v[176:179],a[228:231],a[84:87],v[176:179]\n"
"v_mfma_f32_16x16x32_bf16 v[208:211],a[224:227],a[72:75],v[208:211]\n"
"ds_read_b64_tr_b16 a[252:253],v15 offset:45056\n"
"v_mfma_f32_16x16x32_bf16 v[208:211],a[228:231],a[88:91],v[208:211]\n"
"buffer_load_dwordx4 v[88:91],v8,s[44:47],s88 offen offset:1024 sc1\n"
"v_mfma_f32_16x16x32_bf16 v[240:243],a[224:227],a[76:79],v[240:243]\n"
"ds_read_b64_tr_b16 a[254:255],v15 offset:47104\n"
"v_mfma_f32_16x16x32_bf16 v[240:243],a[228:231],a[92:95],v[240:243]\n"
"s_waitcnt lgkmcnt(8)\n"
"v_mfma_f32_16x16x32_bf16 v[152:155],a[232:235],a[64:67],v[152:155]\n"
"s_add_u32 s89,s89,0x1000\n"
"ds_read_b64_tr_b16 a[192:193],v12 offset:49152\n"
"v_mfma_f32_16x16x32_bf16 v[152:155],a[236:239],a[80:83],v[152:155]\n"
"buffer_load_dwordx4 v[76:79],v8,s[44:47],s89 offen offset:0 sc1\n"
"v_mfma_f32_16x16x32_bf16 v[184:187],a[232:235],a[68:71],v[184:187]\n"
"ds_read_b64_tr_b16 a[194:195],v12 offset:51200\n"
"v_mfma_f32_16x16x32_bf16 v[184:187],a[236:239],a[84:87],v[184:187]\n"
"v_mfma_f32_16x16x32_bf16 v[216:219],a[232:235],a[72:75],v[216:219]\n"
"ds_read_b64_tr_b16 a[196:197],v12 offset:53248\n"
"v_mfma_f32_16x16x32_bf16 v[216:219],a[236:239],a[88:91],v[216:219]\n"
"buffer_load_dwordx4 v[92:95],v8,s[44:47],s89 offen offset:1024 sc1\n"
"v_mfma_f32_16x16x32_bf16 v[248:251],a[232:235],a[76:79],v[248:251]\n"
"s_mov_b32 s58,0x0\n"
"ds_read_b64_tr_b16 a[198:199],v12 offset:55296\n"
"v_mfma_f32_16x16x32_bf16 v[248:251],a[236:239],a[92:95],v[248:251]\n"
"s_waitcnt lgkmcnt(8)\n"
"v_mfma_f32_16x16x32_bf16 v[148:151],a[240:243],a[64:67],v[148:151]\n"
"s_add_u32 m0,s57,0x10000\n"
"ds_read_b64_tr_b16 a[200:201],v12 offset:57344\n"
"v_mfma_f32_16x16x32_bf16 v[148:151],a[244:247],a[80:83],v[148:151]\n"
"buffer_load_dwordx4 v24,s[36:39],s58 offen offset:0 lds\n"
"v_mfma_f32_16x16x32_bf16 v[180:183],a[240:243],a[68:71],v[180:183]\n"
"ds_read_b64_tr_b16 a[202:203],v12 offset:59392\n"
"v_mfma_f32_16x16x32_bf16 v[180:183],a[244:247],a[84:87],v[180:183]\n"
"v_mfma_f32_16x16x32_bf16 v[212:215],a[240:243],a[72:75],v[212:215]\n"
"ds_read_b64_tr_b16 a[204:205],v12 offset:61440\n"
"v_mfma_f32_16x16x32_bf16 v[212:215],a[244:247],a[88:91],v[212:215]\n"
"buffer_load_dwordx4 v25,s[36:39],s58 offen offset:1024 lds\n"
"v_mfma_f32_16x16x32_bf16 v[244:247],a[240:243],a[76:79],v[244:247]\n"
"ds_read_b64_tr_b16 a[206:207],v12 offset:63488\n"
"v_mfma_f32_16x16x32_bf16 v[244:247],a[244:247],a[92:95],v[244:247]\n"
"s_waitcnt lgkmcnt(8)\n"
"v_mfma_f32_16x16x32_bf16 v[156:159],a[248:251],a[64:67],v[156:159]\n"
"ds_read_b64_tr_b16 a[208:209],v13 offset:49152\n"
"v_mfma_f32_16x16x32_bf16 v[156:159],a[252:255],a[80:83],v[156:159]\n"
"buffer_load_dwordx4 v26,s[36:39],s58 offen offset:2048 lds\n"
"v_mfma_f32_16x16x32_bf16 v[188:191],a[248:251],a[68:71],v[188:191]\n"
"ds_read_b64_tr_b16 a[210:211],v13 offset:51200\n"
"v_mfma_f32_16x16x32_bf16 v[188:191],a[252:255],a[84:87],v[188:191]\n"
"v_mfma_f32_16x16x32_bf16 v[220:223],a[248:251],a[72:75],v[220:223]\n"
"ds_read_b64_tr_b16 a[212:213],v13 offset:53248\n"
"v_mfma_f32_16x16x32_bf16 v[220:223],a[252:255],a[88:91],v[220:223]\n"
"buffer_load_dwordx4 v27,s[36:39],s58 offen offset:3072 lds\n"
"v_mfma_f32_16x16x32_bf16 v[252:255],a[248:251],a[76:79],v[252:255]\n"
"s_waitcnt vmcnt(28)\n"
"ds_read_b64_tr_b16 a[214:215],v13 offset:55296\n"
"v_mfma_f32_16x16x32_bf16 v[252:255],a[252:255],a[92:95],v[252:255]\n"
"s_waitcnt lgkmcnt(8)\n"
"v_mfma_f32_16x16x32_bf16 v[128:131],a[192:195],a[96:99],v[128:131]\n"
"s_add_u32 m0,s57,0x11000\n"
"ds_read_b64_tr_b16 a[216:217],v13 offset:57344\n"
"v_mfma_f32_16x16x32_bf16 v[128:131],a[196:199],a[112:115],v[128:131]\n"
"buffer_load_dwordx4 v28,s[36:39],s58 offen offset:0 lds\n"
"v_mfma_f32_16x16x32_bf16 v[160:163],a[192:195],a[100:103],v[160:163]\n"
"ds_read_b64_tr_b16 a[218:219],v13 offset:59392\n"
"v_mfma_f32_16x16x32_bf16 v[160:163],a[196:199],a[116:119],v[160:163]\n"
"v_mfma_f32_16x16x32_bf16 v[192:195],a[192:195],a[104:107],v[192:195]\n"
"ds_read_b64_tr_b16 a[220:221],v13 offset:61440\n"
"v_mfma_f32_16x16x32_bf16 v[192:195],a[196:199],a[120:123],v[192:195]\n"
"buffer_load_dwordx4 v29,s[36:39],s58 offen offset:1024 lds\n"
"v_mfma_f32_16x16x32_bf16 v[224:227],a[192:195],a[108:111],v[224:227]\n"
"ds_read_b64_tr_b16 a[222:223],v13 offset:63488\n"
"v_mfma_f32_16x16x32_bf16 v[224:227],a[196:199],a[124:127],v[224:227]\n"
"s_waitcnt lgkmcnt(8)\n"
"v_mfma_f32_16x16x32_bf16 v[136:139],a[200:203],a[96:99],v[136:139]\n"
"ds_read_b64_tr_b16 a[224:225],v14 offset:49152\n"
"v_mfma_f32_16x16x32_bf16 v[136:139],a[204:207],a[112:115],v[136:139]\n"
"buffer_load_dwordx4 v30,s[36:39],s58 offen offset:2048 lds\n"
"v_mfma_f32_16x16x32_bf16 v[168:171],a[200:203],a[100:103],v[168:171]\n"
"ds_read_b64_tr_b16 a[226:227],v14 offset:51200\n"
"v_mfma_f32_16x16x32_bf16 v[168:171],a[204:207],a[116:119],v[168:171]\n"
"v_mfma_f32_16x16x32_bf16 v[200:203],a[200:203],a[104:107],v[200:203]\n"
"ds_read_b64_tr_b16 a[228:229],v14 offset:53248\n"
"v_mfma_f32_16x16x32_bf16 v[200:203],a[204:207],a[120:123],v[200:203]\n"
"buffer_load_dwordx4 v31,s[36:39],s58 offen offset:3072 lds\n"
"v_mfma_f32_16x16x32_bf16 v[232:235],a[200:203],a[108:111],v[232:235]\n"
"s_mov_b32 s58,0xe000\n"
"ds_read_b64_tr_b16 a[230:231],v14 offset:55296\n"
"v_mfma_f32_16x16x32_bf16 v[232:235],a[204:207],a[124:127],v[232:235]\n"
"s_waitcnt lgkmcnt(8)\n"
"v_mfma_f32_16x16x32_bf16 v[132:135],a[208:211],a[96:99],v[132:135]\n"
"s_add_u32 m0,s57,0x0\n"
"ds_read_b64_tr_b16 a[232:233],v14 offset:57344\n"
"v_mfma_f32_16x16x32_bf16 v[132:135],a[212:215],a[112:115],v[132:135]\n"
"buffer_load_dwordx4 v24,s[36:39],s58 offen offset:0 lds\n"
"v_mfma_f32_16x16x32_bf16 v[164:167],a[208:211],a[100:103],v[164:167]\n"
"ds_read_b64_tr_b16 a[234:235],v14 offset:59392\n"
"v_mfma_f32_16x16x32_bf16 v[164:167],a[212:215],a[116:119],v[164:167]\n"
"v_mfma_f32_16x16x32_bf16 v[196:199],a[208:211],a[104:107],v[196:199]\n"
"ds_read_b64_tr_b16 a[236:237],v14 offset:61440\n"
"v_mfma_f32_16x16x32_bf16 v[196:199],a[212:215],a[120:123],v[196:199]\n"
"buffer_load_dwordx4 v25,s[36:39],s58 offen offset:1024 lds\n"
"v_mfma_f32_16x16x32_bf16 v[228:231],a[208:211],a[108:111],v[228:231]\n"
"ds_read_b64_tr_b16 a[238:239],v14 offset:63488\n"
"v_mfma_f32_16x16x32_bf16 v[228:231],a[212:215],a[124:127],v[228:231]\n"
"s_waitcnt lgkmcnt(8)\n"
"v_mfma_f32_16x16x32_bf16 v[140:143],a[216:219],a[96:99],v[140:143]\n"
"ds_read_b64_tr_b16 a[240:241],v15 offset:49152\n"
"v_mfma_f32_16x16x32_bf16 v[140:143],a[220:223],a[112:115],v[140:143]\n"
"buffer_load_dwordx4 v26,s[36:39],s58 offen offset:2048 lds\n"
"v_mfma_f32_16x16x32_bf16 v[172:175],a[216:219],a[100:103],v[172:175]\n"
"ds_read_b64_tr_b16 a[242:243],v15 offset:51200\n"
"v_mfma_f32_16x16x32_bf16 v[172:175],a[220:223],a[116:119],v[172:175]\n"
"v_mfma_f32_16x16x32_bf16 v[204:207],a[216:219],a[104:107],v[204:207]\n"
"ds_read_b64_tr_b16 a[244:245],v15 offset:53248\n"
"v_mfma_f32_16x16x32_bf16 v[204:207],a[220:223],a[120:123],v[204:207]\n"
"buffer_load_dwordx4 v27,s[36:39],s58 offen offset:3072 lds\n"
"v_mfma_f32_16x16x32_bf16 v[236:239],a[216:219],a[108:111],v[236:239]\n"
"ds_read_b64_tr_b16 a[246:247],v15 offset:55296\n"
"v_mfma_f32_16x16x32_bf16 v[236:239],a[220:223],a[124:127],v[236:239]\n"
"s_waitcnt lgkmcnt(8)\n"
"v_mfma_f32_16x16x32_bf16 v[144:147],a[224:227],a[96:99],v[144:147]\n"
"s_add_u32 m0,s57,0x1000\n"
"ds_read_b64_tr_b16 a[248:249],v15 offset:57344\n"
"v_mfma_f32_16x16x32_bf16 v[144:147],a[228:231],a[112:115],v[144:147]\n"
"buffer_load_dwordx4 v28,s[36:39],s58 offen offset:0 lds\n"
"v_mfma_f32_16x16x32_bf16 v[176:179],a[224:227],a[100:103],v[176:179]\n"
"ds_read_b64_tr_b16 a[250:251],v15 offset:59392\n"
"v_mfma_f32_16x16x32_bf16 v[176:179],a[228:231],a[116:119],v[176:179]\n"
"v_mfma_f32_16x16x32_bf16 v[208:211],a[224:227],a[104:107],v[208:211]\n"
"ds_read_b64_tr_b16 a[252:253],v15 offset:61440\n"
"v_mfma_f32_16x16x32_bf16 v[208:211],a[228:231],a[120:123],v[208:211]\n"
"buffer_load_dwordx4 v29,s[36:39],s58 offen offset:1024 lds\n"
"v_mfma_f32_16x16x32_bf16 v[240:243],a[224:227],a[108:111],v[240:243]\n"
"ds_read_b64_tr_b16 a[254:255],v15 offset:63488\n"
"v_mfma_f32_16x16x32_bf16 v[240:243],a[228:231],a[124:127],v[240:243]\n"
"s_waitcnt lgkmcnt(8)\n"
"v_mfma_f32_16x16x32_bf16 v[152:155],a[232:235],a[96:99],v[152:155]\n"
"ds_read_b64_tr_b16 a[192:193],v16 offset:32768\n"
"v_mfma_f32_16x16x32_bf16 v[152:155],a[236:239],a[112:115],v[152:155]\n"
"buffer_load_dwordx4 v30,s[36:39],s58 offen offset:2048 lds\n"
"v_mfma_f32_16x16x32_bf16 v[184:187],a[232:235],a[100:103],v[184:187]\n"
"ds_read_b64_tr_b16 a[194:195],v16 offset:34816\n"
"v_mfma_f32_16x16x32_bf16 v[184:187],a[236:239],a[116:119],v[184:187]\n"
"v_mfma_f32_16x16x32_bf16 v[216:219],a[232:235],a[104:107],v[216:219]\n"
"ds_read_b64_tr_b16 a[196:197],v16 offset:36864\n"
"v_mfma_f32_16x16x32_bf16 v[216:219],a[236:239],a[120:123],v[216:219]\n"
"buffer_load_dwordx4 v31,s[36:39],s58 offen offset:3072 lds\n"
"v_mfma_f32_16x16x32_bf16 v[248:251],a[232:235],a[108:111],v[248:251]\n"
"ds_read_b64_tr_b16 a[198:199],v16 offset:38912\n"
"v_mfma_f32_16x16x32_bf16 v[248:251],a[236:239],a[124:127],v[248:251]\n"
"s_waitcnt lgkmcnt(8)\n"
"v_mfma_f32_16x16x32_bf16 v[148:151],a[240:243],a[96:99],v[148:151]\n"
"ds_read_b64_tr_b16 a[200:201],v16 offset:40960\n"
"v_mfma_f32_16x16x32_bf16 v[148:151],a[244:247],a[112:115],v[148:151]\n"
"v_mfma_f32_16x16x32_bf16 v[180:183],a[240:243],a[100:103],v[180:183]\n"
"ds_read_b64_tr_b16 a[202:203],v16 offset:43008\n"
"v_mfma_f32_16x16x32_bf16 v[180:183],a[244:247],a[116:119],v[180:183]\n"
"v_mfma_f32_16x16x32_bf16 v[212:215],a[240:243],a[104:107],v[212:215]\n"
"ds_read_b64_tr_b16 a[204:205],v16 offset:45056\n"
"v_mfma_f32_16x16x32_bf16 v[212:215],a[244:247],a[120:123],v[212:215]\n"
"v_mfma_f32_16x16x32_bf16 v[244:247],a[240:243],a[108:111],v[244:247]\n"
"ds_read_b64_tr_b16 a[206:207],v16 offset:47104\n"
"v_mfma_f32_16x16x32_bf16 v[244:247],a[244:247],a[124:127],v[244:247]\n"
"s_waitcnt lgkmcnt(8)\n"
"v_mfma_f32_16x16x32_bf16 v[156:159],a[248:251],a[96:99],v[156:159]\n"
"ds_read_b64_tr_b16 a[208:209],v17 offset:32768\n"
"v_mfma_f32_16x16x32_bf16 v[156:159],a[252:255],a[112:115],v[156:159]\n"
"v_mfma_f32_16x16x32_bf16 v[188:191],a[248:251],a[100:103],v[188:191]\n"
"ds_read_b64_tr_b16 a[210:211],v17 offset:34816\n"
"v_mfma_f32_16x16x32_bf16 v[188:191],a[252:255],a[116:119],v[188:191]\n"
"v_mfma_f32_16x16x32_bf16 v[220:223],a[248:251],a[104:107],v[220:223]\n"
"ds_read_b64_tr_b16 a[212:213],v17 offset:36864\n"
"v_mfma_f32_16x16x32_bf16 v[220:223],a[252:255],a[120:123],v[220:223]\n"
"v_mfma_f32_16x16x32_bf16 v[252:255],a[248:251],a[108:111],v[252:255]\n"
"s_waitcnt vmcnt(32)\n"
"ds_read_b64_tr_b16 a[214:215],v17 offset:38912\n"
"v_mfma_f32_16x16x32_bf16 v[252:255],a[252:255],a[124:127],v[252:255]\n"
"s_waitcnt lgkmcnt(8)\n"
"v_mfma_f32_16x16x32_bf16 v[128:131],a[192:195],a[128:131],v[128:131]\n"
"ds_read_b64_tr_b16 a[216:217],v17 offset:40960\n"
"v_mfma_f32_16x16x32_bf16 v[128:131],a[196:199],a[144:147],v[128:131]\n"
"v_mfma_f32_16x16x32_bf16 v[160:163],a[192:195],a[132:135],v[160:163]\n"
"ds_read_b64_tr_b16 a[218:219],v17 offset:43008\n"
"v_mfma_f32_16x16x32_bf16 v[160:163],a[196:199],a[148:151],v[160:163]\n"
"v_mfma_f32_16x16x32_bf16 v[192:195],a[192:195],a[136:139],v[192:195]\n"
"ds_read_b64_tr_b16 a[220:221],v17 offset:45056\n"
"v_mfma_f32_16x16x32_bf16 v[192:195],a[196:199],a[152:155],v[192:195]\n"
"s_barrier\n"
"v_mfma_f32_16x16x32_bf16 v[224:227],a[192:195],a[140:143],v[224:227]\n"
"ds_read_b64_tr_b16 a[222:223],v17 offset:47104\n"
"v_mfma_f32_16x16x32_bf16 v[224:227],a[196:199],a[156:159],v[224:227]\n"
"s_waitcnt lgkmcnt(8)\n"
"v_mfma_f32_16x16x32_bf16 v[136:139],a[200:203],a[128:131],v[136:139]\n"
"s_mov_b32 s58,0x1c000\n"
"s_add_u32 m0,s57,0x8000\n"
"ds_read_b64_tr_b16 a[224:225],v18 offset:32768\n"
"v_mfma_f32_16x16x32_bf16 v[136:139],a[204:207],a[144:147],v[136:139]\n"
"buffer_load_dwordx4 v24,s[36:39],s58 offen offset:0 lds\n"
"v_mfma_f32_16x16x32_bf16 v[168:171],a[200:203],a[132:135],v[168:171]\n"
"ds_read_b64_tr_b16 a[226:227],v18 offset:34816\n"
"v_mfma_f32_16x16x32_bf16 v[168:171],a[204:207],a[148:151],v[168:171]\n"
"v_mfma_f32_16x16x32_bf16 v[200:203],a[200:203],a[136:139],v[200:203]\n"
"ds_read_b64_tr_b16 a[228:229],v18 offset:36864\n"
"v_mfma_f32_16x16x32_bf16 v[200:203],a[204:207],a[152:155],v[200:203]\n"
"buffer_load_dwordx4 v25,s[36:39],s58 offen offset:1024 lds\n"
"v_mfma_f32_16x16x32_bf16 v[232:235],a[200:203],a[140:143],v[232:235]\n"
"ds_read_b64_tr_b16 a[230:231],v18 offset:38912\n"
"v_mfma_f32_16x16x32_bf16 v[232:235],a[204:207],a[156:159],v[232:235]\n"
"s_waitcnt lgkmcnt(8)\n"
"v_mfma_f32_16x16x32_bf16 v[132:135],a[208:211],a[128:131],v[132:135]\n"
"ds_read_b64_tr_b16 a[232:233],v18 offset:40960\n"
"v_mfma_f32_16x16x32_bf16 v[132:135],a[212:215],a[144:147],v[132:135]\n"
"buffer_load_dwordx4 v26,s[36:39],s58 offen offset:2048 lds\n"
"v_mfma_f32_16x16x32_bf16 v[164:167],a[208:211],a[132:135],v[164:167]\n"
"ds_read_b64_tr_b16 a[234:235],v18 offset:43008\n"
"v_mfma_f32_16x16x32_bf16 v[164:167],a[212:215],a[148:151],v[164:167]\n"
"v_mfma_f32_16x16x32_bf16 v[196:199],a[208:211],a[136:139],v[196:199]\n"
"ds_read_b64_tr_b16 a[236:237],v18 offset:45056\n"
"v_mfma_f32_16x16x32_bf16 v[196:199],a[212:215],a[152:155],v[196:199]\n"
"buffer_load_dwordx4 v27,s[36:39],s58 offen offset:3072 lds\n"
"v_mfma_f32_16x16x32_bf16 v[228:231],a[208:211],a[140:143],v[228:231]\n"
"ds_read_b64_tr_b16 a[238:239],v18 offset:47104\n"
"v_mfma_f32_16x16x32_bf16 v[228:231],a[212:215],a[156:159],v[228:231]\n"
"s_waitcnt lgkmcnt(8)\n"
"v_mfma_f32_16x16x32_bf16 v[140:143],a[216:219],a[128:131],v[140:143]\n"
"s_add_u32 m0,s57,0x9000\n"
"ds_read_b64_tr_b16 a[240:241],v19 offset:32768\n"
"v_mfma_f32_16x16x32_bf16 v[140:143],a[220:223],a[144:147],v[140:143]\n"
"buffer_load_dwordx4 v28,s[36:39],s58 offen offset:0 lds\n"
"v_mfma_f32_16x16x32_bf16 v[172:175],a[216:219],a[132:135],v[172:175]\n"
"ds_read_b64_tr_b16 a[242:243],v19 offset:34816\n"
"v_mfma_f32_16x16x32_bf16 v[172:175],a[220:223],a[148:151],v[172:175]\n"
"v_mfma_f32_16x16x32_bf16 v[204:207],a[216:219],a[136:139],v[204:207]\n"
"ds_read_b64_tr_b16 a[244:245],v19 offset:36864\n"
"v_mfma_f32_16x16x32_bf16 v[204:207],a[220:223],a[152:155],v[204:207]\n"
"buffer_load_dwordx4 v29,s[36:39],s58 offen offset:1024 lds\n"
"v_mfma_f32_16x16x32_bf16 v[236:239],a[216:219],a[140:143],v[236:239]\n"
"ds_read_b64_tr_b16 a[246:247],v19 offset:38912\n"
"v_mfma_f32_16x16x32_bf16 v[236:239],a[220:223],a[156:159],v[236:239]\n"
"s_waitcnt lgkmcnt(8)\n"
"v_mfma_f32_16x16x32_bf16 v[144:147],a[224:227],a[128:131],v[144:147]\n"
"ds_read_b64_tr_b16 a[248:249],v19 offset:40960\n"
"v_mfma_f32_16x16x32_bf16 v[144:147],a[228:231],a[144:147],v[144:147]\n"
"buffer_load_dwordx4 v30,s[36:39],s58 offen offset:2048 lds\n"
"v_mfma_f32_16x16x32_bf16 v[176:179],a[224:227],a[132:135],v[176:179]\n"
"ds_read_b64_tr_b16 a[250:251],v19 offset:43008\n"
"v_mfma_f32_16x16x32_bf16 v[176:179],a[228:231],a[148:151],v[176:179]\n"
"v_mfma_f32_16x16x32_bf16 v[208:211],a[224:227],a[136:139],v[208:211]\n"
"ds_read_b64_tr_b16 a[252:253],v19 offset:45056\n"
"v_mfma_f32_16x16x32_bf16 v[208:211],a[228:231],a[152:155],v[208:211]\n"
"buffer_load_dwordx4 v31,s[36:39],s58 offen offset:3072 lds\n"
"v_mfma_f32_16x16x32_bf16 v[240:243],a[224:227],a[140:143],v[240:243]\n"
"ds_read_b64_tr_b16 a[254:255],v19 offset:47104\n"
"v_mfma_f32_16x16x32_bf16 v[240:243],a[228:231],a[156:159],v[240:243]\n"
"s_waitcnt lgkmcnt(8)\n"
"v_mfma_f32_16x16x32_bf16 v[152:155],a[232:235],a[128:131],v[152:155]\n"
"ds_read_b64_tr_b16 a[192:193],v16 offset:49152\n"
"v_mfma_f32_16x16x32_bf16 v[152:155],a[236:239],a[144:147],v[152:155]\n"
"v_mfma_f32_16x16x32_bf16 v[184:187],a[232:235],a[132:135],v[184:187]\n"
"ds_read_b64_tr_b16 a[194:195],v16 offset:51200\n"
"v_mfma_f32_16x16x32_bf16 v[184:187],a[236:239],a[148:151],v[184:187]\n"
"v_mfma_f32_16x16x32_bf16 v[216:219],a[232:235],a[136:139],v[216:219]\n"
"ds_read_b64_tr_b16 a[196:197],v16 offset:53248\n"
"v_mfma_f32_16x16x32_bf16 v[216:219],a[236:239],a[152:155],v[216:219]\n"
"v_mfma_f32_16x16x32_bf16 v[248:251],a[232:235],a[140:143],v[248:251]\n"
"ds_read_b64_tr_b16 a[198:199],v16 offset:55296\n"
"v_mfma_f32_16x16x32_bf16 v[248:251],a[236:239],a[156:159],v[248:251]\n"
"s_waitcnt lgkmcnt(8)\n"
"v_mfma_f32_16x16x32_bf16 v[148:151],a[240:243],a[128:131],v[148:151]\n"
"ds_read_b64_tr_b16 a[200:201],v16 offset:57344\n"
"v_mfma_f32_16x16x32_bf16 v[148:151],a[244:247],a[144:147],v[148:151]\n"
"v_mfma_f32_16x16x32_bf16 v[180:183],a[240:243],a[132:135],v[180:183]\n"
"ds_read_b64_tr_b16 a[202:203],v16 offset:59392\n"
"v_mfma_f32_16x16x32_bf16 v[180:183],a[244:247],a[148:151],v[180:183]\n"
"v_mfma_f32_16x16x32_bf16 v[212:215],a[240:243],a[136:139],v[212:215]\n"
"ds_read_b64_tr_b16 a[204:205],v16 offset:61440\n"
"v_mfma_f32_16x16x32_bf16 v[212:215],a[244:247],a[152:155],v[212:215]\n"
"v_mfma_f32_16x16x32_bf16 v[244:247],a[240:243],a[140:143],v[244:247]\n"
"ds_read_b64_tr_b16 a[206:207],v16 offset:63488\n"
"v_mfma_f32_16x16x32_bf16 v[244:247],a[244:247],a[156:159],v[244:247]\n"
"s_waitcnt lgkmcnt(8)\n"
"v_mfma_f32_16x16x32_bf16 v[156:159],a[248:251],a[128:131],v[156:159]\n"
"ds_read_b64_tr_b16 a[208:209],v17 offset:49152\n"
"v_mfma_f32_16x16x32_bf16 v[156:159],a[252:255],a[144:147],v[156:159]\n"
"v_mfma_f32_16x16x32_bf16 v[188:191],a[248:251],a[132:135],v[188:191]\n"
"ds_read_b64_tr_b16 a[210:211],v17 offset:51200\n"
"v_mfma_f32_16x16x32_bf16 v[188:191],a[252:255],a[148:151],v[188:191]\n"
"v_mfma_f32_16x16x32_bf16 v[220:223],a[248:251],a[136:139],v[220:223]\n"
"ds_read_b64_tr_b16 a[212:213],v17 offset:53248\n"
"v_mfma_f32_16x16x32_bf16 v[220:223],a[252:255],a[152:155],v[220:223]\n"
"v_mfma_f32_16x16x32_bf16 v[252:255],a[248:251],a[140:143],v[252:255]\n"
"s_waitcnt vmcnt(32)\n"
"ds_read_b64_tr_b16 a[214:215],v17 offset:55296\n"
"v_mfma_f32_16x16x32_bf16 v[252:255],a[252:255],a[156:159],v[252:255]\n"
"s_waitcnt lgkmcnt(8)\n"
"v_mfma_f32_16x16x32_bf16 v[128:131],a[192:195],a[160:163],v[128:131]\n"
"ds_read_b64_tr_b16 a[216:217],v17 offset:57344\n"
"v_mfma_f32_16x16x32_bf16 v[128:131],a[196:199],a[176:179],v[128:131]\n"
"v_mfma_f32_16x16x32_bf16 v[160:163],a[192:195],a[164:167],v[160:163]\n"
"ds_read_b64_tr_b16 a[218:219],v17 offset:59392\n"
"v_mfma_f32_16x16x32_bf16 v[160:163],a[196:199],a[180:183],v[160:163]\n"
"v_mfma_f32_16x16x32_bf16 v[192:195],a[192:195],a[168:171],v[192:195]\n"
"ds_read_b64_tr_b16 a[220:221],v17 offset:61440\n"
"v_mfma_f32_16x16x32_bf16 v[192:195],a[196:199],a[184:187],v[192:195]\n"
"v_mfma_f32_16x16x32_bf16 v[224:227],a[192:195],a[172:175],v[224:227]\n"
"ds_read_b64_tr_b16 a[222:223],v17 offset:63488\n"
"v_mfma_f32_16x16x32_bf16 v[224:227],a[196:199],a[188:191],v[224:227]\n"
"s_waitcnt lgkmcnt(8)\n"
"v_mfma_f32_16x16x32_bf16 v[136:139],a[200:203],a[160:163],v[136:139]\n"
"ds_read_b64_tr_b16 a[224:225],v18 offset:49152\n"
"v_mfma_f32_16x16x32_bf16 v[136:139],a[204:207],a[176:179],v[136:139]\n"
"v_mfma_f32_16x16x32_bf16 v[168:171],a[200:203],a[164:167],v[168:171]\n"
"ds_read_b64_tr_b16 a[226:227],v18 offset:51200\n"
"v_mfma_f32_16x16x32_bf16 v[168:171],a[204:207],a[180:183],v[168:171]\n"
"v_mfma_f32_16x16x32_bf16 v[200:203],a[200:203],a[168:171],v[200:203]\n"
"ds_read_b64_tr_b16 a[228:229],v18 offset:53248\n"
"v_mfma_f32_16x16x32_bf16 v[200:203],a[204:207],a[184:187],v[200:203]\n"
"v_mfma_f32_16x16x32_bf16 v[232:235],a[200:203],a[172:175],v[232:235]\n"
"ds_read_b64_tr_b16 a[230:231],v18 offset:55296\n"
"v_mfma_f32_16x16x32_bf16 v[232:235],a[204:207],a[188:191],v[232:235]\n"
"s_waitcnt lgkmcnt(8)\n"
"v_mfma_f32_16x16x32_bf16 v[132:135],a[208:211],a[160:163],v[132:135]\n"
"ds_read_b64_tr_b16 a[232:233],v18 offset:57344\n"
"v_mfma_f32_16x16x32_bf16 v[132:135],a[212:215],a[176:179],v[132:135]\n"
"v_mfma_f32_16x16x32_bf16 v[164:167],a[208:211],a[164:167],v[164:167]\n"
"ds_read_b64_tr_b16 a[234:235],v18 offset:59392\n"
"v_mfma_f32_16x16x32_bf16 v[164:167],a[212:215],a[180:183],v[164:167]\n"
"v_mfma_f32_16x16x32_bf16 v[196:199],a[208:211],a[168:171],v[196:199]\n"
"ds_read_b64_tr_b16 a[236:237],v18 offset:61440\n"
"v_mfma_f32_16x16x32_bf16 v[196:199],a[212:215],a[184:187],v[196:199]\n"
"v_mfma_f32_16x16x32_bf16 v[228:231],a[208:211],a[172:175],v[228:231]\n"
"ds_read_b64_tr_b16 a[238:239],v18 offset:63488\n"
"v_mfma_f32_16x16x32_bf16 v[228:231],a[212:215],a[188:191],v[228:231]\n"
"s_waitcnt lgkmcnt(8)\n"
"v_mfma_f32_16x16x32_bf16 v[140:143],a[216:219],a[160:163],v[140:143]\n"
"ds_read_b64_tr_b16 a[240:241],v19 offset:49152\n"
"v_mfma_f32_16x16x32_bf16 v[140:143],a[220:223],a[176:179],v[140:143]\n"
"v_mfma_f32_16x16x32_bf16 v[172:175],a[216:219],a[164:167],v[172:175]\n"
"ds_read_b64_tr_b16 a[242:243],v19 offset:51200\n"
"v_mfma_f32_16x16x32_bf16 v[172:175],a[220:223],a[180:183],v[172:175]\n"
"v_mfma_f32_16x16x32_bf16 v[204:207],a[216:219],a[168:171],v[204:207]\n"
"ds_read_b64_tr_b16 a[244:245],v19 offset:53248\n"
"v_mfma_f32_16x16x32_bf16 v[204:207],a[220:223],a[184:187],v[204:207]\n"
"v_mfma_f32_16x16x32_bf16 v[236:239],a[216:219],a[172:175],v[236:239]\n"
"ds_read_b64_tr_b16 a[246:247],v19 offset:55296\n"
"v_mfma_f32_16x16x32_bf16 v[236:239],a[220:223],a[188:191],v[236:239]\n"
"s_waitcnt lgkmcnt(8)\n"
"v_mfma_f32_16x16x32_bf16 v[144:147],a[224:227],a[160:163],v[144:147]\n"
"ds_read_b64_tr_b16 a[248:249],v19 offset:57344\n"
"v_mfma_f32_16x16x32_bf16 v[144:147],a[228:231],a[176:179],v[144:147]\n"
"v_mfma_f32_16x16x32_bf16 v[176:179],a[224:227],a[164:167],v[176:179]\n"
"ds_read_b64_tr_b16 a[250:251],v19 offset:59392\n"
"v_mfma_f32_16x16x32_bf16 v[176:179],a[228:231],a[180:183],v[176:179]\n"
"v_mfma_f32_16x16x32_bf16 v[208:211],a[224:227],a[168:171],v[208:211]\n"
"ds_read_b64_tr_b16 a[252:253],v19 offset:61440\n"
"v_mfma_f32_16x16x32_bf16 v[208:211],a[228:231],a[184:187],v[208:211]\n"
"v_mfma_f32_16x16x32_bf16 v[240:243],a[224:227],a[172:175],v[240:243]\n"
"ds_read_b64_tr_b16 a[254:255],v19 offset:63488\n"
"v_mfma_f32_16x16x32_bf16 v[240:243],a[228:231],a[188:191],v[240:243]\n"
"s_waitcnt lgkmcnt(8)\n"
"v_mfma_f32_16x16x32_bf16 v[152:155],a[232:235],a[160:163],v[152:155]\n"
"ds_read_b64_tr_b16 a[192:193],v20 offset:0\n"
"v_mfma_f32_16x16x32_bf16 v[152:155],a[236:239],a[176:179],v[152:155]\n"
"v_mfma_f32_16x16x32_bf16 v[184:187],a[232:235],a[164:167],v[184:187]\n"
"ds_read_b64_tr_b16 a[194:195],v20 offset:2048\n"
"v_mfma_f32_16x16x32_bf16 v[184:187],a[236:239],a[180:183],v[184:187]\n"
"v_mfma_f32_16x16x32_bf16 v[216:219],a[232:235],a[168:171],v[216:219]\n"
"ds_read_b64_tr_b16 a[196:197],v20 offset:4096\n"
"v_mfma_f32_16x16x32_bf16 v[216:219],a[236:239],a[184:187],v[216:219]\n"
"v_mfma_f32_16x16x32_bf16 v[248:251],a[232:235],a[172:175],v[248:251]\n"
"ds_read_b64_tr_b16 a[198:199],v20 offset:6144\n"
"v_mfma_f32_16x16x32_bf16 v[248:251],a[236:239],a[188:191],v[248:251]\n"
"s_waitcnt lgkmcnt(8)\n"
"v_mfma_f32_16x16x32_bf16 v[148:151],a[240:243],a[160:163],v[148:151]\n"
"ds_read_b64_tr_b16 a[200:201],v20 offset:8192\n"
"v_mfma_f32_16x16x32_bf16 v[148:151],a[244:247],a[176:179],v[148:151]\n"
"v_mfma_f32_16x16x32_bf16 v[180:183],a[240:243],a[164:167],v[180:183]\n"
"ds_read_b64_tr_b16 a[202:203],v20 offset:10240\n"
"v_mfma_f32_16x16x32_bf16 v[180:183],a[244:247],a[180:183],v[180:183]\n"
"v_mfma_f32_16x16x32_bf16 v[212:215],a[240:243],a[168:171],v[212:215]\n"
"ds_read_b64_tr_b16 a[204:205],v20 offset:12288\n"
"v_mfma_f32_16x16x32_bf16 v[212:215],a[244:247],a[184:187],v[212:215]\n"
"v_mfma_f32_16x16x32_bf16 v[244:247],a[240:243],a[172:175],v[244:247]\n"
"ds_read_b64_tr_b16 a[206:207],v20 offset:14336\n"
"v_mfma_f32_16x16x32_bf16 v[244:247],a[244:247],a[188:191],v[244:247]\n"
"s_waitcnt lgkmcnt(8)\n"
"v_mfma_f32_16x16x32_bf16 v[156:159],a[248:251],a[160:163],v[156:159]\n"
"ds_read_b64_tr_b16 a[208:209],v21 offset:0\n"
"v_mfma_f32_16x16x32_bf16 v[156:159],a[252:255],a[176:179],v[156:159]\n"
"v_mfma_f32_16x16x32_bf16 v[188:191],a[248:251],a[164:167],v[188:191]\n"
"ds_read_b64_tr_b16 a[210:211],v21 offset:2048\n"
"v_mfma_f32_16x16x32_bf16 v[188:191],a[252:255],a[180:183],v[188:191]\n"
"v_mfma_f32_16x16x32_bf16 v[220:223],a[248:251],a[168:171],v[220:223]\n"
"ds_read_b64_tr_b16 a[212:213],v21 offset:4096\n"
"v_mfma_f32_16x16x32_bf16 v[220:223],a[252:255],a[184:187],v[220:223]\n"
"v_mfma_f32_16x16x32_bf16 v[252:255],a[248:251],a[172:175],v[252:255]\n"
"s_waitcnt vmcnt(24)\n"
"ds_read_b64_tr_b16 a[214:215],v21 offset:6144\n"
"v_mfma_f32_16x16x32_bf16 v[252:255],a[252:255],a[188:191],v[252:255]\n"
"s_waitcnt lgkmcnt(8)\n"
"v_mfma_f32_16x16x32_bf16 v[128:131],a[192:195],v[64:67],v[128:131]\n"
"ds_read_b64_tr_b16 a[216:217],v21 offset:8192\n"
"v_mfma_f32_16x16x32_bf16 v[128:131],a[196:199],v[80:83],v[128:131]\n"
"v_mfma_f32_16x16x32_bf16 v[160:163],a[192:195],v[68:71],v[160:163]\n"
"ds_read_b64_tr_b16 a[218:219],v21 offset:10240\n"
"v_mfma_f32_16x16x32_bf16 v[160:163],a[196:199],v[84:87],v[160:163]\n"
"v_mfma_f32_16x16x32_bf16 v[192:195],a[192:195],v[72:75],v[192:195]\n"
"ds_read_b64_tr_b16 a[220:221],v21 offset:12288\n"
"v_mfma_f32_16x16x32_bf16 v[192:195],a[196:199],v[88:91],v[192:195]\n"
"s_barrier\n"
"v_mfma_f32_16x16x32_bf16 v[224:227],a[192:195],v[76:79],v[224:227]\n"
"s_mov_b32 s58,0x2a000\n"
"ds_read_b64_tr_b16 a[222:223],v21 offset:14336\n"
"v_mfma_f32_16x16x32_bf16 v[224:227],a[196:199],v[92:95],v[224:227]\n"
"s_waitcnt lgkmcnt(8)\n"
"v_mfma_f32_16x16x32_bf16 v[136:139],a[200:203],v[64:67],v[136:139]\n"
"s_add_u32 m0,s57,0x18000\n"
"ds_read_b64_tr_b16 a[224:225],v22 offset:0\n"
"v_mfma_f32_16x16x32_bf16 v[136:139],a[204:207],v[80:83],v[136:139]\n"
"buffer_load_dwordx4 v24,s[36:39],s58 offen offset:0 lds\n"
"v_mfma_f32_16x16x32_bf16 v[168:171],a[200:203],v[68:71],v[168:171]\n"
"ds_read_b64_tr_b16 a[226:227],v22 offset:2048\n"
"v_mfma_f32_16x16x32_bf16 v[168:171],a[204:207],v[84:87],v[168:171]\n"
"v_mfma_f32_16x16x32_bf16 v[200:203],a[200:203],v[72:75],v[200:203]\n"
"ds_read_b64_tr_b16 a[228:229],v22 offset:4096\n"
"v_mfma_f32_16x16x32_bf16 v[200:203],a[204:207],v[88:91],v[200:203]\n"
"buffer_load_dwordx4 v25,s[36:39],s58 offen offset:1024 lds\n"
"v_mfma_f32_16x16x32_bf16 v[232:235],a[200:203],v[76:79],v[232:235]\n"
"ds_read_b64_tr_b16 a[230:231],v22 offset:6144\n"
"v_mfma_f32_16x16x32_bf16 v[232:235],a[204:207],v[92:95],v[232:235]\n"
"s_waitcnt lgkmcnt(8)\n"
"v_mfma_f32_16x16x32_bf16 v[132:135],a[208:211],v[64:67],v[132:135]\n"
"ds_read_b64_tr_b16 a[232:233],v22 offset:8192\n"
"v_mfma_f32_16x16x32_bf16 v[132:135],a[212:215],v[80:83],v[132:135]\n"
"buffer_load_dwordx4 v26,s[36:39],s58 offen offset:2048 lds\n"
"v_mfma_f32_16x16x32_bf16 v[164:167],a[208:211],v[68:71],v[164:167]\n"
"ds_read_b64_tr_b16 a[234:235],v22 offset:10240\n"
"v_mfma_f32_16x16x32_bf16 v[164:167],a[212:215],v[84:87],v[164:167]\n"
"v_mfma_f32_16x16x32_bf16 v[196:199],a[208:211],v[72:75],v[196:199]\n"
"ds_read_b64_tr_b16 a[236:237],v22 offset:12288\n"
"v_mfma_f32_16x16x32_bf16 v[196:199],a[212:215],v[88:91],v[196:199]\n"
"buffer_load_dwordx4 v27,s[36:39],s58 offen offset:3072 lds\n"
"v_mfma_f32_16x16x32_bf16 v[228:231],a[208:211],v[76:79],v[228:231]\n"
"ds_read_b64_tr_b16 a[238:239],v22 offset:14336\n"
"v_mfma_f32_16x16x32_bf16 v[228:231],a[212:215],v[92:95],v[228:231]\n"
"s_waitcnt lgkmcnt(8)\n"
"v_mfma_f32_16x16x32_bf16 v[140:143],a[216:219],v[64:67],v[140:143]\n"
"s_add_u32 m0,s57,0x19000\n"
"ds_read_b64_tr_b16 a[240:241],v23 offset:0\n"
"v_mfma_f32_16x16x32_bf16 v[140:143],a[220:223],v[80:83],v[140:143]\n"
"buffer_load_dwordx4 v28,s[36:39],s58 offen offset:0 lds\n"
"v_mfma_f32_16x16x32_bf16 v[172:175],a[216:219],v[68:71],v[172:175]\n"
"ds_read_b64_tr_b16 a[242:243],v23 offset:2048\n"
"v_mfma_f32_16x16x32_bf16 v[172:175],a[220:223],v[84:87],v[172:175]\n"
"v_mfma_f32_16x16x32_bf16 v[204:207],a[216:219],v[72:75],v[204:207]\n"
"ds_read_b64_tr_b16 a[244:245],v23 offset:4096\n"
"v_mfma_f32_16x16x32_bf16 v[204:207],a[220:223],v[88:91],v[204:207]\n"
"buffer_load_dwordx4 v29,s[36:39],s58 offen offset:1024 lds\n"
"v_mfma_f32_16x16x32_bf16 v[236:239],a[216:219],v[76:79],v[236:239]\n"
"ds_read_b64_tr_b16 a[246:247],v23 offset:6144\n"
"v_mfma_f32_16x16x32_bf16 v[236:239],a[220:223],v[92:95],v[236:239]\n"
"s_waitcnt lgkmcnt(8)\n"
"v_mfma_f32_16x16x32_bf16 v[144:147],a[224:227],v[64:67],v[144:147]\n"
"ds_read_b64_tr_b16 a[248:249],v23 offset:8192\n"
"v_mfma_f32_16x16x32_bf16 v[144:147],a[228:231],v[80:83],v[144:147]\n"
"buffer_load_dwordx4 v30,s[36:39],s58 offen offset:2048 lds\n"
"v_mfma_f32_16x16x32_bf16 v[176:179],a[224:227],v[68:71],v[176:179]\n"
"ds_read_b64_tr_b16 a[250:251],v23 offset:10240\n"
"v_mfma_f32_16x16x32_bf16 v[176:179],a[228:231],v[84:87],v[176:179]\n"
"v_mul_f32 v128,v128,v136\n"
"v_mul_f32 v129,v129,v137\n"
"v_mfma_f32_16x16x32_bf16 v[208:211],a[224:227],v[72:75],v[208:211]\n"
"ds_read_b64_tr_b16 a[252:253],v23 offset:12288\n"
"v_mul_f32 v130,v130,v138\n"
"v_mfma_f32_16x16x32_bf16 v[208:211],a[228:231],v[88:91],v[208:211]\n"
"buffer_load_dwordx4 v31,s[36:39],s58 offen offset:3072 lds\n"
"v_mfma_f32_16x16x32_bf16 v[240:243],a[224:227],v[76:79],v[240:243]\n"
"ds_read_b64_tr_b16 a[254:255],v23 offset:14336\n"
"v_mul_f32 v131,v131,v139\n"
"v_mfma_f32_16x16x32_bf16 v[240:243],a[228:231],v[92:95],v[240:243]\n"
"s_waitcnt lgkmcnt(8)\n"
"v_mul_f32 v132,v132,v140\n"
"v_mul_f32 v133,v133,v141\n"
"v_mfma_f32_16x16x32_bf16 v[152:155],a[232:235],v[64:67],v[152:155]\n"
"v_mul_f32 v134,v134,v142\n"
"v_mul_f32 v135,v135,v143\n"
"v_mfma_f32_16x16x32_bf16 v[152:155],a[236:239],v[80:83],v[152:155]\n"
"v_cvt_pk_bf16_f32 v128,v128,v129\n"
"v_cvt_pk_bf16_f32 v129,v130,v131\n"
"v_mfma_f32_16x16x32_bf16 v[184:187],a[232:235],v[68:71],v[184:187]\n"
"v_cvt_pk_bf16_f32 v130,v132,v133\n"
"v_cvt_pk_bf16_f32 v131,v134,v135\n"
"v_mfma_f32_16x16x32_bf16 v[184:187],a[236:239],v[84:87],v[184:187]\n"
"s_mov_b32 s59,0x0\n"
"buffer_store_dwordx4 v[128:131],v8,s[60:63],s59 offen offset:0 sc1\n"
"v_mfma_f32_16x16x32_bf16 v[216:219],a[232:235],v[72:75],v[216:219]\n"
"v_mul_f32 v160,v160,v168\n"
"v_mul_f32 v161,v161,v169\n"
"v_mfma_f32_16x16x32_bf16 v[216:219],a[236:239],v[88:91],v[216:219]\n"
"v_mul_f32 v162,v162,v170\n"
"v_mul_f32 v163,v163,v171\n"
"v_mfma_f32_16x16x32_bf16 v[248:251],a[232:235],v[76:79],v[248:251]\n"
"v_mul_f32 v164,v164,v172\n"
"v_mul_f32 v165,v165,v173\n"
"v_mfma_f32_16x16x32_bf16 v[248:251],a[236:239],v[92:95],v[248:251]\n"
"s_waitcnt lgkmcnt(4)\n"
"v_mul_f32 v166,v166,v174\n"
"v_mul_f32 v167,v167,v175\n"
"v_mfma_f32_16x16x32_bf16 v[148:151],a[240:243],v[64:67],v[148:151]\n"
"v_cvt_pk_bf16_f32 v160,v160,v161\n"
"v_cvt_pk_bf16_f32 v161,v162,v163\n"
"v_mfma_f32_16x16x32_bf16 v[148:151],a[244:247],v[80:83],v[148:151]\n"
"s_waitcnt lgkmcnt(0)\n"
"v_cvt_pk_bf16_f32 v162,v164,v165\n"
"v_cvt_pk_bf16_f32 v163,v166,v167\n"
"v_mfma_f32_16x16x32_bf16 v[180:183],a[240:243],v[68:71],v[180:183]\n"
"s_barrier\n"
"v_mfma_f32_16x16x32_bf16 v[180:183],a[244:247],v[84:87],v[180:183]\n"
"s_mov_b32 s59,0xe000\n"
"buffer_store_dwordx4 v[160:163],v8,s[60:63],s59 offen offset:0 sc1\n"
"v_mfma_f32_16x16x32_bf16 v[212:215],a[240:243],v[72:75],v[212:215]\n"
"v_mfma_f32_16x16x32_bf16 v[212:215],a[244:247],v[88:91],v[212:215]\n"
"v_mfma_f32_16x16x32_bf16 v[244:247],a[240:243],v[76:79],v[244:247]\n"
"v_mfma_f32_16x16x32_bf16 v[244:247],a[244:247],v[92:95],v[244:247]\n"
"v_mfma_f32_16x16x32_bf16 v[156:159],a[248:251],v[64:67],v[156:159]\n"
"v_mfma_f32_16x16x32_bf16 v[156:159],a[252:255],v[80:83],v[156:159]\n"
"v_mfma_f32_16x16x32_bf16 v[188:191],a[248:251],v[68:71],v[188:191]\n"
"v_mfma_f32_16x16x32_bf16 v[188:191],a[252:255],v[84:87],v[188:191]\n"
"v_mfma_f32_16x16x32_bf16 v[220:223],a[248:251],v[72:75],v[220:223]\n"
"v_mfma_f32_16x16x32_bf16 v[220:223],a[252:255],v[88:91],v[220:223]\n"
"v_mfma_f32_16x16x32_bf16 v[252:255],a[248:251],v[76:79],v[252:255]\n"
"v_mfma_f32_16x16x32_bf16 v[252:255],a[252:255],v[92:95],v[252:255]\n"
"v_mul_f32 v192,v192,v200\n"
"v_mul_f32 v193,v193,v201\n"
"v_mul_f32 v194,v194,v202\n"
"v_mul_f32 v195,v195,v203\n"
"v_mul_f32 v196,v196,v204\n"
"v_mul_f32 v197,v197,v205\n"
"v_mul_f32 v198,v198,v206\n"
"v_mul_f32 v199,v199,v207\n"
"v_cvt_pk_bf16_f32 v192,v192,v193\n"
"v_cvt_pk_bf16_f32 v193,v194,v195\n"
"v_cvt_pk_bf16_f32 v194,v196,v197\n"
"v_cvt_pk_bf16_f32 v195,v198,v199\n"
"s_mov_b32 s59,0x1c000\n"
"buffer_store_dwordx4 v[192:195],v8,s[60:63],s59 offen offset:0 sc1\n"
"v_mul_f32 v224,v224,v232\n"
"v_mul_f32 v225,v225,v233\n"
"v_mul_f32 v226,v226,v234\n"
"v_mul_f32 v227,v227,v235\n"
"v_mul_f32 v228,v228,v236\n"
"v_mul_f32 v229,v229,v237\n"
"v_mul_f32 v230,v230,v238\n"
"v_mul_f32 v231,v231,v239\n"
"v_cvt_pk_bf16_f32 v224,v224,v225\n"
"v_cvt_pk_bf16_f32 v225,v226,v227\n"
"v_cvt_pk_bf16_f32 v226,v228,v229\n"
"v_cvt_pk_bf16_f32 v227,v230,v231\n"
"s_mov_b32 s59,0x2a000\n"
"buffer_store_dwordx4 v[224:227],v8,s[60:63],s59 offen offset:0 sc1\n"
"v_mul_f32 v144,v144,v152\n"
"v_mul_f32 v145,v145,v153\n"
"v_mul_f32 v146,v146,v154\n"
"v_mul_f32 v147,v147,v155\n"
"v_mul_f32 v148,v148,v156\n"
"v_mul_f32 v149,v149,v157\n"
"v_mul_f32 v150,v150,v158\n"
"v_mul_f32 v151,v151,v159\n"
"v_cvt_pk_bf16_f32 v144,v144,v145\n"
"v_cvt_pk_bf16_f32 v145,v146,v147\n"
"v_cvt_pk_bf16_f32 v146,v148,v149\n"
"v_cvt_pk_bf16_f32 v147,v150,v151\n"
"s_mov_b32 s59,0x0\n"
"buffer_store_dwordx4 v[144:147],v8,s[60:63],s59 offen offset:1024 sc1\n"
"v_mul_f32 v176,v176,v184\n"
"v_mul_f32 v177,v177,v185\n"
"v_mul_f32 v178,v178,v186\n"
"v_mul_f32 v179,v179,v187\n"
"v_mul_f32 v180,v180,v188\n"
"v_mul_f32 v181,v181,v189\n"
"v_mul_f32 v182,v182,v190\n"
"v_mul_f32 v183,v183,v191\n"
"v_cvt_pk_bf16_f32 v176,v176,v177\n"
"v_cvt_pk_bf16_f32 v177,v178,v179\n"
"v_cvt_pk_bf16_f32 v178,v180,v181\n"
"v_cvt_pk_bf16_f32 v179,v182,v183\n"
"s_mov_b32 s59,0xe000\n"
"buffer_store_dwordx4 v[176:179],v8,s[60:63],s59 offen offset:1024 sc1\n"
"v_mul_f32 v208,v208,v216\n"
"v_mul_f32 v209,v209,v217\n"
"v_mul_f32 v210,v210,v218\n"
"v_mul_f32 v211,v211,v219\n"
"v_mul_f32 v212,v212,v220\n"
"v_mul_f32 v213,v213,v221\n"
"v_mul_f32 v214,v214,v222\n"
"v_mul_f32 v215,v215,v223\n"
"v_cvt_pk_bf16_f32 v208,v208,v209\n"
"v_cvt_pk_bf16_f32 v209,v210,v211\n"
"v_cvt_pk_bf16_f32 v210,v212,v213\n"
"v_cvt_pk_bf16_f32 v211,v214,v215\n"
"s_mov_b32 s59,0x1c000\n"
"buffer_store_dwordx4 v[208:211],v8,s[60:63],s59 offen offset:1024 sc1\n"
"v_mul_f32 v240,v240,v248\n"
"v_mul_f32 v241,v241,v249\n"
"v_mul_f32 v242,v242,v250\n"
"v_mul_f32 v243,v243,v251\n"
"v_mul_f32 v244,v244,v252\n"
"v_mul_f32 v245,v245,v253\n"
"v_mul_f32 v246,v246,v254\n"
"v_mul_f32 v247,v247,v255\n"
"v_cvt_pk_bf16_f32 v240,v240,v241\n"
"v_cvt_pk_bf16_f32 v241,v242,v243\n"
"v_cvt_pk_bf16_f32 v242,v244,v245\n"
"v_cvt_pk_bf16_f32 v243,v246,v247\n"
"s_mov_b32 s59,0x2a000\n"
"buffer_store_dwordx4 v[240:243],v8,s[60:63],s59 offen offset:1024 sc1\n"
"s_lshl_b32 s76,s48,5\n"
"s_add_u32 s76,s76,s49\n"
"s_lshl_b32 s76,s76,3\n"
"s_add_u32 s84,s76,0x5000\n"
"s_add_u32 s84,s84,s32\n"
"s_addc_u32 s85,s33,0\n"
"s_lshl_b32 s76,s48,8\n"
"s_add_u32 s82,s76,0x5000\n"
"s_add_u32 s82,s82,s32\n"
"s_addc_u32 s83,s33,0\n"
"s_waitcnt vmcnt(0)\n"
"s_barrier\n"
"s_cmp_eq_u32 s11,0\n"
"s_cbranch_scc0 L_np4_%=\n"
"v_mov_b32 v2,s34\n"
"v_mov_b32 v3,s35\n"
"v_mov_b32 v4,0\n"
"s_mov_b64 exec,1\n"
"global_store_dwordx2 v4,v[2:3],s[84:85] sc1\n"
"s_mov_b64 exec,-1\n"
"L_np4_%=:\n"
"ds_read_b64_tr_b16 a[192:193],v40 offset:0\n"
"ds_read_b64_tr_b16 a[194:195],v40 offset:2048\n"
"ds_read_b64_tr_b16 a[196:197],v40 offset:4096\n"
"ds_read_b64_tr_b16 a[198:199],v40 offset:6144\n"
"ds_read_b64_tr_b16 a[200:201],v41 offset:0\n"
"ds_read_b64_tr_b16 a[202:203],v41 offset:2048\n"
"ds_read_b64_tr_b16 a[204:205],v41 offset:4096\n"
"ds_read_b64_tr_b16 a[206:207],v41 offset:6144\n"
"ds_read_b64_tr_b16 a[208:209],v42 offset:0\n"
"ds_read_b64_tr_b16 a[210:211],v42 offset:2048\n"
"ds_read_b64_tr_b16 a[212:213],v42 offset:4096\n"
"ds_read_b64_tr_b16 a[214:215],v42 offset:6144\n"
"ds_read_b64_tr_b16 a[216:217],v43 offset:0\n"
"ds_read_b64_tr_b16 a[218:219],v43 offset:2048\n"
"ds_read_b64_tr_b16 a[220:221],v43 offset:4096\n"
"ds_read_b64_tr_b16 a[222:223],v43 offset:6144\n"
"ds_read_b64_tr_b16 a[224:225],v32 offset:0\n"
"ds_read_b64_tr_b16 a[226:227],v32 offset:2048\n"
"ds_read_b64_tr_b16 a[228:229],v32 offset:4096\n"
"ds_read_b64_tr_b16 a[230:231],v32 offset:6144\n"
"ds_read_b64_tr_b16 a[232:233],v33 offset:0\n"
"ds_read_b64_tr_b16 a[234:235],v33 offset:2048\n"
"ds_read_b64_tr_b16 a[236:237],v33 offset:4096\n"
"ds_read_b64_tr_b16 a[238:239],v33 offset:6144\n"
"ds_read_b64_tr_b16 a[240:241],v34 offset:0\n"
"ds_read_b64_tr_b16 a[242:243],v34 offset:2048\n"
"ds_read_b64_tr_b16 a[244:245],v34 offset:4096\n"
"ds_read_b64_tr_b16 a[246:247],v34 offset:6144\n"
"ds_read_b64_tr_b16 a[248:249],v35 offset:0\n"
"ds_read_b64_tr_b16 a[250:251],v35 offset:2048\n"
"ds_read_b64_tr_b16 a[252:253],v35 offset:4096\n"
"ds_read_b64_tr_b16 a[254:255],v35 offset:6144\n"
"s_mov_b32 s58,0x38000\n"
"s_add_u32 m0,s57,0x20000\n"
"s_nop 0\n"
"buffer_load_dwordx4 v24,s[36:39],s58 offen offset:0 lds\n"
"buffer_load_dwordx4 v25,s[36:39],s58 offen offset:1024 lds\n"
"buffer_load_dwordx4 v26,s[36:39],s58 offen offset:2048 lds\n"
"buffer_load_dwordx4 v27,s[36:39],s58 offen offset:3072 lds\n"
"s_add_u32 m0,s57,0x21000\n"
"s_nop 0\n"
"buffer_load_dwordx4 v28,s[36:39],s58 offen offset:0 lds\n"
"buffer_load_dwordx4 v29,s[36:39],s58 offen offset:1024 lds\n"
"buffer_load_dwordx4 v30,s[36:39],s58 offen offset:2048 lds\n"
"buffer_load_dwordx4 v31,s[36:39],s58 offen offset:3072 lds\n"
"s_waitcnt lgkmcnt(0)\n"
"s_mov_b32 s58,0x46000\n"
"s_add_u32 m0,s57,0x10000\n"
"s_nop 0\n"
"buffer_load_dwordx4 v24,s[36:39],s58 offen offset:0 lds\n"
"buffer_load_dwordx4 v25,s[36:39],s58 offen offset:1024 lds\n"
"buffer_load_dwordx4 v26,s[36:39],s58 offen offset:2048 lds\n"
"buffer_load_dwordx4 v27,s[36:39],s58 offen offset:3072 lds\n"
"s_add_u32 m0,s57,0x11000\n"
"s_nop 0\n"
"buffer_load_dwordx4 v28,s[36:39],s58 offen offset:0 lds\n"
"buffer_load_dwordx4 v29,s[36:39],s58 offen offset:1024 lds\n"
"buffer_load_dwordx4 v30,s[36:39],s58 offen offset:2048 lds\n"
"buffer_load_dwordx4 v31,s[36:39],s58 offen offset:3072 lds\n"
"s_mov_b32 s58,0x54000\n"
"s_add_u32 m0,s57,0x0\n"
"s_nop 0\n"
"buffer_load_dwordx4 v24,s[36:39],s58 offen offset:0 lds\n"
"buffer_load_dwordx4 v25,s[36:39],s58 offen offset:1024 lds\n"
"buffer_load_dwordx4 v26,s[36:39],s58 offen offset:2048 lds\n"
"buffer_load_dwordx4 v27,s[36:39],s58 offen offset:3072 lds\n"
"s_add_u32 m0,s57,0x1000\n"
"s_nop 0\n"
"buffer_load_dwordx4 v28,s[36:39],s58 offen offset:0 lds\n"
"buffer_load_dwordx4 v29,s[36:39],s58 offen offset:1024 lds\n"
"buffer_load_dwordx4 v30,s[36:39],s58 offen offset:2048 lds\n"
"buffer_load_dwordx4 v31,s[36:39],s58 offen offset:3072 lds\n"
"buffer_load_dwordx4 v[96:99],v36,s[64:67],0 offen offset:0 sc1\n"
"buffer_load_dwordx4 v[100:103],v36,s[64:67],0 offen offset:896 sc1\n"
"buffer_load_dwordx4 v[104:107],v36,s[64:67],0 offen offset:64 sc1\n"
"buffer_load_dwordx4 v[108:111],v36,s[64:67],0 offen offset:960 sc1\n"
"s_add_u32 s76,s11,0\n"
"s_and_b32 s76,s76,3\n"
"s_mul_i32 s86,s76,57344\n"
"s_add_u32 s76,s11,1\n"
"s_and_b32 s76,s76,3\n"
"s_mul_i32 s87,s76,57344\n"
"s_add_u32 s76,s11,2\n"
"s_and_b32 s76,s76,3\n"
"s_mul_i32 s88,s76,57344\n"
"s_add_u32 s76,s11,3\n"
"s_and_b32 s76,s76,3\n"
"s_mul_i32 s89,s76,57344\n"
"v_add_u32 v9,s57,v8\n"
"s_sub_u32 s76,s11,1\n"
"s_and_b32 s76,s76,3\n"
"s_lshl_b32 s76,s76,13\n"
"v_add_u32 v12,s76,v8\n"
"s_sub_u32 s76,s11,2\n"
"s_and_b32 s76,s76,3\n"
"s_lshl_b32 s76,s76,13\n"
"v_add_u32 v13,s76,v8\n"
"s_sub_u32 s76,s11,3\n"
"s_and_b32 s76,s76,3\n"
"s_lshl_b32 s76,s76,13\n"
"v_add_u32 v14,s76,v8\n"
"v_min_u32 v10,27,v0\n"
"v_lshlrev_b32 v10,3,v10\n"
"s_mov_b32 s76,0\n"
"L_hh_lp_%=:\n"
"global_load_dwordx2 v[2:3],v10,s[82:83] sc1\n"
"s_add_u32 s76,s76,1\n"
"s_waitcnt vmcnt(0)\n"
"v_cmp_ne_u64 vcc,s[34:35],v[2:3]\n"
"s_nop 0\n"
"s_cbranch_vccz L_hh_ok_%=\n"
"s_sleep 1\n"
"s_cmp_lt_u32 s76,0x20000\n"
"s_cbranch_scc1 L_hh_lp_%=\n"
"L_hh_ok_%=:\n"
"buffer_load_dwordx4 a[0:3],v8,s[40:43],s86 offen offset:0 sc1\n"
"buffer_load_dwordx4 a[16:19],v8,s[40:43],s86 offen offset:1024 sc1\n"
"buffer_load_dwordx4 a[4:7],v8,s[40:43],s87 offen offset:0 sc1\n"
"buffer_load_dwordx4 a[20:23],v8,s[40:43],s87 offen offset:1024 sc1\n"
"buffer_load_dwordx4 a[8:11],v8,s[40:43],s88 offen offset:0 sc1\n"
"buffer_load_dwordx4 a[24:27],v8,s[40:43],s88 offen offset:1024 sc1\n"
"buffer_load_dwordx4 a[12:15],v8,s[40:43],s89 offen offset:0 sc1\n"
"buffer_load_dwordx4 a[28:31],v8,s[40:43],s89 offen offset:1024 sc1\n"
"buffer_load_dwordx4 a[32:35],v8,s[40:43],s86 offen offset:2048 sc1\n"
"buffer_load_dwordx4 a[48:51],v8,s[40:43],s86 offen offset:3072 sc1\n"
"buffer_load_dwordx4 a[36:39],v8,s[40:43],s87 offen offset:2048 sc1\n"
"buffer_load_dwordx4 a[52:55],v8,s[40:43],s87 offen offset:3072 sc1\n"
"buffer_load_dwordx4 a[40:43],v8,s[40:43],s88 offen offset:2048 sc1\n"
"buffer_load_dwordx4 a[56:59],v8,s[40:43],s88 offen offset:3072 sc1\n"
"buffer_load_dwordx4 a[44:47],v8,s[40:43],s89 offen offset:2048 sc1\n"
"buffer_load_dwordx4 a[60:63],v8,s[40:43],s89 offen offset:3072 sc1\n"
"s_waitcnt vmcnt(15)\n"
"v_mfma_f32_16x16x32_bf16 v[128:131],a[192:195],a[0:3],0\n"
"s_waitcnt vmcnt(14)\n"
"v_mfma_f32_16x16x32_bf16 v[128:131],a[196:199],a[16:19],v[128:131]\n"
"s_waitcnt vmcnt(13)\n"
"v_mfma_f32_16x16x32_bf16 v[144:147],a[192:195],a[4:7],0\n"
"s_add_u32 s86,s86,0x1000\n"
"s_waitcnt vmcnt(12)\n"
"v_mfma_f32_16x16x32_bf16 v[144:147],a[196:199],a[20:23],v[144:147]\n"
"buffer_load_dwordx4 a[64:67],v8,s[40:43],s86 offen offset:0 sc1\n"
"s_waitcnt vmcnt(12)\n"
"v_mfma_f32_16x16x32_bf16 v[160:163],a[192:195],a[8:11],0\n"
"s_waitcnt vmcnt(11)\n"
"v_mfma_f32_16x16x32_bf16 v[160:163],a[196:199],a[24:27],v[160:163]\n"
"s_waitcnt vmcnt(10)\n"
"v_mfma_f32_16x16x32_bf16 v[176:179],a[192:195],a[12:15],0\n"
"s_waitcnt vmcnt(9)\n"
"v_mfma_f32_16x16x32_bf16 v[176:179],a[196:199],a[28:31],v[176:179]\n"
"buffer_load_dwordx4 a[80:83],v8,s[40:43],s86 offen offset:1024 sc1\n"
"v_mfma_f32_16x16x32_bf16 v[132:135],a[200:203],a[0:3],0\n"
"ds_read_b64_tr_b16 a[192:193],v32 offset:32768\n"
"v_mfma_f32_16x16x32_bf16 v[132:135],a[204:207],a[16:19],v[132:135]\n"
"v_mfma_f32_16x16x32_bf16 v[148:151],a[200:203],a[4:7],0\n"
"s_add_u32 s87,s87,0x1000\n"
"ds_read_b64_tr_b16 a[194:195],v32 offset:34816\n"
"v_mfma_f32_16x16x32_bf16 v[148:151],a[204:207],a[20:23],v[148:151]\n"
"buffer_load_dwordx4 a[68:71],v8,s[40:43],s87 offen offset:0 sc1\n"
"v_mfma_f32_16x16x32_bf16 v[164:167],a[200:203],a[8:11],0\n"
"ds_read_b64_tr_b16 a[196:197],v32 offset:36864\n"
"v_mfma_f32_16x16x32_bf16 v[164:167],a[204:207],a[24:27],v[164:167]\n"
"v_mfma_f32_16x16x32_bf16 v[180:183],a[200:203],a[12:15],0\n"
"ds_read_b64_tr_b16 a[198:199],v32 offset:38912\n"
"v_mfma_f32_16x16x32_bf16 v[180:183],a[204:207],a[28:31],v[180:183]\n"
"buffer_load_dwordx4 a[84:87],v8,s[40:43],s87 offen offset:1024 sc1\n"
"v_mfma_f32_16x16x32_bf16 v[136:139],a[208:211],a[0:3],0\n"
"ds_read_b64_tr_b16 a[200:201],v33 offset:32768\n"
"v_mfma_f32_16x16x32_bf16 v[136:139],a[212:215],a[16:19],v[136:139]\n"
"v_mfma_f32_16x16x32_bf16 v[152:155],a[208:211],a[4:7],0\n"
"s_add_u32 s88,s88,0x1000\n"
"ds_read_b64_tr_b16 a[202:203],v33 offset:34816\n"
"v_mfma_f32_16x16x32_bf16 v[152:155],a[212:215],a[20:23],v[152:155]\n"
"buffer_load_dwordx4 a[72:75],v8,s[40:43],s88 offen offset:0 sc1\n"
"v_mfma_f32_16x16x32_bf16 v[168:171],a[208:211],a[8:11],0\n"
"ds_read_b64_tr_b16 a[204:205],v33 offset:36864\n"
"v_mfma_f32_16x16x32_bf16 v[168:171],a[212:215],a[24:27],v[168:171]\n"
"v_mfma_f32_16x16x32_bf16 v[184:187],a[208:211],a[12:15],0\n"
"ds_read_b64_tr_b16 a[206:207],v33 offset:38912\n"
"v_mfma_f32_16x16x32_bf16 v[184:187],a[212:215],a[28:31],v[184:187]\n"
"buffer_load_dwordx4 a[88:91],v8,s[40:43],s88 offen offset:1024 sc1\n"
"v_mfma_f32_16x16x32_bf16 v[140:143],a[216:219],a[0:3],0\n"
"ds_read_b64_tr_b16 a[208:209],v34 offset:32768\n"
"v_mfma_f32_16x16x32_bf16 v[140:143],a[220:223],a[16:19],v[140:143]\n"
"v_mfma_f32_16x16x32_bf16 v[156:159],a[216:219],a[4:7],0\n"
"s_add_u32 s89,s89,0x1000\n"
"ds_read_b64_tr_b16 a[210:211],v34 offset:34816\n"
"v_mfma_f32_16x16x32_bf16 v[156:159],a[220:223],a[20:23],v[156:159]\n"
"buffer_load_dwordx4 a[76:79],v8,s[40:43],s89 offen offset:0 sc1\n"
"v_mfma_f32_16x16x32_bf16 v[172:175],a[216:219],a[8:11],0\n"
"ds_read_b64_tr_b16 a[212:213],v34 offset:36864\n"
"v_mfma_f32_16x16x32_bf16 v[172:175],a[220:223],a[24:27],v[172:175]\n"
"v_mfma_f32_16x16x32_bf16 v[188:191],a[216:219],a[12:15],0\n"
"s_waitcnt vmcnt(7)\n"
"ds_read_b64_tr_b16 a[214:215],v34 offset:38912\n"
"v_mfma_f32_16x16x32_bf16 v[188:191],a[220:223],a[28:31],v[188:191]\n"
"buffer_load_dwordx4 a[92:95],v8,s[40:43],s89 offen offset:1024 sc1\n"
"v_mfma_f32_16x16x32_bf16 v[128:131],a[224:227],a[32:35],v[128:131]\n"
"ds_read_b64_tr_b16 a[216:217],v35 offset:32768\n"
"v_mfma_f32_16x16x32_bf16 v[128:131],a[228:231],a[48:51],v[128:131]\n"
"v_mfma_f32_16x16x32_bf16 v[144:147],a[224:227],a[36:39],v[144:147]\n"
"ds_read_b64_tr_b16 a[218:219],v35 offset:34816\n"
"v_mfma_f32_16x16x32_bf16 v[144:147],a[228:231],a[52:55],v[144:147]\n"
"buffer_load_dwordx4 a[96:99],v8,s[40:43],s86 offen offset:2048 sc1\n"
"v_mfma_f32_16x16x32_bf16 v[160:163],a[224:227],a[40:43],v[160:163]\n"
"ds_read_b64_tr_b16 a[220:221],v35 offset:36864\n"
"v_mfma_f32_16x16x32_bf16 v[160:163],a[228:231],a[56:59],v[160:163]\n"
"v_mfma_f32_16x16x32_bf16 v[176:179],a[224:227],a[44:47],v[176:179]\n"
"ds_read_b64_tr_b16 a[222:223],v35 offset:38912\n"
"v_mfma_f32_16x16x32_bf16 v[176:179],a[228:231],a[60:63],v[176:179]\n"
"buffer_load_dwordx4 a[112:115],v8,s[40:43],s86 offen offset:3072 sc1\n"
"v_mfma_f32_16x16x32_bf16 v[132:135],a[232:235],a[32:35],v[132:135]\n"
"ds_read_b64_tr_b16 a[224:225],v40 offset:32768\n"
"v_mfma_f32_16x16x32_bf16 v[132:135],a[236:239],a[48:51],v[132:135]\n"
"v_mfma_f32_16x16x32_bf16 v[148:151],a[232:235],a[36:39],v[148:151]\n"
"ds_read_b64_tr_b16 a[226:227],v40 offset:34816\n"
"v_mfma_f32_16x16x32_bf16 v[148:151],a[236:239],a[52:55],v[148:151]\n"
"buffer_load_dwordx4 a[100:103],v8,s[40:43],s87 offen offset:2048 sc1\n"
"v_mfma_f32_16x16x32_bf16 v[164:167],a[232:235],a[40:43],v[164:167]\n"
"ds_read_b64_tr_b16 a[228:229],v40 offset:36864\n"
"v_mfma_f32_16x16x32_bf16 v[164:167],a[236:239],a[56:59],v[164:167]\n"
"v_mfma_f32_16x16x32_bf16 v[180:183],a[232:235],a[44:47],v[180:183]\n"
"ds_read_b64_tr_b16 a[230:231],v40 offset:38912\n"
"v_mfma_f32_16x16x32_bf16 v[180:183],a[236:239],a[60:63],v[180:183]\n"
"buffer_load_dwordx4 a[116:119],v8,s[40:43],s87 offen offset:3072 sc1\n"
"v_mfma_f32_16x16x32_bf16 v[136:139],a[240:243],a[32:35],v[136:139]\n"
"ds_read_b64_tr_b16 a[232:233],v41 offset:32768\n"
"v_mfma_f32_16x16x32_bf16 v[136:139],a[244:247],a[48:51],v[136:139]\n"
"v_mfma_f32_16x16x32_bf16 v[152:155],a[240:243],a[36:39],v[152:155]\n"
"ds_read_b64_tr_b16 a[234:235],v41 offset:34816\n"
"v_mfma_f32_16x16x32_bf16 v[152:155],a[244:247],a[52:55],v[152:155]\n"
"buffer_load_dwordx4 a[104:107],v8,s[40:43],s88 offen offset:2048 sc1\n"
"v_mfma_f32_16x16x32_bf16 v[168:171],a[240:243],a[40:43],v[168:171]\n"
"ds_read_b64_tr_b16 a[236:237],v41 offset:36864\n"
"v_mfma_f32_16x16x32_bf16 v[168:171],a[244:247],a[56:59],v[168:171]\n"
"v_mfma_f32_16x16x32_bf16 v[184:187],a[240:243],a[44:47],v[184:187]\n"
"ds_read_b64_tr_b16 a[238:239],v41 offset:38912\n"
"v_mfma_f32_16x16x32_bf16 v[184:187],a[244:247],a[60:63],v[184:187]\n"
"buffer_load_dwordx4 a[120:123],v8,s[40:43],s88 offen offset:3072 sc1\n"
"v_mfma_f32_16x16x32_bf16 v[140:143],a[248:251],a[32:35],v[140:143]\n"
"ds_read_b64_tr_b16 a[240:241],v42 offset:32768\n"
"v_mfma_f32_16x16x32_bf16 v[140:143],a[252:255],a[48:51],v[140:143]\n"
"v_mfma_f32_16x16x32_bf16 v[156:159],a[248:251],a[36:39],v[156:159]\n"
"ds_read_b64_tr_b16 a[242:243],v42 offset:34816\n"
"v_mfma_f32_16x16x32_bf16 v[156:159],a[252:255],a[52:55],v[156:159]\n"
"buffer_load_dwordx4 a[108:111],v8,s[40:43],s89 offen offset:2048 sc1\n"
"v_mfma_f32_16x16x32_bf16 v[172:175],a[248:251],a[40:43],v[172:175]\n"
"ds_read_b64_tr_b16 a[244:245],v42 offset:36864\n"
"v_mfma_f32_16x16x32_bf16 v[172:175],a[252:255],a[56:59],v[172:175]\n"
"v_mfma_f32_16x16x32_bf16 v[188:191],a[248:251],a[44:47],v[188:191]\n"
"s_waitcnt vmcnt(7)\n"
"ds_read_b64_tr_b16 a[246:247],v42 offset:38912\n"
"v_mfma_f32_16x16x32_bf16 v[188:191],a[252:255],a[60:63],v[188:191]\n"
"s_waitcnt lgkmcnt(15)\n"
"buffer_load_dwordx4 a[124:127],v8,s[40:43],s89 offen offset:3072 sc1\n"
"v_mfma_f32_16x16x32_bf16 v[128:131],a[192:195],a[64:67],v[128:131]\n"
"ds_read_b64_tr_b16 a[248:249],v43 offset:32768\n"
"v_mfma_f32_16x16x32_bf16 v[128:131],a[196:199],a[80:83],v[128:131]\n"
"v_mfma_f32_16x16x32_bf16 v[144:147],a[192:195],a[68:71],v[144:147]\n"
"s_add_u32 s86,s86,0x1000\n"
"ds_read_b64_tr_b16 a[250:251],v43 offset:34816\n"
"v_mfma_f32_16x16x32_bf16 v[144:147],a[196:199],a[84:87],v[144:147]\n"
"buffer_load_dwordx4 a[128:131],v8,s[40:43],s86 offen offset:0 sc1\n"
"v_mfma_f32_16x16x32_bf16 v[160:163],a[192:195],a[72:75],v[160:163]\n"
"ds_read_b64_tr_b16 a[252:253],v43 offset:36864\n"
"v_mfma_f32_16x16x32_bf16 v[160:163],a[196:199],a[88:91],v[160:163]\n"
"v_mfma_f32_16x16x32_bf16 v[176:179],a[192:195],a[76:79],v[176:179]\n"
"ds_read_b64_tr_b16 a[254:255],v43 offset:38912\n"
"v_mfma_f32_16x16x32_bf16 v[176:179],a[196:199],a[92:95],v[176:179]\n"
"buffer_load_dwordx4 a[144:147],v8,s[40:43],s86 offen offset:1024 sc1\n"
"v_mfma_f32_16x16x32_bf16 v[132:135],a[200:203],a[64:67],v[132:135]\n"
"ds_read_b64_tr_b16 a[192:193],v44 offset:0\n"
"v_mfma_f32_16x16x32_bf16 v[132:135],a[204:207],a[80:83],v[132:135]\n"
"v_mfma_f32_16x16x32_bf16 v[148:151],a[200:203],a[68:71],v[148:151]\n"
"s_add_u32 s87,s87,0x1000\n"
"ds_read_b64_tr_b16 a[194:195],v44 offset:2048\n"
"v_mfma_f32_16x16x32_bf16 v[148:151],a[204:207],a[84:87],v[148:151]\n"
"buffer_load_dwordx4 a[132:135],v8,s[40:43],s87 offen offset:0 sc1\n"
"v_mfma_f32_16x16x32_bf16 v[164:167],a[200:203],a[72:75],v[164:167]\n"
"ds_read_b64_tr_b16 a[196:197],v44 offset:4096\n"
"v_mfma_f32_16x16x32_bf16 v[164:167],a[204:207],a[88:91],v[164:167]\n"
"v_mfma_f32_16x16x32_bf16 v[180:183],a[200:203],a[76:79],v[180:183]\n"
"ds_read_b64_tr_b16 a[198:199],v44 offset:6144\n"
"v_mfma_f32_16x16x32_bf16 v[180:183],a[204:207],a[92:95],v[180:183]\n"
"buffer_load_dwordx4 a[148:151],v8,s[40:43],s87 offen offset:1024 sc1\n"
"v_mfma_f32_16x16x32_bf16 v[136:139],a[208:211],a[64:67],v[136:139]\n"
"ds_read_b64_tr_b16 a[200:201],v45 offset:0\n"
"v_mfma_f32_16x16x32_bf16 v[136:139],a[212:215],a[80:83],v[136:139]\n"
"v_mfma_f32_16x16x32_bf16 v[152:155],a[208:211],a[68:71],v[152:155]\n"
"s_add_u32 s88,s88,0x1000\n"
"ds_read_b64_tr_b16 a[202:203],v45 offset:2048\n"
"v_mfma_f32_16x16x32_bf16 v[152:155],a[212:215],a[84:87],v[152:155]\n"
"buffer_load_dwordx4 a[136:139],v8,s[40:43],s88 offen offset:0 sc1\n"
"v_mfma_f32_16x16x32_bf16 v[168:171],a[208:211],a[72:75],v[168:171]\n"
"ds_read_b64_tr_b16 a[204:205],v45 offset:4096\n"
"v_mfma_f32_16x16x32_bf16 v[168:171],a[212:215],a[88:91],v[168:171]\n"
"v_mfma_f32_16x16x32_bf16 v[184:187],a[208:211],a[76:79],v[184:187]\n"
"ds_read_b64_tr_b16 a[206:207],v45 offset:6144\n"
"v_mfma_f32_16x16x32_bf16 v[184:187],a[212:215],a[92:95],v[184:187]\n"
"s_waitcnt lgkmcnt(15)\n"
"buffer_load_dwordx4 a[152:155],v8,s[40:43],s88 offen offset:1024 sc1\n"
"v_mfma_f32_16x16x32_bf16 v[140:143],a[216:219],a[64:67],v[140:143]\n"
"ds_read_b64_tr_b16 a[208:209],v46 offset:0\n"
"v_mfma_f32_16x16x32_bf16 v[140:143],a[220:223],a[80:83],v[140:143]\n"
"v_mfma_f32_16x16x32_bf16 v[156:159],a[216:219],a[68:71],v[156:159]\n"
"s_add_u32 s89,s89,0x1000\n"
"ds_read_b64_tr_b16 a[210:211],v46 offset:2048\n"
"v_mfma_f32_16x16x32_bf16 v[156:159],a[220:223],a[84:87],v[156:159]\n"
"buffer_load_dwordx4 a[140:143],v8,s[40:43],s89 offen offset:0 sc1\n"
"v_mfma_f32_16x16x32_bf16 v[172:175],a[216:219],a[72:75],v[172:175]\n"
"ds_read_b64_tr_b16 a[212:213],v46 offset:4096\n"
"v_mfma_f32_16x16x32_bf16 v[172:175],a[220:223],a[88:91],v[172:175]\n"
"v_mfma_f32_16x16x32_bf16 v[188:191],a[216:219],a[76:79],v[188:191]\n"
"s_waitcnt vmcnt(7)\n"
"ds_read_b64_tr_b16 a[214:215],v46 offset:6144\n"
"v_mfma_f32_16x16x32_bf16 v[188:191],a[220:223],a[92:95],v[188:191]\n"
"buffer_load_dwordx4 a[156:159],v8,s[40:43],s89 offen offset:1024 sc1\n"
"v_mfma_f32_16x16x32_bf16 v[128:131],a[224:227],a[96:99],v[128:131]\n"
"ds_read_b64_tr_b16 a[216:217],v47 offset:0\n"
"v_mfma_f32_16x16x32_bf16 v[128:131],a[228:231],a[112:115],v[128:131]\n"
"v_mfma_f32_16x16x32_bf16 v[144:147],a[224:227],a[100:103],v[144:147]\n"
"ds_read_b64_tr_b16 a[218:219],v47 offset:2048\n"
"v_mfma_f32_16x16x32_bf16 v[144:147],a[228:231],a[116:119],v[144:147]\n"
"buffer_load_dwordx4 a[160:163],v8,s[40:43],s86 offen offset:2048 sc1\n"
"v_mfma_f32_16x16x32_bf16 v[160:163],a[224:227],a[104:107],v[160:163]\n"
"ds_read_b64_tr_b16 a[220:221],v47 offset:4096\n"
"v_mfma_f32_16x16x32_bf16 v[160:163],a[228:231],a[120:123],v[160:163]\n"
"v_mfma_f32_16x16x32_bf16 v[176:179],a[224:227],a[108:111],v[176:179]\n"
"ds_read_b64_tr_b16 a[222:223],v47 offset:6144\n"
"v_mfma_f32_16x16x32_bf16 v[176:179],a[228:231],a[124:127],v[176:179]\n"
"buffer_load_dwordx4 a[176:179],v8,s[40:43],s86 offen offset:3072 sc1\n"
"v_mfma_f32_16x16x32_bf16 v[132:135],a[232:235],a[96:99],v[132:135]\n"
"ds_read_b64_tr_b16 a[224:225],v40 offset:0\n"
"v_mfma_f32_16x16x32_bf16 v[132:135],a[236:239],a[112:115],v[132:135]\n"
"v_mfma_f32_16x16x32_bf16 v[148:151],a[232:235],a[100:103],v[148:151]\n"
"ds_read_b64_tr_b16 a[226:227],v40 offset:2048\n"
"v_mfma_f32_16x16x32_bf16 v[148:151],a[236:239],a[116:119],v[148:151]\n"
"buffer_load_dwordx4 a[164:167],v8,s[40:43],s87 offen offset:2048 sc1\n"
"v_mfma_f32_16x16x32_bf16 v[164:167],a[232:235],a[104:107],v[164:167]\n"
"ds_read_b64_tr_b16 a[228:229],v40 offset:4096\n"
"v_mfma_f32_16x16x32_bf16 v[164:167],a[236:239],a[120:123],v[164:167]\n"
"v_mfma_f32_16x16x32_bf16 v[180:183],a[232:235],a[108:111],v[180:183]\n"
"ds_read_b64_tr_b16 a[230:231],v40 offset:6144\n"
"v_mfma_f32_16x16x32_bf16 v[180:183],a[236:239],a[124:127],v[180:183]\n"
"s_waitcnt lgkmcnt(15)\n"
"buffer_load_dwordx4 a[180:183],v8,s[40:43],s87 offen offset:3072 sc1\n"
"v_mfma_f32_16x16x32_bf16 v[136:139],a[240:243],a[96:99],v[136:139]\n"
"ds_read_b64_tr_b16 a[232:233],v41 offset:0\n"
"v_mfma_f32_16x16x32_bf16 v[136:139],a[244:247],a[112:115],v[136:139]\n"
"v_mfma_f32_16x16x32_bf16 v[152:155],a[240:243],a[100:103],v[152:155]\n"
"ds_read_b64_tr_b16 a[234:235],v41 offset:2048\n"
"v_mfma_f32_16x16x32_bf16 v[152:155],a[244:247],a[116:119],v[152:155]\n"
"buffer_load_dwordx4 a[168:171],v8,s[40:43],s88 offen offset:2048 sc1\n"
"v_mfma_f32_16x16x32_bf16 v[168:171],a[240:243],a[104:107],v[168:171]\n"
"ds_read_b64_tr_b16 a[236:237],v41 offset:4096\n"
"v_mfma_f32_16x16x32_bf16 v[168:171],a[244:247],a[120:123],v[168:171]\n"
"v_mfma_f32_16x16x32_bf16 v[184:187],a[240:243],a[108:111],v[184:187]\n"
"ds_read_b64_tr_b16 a[238:239],v41 offset:6144\n"
"v_mfma_f32_16x16x32_bf16 v[184:187],a[244:247],a[124:127],v[184:187]\n"
"buffer_load_dwordx4 a[184:187],v8,s[40:43],s88 offen offset:3072 sc1\n"
"v_mfma_f32_16x16x32_bf16 v[140:143],a[248:251],a[96:99],v[140:143]\n"
"ds_read_b64_tr_b16 a[240:241],v42 offset:0\n"
"v_mfma_f32_16x16x32_bf16 v[140:143],a[252:255],a[112:115],v[140:143]\n"
"v_mfma_f32_16x16x32_bf16 v[156:159],a[248:251],a[100:103],v[156:159]\n"
"ds_read_b64_tr_b16 a[242:243],v42 offset:2048\n"
"v_mfma_f32_16x16x32_bf16 v[156:159],a[252:255],a[116:119],v[156:159]\n"
"buffer_load_dwordx4 a[172:175],v8,s[40:43],s89 offen offset:2048 sc1\n"
"v_mfma_f32_16x16x32_bf16 v[172:175],a[248:251],a[104:107],v[172:175]\n"
"ds_read_b64_tr_b16 a[244:245],v42 offset:4096\n"
"v_mfma_f32_16x16x32_bf16 v[172:175],a[252:255],a[120:123],v[172:175]\n"
"v_mfma_f32_16x16x32_bf16 v[188:191],a[248:251],a[108:111],v[188:191]\n"
"s_waitcnt vmcnt(7)\n"
"ds_read_b64_tr_b16 a[246:247],v42 offset:6144\n"
"v_mfma_f32_16x16x32_bf16 v[188:191],a[252:255],a[124:127],v[188:191]\n"
"buffer_load_dwordx4 a[188:191],v8,s[40:43],s89 offen offset:3072 sc1\n"
"v_mfma_f32_16x16x32_bf16 v[128:131],a[192:195],a[128:131],v[128:131]\n"
"ds_read_b64_tr_b16 a[248:249],v43 offset:0\n"
"v_mfma_f32_16x16x32_bf16 v[128:131],a[196:199],a[144:147],v[128:131]\n"
"v_mfma_f32_16x16x32_bf16 v[144:147],a[192:195],a[132:135],v[144:147]\n"
"s_add_u32 s86,s86,0x1000\n"
"ds_read_b64_tr_b16 a[250:251],v43 offset:2048\n"
"v_mfma_f32_16x16x32_bf16 v[144:147],a[196:199],a[148:151],v[144:147]\n"
"buffer_load_dwordx4 v[64:67],v8,s[40:43],s86 offen offset:0 sc1\n"
"v_mfma_f32_16x16x32_bf16 v[160:163],a[192:195],a[136:139],v[160:163]\n"
"ds_read_b64_tr_b16 a[252:253],v43 offset:4096\n"
"v_mfma_f32_16x16x32_bf16 v[160:163],a[196:199],a[152:155],v[160:163]\n"
"v_mfma_f32_16x16x32_bf16 v[176:179],a[192:195],a[140:143],v[176:179]\n"
"ds_read_b64_tr_b16 a[254:255],v43 offset:6144\n"
"v_mfma_f32_16x16x32_bf16 v[176:179],a[196:199],a[156:159],v[176:179]\n"
"s_waitcnt lgkmcnt(15)\n"
"buffer_load_dwordx4 v[80:83],v8,s[40:43],s86 offen offset:1024 sc1\n"
"v_mfma_f32_16x16x32_bf16 v[132:135],a[200:203],a[128:131],v[132:135]\n"
"ds_read_b64_tr_b16 a[192:193],v32 offset:0\n"
"v_mfma_f32_16x16x32_bf16 v[132:135],a[204:207],a[144:147],v[132:135]\n"
"v_mfma_f32_16x16x32_bf16 v[148:151],a[200:203],a[132:135],v[148:151]\n"
"s_add_u32 s87,s87,0x1000\n"
"ds_read_b64_tr_b16 a[194:195],v32 offset:2048\n"
"v_mfma_f32_16x16x32_bf16 v[148:151],a[204:207],a[148:151],v[148:151]\n"
"buffer_load_dwordx4 v[68:71],v8,s[40:43],s87 offen offset:0 sc1\n"
"v_mfma_f32_16x16x32_bf16 v[164:167],a[200:203],a[136:139],v[164:167]\n"
"ds_read_b64_tr_b16 a[196:197],v32 offset:4096\n"
"v_mfma_f32_16x16x32_bf16 v[164:167],a[204:207],a[152:155],v[164:167]\n"
"v_mfma_f32_16x16x32_bf16 v[180:183],a[200:203],a[140:143],v[180:183]\n"
"ds_read_b64_tr_b16 a[198:199],v32 offset:6144\n"
"v_mfma_f32_16x16x32_bf16 v[180:183],a[204:207],a[156:159],v[180:183]\n"
"buffer_load_dwordx4 v[84:87],v8,s[40:43],s87 offen offset:1024 sc1\n"
"v_mfma_f32_16x16x32_bf16 v[136:139],a[208:211],a[128:131],v[136:139]\n"
"ds_read_b64_tr_b16 a[200:201],v33 offset:0\n"
"v_mfma_f32_16x16x32_bf16 v[136:139],a[212:215],a[144:147],v[136:139]\n"
"v_mfma_f32_16x16x32_bf16 v[152:155],a[208:211],a[132:135],v[152:155]\n"
"s_add_u32 s88,s88,0x1000\n"
"ds_read_b64_tr_b16 a[202:203],v33 offset:2048\n"
"v_mfma_f32_16x16x32_bf16 v[152:155],a[212:215],a[148:151],v[152:155]\n"
"buffer_load_dwordx4 v[72:75],v8,s[40:43],s88 offen offset:0 sc1\n"
"v_mfma_f32_16x16x32_bf16 v[168:171],a[208:211],a[136:139],v[168:171]\n"
"ds_read_b64_tr_b16 a[204:205],v33 offset:4096\n"
"v_mfma_f32_16x16x32_bf16 v[168:171],a[212:215],a[152:155],v[168:171]\n"
"v_mfma_f32_16x16x32_bf16 v[184:187],a[208:211],a[140:143],v[184:187]\n"
"ds_read_b64_tr_b16 a[206:207],v33 offset:6144\n"
"v_mfma_f32_16x16x32_bf16 v[184:187],a[212:215],a[156:159],v[184:187]\n"
"buffer_load_dwordx4 v[88:91],v8,s[40:43],s88 offen offset:1024 sc1\n"
"v_mfma_f32_16x16x32_bf16 v[140:143],a[216:219],a[128:131],v[140:143]\n"
"ds_read_b64_tr_b16 a[208:209],v34 offset:0\n"
"v_mfma_f32_16x16x32_bf16 v[140:143],a[220:223],a[144:147],v[140:143]\n"
"v_mfma_f32_16x16x32_bf16 v[156:159],a[216:219],a[132:135],v[156:159]\n"
"s_add_u32 s89,s89,0x1000\n"
"ds_read_b64_tr_b16 a[210:211],v34 offset:2048\n"
"v_mfma_f32_16x16x32_bf16 v[156:159],a[220:223],a[148:151],v[156:159]\n"
"buffer_load_dwordx4 v[76:79],v8,s[40:43],s89 offen offset:0 sc1\n"
"v_mfma_f32_16x16x32_bf16 v[172:175],a[216:219],a[136:139],v[172:175]\n"
"ds_read_b64_tr_b16 a[212:213],v34 offset:4096\n"
"v_mfma_f32_16x16x32_bf16 v[172:175],a[220:223],a[152:155],v[172:175]\n"
"v_mfma_f32_16x16x32_bf16 v[188:191],a[216:219],a[140:143],v[188:191]\n"
"s_waitcnt vmcnt(7)\n"
"ds_read_b64_tr_b16 a[214:215],v34 offset:6144\n"
"v_mfma_f32_16x16x32_bf16 v[188:191],a[220:223],a[156:159],v[188:191]\n"
"s_waitcnt lgkmcnt(15)\n"
"buffer_load_dwordx4 v[92:95],v8,s[40:43],s89 offen offset:1024 sc1\n"
"v_mfma_f32_16x16x32_bf16 v[128:131],a[224:227],a[160:163],v[128:131]\n"
"ds_read_b64_tr_b16 a[216:217],v35 offset:0\n"
"v_mfma_f32_16x16x32_bf16 v[128:131],a[228:231],a[176:179],v[128:131]\n"
"v_mfma_f32_16x16x32_bf16 v[144:147],a[224:227],a[164:167],v[144:147]\n"
"ds_read_b64_tr_b16 a[218:219],v35 offset:2048\n"
"v_mfma_f32_16x16x32_bf16 v[144:147],a[228:231],a[180:183],v[144:147]\n"
"v_mfma_f32_16x16x32_bf16 v[160:163],a[224:227],a[168:171],v[160:163]\n"
"ds_read_b64_tr_b16 a[220:221],v35 offset:4096\n"
"v_mfma_f32_16x16x32_bf16 v[160:163],a[228:231],a[184:187],v[160:163]\n"
"v_mfma_f32_16x16x32_bf16 v[176:179],a[224:227],a[172:175],v[176:179]\n"
"ds_read_b64_tr_b16 a[222:223],v35 offset:6144\n"
"v_mfma_f32_16x16x32_bf16 v[176:179],a[228:231],a[188:191],v[176:179]\n"
"v_mfma_f32_16x16x32_bf16 v[132:135],a[232:235],a[160:163],v[132:135]\n"
"v_mfma_f32_16x16x32_bf16 v[132:135],a[236:239],a[176:179],v[132:135]\n"
"v_mfma_f32_16x16x32_bf16 v[148:151],a[232:235],a[164:167],v[148:151]\n"
"v_mfma_f32_16x16x32_bf16 v[148:151],a[236:239],a[180:183],v[148:151]\n"
"v_mfma_f32_16x16x32_bf16 v[164:167],a[232:235],a[168:171],v[164:167]\n"
"v_mfma_f32_16x16x32_bf16 v[164:167],a[236:239],a[184:187],v[164:167]\n"
"v_mfma_f32_16x16x32_bf16 v[180:183],a[232:235],a[172:175],v[180:183]\n"
"v_mfma_f32_16x16x32_bf16 v[180:183],a[236:239],a[188:191],v[180:183]\n"
"v_mfma_f32_16x16x32_bf16 v[136:139],a[240:243],a[160:163],v[136:139]\n"
"v_mfma_f32_16x16x32_bf16 v[136:139],a[244:247],a[176:179],v[136:139]\n"
"v_mfma_f32_16x16x32_bf16 v[152:155],a[240:243],a[164:167],v[152:155]\n"
"v_mfma_f32_16x16x32_bf16 v[152:155],a[244:247],a[180:183],v[152:155]\n"
"v_mfma_f32_16x16x32_bf16 v[168:171],a[240:243],a[168:171],v[168:171]\n"
"v_mfma_f32_16x16x32_bf16 v[168:171],a[244:247],a[184:187],v[168:171]\n"
"v_mfma_f32_16x16x32_bf16 v[184:187],a[240:243],a[172:175],v[184:187]\n"
"v_mfma_f32_16x16x32_bf16 v[184:187],a[244:247],a[188:191],v[184:187]\n"
"s_waitcnt lgkmcnt(15)\n"
"v_mfma_f32_16x16x32_bf16 v[140:143],a[248:251],a[160:163],v[140:143]\n"
"v_mfma_f32_16x16x32_bf16 v[140:143],a[252:255],a[176:179],v[140:143]\n"
"v_mfma_f32_16x16x32_bf16 v[156:159],a[248:251],a[164:167],v[156:159]\n"
"v_mfma_f32_16x16x32_bf16 v[156:159],a[252:255],a[180:183],v[156:159]\n"
"v_mfma_f32_16x16x32_bf16 v[172:175],a[248:251],a[168:171],v[172:175]\n"
"v_mfma_f32_16x16x32_bf16 v[172:175],a[252:255],a[184:187],v[172:175]\n"
"v_mfma_f32_16x16x32_bf16 v[188:191],a[248:251],a[172:175],v[188:191]\n"
"s_waitcnt vmcnt(0)\n"
"v_mfma_f32_16x16x32_bf16 v[188:191],a[252:255],a[188:191],v[188:191]\n"
"s_waitcnt lgkmcnt(12)\n"
"v_mfma_f32_16x16x32_bf16 v[128:131],a[192:195],v[64:67],v[128:131]\n"
"v_mfma_f32_16x16x32_bf16 v[128:131],a[196:199],v[80:83],v[128:131]\n"
"v_mfma_f32_16x16x32_bf16 v[144:147],a[192:195],v[68:71],v[144:147]\n"
"v_mfma_f32_16x16x32_bf16 v[144:147],a[196:199],v[84:87],v[144:147]\n"
"v_mfma_f32_16x16x32_bf16 v[160:163],a[192:195],v[72:75],v[160:163]\n"
"v_mfma_f32_16x16x32_bf16 v[160:163],a[196:199],v[88:91],v[160:163]\n"
"v_mfma_f32_16x16x32_bf16 v[176:179],a[192:195],v[76:79],v[176:179]\n"
"v_mfma_f32_16x16x32_bf16 v[176:179],a[196:199],v[92:95],v[176:179]\n"
"s_waitcnt lgkmcnt(8)\n"
"v_mfma_f32_16x16x32_bf16 v[132:135],a[200:203],v[64:67],v[132:135]\n"
"v_mfma_f32_16x16x32_bf16 v[132:135],a[204:207],v[80:83],v[132:135]\n"
"v_mfma_f32_16x16x32_bf16 v[148:151],a[200:203],v[68:71],v[148:151]\n"
"v_mfma_f32_16x16x32_bf16 v[148:151],a[204:207],v[84:87],v[148:151]\n"
"v_mfma_f32_16x16x32_bf16 v[164:167],a[200:203],v[72:75],v[164:167]\n"
"v_mfma_f32_16x16x32_bf16 v[164:167],a[204:207],v[88:91],v[164:167]\n"
"v_mfma_f32_16x16x32_bf16 v[180:183],a[200:203],v[76:79],v[180:183]\n"
"v_mfma_f32_16x16x32_bf16 v[180:183],a[204:207],v[92:95],v[180:183]\n"
"s_waitcnt lgkmcnt(4)\n"
"v_mfma_f32_16x16x32_bf16 v[136:139],a[208:211],v[64:67],v[136:139]\n"
"v_mfma_f32_16x16x32_bf16 v[136:139],a[212:215],v[80:83],v[136:139]\n"
"v_mfma_f32_16x16x32_bf16 v[152:155],a[208:211],v[68:71],v[152:155]\n"
"v_mfma_f32_16x16x32_bf16 v[152:155],a[212:215],v[84:87],v[152:155]\n"
"v_mfma_f32_16x16x32_bf16 v[168:171],a[208:211],v[72:75],v[168:171]\n"
"v_mfma_f32_16x16x32_bf16 v[168:171],a[212:215],v[88:91],v[168:171]\n"
"v_mfma_f32_16x16x32_bf16 v[184:187],a[208:211],v[76:79],v[184:187]\n"
"v_mfma_f32_16x16x32_bf16 v[184:187],a[212:215],v[92:95],v[184:187]\n"
"s_waitcnt lgkmcnt(0)\n"
"v_mfma_f32_16x16x32_bf16 v[140:143],a[216:219],v[64:67],v[140:143]\n"
"v_mfma_f32_16x16x32_bf16 v[140:143],a[220:223],v[80:83],v[140:143]\n"
"v_mfma_f32_16x16x32_bf16 v[156:159],a[216:219],v[68:71],v[156:159]\n"
"v_mfma_f32_16x16x32_bf16 v[156:159],a[220:223],v[84:87],v[156:159]\n"
"v_mfma_f32_16x16x32_bf16 v[172:175],a[216:219],v[72:75],v[172:175]\n"
"v_mfma_f32_16x16x32_bf16 v[172:175],a[220:223],v[88:91],v[172:175]\n"
"v_mfma_f32_16x16x32_bf16 v[188:191],a[216:219],v[76:79],v[188:191]\n"
"v_mfma_f32_16x16x32_bf16 v[188:191],a[220:223],v[92:95],v[188:191]\n"
"ds_write_b128 v9,v[144:147] offset:0\n"
"ds_write_b128 v9,v[148:151] offset:1024\n"
"ds_write_b128 v9,v[152:155] offset:2048\n"
"s_nop 0\n"
"ds_write_b128 v9,v[156:159] offset:3072\n"
"ds_write_b128 v9,v[160:163] offset:4096\n"
"ds_write_b128 v9,v[164:167] offset:5120\n"
"ds_write_b128 v9,v[168:171] offset:6144\n"
"ds_write_b128 v9,v[172:175] offset:7168\n"
"ds_write_b128 v9,v[176:179] offset:32768\n"
"ds_write_b128 v9,v[180:183] offset:33792\n"
"ds_write_b128 v9,v[184:187] offset:34816\n"
"ds_write_b128 v9,v[188:191] offset:35840\n"
"v_pk_add_f32 v[96:97],v[96:97],v[128:129]\n"
"v_pk_add_f32 v[98:99],v[98:99],v[130:131]\n"
"v_pk_add_f32 v[100:101],v[100:101],v[132:133]\n"
"v_pk_add_f32 v[102:103],v[102:103],v[134:135]\n"
"v_pk_add_f32 v[104:105],v[104:105],v[136:137]\n"
"v_pk_add_f32 v[106:107],v[106:107],v[138:139]\n"
"v_pk_add_f32 v[108:109],v[108:109],v[140:141]\n"
"v_pk_add_f32 v[110:111],v[110:111],v[142:143]\n"
"s_waitcnt lgkmcnt(0)\n"
"s_barrier\n"
"ds_read_b128 v[192:195],v12 offset:0\n"
"ds_read_b128 v[196:199],v12 offset:1024\n"
"ds_read_b128 v[200:203],v12 offset:2048\n"
"ds_read_b128 v[204:207],v12 offset:3072\n"
"ds_read_b128 v[208:211],v13 offset:4096\n"
"ds_read_b128 v[212:215],v13 offset:5120\n"
"ds_read_b128 v[216:219],v13 offset:6144\n"
"ds_read_b128 v[220:223],v13 offset:7168\n"
"ds_read_b128 v[224:227],v14 offset:32768\n"
"ds_read_b128 v[228:231],v14 offset:33792\n"
"ds_read_b128 v[232:235],v14 offset:34816\n"
"ds_read_b128 v[236:239],v14 offset:35840\n"
"s_waitcnt lgkmcnt(11)\n"
"v_pk_add_f32 v[96:97],v[96:97],v[192:193]\n"
"v_pk_add_f32 v[98:99],v[98:99],v[194:195]\n"
"s_waitcnt lgkmcnt(10)\n"
"v_pk_add_f32 v[100:101],v[100:101],v[196:197]\n"
"v_pk_add_f32 v[102:103],v[102:103],v[198:199]\n"
"s_waitcnt lgkmcnt(9)\n"
"v_pk_add_f32 v[104:105],v[104:105],v[200:201]\n"
"v_pk_add_f32 v[106:107],v[106:107],v[202:203]\n"
"s_waitcnt lgkmcnt(8)\n"
"v_pk_add_f32 v[108:109],v[108:109],v[204:205]\n"
"v_pk_add_f32 v[110:111],v[110:111],v[206:207]\n"
"s_waitcnt lgkmcnt(7)\n"
"v_pk_add_f32 v[96:97],v[96:97],v[208:209]\n"
"v_pk_add_f32 v[98:99],v[98:99],v[210:211]\n"
"s_waitcnt lgkmcnt(6)\n"
"v_pk_add_f32 v[100:101],v[100:101],v[212:213]\n"
"v_pk_add_f32 v[102:103],v[102:103],v[214:215]\n"
"s_waitcnt lgkmcnt(5)\n"
"v_pk_add_f32 v[104:105],v[104:105],v[216:217]\n"
"v_pk_add_f32 v[106:107],v[106:107],v[218:219]\n"
"s_waitcnt lgkmcnt(4)\n"
"v_pk_add_f32 v[108:109],v[108:109],v[220:221]\n"
"v_pk_add_f32 v[110:111],v[110:111],v[222:223]\n"
"s_waitcnt lgkmcnt(3)\n"
"v_pk_add_f32 v[96:97],v[96:97],v[224:225]\n"
"v_pk_add_f32 v[98:99],v[98:99],v[226:227]\n"
"s_waitcnt lgkmcnt(2)\n"
"v_pk_add_f32 v[100:101],v[100:101],v[228:229]\n"
"v_pk_add_f32 v[102:103],v[102:103],v[230:231]\n"
"s_waitcnt lgkmcnt(1)\n"
"v_pk_add_f32 v[104:105],v[104:105],v[232:233]\n"
"v_pk_add_f32 v[106:107],v[106:107],v[234:235]\n"
"s_waitcnt lgkmcnt(0)\n"
"v_pk_add_f32 v[108:109],v[108:109],v[236:237]\n"
"v_pk_add_f32 v[110:111],v[110:111],v[238:239]\n"
"v_cvt_pk_bf16_f32 v96,v96,v97\n"
"v_cvt_pk_bf16_f32 v97,v98,v99\n"
"buffer_store_dwordx2 v[96:97],v37,s[68:71],0 offen offset:0\n"
"v_cvt_pk_bf16_f32 v100,v100,v101\n"
"v_cvt_pk_bf16_f32 v101,v102,v103\n"
"buffer_store_dwordx2 v[100:101],v37,s[68:71],0 offen offset:32\n"
"v_cvt_pk_bf16_f32 v104,v104,v105\n"
"v_cvt_pk_bf16_f32 v105,v106,v107\n"
"buffer_store_dwordx2 v[104:105],v37,s[68:71],0 offen offset:64\n"
"v_cvt_pk_bf16_f32 v108,v108,v109\n"
"v_cvt_pk_bf16_f32 v109,v110,v111\n"
"buffer_store_dwordx2 v[108:109],v37,s[68:71],0 offen offset:96\n"
"L_done_%=:\n"
"s_xor_b32 s76,s10,255\n"
"s_or_b32 s76,s76,s11\n"
"s_cmp_eq_u32 s76,0\n"
"s_cbranch_scc0 L_exit_%=\n"
"v_lshlrev_b32 v10,3,v0\n"
"s_mov_b32 s76,0\n"
"s_add_u32 s82,s76,0x1000\n"
"s_add_u32 s82,s82,s32\n"
"s_addc_u32 s83,s33,0\n"
"s_mov_b32 s76,0\n"
"L_ep0_lp_%=:\n"
"global_load_dwordx2 v[2:3],v10,s[82:83] sc1\n"
"s_add_u32 s76,s76,1\n"
"s_waitcnt vmcnt(0)\n"
"v_cmp_ne_u64 vcc,s[34:35],v[2:3]\n"
"s_nop 0\n"
"s_cbranch_vccz L_ep0_ok_%=\n"
"s_sleep 1\n"
"s_cmp_lt_u32 s76,0x20000\n"
"s_cbranch_scc1 L_ep0_lp_%=\n"
"L_ep0_ok_%=:\n"
"s_mov_b32 s76,512\n"
"s_add_u32 s82,s76,0x1000\n"
"s_add_u32 s82,s82,s32\n"
"s_addc_u32 s83,s33,0\n"
"s_mov_b32 s76,0\n"
"L_ep1_lp_%=:\n"
"global_load_dwordx2 v[2:3],v10,s[82:83] sc1\n"
"s_add_u32 s76,s76,1\n"
"s_waitcnt vmcnt(0)\n"
"v_cmp_ne_u64 vcc,s[34:35],v[2:3]\n"
"s_nop 0\n"
"s_cbranch_vccz L_ep1_ok_%=\n"
"s_sleep 1\n"
"s_cmp_lt_u32 s76,0x20000\n"
"s_cbranch_scc1 L_ep1_lp_%=\n"
"L_ep1_ok_%=:\n"
"s_mov_b32 s76,1024\n"
"s_add_u32 s82,s76,0x1000\n"
"s_add_u32 s82,s82,s32\n"
"s_addc_u32 s83,s33,0\n"
"s_mov_b32 s76,0\n"
"L_ep2_lp_%=:\n"
"global_load_dwordx2 v[2:3],v10,s[82:83] sc1\n"
"s_add_u32 s76,s76,1\n"
"s_waitcnt vmcnt(0)\n"
"v_cmp_ne_u64 vcc,s[34:35],v[2:3]\n"
"s_nop 0\n"
"s_cbranch_vccz L_ep2_ok_%=\n"
"s_sleep 1\n"
"s_cmp_lt_u32 s76,0x20000\n"
"s_cbranch_scc1 L_ep2_lp_%=\n"
"L_ep2_ok_%=:\n"
"s_mov_b32 s76,1536\n"
"s_add_u32 s82,s76,0x1000\n"
"s_add_u32 s82,s82,s32\n"
"s_addc_u32 s83,s33,0\n"
"s_mov_b32 s76,0\n"
"L_ep3_lp_%=:\n"
"global_load_dwordx2 v[2:3],v10,s[82:83] sc1\n"
"s_add_u32 s76,s76,1\n"
"s_waitcnt vmcnt(0)\n"
"v_cmp_ne_u64 vcc,s[34:35],v[2:3]\n"
"s_nop 0\n"
"s_cbranch_vccz L_ep3_ok_%=\n"
"s_sleep 1\n"
"s_cmp_lt_u32 s76,0x20000\n"
"s_cbranch_scc1 L_ep3_lp_%=\n"
"L_ep3_ok_%=:\n"
"s_cmp_eq_u32 s11,0\n"
"s_cbranch_scc0 L_np5_%=\n"
"v_mov_b32 v2,s34\n"
"v_mov_b32 v3,s35\n"
"v_mov_b32 v4,0\n"
"s_mov_b64 exec,1\n"
"global_store_dwordx2 v4,v[2:3],s[32:33] sc1\n"
"s_mov_b64 exec,-1\n"
"L_np5_%=:\n"
"L_exit_%=:\n"
"s_branch L_fin_%=\n"
"L_pubkvk_%=:\n"
"s_cmp_eq_u32 s11,0\n"
"s_cbranch_scc0 L_np6_%=\n"
"v_mov_b32 v48,s34\n"
"v_mov_b32 v49,s35\n"
"v_mov_b32 v50,0\n"
"s_mov_b64 exec,1\n"
"global_store_dwordx2 v50,v[48:49],s[84:85] sc1\n"
"s_mov_b64 exec,-1\n"
"L_np6_%=:\n"
"s_branch L_pubkvk_ret_%=\n"
"L_kfk_%=:\n"
"s_mov_b32 s96,0\n"
"L_kfk_lp_%=:\n"
"global_load_dwordx2 v[2:3],v5,s[82:83] sc1\n"
"s_add_u32 s96,s96,1\n"
"s_waitcnt vmcnt(0)\n"
"v_cmp_ne_u64 vcc,s[34:35],v[2:3]\n"
"s_nop 0\n"
"s_cbranch_vccz L_kfk_ret_%=\n"
"s_sleep 1\n"
"s_cmp_lt_u32 s96,0x20000\n"
"s_cbranch_scc1 L_kfk_lp_%=\n"
"s_branch L_kfk_ret_%=\n"
"L_oncek_%=:\n"
"s_lshl_b32 s76,s10,3\n"
"s_add_u32 s82,s76,0x1000\n"
"s_add_u32 s82,s82,s32\n"
"s_addc_u32 s83,s33,0\n"
"s_cmp_eq_u32 s11,0\n"
"s_cbranch_scc0 L_np7_%=\n"
"v_mov_b32 v2,s34\n"
"v_mov_b32 v3,s35\n"
"v_mov_b32 v4,0\n"
"s_mov_b64 exec,1\n"
"global_store_dwordx2 v4,v[2:3],s[82:83] sc1\n"
"s_mov_b64 exec,-1\n"
"L_np7_%=:\n"
"s_branch L_oncek_ret_%=\n"
"L_lnsk_%=:\n"
"s_mov_b32 s76,0\n"
"L_lnsk_lp_%=:\n"
"global_load_dwordx2 v[54:55],v53,s[82:83] sc1\n"
"s_add_u32 s76,s76,1\n"
"s_waitcnt vmcnt(0)\n"
"v_cmp_ne_u64 vcc,s[34:35],v[54:55]\n"
"s_nop 0\n"
"s_cbranch_vccz L_lnsk_ret_%=\n"
"s_sleep 1\n"
"s_cmp_lt_u32 s76,0x20000\n"
"s_cbranch_scc1 L_lnsk_lp_%=\n"
"s_branch L_lnsk_ret_%=\n"
"L_pubkvv_%=:\n"
"s_cmp_eq_u32 s11,0\n"
"s_cbranch_scc0 L_np8_%=\n"
"v_mov_b32 v48,s34\n"
"v_mov_b32 v49,s35\n"
"v_mov_b32 v50,0\n"
"s_mov_b64 exec,1\n"
"global_store_dwordx2 v50,v[48:49],s[84:85] sc1\n"
"s_mov_b64 exec,-1\n"
"L_np8_%=:\n"
"s_branch L_pubkvv_ret_%=\n"
"L_kfv_%=:\n"
"s_mov_b32 s96,0\n"
"L_kfv_lp_%=:\n"
"global_load_dwordx2 v[2:3],v5,s[82:83] sc1\n"
"s_add_u32 s96,s96,1\n"
"s_waitcnt vmcnt(0)\n"
"v_cmp_ne_u64 vcc,s[34:35],v[2:3]\n"
"s_nop 0\n"
"s_cbranch_vccz L_kfv_ret_%=\n"
"s_sleep 1\n"
"s_cmp_lt_u32 s96,0x20000\n"
"s_cbranch_scc1 L_kfv_lp_%=\n"
"s_branch L_kfv_ret_%=\n"
"L_oncev_%=:\n"
"s_lshl_b32 s76,s10,3\n"
"s_add_u32 s82,s76,0x1000\n"
"s_add_u32 s82,s82,s32\n"
"s_addc_u32 s83,s33,0\n"
"s_cmp_eq_u32 s11,0\n"
"s_cbranch_scc0 L_np9_%=\n"
"v_mov_b32 v2,s34\n"
"v_mov_b32 v3,s35\n"
"v_mov_b32 v4,0\n"
"s_mov_b64 exec,1\n"
"global_store_dwordx2 v4,v[2:3],s[82:83] sc1\n"
"s_mov_b64 exec,-1\n"
"L_np9_%=:\n"
"s_branch L_oncev_ret_%=\n"
"L_lnsv_%=:\n"
"s_mov_b32 s76,0\n"
"L_lnsv_lp_%=:\n"
"global_load_dwordx2 v[54:55],v53,s[82:83] sc1\n"
"s_add_u32 s76,s76,1\n"
"s_waitcnt vmcnt(0)\n"
"v_cmp_ne_u64 vcc,s[34:35],v[54:55]\n"
"s_nop 0\n"
"s_cbranch_vccz L_lnsv_ret_%=\n"
"s_sleep 1\n"
"s_cmp_lt_u32 s76,0x20000\n"
"s_cbranch_scc1 L_lnsv_lp_%=\n"
"s_branch L_lnsv_ret_%=\n"
"L_pro1_%=:\n"
"global_load_dwordx2 v[2:3],v5,s[82:83] offset:64 sc1\n"
"ds_read_b128 a[192:195],v9 offset:32768\n"
"ds_read_b128 a[196:199],v9 offset:33792\n"
"ds_read_b128 a[200:203],v9 offset:34816\n"
"ds_read_b128 a[204:207],v9 offset:35840\n"
"ds_read_b128 a[208:211],v9 offset:49152\n"
"ds_read_b128 a[212:215],v9 offset:50176\n"
"ds_read_b128 a[216:219],v9 offset:51200\n"
"ds_read_b128 a[220:223],v9 offset:52224\n"
"ds_read_b128 a[224:227],v9 offset:36864\n"
"ds_read_b128 a[228:231],v9 offset:37888\n"
"s_waitcnt lgkmcnt(6)\n"
"v_mfma_f32_16x16x32_bf16 v[96:99],a[192:195],a[128:131],v[16:19]\n"
"s_mov_b32 s59,0x0\n"
"ds_read_b128 a[232:235],v9 offset:38912\n"
"v_mov_b32 v128,0\n"
"v_mfma_f32_16x16x32_bf16 v[96:99],a[196:199],a[132:135],v[96:99]\n"
"buffer_store_dwordx4 a[128:131],v8,s[60:63],s59 offen offset:0 sc1\n"
"v_mfma_f32_16x16x32_bf16 v[96:99],a[200:203],a[136:139],v[96:99]\n"
"ds_read_b128 a[236:239],v9 offset:39936\n"
"v_mov_b32 v129,0\n"
"v_mfma_f32_16x16x32_bf16 v[96:99],a[204:207],a[140:143],v[96:99]\n"
"s_waitcnt lgkmcnt(4)\n"
"v_mov_b32 v130,0\n"
"v_mov_b32 v131,0\n"
"v_mfma_f32_16x16x32_bf16 v[96:99],a[208:211],a[144:147],v[96:99]\n"
"ds_read_b128 a[240:243],v9 offset:53248\n"
"v_mov_b32 v132,0\n"
"v_mfma_f32_16x16x32_bf16 v[96:99],a[212:215],a[148:151],v[96:99]\n"
"v_mov_b32 v133,0\n"
"v_mov_b32 v134,0\n"
"v_mfma_f32_16x16x32_bf16 v[96:99],a[216:219],a[152:155],v[96:99]\n"
"ds_read_b128 a[244:247],v9 offset:54272\n"
"v_mov_b32 v135,0\n"
"v_mfma_f32_16x16x32_bf16 v[96:99],a[220:223],a[156:159],v[96:99]\n"
"v_mov_b32 v136,0\n"
"v_mov_b32 v137,0\n"
"v_mfma_f32_16x16x32_bf16 v[112:115],a[192:195],a[160:163],v[20:23]\n"
"ds_read_b128 a[248:251],v9 offset:55296\n"
"v_mov_b32 v138,0\n"
"v_mfma_f32_16x16x32_bf16 v[112:115],a[196:199],a[164:167],v[112:115]\n"
"buffer_store_dwordx4 a[132:135],v8,s[60:63],s59 offen offset:1024 sc1\n"
"v_mfma_f32_16x16x32_bf16 v[112:115],a[200:203],a[168:171],v[112:115]\n"
"ds_read_b128 a[252:255],v9 offset:56320\n"
"v_mov_b32 v139,0\n"
"v_mfma_f32_16x16x32_bf16 v[112:115],a[204:207],a[172:175],v[112:115]\n"
"v_mov_b32 v140,0\n"
"v_mov_b32 v141,0\n"
"v_mfma_f32_16x16x32_bf16 v[112:115],a[208:211],a[176:179],v[112:115]\n"
"ds_read_b128 a[192:195],v9 offset:40960\n"
"v_mov_b32 v142,0\n"
"v_mfma_f32_16x16x32_bf16 v[112:115],a[212:215],a[180:183],v[112:115]\n"
"v_mov_b32 v143,0\n"
"v_mov_b32 v144,0\n"
"v_mfma_f32_16x16x32_bf16 v[112:115],a[216:219],a[184:187],v[112:115]\n"
"ds_read_b128 a[196:199],v9 offset:41984\n"
"v_mov_b32 v145,0\n"
"v_mfma_f32_16x16x32_bf16 v[112:115],a[220:223],a[188:191],v[112:115]\n"
"s_waitcnt lgkmcnt(6)\n"
"v_mov_b32 v146,0\n"
"v_mov_b32 v147,0\n"
"v_mfma_f32_16x16x32_bf16 v[100:103],a[224:227],a[128:131],v[16:19]\n"
"ds_read_b128 a[200:203],v9 offset:43008\n"
"v_mov_b32 v148,0\n"
"v_mfma_f32_16x16x32_bf16 v[100:103],a[228:231],a[132:135],v[100:103]\n"
"buffer_store_dwordx4 a[136:139],v8,s[60:63],s59 offen offset:2048 sc1\n"
"v_mfma_f32_16x16x32_bf16 v[100:103],a[232:235],a[136:139],v[100:103]\n"
"ds_read_b128 a[204:207],v9 offset:44032\n"
"v_mov_b32 v149,0\n"
"v_mfma_f32_16x16x32_bf16 v[100:103],a[236:239],a[140:143],v[100:103]\n"
"s_waitcnt lgkmcnt(4)\n"
"v_mov_b32 v150,0\n"
"v_mov_b32 v151,0\n"
"v_mfma_f32_16x16x32_bf16 v[100:103],a[240:243],a[144:147],v[100:103]\n"
"ds_read_b128 a[208:211],v9 offset:57344\n"
"v_mov_b32 v152,0\n"
"v_mfma_f32_16x16x32_bf16 v[100:103],a[244:247],a[148:151],v[100:103]\n"
"v_mov_b32 v153,0\n"
"v_mov_b32 v154,0\n"
"v_mfma_f32_16x16x32_bf16 v[100:103],a[248:251],a[152:155],v[100:103]\n"
"ds_read_b128 a[212:215],v9 offset:58368\n"
"v_mov_b32 v155,0\n"
"v_mfma_f32_16x16x32_bf16 v[100:103],a[252:255],a[156:159],v[100:103]\n"
"v_mov_b32 v156,0\n"
"v_mov_b32 v157,0\n"
"v_mfma_f32_16x16x32_bf16 v[116:119],a[224:227],a[160:163],v[20:23]\n"
"s_waitcnt vmcnt(3)\n"
"ds_read_b128 a[216:219],v9 offset:59392\n"
"v_mov_b32 v158,0\n"
"v_mfma_f32_16x16x32_bf16 v[116:119],a[228:231],a[164:167],v[116:119]\n"
"v_cmp_ne_u64 vcc,s[34:35],v[2:3]\n"
"buffer_store_dwordx4 a[140:143],v8,s[60:63],s59 offen offset:3072 sc1\n"
"v_mfma_f32_16x16x32_bf16 v[116:119],a[232:235],a[168:171],v[116:119]\n"
"ds_read_b128 a[220:223],v9 offset:60416\n"
"v_mov_b32 v159,0\n"
"v_mfma_f32_16x16x32_bf16 v[116:119],a[236:239],a[172:175],v[116:119]\n"
"v_mov_b32 v160,0\n"
"v_mov_b32 v161,0\n"
"v_mfma_f32_16x16x32_bf16 v[116:119],a[240:243],a[176:179],v[116:119]\n"
"s_cbranch_vccnz L_vf1_%=\n"
"L_vf1_ret_%=:\n"
"ds_read_b128 a[224:227],v9 offset:45056\n"
"v_mov_b32 v162,0\n"
"v_mfma_f32_16x16x32_bf16 v[116:119],a[244:247],a[180:183],v[116:119]\n"
"v_mov_b32 v163,0\n"
"v_mov_b32 v164,0\n"
"v_mfma_f32_16x16x32_bf16 v[116:119],a[248:251],a[184:187],v[116:119]\n"
"ds_read_b128 a[228:231],v9 offset:46080\n"
"v_mov_b32 v165,0\n"
"v_mfma_f32_16x16x32_bf16 v[116:119],a[252:255],a[188:191],v[116:119]\n"
"s_waitcnt lgkmcnt(6)\n"
"v_mov_b32 v166,0\n"
"v_mov_b32 v167,0\n"
"v_mfma_f32_16x16x32_bf16 v[104:107],a[192:195],a[128:131],v[16:19]\n"
"s_mov_b32 s59,0x1000\n"
"ds_read_b128 a[232:235],v9 offset:47104\n"
"v_mov_b32 v168,0\n"
"v_mfma_f32_16x16x32_bf16 v[104:107],a[196:199],a[132:135],v[104:107]\n"
"buffer_store_dwordx4 a[144:147],v8,s[60:63],s59 offen offset:0 sc1\n"
"v_mfma_f32_16x16x32_bf16 v[104:107],a[200:203],a[136:139],v[104:107]\n"
"ds_read_b128 a[236:239],v9 offset:48128\n"
"v_mov_b32 v169,0\n"
"v_mfma_f32_16x16x32_bf16 v[104:107],a[204:207],a[140:143],v[104:107]\n"
"s_waitcnt lgkmcnt(4)\n"
"v_mov_b32 v170,0\n"
"v_mov_b32 v171,0\n"
"v_mfma_f32_16x16x32_bf16 v[104:107],a[208:211],a[144:147],v[104:107]\n"
"ds_read_b128 a[240:243],v9 offset:61440\n"
"v_mov_b32 v172,0\n"
"v_mfma_f32_16x16x32_bf16 v[104:107],a[212:215],a[148:151],v[104:107]\n"
"v_mov_b32 v173,0\n"
"v_mov_b32 v174,0\n"
"v_mfma_f32_16x16x32_bf16 v[104:107],a[216:219],a[152:155],v[104:107]\n"
"ds_read_b128 a[244:247],v9 offset:62464\n"
"v_mov_b32 v175,0\n"
"v_mfma_f32_16x16x32_bf16 v[104:107],a[220:223],a[156:159],v[104:107]\n"
"v_mov_b32 v176,0\n"
"v_mov_b32 v177,0\n"
"v_mfma_f32_16x16x32_bf16 v[120:123],a[192:195],a[160:163],v[20:23]\n"
"ds_read_b128 a[248:251],v9 offset:63488\n"
"v_mov_b32 v178,0\n"
"v_mfma_f32_16x16x32_bf16 v[120:123],a[196:199],a[164:167],v[120:123]\n"
"buffer_store_dwordx4 a[148:151],v8,s[60:63],s59 offen offset:1024 sc1\n"
"v_mfma_f32_16x16x32_bf16 v[120:123],a[200:203],a[168:171],v[120:123]\n"
"s_waitcnt lgkmcnt(3)\n"
"ds_read_b128 a[252:255],v9 offset:64512\n"
"v_mov_b32 v179,0\n"
"v_mfma_f32_16x16x32_bf16 v[120:123],a[204:207],a[172:175],v[120:123]\n"
"v_mov_b32 v180,0\n"
"v_mov_b32 v181,0\n"
"v_mfma_f32_16x16x32_bf16 v[120:123],a[208:211],a[176:179],v[120:123]\n"
"v_mov_b32 v182,0\n"
"v_mov_b32 v183,0\n"
"v_mfma_f32_16x16x32_bf16 v[120:123],a[212:215],a[180:183],v[120:123]\n"
"v_mov_b32 v184,0\n"
"v_mov_b32 v185,0\n"
"v_mfma_f32_16x16x32_bf16 v[120:123],a[216:219],a[184:187],v[120:123]\n"
"v_mov_b32 v186,0\n"
"v_mov_b32 v187,0\n"
"v_mfma_f32_16x16x32_bf16 v[120:123],a[220:223],a[188:191],v[120:123]\n"
"v_mov_b32 v188,0\n"
"v_mov_b32 v189,0\n"
"v_mfma_f32_16x16x32_bf16 v[108:111],a[224:227],a[128:131],v[16:19]\n"
"v_mov_b32 v190,0\n"
"v_mov_b32 v191,0\n"
"v_mfma_f32_16x16x32_bf16 v[108:111],a[228:231],a[132:135],v[108:111]\n"
"buffer_store_dwordx4 a[152:155],v8,s[60:63],s59 offen offset:2048 sc1\n"
"v_mfma_f32_16x16x32_bf16 v[108:111],a[232:235],a[136:139],v[108:111]\n"
"v_mov_b32 v192,0\n"
"v_mov_b32 v193,0\n"
"v_mfma_f32_16x16x32_bf16 v[108:111],a[236:239],a[140:143],v[108:111]\n"
"s_waitcnt lgkmcnt(0)\n"
"v_mov_b32 v194,0\n"
"v_mov_b32 v195,0\n"
"v_mfma_f32_16x16x32_bf16 v[108:111],a[240:243],a[144:147],v[108:111]\n"
"v_mov_b32 v196,0\n"
"v_mov_b32 v197,0\n"
"v_mfma_f32_16x16x32_bf16 v[108:111],a[244:247],a[148:151],v[108:111]\n"
"v_mov_b32 v198,0\n"
"v_mov_b32 v199,0\n"
"v_mfma_f32_16x16x32_bf16 v[108:111],a[248:251],a[152:155],v[108:111]\n"
"v_mov_b32 v200,0\n"
"v_mov_b32 v201,0\n"
"v_mfma_f32_16x16x32_bf16 v[108:111],a[252:255],a[156:159],v[108:111]\n"
"v_mov_b32 v202,0\n"
"v_mov_b32 v203,0\n"
"v_mfma_f32_16x16x32_bf16 v[124:127],a[224:227],a[160:163],v[20:23]\n"
"v_mov_b32 v204,0\n"
"v_mov_b32 v205,0\n"
"v_mfma_f32_16x16x32_bf16 v[124:127],a[228:231],a[164:167],v[124:127]\n"
"buffer_store_dwordx4 a[156:159],v8,s[60:63],s59 offen offset:3072 sc1\n"
"v_mfma_f32_16x16x32_bf16 v[124:127],a[232:235],a[168:171],v[124:127]\n"
"v_mov_b32 v206,0\n"
"v_mov_b32 v207,0\n"
"v_mfma_f32_16x16x32_bf16 v[124:127],a[236:239],a[172:175],v[124:127]\n"
"v_mov_b32 v208,0\n"
"v_mov_b32 v209,0\n"
"v_mfma_f32_16x16x32_bf16 v[124:127],a[240:243],a[176:179],v[124:127]\n"
"v_mov_b32 v210,0\n"
"v_mov_b32 v211,0\n"
"v_mfma_f32_16x16x32_bf16 v[124:127],a[244:247],a[180:183],v[124:127]\n"
"v_mov_b32 v212,0\n"
"v_mov_b32 v213,0\n"
"v_mfma_f32_16x16x32_bf16 v[124:127],a[248:251],a[184:187],v[124:127]\n"
"v_mov_b32 v214,0\n"
"v_mov_b32 v215,0\n"
"v_mfma_f32_16x16x32_bf16 v[124:127],a[252:255],a[188:191],v[124:127]\n"
"v_mov_b32 v216,0\n"
"v_mov_b32 v217,0\n"
"s_barrier\n"
"s_mov_b32 s59,0x2000\n"
"buffer_store_dwordx4 a[160:163],v8,s[60:63],s59 offen offset:0 sc1\n"
"v_pk_mov_b32 v[218:219],0,0\n"
"v_pk_mov_b32 v[220:221],0,0\n"
"v_pk_mov_b32 v[222:223],0,0\n"
"v_pk_mov_b32 v[224:225],0,0\n"
"v_pk_mov_b32 v[226:227],0,0\n"
"v_pk_mov_b32 v[228:229],0,0\n"
"v_pk_mov_b32 v[230:231],0,0\n"
"v_pk_mov_b32 v[232:233],0,0\n"
"v_pk_mov_b32 v[234:235],0,0\n"
"v_pk_mov_b32 v[236:237],0,0\n"
"v_pk_mov_b32 v[238:239],0,0\n"
"v_pk_mov_b32 v[240:241],0,0\n"
"v_pk_mov_b32 v[242:243],0,0\n"
"v_pk_mov_b32 v[244:245],0,0\n"
"v_pk_mov_b32 v[246:247],0,0\n"
"v_pk_mov_b32 v[248:249],0,0\n"
"v_pk_mov_b32 v[250:251],0,0\n"
"v_pk_mov_b32 v[252:253],0,0\n"
"v_pk_mov_b32 v[254:255],0,0\n"
"s_branch L_slow_pro1a_%=\n"
"L_slowret_pro1a_%=:\n"
"buffer_store_dwordx4 a[164:167],v8,s[60:63],s59 offen offset:1024 sc1\n"
"v_exp_f32 v96,v96\n"
"v_exp_f32 v97,v97\n"
"v_add_f32 v12,v12,v96\n"
"buffer_store_dwordx4 a[168:171],v8,s[60:63],s59 offen offset:2048 sc1\n"
"v_exp_f32 v98,v98\n"
"v_add_f32 v12,v12,v97\n"
"v_exp_f32 v99,v99\n"
"buffer_store_dwordx4 a[172:175],v8,s[60:63],s59 offen offset:3072 sc1\n"
"v_add_f32 v12,v12,v98\n"
"v_exp_f32 v100,v100\n"
"v_add_f32 v12,v12,v99\n"
"s_mov_b32 s59,0x3000\n"
"buffer_store_dwordx4 a[176:179],v8,s[60:63],s59 offen offset:0 sc1\n"
"v_exp_f32 v101,v101\n"
"v_add_f32 v12,v12,v100\n"
"v_exp_f32 v102,v102\n"
"buffer_store_dwordx4 a[180:183],v8,s[60:63],s59 offen offset:1024 sc1\n"
"v_add_f32 v12,v12,v101\n"
"v_exp_f32 v103,v103\n"
"v_add_f32 v12,v12,v102\n"
"buffer_store_dwordx4 a[184:187],v8,s[60:63],s59 offen offset:2048 sc1\n"
"v_exp_f32 v104,v104\n"
"v_add_f32 v12,v12,v103\n"
"v_exp_f32 v105,v105\n"
"buffer_store_dwordx4 a[188:191],v8,s[60:63],s59 offen offset:3072 sc1\n"
"v_add_f32 v12,v12,v104\n"
"v_exp_f32 v106,v106\n"
"v_add_f32 v12,v12,v105\n"
"s_add_u32 m0,s57,0x8000\n"
"s_nop 0\n"
"buffer_load_dwordx4 v10,s[36:39],s49 offen offset:0 sc1 lds\n"
"v_exp_f32 v107,v107\n"
"v_add_f32 v12,v12,v106\n"
"v_exp_f32 v108,v108\n"
"buffer_load_dwordx4 v10,s[36:39],s49 offen offset:1024 sc1 lds\n"
"v_add_f32 v12,v12,v107\n"
"ds_read_b128 a[192:195],v9 offset:0\n"
"v_exp_f32 v109,v109\n"
"buffer_load_dwordx4 v10,s[36:39],s49 offen offset:2048 sc1 lds\n"
"v_add_f32 v12,v12,v108\n"
"ds_read_b128 a[196:199],v9 offset:1024\n"
"v_exp_f32 v110,v110\n"
"buffer_load_dwordx4 v10,s[36:39],s49 offen offset:3072 sc1 lds\n"
"v_add_f32 v12,v12,v109\n"
"ds_read_b128 a[200:203],v9 offset:2048\n"
"v_exp_f32 v111,v111\n"
"s_add_u32 m0,m0,0x1000\n"
"s_nop 0\n"
"buffer_load_dwordx4 v7,s[36:39],s49 offen offset:0 sc1 lds\n"
"v_add_f32 v12,v12,v110\n"
"ds_read_b128 a[204:207],v9 offset:3072\n"
"v_exp_f32 v112,v112\n"
"buffer_load_dwordx4 v7,s[36:39],s49 offen offset:1024 sc1 lds\n"
"v_add_f32 v12,v12,v111\n"
"ds_read_b128 a[208:211],v9 offset:16384\n"
"v_exp_f32 v113,v113\n"
"buffer_load_dwordx4 v7,s[36:39],s49 offen offset:2048 sc1 lds\n"
"v_add_f32 v13,v13,v112\n"
"ds_read_b128 a[212:215],v9 offset:17408\n"
"v_exp_f32 v114,v114\n"
"buffer_load_dwordx4 v7,s[36:39],s49 offen offset:3072 sc1 lds\n"
"v_add_f32 v13,v13,v113\n"
"ds_read_b128 a[216:219],v9 offset:18432\n"
"v_exp_f32 v115,v115\n"
"s_add_u32 m0,s57,0x18000\n"
"s_nop 0\n"
"buffer_load_dwordx4 v10,s[36:39],s48 offen offset:0 sc1 lds\n"
"v_add_f32 v13,v13,v114\n"
"ds_read_b128 a[220:223],v9 offset:19456\n"
"v_exp_f32 v116,v116\n"
"buffer_load_dwordx4 v10,s[36:39],s48 offen offset:1024 sc1 lds\n"
"v_add_f32 v13,v13,v115\n"
"ds_read_b128 a[224:227],v9 offset:4096\n"
"v_exp_f32 v117,v117\n"
"buffer_load_dwordx4 v10,s[36:39],s48 offen offset:2048 sc1 lds\n"
"v_add_f32 v13,v13,v116\n"
"ds_read_b128 a[228:231],v9 offset:5120\n"
"buffer_load_dwordx4 v10,s[36:39],s48 offen offset:3072 sc1 lds\n"
"s_add_u32 m0,m0,0x1000\n"
"s_nop 0\n"
"buffer_load_dwordx4 v7,s[36:39],s48 offen offset:0 sc1 lds\n"
"s_add_u32 s49,s49,0x10000\n"
"s_add_u32 s53,s53,64\n"
"s_sub_u32 s50,s50,1\n"
"s_branch L_even_%=\n"
"L_lastb_%=:\n"
"s_waitcnt lgkmcnt(9)\n"
"v_mfma_f32_16x16x32_bf16 v[96:99],a[192:195],a[128:131],v[16:19]\n"
"ds_read_b128 a[232:235],v9 offset:38912\n"
"v_add_f32 v13,v13,v85\n"
"s_waitcnt lgkmcnt(9)\n"
"v_mfma_f32_16x16x32_bf16 v[96:99],a[196:199],a[132:135],v[96:99]\n"
"buffer_load_dwordx4 v7,s[36:39],s48 offen offset:1024 sc1 lds\n"
"s_waitcnt lgkmcnt(8)\n"
"v_mfma_f32_16x16x32_bf16 v[96:99],a[200:203],a[136:139],v[96:99]\n"
"ds_read_b128 a[236:239],v9 offset:39936\n"
"v_cvt_pk_bf16_f32 v40,v64,v65\n"
"s_waitcnt lgkmcnt(8)\n"
"v_mfma_f32_16x16x32_bf16 v[96:99],a[204:207],a[140:143],v[96:99]\n"
"s_waitcnt lgkmcnt(4)\n"
"v_exp_f32 v86,v86\n"
"v_mfma_f32_16x16x32_bf16 v[96:99],a[208:211],a[144:147],v[96:99]\n"
"ds_read_b128 a[240:243],v9 offset:53248\n"
"v_cvt_pk_bf16_f32 v41,v66,v67\n"
"v_mfma_f32_16x16x32_bf16 v[96:99],a[212:215],a[148:151],v[96:99]\n"
"buffer_load_dwordx4 v7,s[36:39],s48 offen offset:2048 sc1 lds\n"
"v_mfma_f32_16x16x32_bf16 v[96:99],a[216:219],a[152:155],v[96:99]\n"
"ds_read_b128 a[244:247],v9 offset:54272\n"
"v_cvt_pk_bf16_f32 v42,v68,v69\n"
"v_mfma_f32_16x16x32_bf16 v[96:99],a[220:223],a[156:159],v[96:99]\n"
"v_exp_f32 v87,v87\n"
"v_mfma_f32_16x16x32_bf16 v[112:115],a[192:195],a[160:163],v[20:23]\n"
"ds_read_b128 a[248:251],v9 offset:55296\n"
"v_cvt_pk_bf16_f32 v43,v70,v71\n"
"v_mfma_f32_16x16x32_bf16 v[112:115],a[196:199],a[164:167],v[112:115]\n"
"buffer_load_dwordx4 v7,s[36:39],s48 offen offset:3072 sc1 lds\n"
"v_mfma_f32_16x16x32_bf16 v[112:115],a[200:203],a[168:171],v[112:115]\n"
"s_add_u32 s48,s48,0x10000\n"
"ds_read_b128 a[252:255],v9 offset:56320\n"
"v_mov_b32 v1,0\n"
"v_mfma_f32_16x16x32_bf16 v[112:115],a[204:207],a[172:175],v[112:115]\n"
"v_exp_f32 v88,v88\n"
"v_mfma_f32_16x16x32_bf16 v[112:115],a[208:211],a[176:179],v[112:115]\n"
"s_add_u32 s48,s78,0x130000\n"
"ds_read_b128 a[192:195],v9 offset:40960\n"
"v_max3_f32 v11,v96,v97,v98\n"
"v_mfma_f32_16x16x32_bf16 v[112:115],a[212:215],a[180:183],v[112:115]\n"
"v_cvt_pk_bf16_f32 v44,v72,v73\n"
"v_cvt_pk_bf16_f32 v45,v74,v75\n"
"v_mfma_f32_16x16x32_bf16 v[112:115],a[216:219],a[184:187],v[112:115]\n"
"ds_read_b128 a[196:199],v9 offset:41984\n"
"v_max_f32 v11,v11,v99\n"
"v_mfma_f32_16x16x32_bf16 v[112:115],a[220:223],a[188:191],v[112:115]\n"
"s_waitcnt lgkmcnt(6)\n"
"v_exp_f32 v89,v89\n"
"v_mfma_f32_16x16x32_bf16 v[100:103],a[224:227],a[128:131],v[16:19]\n"
"ds_read_b128 a[200:203],v9 offset:43008\n"
"v_cvt_pk_bf16_f32 v46,v76,v77\n"
"v_mfma_f32_16x16x32_bf16 v[100:103],a[228:231],a[132:135],v[100:103]\n"
"v_exp_f32 v90,v90\n"
"v_mfma_f32_16x16x32_bf16 v[100:103],a[232:235],a[136:139],v[100:103]\n"
"ds_read_b128 a[204:207],v9 offset:44032\n"
"v_cvt_pk_bf16_f32 v47,v78,v79\n"
"v_mfma_f32_16x16x32_bf16 v[100:103],a[236:239],a[140:143],v[100:103]\n"
"s_waitcnt lgkmcnt(4)\n"
"v_exp_f32 v91,v91\n"
"v_mfma_f32_16x16x32_bf16 v[100:103],a[240:243],a[144:147],v[100:103]\n"
"ds_read_b128 a[208:211],v9 offset:57344\n"
"v_max3_f32 v11,v11,v112,v113\n"
"v_mfma_f32_16x16x32_bf16 v[100:103],a[244:247],a[148:151],v[100:103]\n"
"v_exp_f32 v92,v92\n"
"v_mfma_f32_16x16x32_bf16 v[100:103],a[248:251],a[152:155],v[100:103]\n"
"ds_read_b128 a[212:215],v9 offset:58368\n"
"v_max3_f32 v11,v11,v114,v115\n"
"v_mfma_f32_16x16x32_bf16 v[100:103],a[252:255],a[156:159],v[100:103]\n"
"v_exp_f32 v93,v93\n"
"v_mfma_f32_16x16x32_bf16 v[116:119],a[224:227],a[160:163],v[20:23]\n"
"ds_read_b128 a[216:219],v9 offset:59392\n"
"v_cvt_pk_bf16_f32 v48,v80,v81\n"
"v_mfma_f32_16x16x32_bf16 v[116:119],a[228:231],a[164:167],v[116:119]\n"
"v_exp_f32 v94,v94\n"
"v_mfma_f32_16x16x32_bf16 v[116:119],a[232:235],a[168:171],v[116:119]\n"
"ds_read_b128 a[220:223],v9 offset:60416\n"
"v_cvt_pk_bf16_f32 v49,v82,v83\n"
"v_mfma_f32_16x16x32_bf16 v[116:119],a[236:239],a[172:175],v[116:119]\n"
"v_exp_f32 v95,v95\n"
"v_mfma_f32_16x16x32_bf16 v[116:119],a[240:243],a[176:179],v[116:119]\n"
"ds_read_b128 a[224:227],v9 offset:45056\n"
"v_max3_f32 v11,v11,v100,v101\n"
"v_mfma_f32_16x16x32_bf16 v[116:119],a[244:247],a[180:183],v[116:119]\n"
"v_cvt_pk_bf16_f32 v50,v84,v85\n"
"v_add_f32 v13,v13,v86\n"
"v_mfma_f32_16x16x32_bf16 v[116:119],a[248:251],a[184:187],v[116:119]\n"
"ds_read_b128 a[228:231],v9 offset:46080\n"
"v_max3_f32 v11,v11,v102,v103\n"
"v_mfma_f32_16x16x32_bf16 v[116:119],a[252:255],a[188:191],v[116:119]\n"
"s_waitcnt lgkmcnt(6)\n"
"v_add_f32 v13,v13,v87\n"
"v_cvt_pk_bf16_f32 v51,v86,v87\n"
"v_mfma_f32_16x16x32_bf16 v[104:107],a[192:195],a[128:131],v[16:19]\n"
"ds_read_b128 a[232:235],v9 offset:47104\n"
"v_add_f32 v13,v13,v88\n"
"v_mfma_f32_16x16x32_bf16 v[104:107],a[196:199],a[132:135],v[104:107]\n"
"v_add_f32 v13,v13,v89\n"
"v_cvt_pk_bf16_f32 v52,v88,v89\n"
"v_mfma_f32_16x16x32_bf16 v[104:107],a[200:203],a[136:139],v[104:107]\n"
"ds_read_b128 a[236:239],v9 offset:48128\n"
"v_add_f32 v13,v13,v90\n"
"v_mfma_f32_16x16x32_bf16 v[104:107],a[204:207],a[140:143],v[104:107]\n"
"s_waitcnt lgkmcnt(4)\n"
"v_add_f32 v13,v13,v91\n"
"v_cvt_pk_bf16_f32 v53,v90,v91\n"
"v_mfma_f32_16x16x32_bf16 v[104:107],a[208:211],a[144:147],v[104:107]\n"
"ds_read_b128 a[240:243],v9 offset:61440\n"
"v_max3_f32 v11,v11,v116,v117\n"
"v_mfma_f32_16x16x32_bf16 v[104:107],a[212:215],a[148:151],v[104:107]\n"
"v_add_f32 v13,v13,v92\n"
"v_add_f32 v13,v13,v93\n"
"v_mfma_f32_16x16x32_bf16 v[104:107],a[216:219],a[152:155],v[104:107]\n"
"ds_read_b128 a[244:247],v9 offset:62464\n"
"v_max3_f32 v11,v11,v118,v119\n"
"v_mfma_f32_16x16x32_bf16 v[104:107],a[220:223],a[156:159],v[104:107]\n"
"v_cvt_pk_bf16_f32 v54,v92,v93\n"
"v_add_f32 v13,v13,v94\n"
"v_mfma_f32_16x16x32_bf16 v[120:123],a[192:195],a[160:163],v[20:23]\n"
"ds_read_b128 a[248:251],v9 offset:63488\n"
"v_add_f32 v13,v13,v95\n"
"v_mfma_f32_16x16x32_bf16 v[120:123],a[196:199],a[164:167],v[120:123]\n"
"v_cvt_pk_bf16_f32 v55,v94,v95\n"
"v_mfma_f32_16x16x32_bf16 v[120:123],a[200:203],a[168:171],v[120:123]\n"
"s_waitcnt lgkmcnt(3)\n"
"ds_read_b128 a[252:255],v9 offset:64512\n"
"v_mfma_f32_16x16x32_bf16 v[120:123],a[204:207],a[172:175],v[120:123]\n"
"s_waitcnt vmcnt(8)\n"
"s_barrier\n"
"v_mfma_f32_16x16x32_bf16 v[120:123],a[208:211],a[176:179],v[120:123]\n"
"s_add_u32 m0,s57,0x8000\n"
"ds_read_b128 a[192:195],v8 offset:0\n"
"v_max3_f32 v11,v11,v104,v105\n"
"v_mfma_f32_16x16x32_bf16 v[120:123],a[212:215],a[180:183],v[120:123]\n"
"buffer_load_dwordx4 v10,s[36:39],s49 offen offset:0 sc1 lds\n"
"v_mfma_f32_16x16x32_bf16 v[120:123],a[216:219],a[184:187],v[120:123]\n"
"ds_read_b128 a[196:199],v8 offset:8192\n"
"v_max3_f32 v11,v11,v106,v107\n"
"v_mfma_f32_16x16x32_bf16 v[120:123],a[220:223],a[188:191],v[120:123]\n"
"v_mfma_f32_16x16x32_bf16 v[108:111],a[224:227],a[128:131],v[16:19]\n"
"ds_read_b128 a[200:203],v8 offset:1024\n"
"v_mfma_f32_16x16x32_bf16 v[108:111],a[228:231],a[132:135],v[108:111]\n"
"buffer_load_dwordx4 v10,s[36:39],s49 offen offset:1024 sc1 lds\n"
"v_mfma_f32_16x16x32_bf16 v[108:111],a[232:235],a[136:139],v[108:111]\n"
"ds_read_b128 a[204:207],v8 offset:9216\n"
"v_mfma_f32_16x16x32_bf16 v[108:111],a[236:239],a[140:143],v[108:111]\n"
"s_waitcnt lgkmcnt(4)\n"
"v_mfma_f32_16x16x32_bf16 v[108:111],a[240:243],a[144:147],v[108:111]\n"
"ds_read_b128 a[208:211],v8 offset:2048\n"
"v_max3_f32 v11,v11,v120,v121\n"
"v_mfma_f32_16x16x32_bf16 v[108:111],a[244:247],a[148:151],v[108:111]\n"
"buffer_load_dwordx4 v10,s[36:39],s49 offen offset:2048 sc1 lds\n"
"v_mfma_f32_16x16x32_bf16 v[108:111],a[248:251],a[152:155],v[108:111]\n"
"ds_read_b128 a[212:215],v8 offset:10240\n"
"v_max3_f32 v11,v11,v122,v123\n"
"v_mfma_f32_16x16x32_bf16 v[108:111],a[252:255],a[156:159],v[108:111]\n"
"v_mfma_f32_16x16x32_bf16 v[124:127],a[224:227],a[160:163],v[20:23]\n"
"ds_read_b128 a[216:219],v8 offset:3072\n"
"v_mfma_f32_16x16x32_bf16 v[124:127],a[228:231],a[164:167],v[124:127]\n"
"buffer_load_dwordx4 v10,s[36:39],s49 offen offset:3072 sc1 lds\n"
"v_mfma_f32_16x16x32_bf16 v[124:127],a[232:235],a[168:171],v[124:127]\n"
"ds_read_b128 a[220:223],v8 offset:11264\n"
"v_mfma_f32_16x16x32_bf16 v[124:127],a[236:239],a[172:175],v[124:127]\n"
"v_mfma_f32_16x16x32_bf16 v[124:127],a[240:243],a[176:179],v[124:127]\n"
"s_add_u32 m0,m0,0x1000\n"
"ds_read_b128 a[224:227],v8 offset:4096\n"
"v_max3_f32 v11,v11,v108,v109\n"
"v_mfma_f32_16x16x32_bf16 v[124:127],a[244:247],a[180:183],v[124:127]\n"
"buffer_load_dwordx4 v7,s[36:39],s49 offen offset:0 sc1 lds\n"
"v_mfma_f32_16x16x32_bf16 v[124:127],a[248:251],a[184:187],v[124:127]\n"
"s_cmp_le_i32 s50,s52\n"
"ds_read_b128 a[228:231],v8 offset:12288\n"
"v_max3_f32 v11,v11,v110,v111\n"
"v_mfma_f32_16x16x32_bf16 v[124:127],a[252:255],a[188:191],v[124:127]\n"
"s_waitcnt lgkmcnt(6)\n"
"v_mfma_f32_16x16x32_bf16 v[128:131],a[192:195],v[40:43],v[128:131]\n"
"s_cselect_b32 s51,s80,s81\n"
"ds_read_b128 a[232:235],v8 offset:5120\n"
"v_mfma_f32_16x16x32_bf16 v[128:131],a[196:199],v[44:47],v[128:131]\n"
"buffer_load_dwordx4 v7,s[36:39],s49 offen offset:1024 sc1 lds\n"
"v_mfma_f32_16x16x32_bf16 v[192:195],a[192:195],v[48:51],v[192:195]\n"
"s_barrier\n"
"ds_read_b128 a[236:239],v8 offset:13312\n"
"v_mfma_f32_16x16x32_bf16 v[192:195],a[196:199],v[52:55],v[192:195]\n"
"v_mfma_f32_16x16x32_bf16 v[132:135],a[200:203],v[40:43],v[132:135]\n"
"ds_read_b128 a[240:243],v8 offset:6144\n"
"v_max3_f32 v11,v11,v124,v125\n"
"v_mfma_f32_16x16x32_bf16 v[132:135],a[204:207],v[44:47],v[132:135]\n"
"v_max3_f32 v11,v11,v126,v127\n"
"v_cmp_nge_f32 vcc,s51,v11\n"
"v_mfma_f32_16x16x32_bf16 v[196:199],a[200:203],v[48:51],v[196:199]\n"
"s_waitcnt lgkmcnt(5)\n"
"ds_read_b128 a[244:247],v8 offset:14336\n"
"v_mfma_f32_16x16x32_bf16 v[196:199],a[204:207],v[52:55],v[196:199]\n"
"s_cbranch_vccnz L_slow_lastb_%=\n"
"L_slowret_lastb_%=:\n"
"v_exp_f32 v96,v96\n"
"v_mfma_f32_16x16x32_bf16 v[136:139],a[208:211],v[40:43],v[136:139]\n"
"ds_read_b128 a[248:251],v8 offset:7168\n"
"v_add_f32 v12,v12,v96\n"
"v_mfma_f32_16x16x32_bf16 v[136:139],a[212:215],v[44:47],v[136:139]\n"
"buffer_load_dwordx4 v7,s[36:39],s49 offen offset:2048 sc1 lds\n"
"v_mfma_f32_16x16x32_bf16 v[200:203],a[208:211],v[48:51],v[200:203]\n"
"ds_read_b128 a[252:255],v8 offset:15360\n"
"v_mfma_f32_16x16x32_bf16 v[200:203],a[212:215],v[52:55],v[200:203]\n"
"v_exp_f32 v97,v97\n"
"v_mfma_f32_16x16x32_bf16 v[140:143],a[216:219],v[40:43],v[140:143]\n"
"ds_read_b128 a[192:195],v8 offset:16384\n"
"v_add_f32 v12,v12,v97\n"
"v_mfma_f32_16x16x32_bf16 v[140:143],a[220:223],v[44:47],v[140:143]\n"
"buffer_load_dwordx4 v7,s[36:39],s49 offen offset:3072 sc1 lds\n"
"v_mfma_f32_16x16x32_bf16 v[204:207],a[216:219],v[48:51],v[204:207]\n"
"s_add_u32 s49,s49,0x10000\n"
"ds_read_b128 a[196:199],v8 offset:24576\n"
"v_mfma_f32_16x16x32_bf16 v[204:207],a[220:223],v[52:55],v[204:207]\n"
"s_waitcnt lgkmcnt(6)\n"
"v_exp_f32 v98,v98\n"
"v_mfma_f32_16x16x32_bf16 v[144:147],a[224:227],v[40:43],v[144:147]\n"
"s_add_u32 m0,s57,0x18000\n"
"ds_read_b128 a[200:203],v8 offset:17408\n"
"v_add_f32 v12,v12,v98\n"
"v_mfma_f32_16x16x32_bf16 v[144:147],a[228:231],v[44:47],v[144:147]\n"
"buffer_load_dwordx4 v10,s[36:39],s48 offen offset:0 sc1 lds\n"
"v_mfma_f32_16x16x32_bf16 v[208:211],a[224:227],v[48:51],v[208:211]\n"
"ds_read_b128 a[204:207],v8 offset:25600\n"
"v_mfma_f32_16x16x32_bf16 v[208:211],a[228:231],v[52:55],v[208:211]\n"
"v_exp_f32 v99,v99\n"
"v_mfma_f32_16x16x32_bf16 v[148:151],a[232:235],v[40:43],v[148:151]\n"
"ds_read_b128 a[208:211],v8 offset:18432\n"
"v_add_f32 v12,v12,v99\n"
"v_mfma_f32_16x16x32_bf16 v[148:151],a[236:239],v[44:47],v[148:151]\n"
"v_exp_f32 v100,v100\n"
"v_mfma_f32_16x16x32_bf16 v[212:215],a[232:235],v[48:51],v[212:215]\n"
"ds_read_b128 a[212:215],v8 offset:26624\n"
"v_add_f32 v12,v12,v100\n"
"v_mfma_f32_16x16x32_bf16 v[212:215],a[236:239],v[52:55],v[212:215]\n"
"s_waitcnt lgkmcnt(6)\n"
"v_exp_f32 v101,v101\n"
"v_mfma_f32_16x16x32_bf16 v[152:155],a[240:243],v[40:43],v[152:155]\n"
"ds_read_b128 a[216:219],v8 offset:19456\n"
"v_add_f32 v12,v12,v101\n"
"v_mfma_f32_16x16x32_bf16 v[152:155],a[244:247],v[44:47],v[152:155]\n"
"buffer_load_dwordx4 v10,s[36:39],s48 offen offset:1024 sc1 lds\n"
"v_mfma_f32_16x16x32_bf16 v[216:219],a[240:243],v[48:51],v[216:219]\n"
"ds_read_b128 a[220:223],v8 offset:27648\n"
"v_mfma_f32_16x16x32_bf16 v[216:219],a[244:247],v[52:55],v[216:219]\n"
"v_exp_f32 v102,v102\n"
"v_mfma_f32_16x16x32_bf16 v[156:159],a[248:251],v[40:43],v[156:159]\n"
"ds_read_b128 a[224:227],v8 offset:20480\n"
"v_add_f32 v12,v12,v102\n"
"v_mfma_f32_16x16x32_bf16 v[156:159],a[252:255],v[44:47],v[156:159]\n"
"v_exp_f32 v103,v103\n"
"v_mfma_f32_16x16x32_bf16 v[220:223],a[248:251],v[48:51],v[220:223]\n"
"ds_read_b128 a[228:231],v8 offset:28672\n"
"v_add_f32 v12,v12,v103\n"
"v_mfma_f32_16x16x32_bf16 v[220:223],a[252:255],v[52:55],v[220:223]\n"
"s_waitcnt lgkmcnt(6)\n"
"v_exp_f32 v104,v104\n"
"v_mfma_f32_16x16x32_bf16 v[160:163],a[192:195],v[40:43],v[160:163]\n"
"ds_read_b128 a[232:235],v8 offset:21504\n"
"v_add_f32 v12,v12,v104\n"
"v_mfma_f32_16x16x32_bf16 v[160:163],a[196:199],v[44:47],v[160:163]\n"
"buffer_load_dwordx4 v10,s[36:39],s48 offen offset:2048 sc1 lds\n"
"v_mfma_f32_16x16x32_bf16 v[224:227],a[192:195],v[48:51],v[224:227]\n"
"ds_read_b128 a[236:239],v8 offset:29696\n"
"v_mfma_f32_16x16x32_bf16 v[224:227],a[196:199],v[52:55],v[224:227]\n"
"v_exp_f32 v105,v105\n"
"v_mfma_f32_16x16x32_bf16 v[164:167],a[200:203],v[40:43],v[164:167]\n"
"ds_read_b128 a[240:243],v8 offset:22528\n"
"v_add_f32 v12,v12,v105\n"
"v_mfma_f32_16x16x32_bf16 v[164:167],a[204:207],v[44:47],v[164:167]\n"
"v_exp_f32 v106,v106\n"
"v_mfma_f32_16x16x32_bf16 v[228:231],a[200:203],v[48:51],v[228:231]\n"
"s_add_u32 s53,s53,64\n"
"ds_read_b128 a[244:247],v8 offset:30720\n"
"v_add_f32 v12,v12,v106\n"
"v_mfma_f32_16x16x32_bf16 v[228:231],a[204:207],v[52:55],v[228:231]\n"
"s_waitcnt lgkmcnt(6)\n"
"v_exp_f32 v107,v107\n"
"v_mfma_f32_16x16x32_bf16 v[168:171],a[208:211],v[40:43],v[168:171]\n"
"s_sub_u32 s50,s50,1\n"
"ds_read_b128 a[248:251],v8 offset:23552\n"
"v_add_f32 v12,v12,v107\n"
"v_mfma_f32_16x16x32_bf16 v[168:171],a[212:215],v[44:47],v[168:171]\n"
"buffer_load_dwordx4 v10,s[36:39],s48 offen offset:3072 sc1 lds\n"
"v_mfma_f32_16x16x32_bf16 v[232:235],a[208:211],v[48:51],v[232:235]\n"
"ds_read_b128 a[252:255],v8 offset:31744\n"
"v_mfma_f32_16x16x32_bf16 v[232:235],a[212:215],v[52:55],v[232:235]\n"
"v_exp_f32 v108,v108\n"
"v_mfma_f32_16x16x32_bf16 v[172:175],a[216:219],v[40:43],v[172:175]\n"
"v_exp_f32 v109,v109\n"
"v_mfma_f32_16x16x32_bf16 v[172:175],a[220:223],v[44:47],v[172:175]\n"
"v_exp_f32 v110,v110\n"
"v_mfma_f32_16x16x32_bf16 v[236:239],a[216:219],v[48:51],v[236:239]\n"
"v_exp_f32 v111,v111\n"
"v_mfma_f32_16x16x32_bf16 v[236:239],a[220:223],v[52:55],v[236:239]\n"
"s_waitcnt lgkmcnt(4)\n"
"v_exp_f32 v112,v112\n"
"v_mfma_f32_16x16x32_bf16 v[176:179],a[224:227],v[40:43],v[176:179]\n"
"v_exp_f32 v113,v113\n"
"v_mfma_f32_16x16x32_bf16 v[176:179],a[228:231],v[44:47],v[176:179]\n"
"s_waitcnt vmcnt(4)\n"
"s_barrier\n"
"v_add_f32 v12,v12,v108\n"
"v_add_f32 v12,v12,v109\n"
"v_mfma_f32_16x16x32_bf16 v[240:243],a[224:227],v[48:51],v[240:243]\n"
"ds_read_b128 a[192:195],v8 offset:32768\n"
"v_add_f32 v12,v12,v110\n"
"v_mfma_f32_16x16x32_bf16 v[240:243],a[228:231],v[52:55],v[240:243]\n"
"v_exp_f32 v114,v114\n"
"v_mfma_f32_16x16x32_bf16 v[180:183],a[232:235],v[40:43],v[180:183]\n"
"s_add_u32 m0,m0,0x1000\n"
"ds_read_b128 a[196:199],v8 offset:40960\n"
"v_add_f32 v12,v12,v111\n"
"v_mfma_f32_16x16x32_bf16 v[180:183],a[236:239],v[44:47],v[180:183]\n"
"buffer_load_dwordx4 v7,s[36:39],s48 offen offset:0 sc1 lds\n"
"v_mfma_f32_16x16x32_bf16 v[244:247],a[232:235],v[48:51],v[244:247]\n"
"ds_read_b128 a[200:203],v8 offset:33792\n"
"v_add_f32 v13,v13,v112\n"
"v_mfma_f32_16x16x32_bf16 v[244:247],a[236:239],v[52:55],v[244:247]\n"
"s_waitcnt lgkmcnt(3)\n"
"v_exp_f32 v115,v115\n"
"v_mfma_f32_16x16x32_bf16 v[184:187],a[240:243],v[40:43],v[184:187]\n"
"ds_read_b128 a[204:207],v8 offset:41984\n"
"v_add_f32 v13,v13,v113\n"
"v_mfma_f32_16x16x32_bf16 v[184:187],a[244:247],v[44:47],v[184:187]\n"
"buffer_load_dwordx4 v7,s[36:39],s48 offen offset:1024 sc1 lds\n"
"v_mfma_f32_16x16x32_bf16 v[248:251],a[240:243],v[48:51],v[248:251]\n"
"ds_read_b128 a[208:211],v8 offset:34816\n"
"v_add_f32 v13,v13,v114\n"
"v_mfma_f32_16x16x32_bf16 v[248:251],a[244:247],v[52:55],v[248:251]\n"
"v_exp_f32 v116,v116\n"
"v_mfma_f32_16x16x32_bf16 v[188:191],a[248:251],v[40:43],v[188:191]\n"
"s_cmp_lg_u32 s76,0\n"
"ds_read_b128 a[212:215],v8 offset:43008\n"
"v_add_f32 v13,v13,v115\n"
"v_mfma_f32_16x16x32_bf16 v[188:191],a[252:255],v[44:47],v[188:191]\n"
"global_load_dwordx2 v[2:3],v1,s[84:85] sc1\n"
"v_mfma_f32_16x16x32_bf16 v[252:255],a[248:251],v[48:51],v[252:255]\n"
"s_waitcnt lgkmcnt(4)\n"
"ds_read_b128 a[216:219],v8 offset:35840\n"
"v_add_f32 v13,v13,v116\n"
"v_mfma_f32_16x16x32_bf16 v[252:255],a[252:255],v[52:55],v[252:255]\n"
"s_cbranch_scc1 L_resc_lastb_%=\n"
"L_rescret_lastb_%=:\n"
"v_exp_f32 v117,v117\n"
"v_exp_f32 v118,v118\n"
"v_exp_f32 v119,v119\n"
"v_exp_f32 v120,v120\n"
"v_exp_f32 v121,v121\n"
"v_exp_f32 v122,v122\n"
"v_exp_f32 v123,v123\n"
"v_exp_f32 v124,v124\n"
"v_exp_f32 v125,v125\n"
"v_exp_f32 v126,v126\n"
"v_exp_f32 v127,v127\n"
"v_cvt_pk_bf16_f32 v40,v96,v97\n"
"v_cvt_pk_bf16_f32 v41,v98,v99\n"
"v_cvt_pk_bf16_f32 v42,v100,v101\n"
"v_cvt_pk_bf16_f32 v43,v102,v103\n"
"v_cvt_pk_bf16_f32 v44,v104,v105\n"
"v_cvt_pk_bf16_f32 v45,v106,v107\n"
"v_cvt_pk_bf16_f32 v46,v108,v109\n"
"v_cvt_pk_bf16_f32 v47,v110,v111\n"
"v_add_f32 v13,v13,v117\n"
"v_add_f32 v13,v13,v118\n"
"v_add_f32 v13,v13,v119\n"
"v_add_f32 v13,v13,v120\n"
"v_add_f32 v13,v13,v121\n"
"v_add_f32 v13,v13,v122\n"
"v_add_f32 v13,v13,v123\n"
"v_add_f32 v13,v13,v124\n"
"v_add_f32 v13,v13,v125\n"
"v_add_f32 v13,v13,v126\n"
"v_add_f32 v13,v13,v127\n"
"v_cvt_pk_bf16_f32 v48,v112,v113\n"
"v_cvt_pk_bf16_f32 v49,v114,v115\n"
"v_cvt_pk_bf16_f32 v50,v116,v117\n"
"v_cvt_pk_bf16_f32 v51,v118,v119\n"
"v_cvt_pk_bf16_f32 v52,v120,v121\n"
"v_cvt_pk_bf16_f32 v53,v122,v123\n"
"v_cvt_pk_bf16_f32 v54,v124,v125\n"
"v_cvt_pk_bf16_f32 v55,v126,v127\n"
"s_waitcnt vmcnt(0)\n"
"v_cmp_ne_u64 vcc,s[34:35],v[2:3]\n"
"s_nop 0\n"
"s_cbranch_vccnz L_qf_%=\n"
"L_qf_ret_%=:\n"
"s_mov_b32 s59,0x0\n"
"buffer_load_dwordx4 a[128:131],v8,s[60:63],s59 offen offset:0 sc1\n"
"buffer_load_dwordx4 a[132:135],v8,s[60:63],s59 offen offset:1024 sc1\n"
"v_mfma_f32_16x16x32_bf16 v[128:131],a[192:195],v[40:43],v[128:131]\n"
"ds_read_b128 a[220:223],v8 offset:44032\n"
"v_mfma_f32_16x16x32_bf16 v[128:131],a[196:199],v[44:47],v[128:131]\n"
"buffer_load_dwordx4 v7,s[36:39],s48 offen offset:2048 sc1 lds\n"
"v_mfma_f32_16x16x32_bf16 v[192:195],a[192:195],v[48:51],v[192:195]\n"
"ds_read_b128 a[224:227],v8 offset:36864\n"
"v_mfma_f32_16x16x32_bf16 v[192:195],a[196:199],v[52:55],v[192:195]\n"
"s_waitcnt lgkmcnt(5)\n"
"v_mfma_f32_16x16x32_bf16 v[132:135],a[200:203],v[40:43],v[132:135]\n"
"ds_read_b128 a[228:231],v8 offset:45056\n"
"v_mfma_f32_16x16x32_bf16 v[132:135],a[204:207],v[44:47],v[132:135]\n"
"buffer_load_dwordx4 v7,s[36:39],s48 offen offset:3072 sc1 lds\n"
"v_mfma_f32_16x16x32_bf16 v[196:199],a[200:203],v[48:51],v[196:199]\n"
"ds_read_b128 a[232:235],v8 offset:37888\n"
"v_accvgpr_write_b32 a0,v128\n"
"v_mfma_f32_16x16x32_bf16 v[196:199],a[204:207],v[52:55],v[196:199]\n"
"s_waitcnt lgkmcnt(5)\n"
"v_accvgpr_write_b32 a1,v129\n"
"v_accvgpr_write_b32 a2,v130\n"
"v_mfma_f32_16x16x32_bf16 v[136:139],a[208:211],v[40:43],v[136:139]\n"
"ds_read_b128 a[236:239],v8 offset:46080\n"
"v_accvgpr_write_b32 a3,v131\n"
"v_mfma_f32_16x16x32_bf16 v[136:139],a[212:215],v[44:47],v[136:139]\n"
"buffer_load_dwordx4 a[136:139],v8,s[60:63],s59 offen offset:2048 sc1\n"
"v_mfma_f32_16x16x32_bf16 v[200:203],a[208:211],v[48:51],v[200:203]\n"
"ds_read_b128 a[240:243],v8 offset:38912\n"
"v_accvgpr_write_b32 a64,v192\n"
"v_mfma_f32_16x16x32_bf16 v[200:203],a[212:215],v[52:55],v[200:203]\n"
"s_waitcnt lgkmcnt(5)\n"
"v_accvgpr_write_b32 a65,v193\n"
"v_accvgpr_write_b32 a66,v194\n"
"v_mfma_f32_16x16x32_bf16 v[140:143],a[216:219],v[40:43],v[140:143]\n"
"ds_read_b128 a[244:247],v8 offset:47104\n"
"v_accvgpr_write_b32 a67,v195\n"
"v_mfma_f32_16x16x32_bf16 v[140:143],a[220:223],v[44:47],v[140:143]\n"
"buffer_load_dwordx4 a[140:143],v8,s[60:63],s59 offen offset:3072 sc1\n"
"v_mfma_f32_16x16x32_bf16 v[204:207],a[216:219],v[48:51],v[204:207]\n"
"ds_read_b128 a[248:251],v8 offset:39936\n"
"v_accvgpr_write_b32 a4,v132\n"
"v_mfma_f32_16x16x32_bf16 v[204:207],a[220:223],v[52:55],v[204:207]\n"
"s_waitcnt lgkmcnt(5)\n"
"v_accvgpr_write_b32 a5,v133\n"
"v_accvgpr_write_b32 a6,v134\n"
"v_mfma_f32_16x16x32_bf16 v[144:147],a[224:227],v[40:43],v[144:147]\n"
"s_mov_b32 s59,0x1000\n"
"ds_read_b128 a[252:255],v8 offset:48128\n"
"v_accvgpr_write_b32 a7,v135\n"
"v_mfma_f32_16x16x32_bf16 v[144:147],a[228:231],v[44:47],v[144:147]\n"
"buffer_load_dwordx4 a[144:147],v8,s[60:63],s59 offen offset:0 sc1\n"
"v_mfma_f32_16x16x32_bf16 v[208:211],a[224:227],v[48:51],v[208:211]\n"
"ds_read_b128 a[192:195],v8 offset:49152\n"
"v_accvgpr_write_b32 a68,v196\n"
"v_mfma_f32_16x16x32_bf16 v[208:211],a[228:231],v[52:55],v[208:211]\n"
"s_waitcnt lgkmcnt(5)\n"
"v_accvgpr_write_b32 a69,v197\n"
"v_accvgpr_write_b32 a70,v198\n"
"v_mfma_f32_16x16x32_bf16 v[148:151],a[232:235],v[40:43],v[148:151]\n"
"ds_read_b128 a[196:199],v8 offset:57344\n"
"v_accvgpr_write_b32 a71,v199\n"
"v_mfma_f32_16x16x32_bf16 v[148:151],a[236:239],v[44:47],v[148:151]\n"
"buffer_load_dwordx4 a[148:151],v8,s[60:63],s59 offen offset:1024 sc1\n"
"v_mfma_f32_16x16x32_bf16 v[212:215],a[232:235],v[48:51],v[212:215]\n"
"ds_read_b128 a[200:203],v8 offset:50176\n"
"v_accvgpr_write_b32 a8,v136\n"
"v_mfma_f32_16x16x32_bf16 v[212:215],a[236:239],v[52:55],v[212:215]\n"
"s_waitcnt lgkmcnt(5)\n"
"v_accvgpr_write_b32 a9,v137\n"
"v_accvgpr_write_b32 a10,v138\n"
"v_mfma_f32_16x16x32_bf16 v[152:155],a[240:243],v[40:43],v[152:155]\n"
"ds_read_b128 a[204:207],v8 offset:58368\n"
"v_accvgpr_write_b32 a11,v139\n"
"v_mfma_f32_16x16x32_bf16 v[152:155],a[244:247],v[44:47],v[152:155]\n"
"buffer_load_dwordx4 a[152:155],v8,s[60:63],s59 offen offset:2048 sc1\n"
"v_mfma_f32_16x16x32_bf16 v[216:219],a[240:243],v[48:51],v[216:219]\n"
"ds_read_b128 a[208:211],v8 offset:51200\n"
"v_accvgpr_write_b32 a72,v200\n"
"v_mfma_f32_16x16x32_bf16 v[216:219],a[244:247],v[52:55],v[216:219]\n"
"s_waitcnt lgkmcnt(5)\n"
"v_accvgpr_write_b32 a73,v201\n"
"v_accvgpr_write_b32 a74,v202\n"
"v_mfma_f32_16x16x32_bf16 v[156:159],a[248:251],v[40:43],v[156:159]\n"
"ds_read_b128 a[212:215],v8 offset:59392\n"
"v_accvgpr_write_b32 a75,v203\n"
"v_mfma_f32_16x16x32_bf16 v[156:159],a[252:255],v[44:47],v[156:159]\n"
"buffer_load_dwordx4 a[156:159],v8,s[60:63],s59 offen offset:3072 sc1\n"
"v_mfma_f32_16x16x32_bf16 v[220:223],a[248:251],v[48:51],v[220:223]\n"
"ds_read_b128 a[216:219],v8 offset:52224\n"
"v_accvgpr_write_b32 a12,v140\n"
"v_mfma_f32_16x16x32_bf16 v[220:223],a[252:255],v[52:55],v[220:223]\n"
"s_waitcnt lgkmcnt(5)\n"
"v_accvgpr_write_b32 a13,v141\n"
"v_accvgpr_write_b32 a14,v142\n"
"v_mfma_f32_16x16x32_bf16 v[160:163],a[192:195],v[40:43],v[160:163]\n"
"s_mov_b32 s59,0x2000\n"
"ds_read_b128 a[220:223],v8 offset:60416\n"
"v_accvgpr_write_b32 a15,v143\n"
"v_mfma_f32_16x16x32_bf16 v[160:163],a[196:199],v[44:47],v[160:163]\n"
"buffer_load_dwordx4 a[160:163],v8,s[60:63],s59 offen offset:0 sc1\n"
"v_mfma_f32_16x16x32_bf16 v[224:227],a[192:195],v[48:51],v[224:227]\n"
"ds_read_b128 a[224:227],v8 offset:53248\n"
"v_accvgpr_write_b32 a76,v204\n"
"v_mfma_f32_16x16x32_bf16 v[224:227],a[196:199],v[52:55],v[224:227]\n"
"s_waitcnt lgkmcnt(5)\n"
"v_accvgpr_write_b32 a77,v205\n"
"v_accvgpr_write_b32 a78,v206\n"
"v_mfma_f32_16x16x32_bf16 v[164:167],a[200:203],v[40:43],v[164:167]\n"
"ds_read_b128 a[228:231],v8 offset:61440\n"
"v_accvgpr_write_b32 a79,v207\n"
"v_mfma_f32_16x16x32_bf16 v[164:167],a[204:207],v[44:47],v[164:167]\n"
"buffer_load_dwordx4 a[164:167],v8,s[60:63],s59 offen offset:1024 sc1\n"
"v_mfma_f32_16x16x32_bf16 v[228:231],a[200:203],v[48:51],v[228:231]\n"
"ds_read_b128 a[232:235],v8 offset:54272\n"
"v_accvgpr_write_b32 a16,v144\n"
"v_mfma_f32_16x16x32_bf16 v[228:231],a[204:207],v[52:55],v[228:231]\n"
"s_waitcnt lgkmcnt(5)\n"
"v_accvgpr_write_b32 a17,v145\n"
"v_accvgpr_write_b32 a18,v146\n"
"v_mfma_f32_16x16x32_bf16 v[168:171],a[208:211],v[40:43],v[168:171]\n"
"ds_read_b128 a[236:239],v8 offset:62464\n"
"v_accvgpr_write_b32 a19,v147\n"
"v_mfma_f32_16x16x32_bf16 v[168:171],a[212:215],v[44:47],v[168:171]\n"
"buffer_load_dwordx4 a[168:171],v8,s[60:63],s59 offen offset:2048 sc1\n"
"v_mfma_f32_16x16x32_bf16 v[232:235],a[208:211],v[48:51],v[232:235]\n"
"ds_read_b128 a[240:243],v8 offset:55296\n"
"v_accvgpr_write_b32 a80,v208\n"
"v_mfma_f32_16x16x32_bf16 v[232:235],a[212:215],v[52:55],v[232:235]\n"
"s_waitcnt lgkmcnt(5)\n"
"v_accvgpr_write_b32 a81,v209\n"
"v_accvgpr_write_b32 a82,v210\n"
"v_mfma_f32_16x16x32_bf16 v[172:175],a[216:219],v[40:43],v[172:175]\n"
"ds_read_b128 a[244:247],v8 offset:63488\n"
"v_accvgpr_write_b32 a83,v211\n"
"v_mfma_f32_16x16x32_bf16 v[172:175],a[220:223],v[44:47],v[172:175]\n"
"buffer_load_dwordx4 a[172:175],v8,s[60:63],s59 offen offset:3072 sc1\n"
"v_mfma_f32_16x16x32_bf16 v[236:239],a[216:219],v[48:51],v[236:239]\n"
"ds_read_b128 a[248:251],v8 offset:56320\n"
"v_accvgpr_write_b32 a20,v148\n"
"v_mfma_f32_16x16x32_bf16 v[236:239],a[220:223],v[52:55],v[236:239]\n"
"s_waitcnt lgkmcnt(5)\n"
"v_accvgpr_write_b32 a21,v149\n"
"v_accvgpr_write_b32 a22,v150\n"
"v_mfma_f32_16x16x32_bf16 v[176:179],a[224:227],v[40:43],v[176:179]\n"
"s_mov_b32 s59,0x3000\n"
"ds_read_b128 a[252:255],v8 offset:64512\n"
"v_accvgpr_write_b32 a23,v151\n"
"v_mfma_f32_16x16x32_bf16 v[176:179],a[228:231],v[44:47],v[176:179]\n"
"buffer_load_dwordx4 a[176:179],v8,s[60:63],s59 offen offset:0 sc1\n"
"v_mfma_f32_16x16x32_bf16 v[240:243],a[224:227],v[48:51],v[240:243]\n"
"v_accvgpr_write_b32 a84,v212\n"
"v_accvgpr_write_b32 a85,v213\n"
"v_mfma_f32_16x16x32_bf16 v[240:243],a[228:231],v[52:55],v[240:243]\n"
"s_waitcnt lgkmcnt(4)\n"
"v_accvgpr_write_b32 a86,v214\n"
"v_accvgpr_write_b32 a87,v215\n"
"v_mfma_f32_16x16x32_bf16 v[180:183],a[232:235],v[40:43],v[180:183]\n"
"v_accvgpr_write_b32 a24,v152\n"
"v_accvgpr_write_b32 a25,v153\n"
"v_mfma_f32_16x16x32_bf16 v[180:183],a[236:239],v[44:47],v[180:183]\n"
"buffer_load_dwordx4 a[180:183],v8,s[60:63],s59 offen offset:1024 sc1\n"
"v_mfma_f32_16x16x32_bf16 v[244:247],a[232:235],v[48:51],v[244:247]\n"
"v_accvgpr_write_b32 a26,v154\n"
"v_accvgpr_write_b32 a27,v155\n"
"v_mfma_f32_16x16x32_bf16 v[244:247],a[236:239],v[52:55],v[244:247]\n"
"s_waitcnt lgkmcnt(2)\n"
"v_accvgpr_write_b32 a88,v216\n"
"v_accvgpr_write_b32 a89,v217\n"
"v_mfma_f32_16x16x32_bf16 v[184:187],a[240:243],v[40:43],v[184:187]\n"
"v_accvgpr_write_b32 a90,v218\n"
"v_accvgpr_write_b32 a91,v219\n"
"v_mfma_f32_16x16x32_bf16 v[184:187],a[244:247],v[44:47],v[184:187]\n"
"buffer_load_dwordx4 a[184:187],v8,s[60:63],s59 offen offset:2048 sc1\n"
"v_mfma_f32_16x16x32_bf16 v[248:251],a[240:243],v[48:51],v[248:251]\n"
"v_accvgpr_write_b32 a28,v156\n"
"v_accvgpr_write_b32 a29,v157\n"
"v_mfma_f32_16x16x32_bf16 v[248:251],a[244:247],v[52:55],v[248:251]\n"
"s_waitcnt lgkmcnt(0)\n"
"v_accvgpr_write_b32 a30,v158\n"
"v_accvgpr_write_b32 a31,v159\n"
"v_mfma_f32_16x16x32_bf16 v[188:191],a[248:251],v[40:43],v[188:191]\n"
"v_accvgpr_write_b32 a92,v220\n"
"v_accvgpr_write_b32 a93,v221\n"
"v_mfma_f32_16x16x32_bf16 v[188:191],a[252:255],v[44:47],v[188:191]\n"
"buffer_load_dwordx4 a[188:191],v8,s[60:63],s59 offen offset:3072 sc1\n"
"v_mfma_f32_16x16x32_bf16 v[252:255],a[248:251],v[48:51],v[252:255]\n"
"v_accvgpr_write_b32 a94,v222\n"
"v_accvgpr_write_b32 a95,v223\n"
"v_mfma_f32_16x16x32_bf16 v[252:255],a[252:255],v[52:55],v[252:255]\n"
"v_accvgpr_write_b32 a32,v160\n"
"v_accvgpr_write_b32 a33,v161\n"
"v_accvgpr_write_b32 a34,v162\n"
"v_accvgpr_write_b32 a35,v163\n"
"v_accvgpr_write_b32 a96,v224\n"
"v_accvgpr_write_b32 a97,v225\n"
"v_accvgpr_write_b32 a98,v226\n"
"v_accvgpr_write_b32 a99,v227\n"
"v_accvgpr_write_b32 a36,v164\n"
"v_accvgpr_write_b32 a37,v165\n"
"v_accvgpr_write_b32 a38,v166\n"
"v_accvgpr_write_b32 a39,v167\n"
"v_accvgpr_write_b32 a100,v228\n"
"v_accvgpr_write_b32 a101,v229\n"
"v_accvgpr_write_b32 a102,v230\n"
"v_accvgpr_write_b32 a103,v231\n"
"v_accvgpr_write_b32 a40,v168\n"
"v_accvgpr_write_b32 a41,v169\n"
"v_accvgpr_write_b32 a42,v170\n"
"v_accvgpr_write_b32 a43,v171\n"
"v_accvgpr_write_b32 a104,v232\n"
"v_accvgpr_write_b32 a105,v233\n"
"v_accvgpr_write_b32 a106,v234\n"
"v_accvgpr_write_b32 a107,v235\n"
"v_accvgpr_write_b32 a44,v172\n"
"v_accvgpr_write_b32 a45,v173\n"
"v_accvgpr_write_b32 a46,v174\n"
"v_accvgpr_write_b32 a47,v175\n"
"v_accvgpr_write_b32 a108,v236\n"
"v_accvgpr_write_b32 a109,v237\n"
"v_accvgpr_write_b32 a110,v238\n"
"v_accvgpr_write_b32 a111,v239\n"
"v_accvgpr_write_b32 a48,v176\n"
"v_accvgpr_write_b32 a49,v177\n"
"v_accvgpr_write_b32 a50,v178\n"
"v_accvgpr_write_b32 a51,v179\n"
"v_accvgpr_write_b32 a112,v240\n"
"v_accvgpr_write_b32 a113,v241\n"
"v_accvgpr_write_b32 a114,v242\n"
"v_accvgpr_write_b32 a115,v243\n"
"v_accvgpr_write_b32 a52,v180\n"
"v_accvgpr_write_b32 a53,v181\n"
"v_accvgpr_write_b32 a54,v182\n"
"v_accvgpr_write_b32 a55,v183\n"
"v_accvgpr_write_b32 a116,v244\n"
"v_accvgpr_write_b32 a117,v245\n"
"v_accvgpr_write_b32 a118,v246\n"
"v_accvgpr_write_b32 a119,v247\n"
"v_accvgpr_write_b32 a56,v184\n"
"v_accvgpr_write_b32 a57,v185\n"
"v_accvgpr_write_b32 a58,v186\n"
"v_accvgpr_write_b32 a59,v187\n"
"v_accvgpr_write_b32 a120,v248\n"
"v_accvgpr_write_b32 a121,v249\n"
"v_accvgpr_write_b32 a122,v250\n"
"v_accvgpr_write_b32 a123,v251\n"
"v_accvgpr_write_b32 a60,v188\n"
"v_accvgpr_write_b32 a61,v189\n"
"v_accvgpr_write_b32 a62,v190\n"
"v_accvgpr_write_b32 a63,v191\n"
"v_accvgpr_write_b32 a124,v252\n"
"v_accvgpr_write_b32 a125,v253\n"
"v_accvgpr_write_b32 a126,v254\n"
"v_accvgpr_write_b32 a127,v255\n"
"v_mov_b32 v56,v12\n"
"v_mov_b32 v57,v13\n"
"s_nop 0\n"
"v_permlane32_swap_b32 v12,v56\n"
"v_permlane32_swap_b32 v13,v57\n"
"v_add_f32 v12,v12,v56\n"
"v_add_f32 v13,v13,v57\n"
"v_mov_b32 v56,v12\n"
"v_mov_b32 v57,v13\n"
"s_nop 0\n"
"v_permlane16_swap_b32 v12,v56\n"
"v_permlane16_swap_b32 v13,v57\n"
"v_add_f32 v12,v12,v56\n"
"v_add_f32 v13,v13,v57\n"
"s_lshl_b32 s79,s93,1\n"
"s_sub_u32 s79,13,s79\n"
"s_sub_u32 s50,s79,1\n"
"s_mov_b32 s56,3\n"
"s_mov_b32 s55,s78\n"
"s_add_u32 s49,s78,0x138000\n"
"s_add_u32 s48,s78,0x150000\n"
"s_lshl_b32 s53,s93,7\n"
"s_lshl_b32 s59,s11,5\n"
"s_sub_u32 s53,s53,s59\n"
"s_add_u32 s53,s53,-704\n"
"v_mov_b32 v60,v12\n"
"v_mov_b32 v61,v13\n"
"v_mov_b32 v12,0\n"
"v_mov_b32 v13,0\n"
"v_mov_b32 v14,0\n"
"v_mov_b32 v15,0\n"
"v_mov_b32 v16,0\n"
"v_mov_b32 v17,0\n"
"v_mov_b32 v18,0\n"
"v_mov_b32 v19,0\n"
"v_mov_b32 v20,0\n"
"v_mov_b32 v21,0\n"
"v_mov_b32 v22,0\n"
"v_mov_b32 v23,0\n"
"s_waitcnt vmcnt(14)\n"
"s_barrier\n"
"ds_read_b128 a[192:195],v9 offset:32768\n"
"ds_read_b128 a[196:199],v9 offset:33792\n"
"ds_read_b128 a[200:203],v9 offset:34816\n"
"ds_read_b128 a[204:207],v9 offset:35840\n"
"ds_read_b128 a[208:211],v9 offset:49152\n"
"ds_read_b128 a[212:215],v9 offset:50176\n"
"ds_read_b128 a[216:219],v9 offset:51200\n"
"ds_read_b128 a[220:223],v9 offset:52224\n"
"ds_read_b128 a[224:227],v9 offset:36864\n"
"ds_read_b128 a[228:231],v9 offset:37888\n"
"s_waitcnt vmcnt(0) lgkmcnt(6)\n"
"v_mfma_f32_16x16x32_bf16 v[96:99],a[192:195],a[128:131],v[16:19]\n"
"ds_read_b128 a[232:235],v9 offset:38912\n"
"v_mov_b32 v128,0\n"
"v_mfma_f32_16x16x32_bf16 v[96:99],a[196:199],a[132:135],v[96:99]\n"
"v_mov_b32 v129,0\n"
"v_mov_b32 v130,0\n"
"v_mfma_f32_16x16x32_bf16 v[96:99],a[200:203],a[136:139],v[96:99]\n"
"s_add_u32 m0,s57,0x8000\n"
"ds_read_b128 a[236:239],v9 offset:39936\n"
"v_mov_b32 v131,0\n"
"v_mfma_f32_16x16x32_bf16 v[96:99],a[204:207],a[140:143],v[96:99]\n"
"s_waitcnt lgkmcnt(4)\n"
"buffer_load_dwordx4 v10,s[36:39],s49 offen offset:0 sc1 lds\n"
"v_mfma_f32_16x16x32_bf16 v[96:99],a[208:211],a[144:147],v[96:99]\n"
"ds_read_b128 a[240:243],v9 offset:53248\n"
"v_mov_b32 v132,0\n"
"v_mfma_f32_16x16x32_bf16 v[96:99],a[212:215],a[148:151],v[96:99]\n"
"v_mov_b32 v133,0\n"
"v_mov_b32 v134,0\n"
"v_mfma_f32_16x16x32_bf16 v[96:99],a[216:219],a[152:155],v[96:99]\n"
"ds_read_b128 a[244:247],v9 offset:54272\n"
"v_mov_b32 v135,0\n"
"v_mfma_f32_16x16x32_bf16 v[96:99],a[220:223],a[156:159],v[96:99]\n"
"v_mov_b32 v136,0\n"
"v_mov_b32 v137,0\n"
"v_mfma_f32_16x16x32_bf16 v[112:115],a[192:195],a[160:163],v[20:23]\n"
"ds_read_b128 a[248:251],v9 offset:55296\n"
"v_mov_b32 v138,0\n"
"v_mfma_f32_16x16x32_bf16 v[112:115],a[196:199],a[164:167],v[112:115]\n"
"v_mov_b32 v139,0\n"
"v_mov_b32 v140,0\n"
"v_mfma_f32_16x16x32_bf16 v[112:115],a[200:203],a[168:171],v[112:115]\n"
"ds_read_b128 a[252:255],v9 offset:56320\n"
"v_mov_b32 v141,0\n"
"v_mfma_f32_16x16x32_bf16 v[112:115],a[204:207],a[172:175],v[112:115]\n"
"buffer_load_dwordx4 v10,s[36:39],s49 offen offset:1024 sc1 lds\n"
"v_mfma_f32_16x16x32_bf16 v[112:115],a[208:211],a[176:179],v[112:115]\n"
"ds_read_b128 a[192:195],v9 offset:40960\n"
"v_mov_b32 v142,0\n"
"v_mfma_f32_16x16x32_bf16 v[112:115],a[212:215],a[180:183],v[112:115]\n"
"v_mov_b32 v143,0\n"
"v_mov_b32 v144,0\n"
"v_mfma_f32_16x16x32_bf16 v[112:115],a[216:219],a[184:187],v[112:115]\n"
"ds_read_b128 a[196:199],v9 offset:41984\n"
"v_mov_b32 v145,0\n"
"v_mfma_f32_16x16x32_bf16 v[112:115],a[220:223],a[188:191],v[112:115]\n"
"s_waitcnt lgkmcnt(6)\n"
"v_mov_b32 v146,0\n"
"v_mov_b32 v147,0\n"
"v_mfma_f32_16x16x32_bf16 v[100:103],a[224:227],a[128:131],v[16:19]\n"
"ds_read_b128 a[200:203],v9 offset:43008\n"
"v_mov_b32 v148,0\n"
"v_mfma_f32_16x16x32_bf16 v[100:103],a[228:231],a[132:135],v[100:103]\n"
"v_mov_b32 v149,0\n"
"v_mov_b32 v150,0\n"
"v_mfma_f32_16x16x32_bf16 v[100:103],a[232:235],a[136:139],v[100:103]\n"
"ds_read_b128 a[204:207],v9 offset:44032\n"
"v_mov_b32 v151,0\n"
"v_mfma_f32_16x16x32_bf16 v[100:103],a[236:239],a[140:143],v[100:103]\n"
"s_waitcnt lgkmcnt(4)\n"
"buffer_load_dwordx4 v10,s[36:39],s49 offen offset:2048 sc1 lds\n"
"v_mfma_f32_16x16x32_bf16 v[100:103],a[240:243],a[144:147],v[100:103]\n"
"ds_read_b128 a[208:211],v9 offset:57344\n"
"v_mov_b32 v152,0\n"
"v_mfma_f32_16x16x32_bf16 v[100:103],a[244:247],a[148:151],v[100:103]\n"
"v_mov_b32 v153,0\n"
"v_mov_b32 v154,0\n"
"v_mfma_f32_16x16x32_bf16 v[100:103],a[248:251],a[152:155],v[100:103]\n"
"ds_read_b128 a[212:215],v9 offset:58368\n"
"v_mov_b32 v155,0\n"
"v_mfma_f32_16x16x32_bf16 v[100:103],a[252:255],a[156:159],v[100:103]\n"
"v_mov_b32 v156,0\n"
"v_mov_b32 v157,0\n"
"v_mfma_f32_16x16x32_bf16 v[116:119],a[224:227],a[160:163],v[20:23]\n"
"ds_read_b128 a[216:219],v9 offset:59392\n"
"v_mov_b32 v158,0\n"
"v_mfma_f32_16x16x32_bf16 v[116:119],a[228:231],a[164:167],v[116:119]\n"
"v_mov_b32 v159,0\n"
"v_mov_b32 v160,0\n"
"v_mfma_f32_16x16x32_bf16 v[116:119],a[232:235],a[168:171],v[116:119]\n"
"ds_read_b128 a[220:223],v9 offset:60416\n"
"v_mov_b32 v161,0\n"
"v_mfma_f32_16x16x32_bf16 v[116:119],a[236:239],a[172:175],v[116:119]\n"
"buffer_load_dwordx4 v10,s[36:39],s49 offen offset:3072 sc1 lds\n"
"v_mfma_f32_16x16x32_bf16 v[116:119],a[240:243],a[176:179],v[116:119]\n"
"ds_read_b128 a[224:227],v9 offset:45056\n"
"v_mov_b32 v162,0\n"
"v_mfma_f32_16x16x32_bf16 v[116:119],a[244:247],a[180:183],v[116:119]\n"
"v_mov_b32 v163,0\n"
"v_mov_b32 v164,0\n"
"v_mfma_f32_16x16x32_bf16 v[116:119],a[248:251],a[184:187],v[116:119]\n"
"ds_read_b128 a[228:231],v9 offset:46080\n"
"v_mov_b32 v165,0\n"
"v_mfma_f32_16x16x32_bf16 v[116:119],a[252:255],a[188:191],v[116:119]\n"
"s_waitcnt lgkmcnt(6)\n"
"v_mov_b32 v166,0\n"
"v_mov_b32 v167,0\n"
"v_mfma_f32_16x16x32_bf16 v[104:107],a[192:195],a[128:131],v[16:19]\n"
"ds_read_b128 a[232:235],v9 offset:47104\n"
"v_mov_b32 v168,0\n"
"v_mfma_f32_16x16x32_bf16 v[104:107],a[196:199],a[132:135],v[104:107]\n"
"v_mov_b32 v169,0\n"
"v_mov_b32 v170,0\n"
"v_mfma_f32_16x16x32_bf16 v[104:107],a[200:203],a[136:139],v[104:107]\n"
"s_add_u32 m0,m0,0x1000\n"
"ds_read_b128 a[236:239],v9 offset:48128\n"
"v_mov_b32 v171,0\n"
"v_mfma_f32_16x16x32_bf16 v[104:107],a[204:207],a[140:143],v[104:107]\n"
"s_waitcnt lgkmcnt(4)\n"
"buffer_load_dwordx4 v7,s[36:39],s49 offen offset:0 sc1 lds\n"
"v_mfma_f32_16x16x32_bf16 v[104:107],a[208:211],a[144:147],v[104:107]\n"
"ds_read_b128 a[240:243],v9 offset:61440\n"
"v_mov_b32 v172,0\n"
"v_mfma_f32_16x16x32_bf16 v[104:107],a[212:215],a[148:151],v[104:107]\n"
"v_mov_b32 v173,0\n"
"v_mov_b32 v174,0\n"
"v_mfma_f32_16x16x32_bf16 v[104:107],a[216:219],a[152:155],v[104:107]\n"
"ds_read_b128 a[244:247],v9 offset:62464\n"
"v_mov_b32 v175,0\n"
"v_mfma_f32_16x16x32_bf16 v[104:107],a[220:223],a[156:159],v[104:107]\n"
"v_mov_b32 v176,0\n"
"v_mov_b32 v177,0\n"
"v_mfma_f32_16x16x32_bf16 v[120:123],a[192:195],a[160:163],v[20:23]\n"
"ds_read_b128 a[248:251],v9 offset:63488\n"
"v_mov_b32 v178,0\n"
"v_mfma_f32_16x16x32_bf16 v[120:123],a[196:199],a[164:167],v[120:123]\n"
"v_mov_b32 v179,0\n"
"v_mov_b32 v180,0\n"
"v_mfma_f32_16x16x32_bf16 v[120:123],a[200:203],a[168:171],v[120:123]\n"
"s_waitcnt lgkmcnt(3)\n"
"ds_read_b128 a[252:255],v9 offset:64512\n"
"v_mov_b32 v181,0\n"
"v_mfma_f32_16x16x32_bf16 v[120:123],a[204:207],a[172:175],v[120:123]\n"
"buffer_load_dwordx4 v7,s[36:39],s49 offen offset:1024 sc1 lds\n"
"v_mfma_f32_16x16x32_bf16 v[120:123],a[208:211],a[176:179],v[120:123]\n"
"v_mov_b32 v182,0\n"
"v_mov_b32 v183,0\n"
"v_mfma_f32_16x16x32_bf16 v[120:123],a[212:215],a[180:183],v[120:123]\n"
"v_mov_b32 v184,0\n"
"v_mov_b32 v185,0\n"
"v_mfma_f32_16x16x32_bf16 v[120:123],a[216:219],a[184:187],v[120:123]\n"
"v_mov_b32 v186,0\n"
"v_mov_b32 v187,0\n"
"v_mfma_f32_16x16x32_bf16 v[120:123],a[220:223],a[188:191],v[120:123]\n"
"v_mov_b32 v188,0\n"
"v_mov_b32 v189,0\n"
"v_mfma_f32_16x16x32_bf16 v[108:111],a[224:227],a[128:131],v[16:19]\n"
"v_mov_b32 v190,0\n"
"v_mov_b32 v191,0\n"
"v_mfma_f32_16x16x32_bf16 v[108:111],a[228:231],a[132:135],v[108:111]\n"
"v_mov_b32 v192,0\n"
"v_mov_b32 v193,0\n"
"v_mfma_f32_16x16x32_bf16 v[108:111],a[232:235],a[136:139],v[108:111]\n"
"v_mov_b32 v194,0\n"
"v_mov_b32 v195,0\n"
"v_mfma_f32_16x16x32_bf16 v[108:111],a[236:239],a[140:143],v[108:111]\n"
"s_waitcnt lgkmcnt(0)\n"
"buffer_load_dwordx4 v7,s[36:39],s49 offen offset:2048 sc1 lds\n"
"v_mfma_f32_16x16x32_bf16 v[108:111],a[240:243],a[144:147],v[108:111]\n"
"v_mov_b32 v196,0\n"
"v_mov_b32 v197,0\n"
"v_mfma_f32_16x16x32_bf16 v[108:111],a[244:247],a[148:151],v[108:111]\n"
"v_mov_b32 v198,0\n"
"v_mov_b32 v199,0\n"
"v_mfma_f32_16x16x32_bf16 v[108:111],a[248:251],a[152:155],v[108:111]\n"
"v_mov_b32 v200,0\n"
"v_mov_b32 v201,0\n"
"v_mfma_f32_16x16x32_bf16 v[108:111],a[252:255],a[156:159],v[108:111]\n"
"v_mov_b32 v202,0\n"
"v_mov_b32 v203,0\n"
"v_mfma_f32_16x16x32_bf16 v[124:127],a[224:227],a[160:163],v[20:23]\n"
"v_mov_b32 v204,0\n"
"v_mov_b32 v205,0\n"
"v_mfma_f32_16x16x32_bf16 v[124:127],a[228:231],a[164:167],v[124:127]\n"
"v_mov_b32 v206,0\n"
"v_mov_b32 v207,0\n"
"v_mfma_f32_16x16x32_bf16 v[124:127],a[232:235],a[168:171],v[124:127]\n"
"v_mov_b32 v208,0\n"
"v_mov_b32 v209,0\n"
"v_mfma_f32_16x16x32_bf16 v[124:127],a[236:239],a[172:175],v[124:127]\n"
"buffer_load_dwordx4 v7,s[36:39],s49 offen offset:3072 sc1 lds\n"
"v_mfma_f32_16x16x32_bf16 v[124:127],a[240:243],a[176:179],v[124:127]\n"
"v_mov_b32 v210,0\n"
"v_mov_b32 v211,0\n"
"v_mfma_f32_16x16x32_bf16 v[124:127],a[244:247],a[180:183],v[124:127]\n"
"v_mov_b32 v212,0\n"
"v_mov_b32 v213,0\n"
"v_mfma_f32_16x16x32_bf16 v[124:127],a[248:251],a[184:187],v[124:127]\n"
"v_mov_b32 v214,0\n"
"v_mov_b32 v215,0\n"
"v_mfma_f32_16x16x32_bf16 v[124:127],a[252:255],a[188:191],v[124:127]\n"
"v_mov_b32 v216,0\n"
"v_mov_b32 v217,0\n"
"s_barrier\n"
"s_add_u32 m0,s57,0x18000\n"
"s_nop 0\n"
"buffer_load_dwordx4 v10,s[36:39],s48 offen offset:0 sc1 lds\n"
"v_pk_mov_b32 v[218:219],0,0\n"
"v_pk_mov_b32 v[220:221],0,0\n"
"v_pk_mov_b32 v[222:223],0,0\n"
"v_pk_mov_b32 v[224:225],0,0\n"
"v_pk_mov_b32 v[226:227],0,0\n"
"v_pk_mov_b32 v[228:229],0,0\n"
"v_pk_mov_b32 v[230:231],0,0\n"
"v_pk_mov_b32 v[232:233],0,0\n"
"v_pk_mov_b32 v[234:235],0,0\n"
"v_pk_mov_b32 v[236:237],0,0\n"
"v_pk_mov_b32 v[238:239],0,0\n"
"v_pk_mov_b32 v[240:241],0,0\n"
"v_pk_mov_b32 v[242:243],0,0\n"
"v_pk_mov_b32 v[244:245],0,0\n"
"v_pk_mov_b32 v[246:247],0,0\n"
"v_pk_mov_b32 v[248:249],0,0\n"
"v_pk_mov_b32 v[250:251],0,0\n"
"v_pk_mov_b32 v[252:253],0,0\n"
"v_pk_mov_b32 v[254:255],0,0\n"
"s_branch L_slow_pro1b_%=\n"
"L_slowret_pro1b_%=:\n"
"buffer_load_dwordx4 v10,s[36:39],s48 offen offset:1024 sc1 lds\n"
"v_exp_f32 v96,v96\n"
"v_exp_f32 v97,v97\n"
"v_add_f32 v12,v12,v96\n"
"v_exp_f32 v98,v98\n"
"buffer_load_dwordx4 v10,s[36:39],s48 offen offset:2048 sc1 lds\n"
"v_add_f32 v12,v12,v97\n"
"v_exp_f32 v99,v99\n"
"v_add_f32 v12,v12,v98\n"
"v_exp_f32 v100,v100\n"
"buffer_load_dwordx4 v10,s[36:39],s48 offen offset:3072 sc1 lds\n"
"v_add_f32 v12,v12,v99\n"
"v_exp_f32 v101,v101\n"
"v_add_f32 v12,v12,v100\n"
"v_exp_f32 v102,v102\n"
"s_add_u32 m0,m0,0x1000\n"
"s_nop 0\n"
"buffer_load_dwordx4 v7,s[36:39],s48 offen offset:0 sc1 lds\n"
"v_add_f32 v12,v12,v101\n"
"v_exp_f32 v103,v103\n"
"v_add_f32 v12,v12,v102\n"
"v_exp_f32 v104,v104\n"
"v_add_f32 v12,v12,v103\n"
"v_exp_f32 v105,v105\n"
"v_add_f32 v12,v12,v104\n"
"v_exp_f32 v106,v106\n"
"v_add_f32 v12,v12,v105\n"
"v_exp_f32 v107,v107\n"
"v_add_f32 v12,v12,v106\n"
"v_exp_f32 v108,v108\n"
"v_add_f32 v12,v12,v107\n"
"ds_read_b128 a[192:195],v9 offset:0\n"
"v_exp_f32 v109,v109\n"
"v_add_f32 v12,v12,v108\n"
"ds_read_b128 a[196:199],v9 offset:1024\n"
"v_exp_f32 v110,v110\n"
"v_add_f32 v12,v12,v109\n"
"ds_read_b128 a[200:203],v9 offset:2048\n"
"v_exp_f32 v111,v111\n"
"v_add_f32 v12,v12,v110\n"
"ds_read_b128 a[204:207],v9 offset:3072\n"
"v_exp_f32 v112,v112\n"
"v_add_f32 v12,v12,v111\n"
"ds_read_b128 a[208:211],v9 offset:16384\n"
"v_exp_f32 v113,v113\n"
"v_add_f32 v13,v13,v112\n"
"ds_read_b128 a[212:215],v9 offset:17408\n"
"v_exp_f32 v114,v114\n"
"v_add_f32 v13,v13,v113\n"
"ds_read_b128 a[216:219],v9 offset:18432\n"
"v_exp_f32 v115,v115\n"
"v_add_f32 v13,v13,v114\n"
"ds_read_b128 a[220:223],v9 offset:19456\n"
"v_exp_f32 v116,v116\n"
"v_add_f32 v13,v13,v115\n"
"ds_read_b128 a[224:227],v9 offset:4096\n"
"v_exp_f32 v117,v117\n"
"v_add_f32 v13,v13,v116\n"
"ds_read_b128 a[228:231],v9 offset:5120\n"
"s_add_u32 s49,s49,0x10000\n"
"s_add_u32 s53,s53,64\n"
"s_sub_u32 s50,s50,1\n"
"s_branch L_even_%=\n"
"L_slow_pro0_%=:\n"
"s_cmp_le_i32 s50,s52\n"
"s_cbranch_scc0 L_nomask_pro0_%=\n"
"s_cmp_lt_i32 s53,-32\n"
"s_cbranch_scc1 L_mdone_pro0_%=\n"
"s_cmp_eq_i32 s53,0\n"
"s_cbranch_scc0 L_m2_pro0_%=\n"
"v_cndmask_b32 v64,v64,v27,s[86:87]\n"
"v_cndmask_b32 v65,v65,v27,s[88:89]\n"
"v_cndmask_b32 v66,v66,v27,s[90:91]\n"
"v_cndmask_b32 v67,v67,v27,s[22:23]\n"
"v_mov_b32 v68,v27\n"
"v_mov_b32 v69,v27\n"
"v_mov_b32 v70,v27\n"
"v_mov_b32 v71,v27\n"
"v_cndmask_b32 v84,v84,v27,s[86:87]\n"
"v_cndmask_b32 v85,v85,v27,s[88:89]\n"
"v_cndmask_b32 v86,v86,v27,s[90:91]\n"
"v_cndmask_b32 v87,v87,v27,s[22:23]\n"
"v_mov_b32 v72,v27\n"
"v_mov_b32 v73,v27\n"
"v_mov_b32 v74,v27\n"
"v_mov_b32 v75,v27\n"
"v_mov_b32 v88,v27\n"
"v_mov_b32 v89,v27\n"
"v_mov_b32 v90,v27\n"
"v_mov_b32 v91,v27\n"
"v_mov_b32 v76,v27\n"
"v_mov_b32 v77,v27\n"
"v_mov_b32 v78,v27\n"
"v_mov_b32 v79,v27\n"
"v_mov_b32 v92,v27\n"
"v_mov_b32 v93,v27\n"
"v_mov_b32 v94,v27\n"
"v_mov_b32 v95,v27\n"
"s_branch L_mdone_pro0_%=\n"
"L_m2_pro0_%=:\n"
"s_cmp_eq_i32 s53,-32\n"
"s_cbranch_scc0 L_m3_pro0_%=\n"
"v_cndmask_b32 v72,v72,v27,s[86:87]\n"
"v_cndmask_b32 v73,v73,v27,s[88:89]\n"
"v_cndmask_b32 v74,v74,v27,s[90:91]\n"
"v_cndmask_b32 v75,v75,v27,s[22:23]\n"
"v_mov_b32 v76,v27\n"
"v_mov_b32 v77,v27\n"
"v_mov_b32 v78,v27\n"
"v_mov_b32 v79,v27\n"
"v_cndmask_b32 v92,v92,v27,s[86:87]\n"
"v_cndmask_b32 v93,v93,v27,s[88:89]\n"
"v_cndmask_b32 v94,v94,v27,s[90:91]\n"
"v_cndmask_b32 v95,v95,v27,s[22:23]\n"
"s_branch L_mdone_pro0_%=\n"
"L_m3_pro0_%=:\n"
"v_mov_b32 v64,v27\n"
"v_mov_b32 v65,v27\n"
"v_mov_b32 v66,v27\n"
"v_mov_b32 v67,v27\n"
"v_mov_b32 v80,v27\n"
"v_mov_b32 v81,v27\n"
"v_mov_b32 v82,v27\n"
"v_mov_b32 v83,v27\n"
"v_mov_b32 v68,v27\n"
"v_mov_b32 v69,v27\n"
"v_mov_b32 v70,v27\n"
"v_mov_b32 v71,v27\n"
"v_mov_b32 v84,v27\n"
"v_mov_b32 v85,v27\n"
"v_mov_b32 v86,v27\n"
"v_mov_b32 v87,v27\n"
"v_mov_b32 v72,v27\n"
"v_mov_b32 v73,v27\n"
"v_mov_b32 v74,v27\n"
"v_mov_b32 v75,v27\n"
"v_mov_b32 v88,v27\n"
"v_mov_b32 v89,v27\n"
"v_mov_b32 v90,v27\n"
"v_mov_b32 v91,v27\n"
"v_mov_b32 v76,v27\n"
"v_mov_b32 v77,v27\n"
"v_mov_b32 v78,v27\n"
"v_mov_b32 v79,v27\n"
"v_mov_b32 v92,v27\n"
"v_mov_b32 v93,v27\n"
"v_mov_b32 v94,v27\n"
"v_mov_b32 v95,v27\n"
"L_mdone_pro0_%=:\n"
"L_nomask_pro0_%=:\n"
"v_max3_f32 v56,v64,v65,v66\n"
"v_max3_f32 v58,v80,v81,v82\n"
"v_max3_f32 v56,v56,v67,v68\n"
"v_max3_f32 v58,v58,v83,v84\n"
"v_max3_f32 v56,v56,v69,v70\n"
"v_max3_f32 v58,v58,v85,v86\n"
"v_max3_f32 v56,v56,v71,v72\n"
"v_max3_f32 v58,v58,v87,v88\n"
"v_max3_f32 v56,v56,v73,v74\n"
"v_max3_f32 v58,v58,v89,v90\n"
"v_max3_f32 v56,v56,v75,v76\n"
"v_max3_f32 v58,v58,v91,v92\n"
"v_max3_f32 v56,v56,v77,v78\n"
"v_max3_f32 v58,v58,v93,v94\n"
"v_max_f32 v56,v56,v79\n"
"v_max_f32 v58,v58,v95\n"
"v_mov_b32 v57,v56\n"
"v_mov_b32 v59,v58\n"
"s_nop 0\n"
"v_permlane32_swap_b32 v56,v57\n"
"v_permlane32_swap_b32 v58,v59\n"
"v_max_f32 v56,v56,v57\n"
"v_max_f32 v58,v58,v59\n"
"v_mov_b32 v57,v56\n"
"v_mov_b32 v59,v58\n"
"s_nop 0\n"
"v_permlane16_swap_b32 v56,v57\n"
"v_permlane16_swap_b32 v58,v59\n"
"v_max_f32 v56,v56,v57\n"
"v_max_f32 v58,v58,v59\n"
"v_max_f32 v56,0xf149f2ca,v56\n"
"v_max_f32 v58,0xf149f2ca,v58\n"
"v_add_f32 v14,v14,v56\n"
"v_pk_add_f32 v[64:65],v[64:65],v[56:57] op_sel_hi:[1,0] neg_lo:[0,1] neg_hi:[0,1]\n"
"v_pk_add_f32 v[66:67],v[66:67],v[56:57] op_sel_hi:[1,0] neg_lo:[0,1] neg_hi:[0,1]\n"
"v_pk_add_f32 v[68:69],v[68:69],v[56:57] op_sel_hi:[1,0] neg_lo:[0,1] neg_hi:[0,1]\n"
"v_pk_add_f32 v[70:71],v[70:71],v[56:57] op_sel_hi:[1,0] neg_lo:[0,1] neg_hi:[0,1]\n"
"v_pk_add_f32 v[72:73],v[72:73],v[56:57] op_sel_hi:[1,0] neg_lo:[0,1] neg_hi:[0,1]\n"
"v_pk_add_f32 v[74:75],v[74:75],v[56:57] op_sel_hi:[1,0] neg_lo:[0,1] neg_hi:[0,1]\n"
"v_pk_add_f32 v[76:77],v[76:77],v[56:57] op_sel_hi:[1,0] neg_lo:[0,1] neg_hi:[0,1]\n"
"v_pk_add_f32 v[78:79],v[78:79],v[56:57] op_sel_hi:[1,0] neg_lo:[0,1] neg_hi:[0,1]\n"
"v_sub_f32 v16,0,v14\n"
"v_sub_f32 v17,0,v14\n"
"v_sub_f32 v18,0,v14\n"
"v_sub_f32 v19,0,v14\n"
"v_add_f32 v15,v15,v58\n"
"v_pk_add_f32 v[80:81],v[80:81],v[58:59] op_sel_hi:[1,0] neg_lo:[0,1] neg_hi:[0,1]\n"
"v_pk_add_f32 v[82:83],v[82:83],v[58:59] op_sel_hi:[1,0] neg_lo:[0,1] neg_hi:[0,1]\n"
"v_pk_add_f32 v[84:85],v[84:85],v[58:59] op_sel_hi:[1,0] neg_lo:[0,1] neg_hi:[0,1]\n"
"v_pk_add_f32 v[86:87],v[86:87],v[58:59] op_sel_hi:[1,0] neg_lo:[0,1] neg_hi:[0,1]\n"
"v_pk_add_f32 v[88:89],v[88:89],v[58:59] op_sel_hi:[1,0] neg_lo:[0,1] neg_hi:[0,1]\n"
"v_pk_add_f32 v[90:91],v[90:91],v[58:59] op_sel_hi:[1,0] neg_lo:[0,1] neg_hi:[0,1]\n"
"v_pk_add_f32 v[92:93],v[92:93],v[58:59] op_sel_hi:[1,0] neg_lo:[0,1] neg_hi:[0,1]\n"
"v_pk_add_f32 v[94:95],v[94:95],v[58:59] op_sel_hi:[1,0] neg_lo:[0,1] neg_hi:[0,1]\n"
"v_sub_f32 v20,0,v15\n"
"v_sub_f32 v21,0,v15\n"
"v_sub_f32 v22,0,v15\n"
"v_sub_f32 v23,0,v15\n"
"s_branch L_slowret_pro0_%=\n"
"L_slow_pro1a_%=:\n"
"s_cmp_le_i32 s50,s52\n"
"s_cbranch_scc0 L_nomask_pro1a_%=\n"
"s_cmp_lt_i32 s53,-32\n"
"s_cbranch_scc1 L_mdone_pro1a_%=\n"
"s_cmp_eq_i32 s53,0\n"
"s_cbranch_scc0 L_m2_pro1a_%=\n"
"v_cndmask_b32 v96,v96,v27,s[86:87]\n"
"v_cndmask_b32 v97,v97,v27,s[88:89]\n"
"v_cndmask_b32 v98,v98,v27,s[90:91]\n"
"v_cndmask_b32 v99,v99,v27,s[22:23]\n"
"v_mov_b32 v100,v27\n"
"v_mov_b32 v101,v27\n"
"v_mov_b32 v102,v27\n"
"v_mov_b32 v103,v27\n"
"v_cndmask_b32 v116,v116,v27,s[86:87]\n"
"v_cndmask_b32 v117,v117,v27,s[88:89]\n"
"v_cndmask_b32 v118,v118,v27,s[90:91]\n"
"v_cndmask_b32 v119,v119,v27,s[22:23]\n"
"v_mov_b32 v104,v27\n"
"v_mov_b32 v105,v27\n"
"v_mov_b32 v106,v27\n"
"v_mov_b32 v107,v27\n"
"v_mov_b32 v120,v27\n"
"v_mov_b32 v121,v27\n"
"v_mov_b32 v122,v27\n"
"v_mov_b32 v123,v27\n"
"v_mov_b32 v108,v27\n"
"v_mov_b32 v109,v27\n"
"v_mov_b32 v110,v27\n"
"v_mov_b32 v111,v27\n"
"v_mov_b32 v124,v27\n"
"v_mov_b32 v125,v27\n"
"v_mov_b32 v126,v27\n"
"v_mov_b32 v127,v27\n"
"s_branch L_mdone_pro1a_%=\n"
"L_m2_pro1a_%=:\n"
"s_cmp_eq_i32 s53,-32\n"
"s_cbranch_scc0 L_m3_pro1a_%=\n"
"v_cndmask_b32 v104,v104,v27,s[86:87]\n"
"v_cndmask_b32 v105,v105,v27,s[88:89]\n"
"v_cndmask_b32 v106,v106,v27,s[90:91]\n"
"v_cndmask_b32 v107,v107,v27,s[22:23]\n"
"v_mov_b32 v108,v27\n"
"v_mov_b32 v109,v27\n"
"v_mov_b32 v110,v27\n"
"v_mov_b32 v111,v27\n"
"v_cndmask_b32 v124,v124,v27,s[86:87]\n"
"v_cndmask_b32 v125,v125,v27,s[88:89]\n"
"v_cndmask_b32 v126,v126,v27,s[90:91]\n"
"v_cndmask_b32 v127,v127,v27,s[22:23]\n"
"s_branch L_mdone_pro1a_%=\n"
"L_m3_pro1a_%=:\n"
"v_mov_b32 v96,v27\n"
"v_mov_b32 v97,v27\n"
"v_mov_b32 v98,v27\n"
"v_mov_b32 v99,v27\n"
"v_mov_b32 v112,v27\n"
"v_mov_b32 v113,v27\n"
"v_mov_b32 v114,v27\n"
"v_mov_b32 v115,v27\n"
"v_mov_b32 v100,v27\n"
"v_mov_b32 v101,v27\n"
"v_mov_b32 v102,v27\n"
"v_mov_b32 v103,v27\n"
"v_mov_b32 v116,v27\n"
"v_mov_b32 v117,v27\n"
"v_mov_b32 v118,v27\n"
"v_mov_b32 v119,v27\n"
"v_mov_b32 v104,v27\n"
"v_mov_b32 v105,v27\n"
"v_mov_b32 v106,v27\n"
"v_mov_b32 v107,v27\n"
"v_mov_b32 v120,v27\n"
"v_mov_b32 v121,v27\n"
"v_mov_b32 v122,v27\n"
"v_mov_b32 v123,v27\n"
"v_mov_b32 v108,v27\n"
"v_mov_b32 v109,v27\n"
"v_mov_b32 v110,v27\n"
"v_mov_b32 v111,v27\n"
"v_mov_b32 v124,v27\n"
"v_mov_b32 v125,v27\n"
"v_mov_b32 v126,v27\n"
"v_mov_b32 v127,v27\n"
"L_mdone_pro1a_%=:\n"
"L_nomask_pro1a_%=:\n"
"v_max3_f32 v56,v96,v97,v98\n"
"v_max3_f32 v58,v112,v113,v114\n"
"v_max3_f32 v56,v56,v99,v100\n"
"v_max3_f32 v58,v58,v115,v116\n"
"v_max3_f32 v56,v56,v101,v102\n"
"v_max3_f32 v58,v58,v117,v118\n"
"v_max3_f32 v56,v56,v103,v104\n"
"v_max3_f32 v58,v58,v119,v120\n"
"v_max3_f32 v56,v56,v105,v106\n"
"v_max3_f32 v58,v58,v121,v122\n"
"v_max3_f32 v56,v56,v107,v108\n"
"v_max3_f32 v58,v58,v123,v124\n"
"v_max3_f32 v56,v56,v109,v110\n"
"v_max3_f32 v58,v58,v125,v126\n"
"v_max_f32 v56,v56,v111\n"
"v_max_f32 v58,v58,v127\n"
"v_mov_b32 v57,v56\n"
"v_mov_b32 v59,v58\n"
"s_nop 0\n"
"v_permlane32_swap_b32 v56,v57\n"
"v_permlane32_swap_b32 v58,v59\n"
"v_max_f32 v56,v56,v57\n"
"v_max_f32 v58,v58,v59\n"
"v_mov_b32 v57,v56\n"
"v_mov_b32 v59,v58\n"
"s_nop 0\n"
"v_permlane16_swap_b32 v56,v57\n"
"v_permlane16_swap_b32 v58,v59\n"
"v_max_f32 v56,v56,v57\n"
"v_max_f32 v58,v58,v59\n"
"v_max_f32 v56,0xf149f2ca,v56\n"
"v_max_f32 v58,0xf149f2ca,v58\n"
"v_add_f32 v14,v14,v56\n"
"v_pk_add_f32 v[96:97],v[96:97],v[56:57] op_sel_hi:[1,0] neg_lo:[0,1] neg_hi:[0,1]\n"
"v_pk_add_f32 v[98:99],v[98:99],v[56:57] op_sel_hi:[1,0] neg_lo:[0,1] neg_hi:[0,1]\n"
"v_pk_add_f32 v[100:101],v[100:101],v[56:57] op_sel_hi:[1,0] neg_lo:[0,1] neg_hi:[0,1]\n"
"v_pk_add_f32 v[102:103],v[102:103],v[56:57] op_sel_hi:[1,0] neg_lo:[0,1] neg_hi:[0,1]\n"
"v_pk_add_f32 v[104:105],v[104:105],v[56:57] op_sel_hi:[1,0] neg_lo:[0,1] neg_hi:[0,1]\n"
"v_pk_add_f32 v[106:107],v[106:107],v[56:57] op_sel_hi:[1,0] neg_lo:[0,1] neg_hi:[0,1]\n"
"v_pk_add_f32 v[108:109],v[108:109],v[56:57] op_sel_hi:[1,0] neg_lo:[0,1] neg_hi:[0,1]\n"
"v_pk_add_f32 v[110:111],v[110:111],v[56:57] op_sel_hi:[1,0] neg_lo:[0,1] neg_hi:[0,1]\n"
"v_sub_f32 v16,0,v14\n"
"v_sub_f32 v17,0,v14\n"
"v_sub_f32 v18,0,v14\n"
"v_sub_f32 v19,0,v14\n"
"v_add_f32 v15,v15,v58\n"
"v_pk_add_f32 v[112:113],v[112:113],v[58:59] op_sel_hi:[1,0] neg_lo:[0,1] neg_hi:[0,1]\n"
"v_pk_add_f32 v[114:115],v[114:115],v[58:59] op_sel_hi:[1,0] neg_lo:[0,1] neg_hi:[0,1]\n"
"v_pk_add_f32 v[116:117],v[116:117],v[58:59] op_sel_hi:[1,0] neg_lo:[0,1] neg_hi:[0,1]\n"
"v_pk_add_f32 v[118:119],v[118:119],v[58:59] op_sel_hi:[1,0] neg_lo:[0,1] neg_hi:[0,1]\n"
"v_pk_add_f32 v[120:121],v[120:121],v[58:59] op_sel_hi:[1,0] neg_lo:[0,1] neg_hi:[0,1]\n"
"v_pk_add_f32 v[122:123],v[122:123],v[58:59] op_sel_hi:[1,0] neg_lo:[0,1] neg_hi:[0,1]\n"
"v_pk_add_f32 v[124:125],v[124:125],v[58:59] op_sel_hi:[1,0] neg_lo:[0,1] neg_hi:[0,1]\n"
"v_pk_add_f32 v[126:127],v[126:127],v[58:59] op_sel_hi:[1,0] neg_lo:[0,1] neg_hi:[0,1]\n"
"v_sub_f32 v20,0,v15\n"
"v_sub_f32 v21,0,v15\n"
"v_sub_f32 v22,0,v15\n"
"v_sub_f32 v23,0,v15\n"
"s_branch L_slowret_pro1a_%=\n"
"L_slow_pro1b_%=:\n"
"s_cmp_le_i32 s50,s52\n"
"s_cbranch_scc0 L_nomask_pro1b_%=\n"
"s_cmp_lt_i32 s53,-32\n"
"s_cbranch_scc1 L_mdone_pro1b_%=\n"
"s_cmp_eq_i32 s53,0\n"
"s_cbranch_scc0 L_m2_pro1b_%=\n"
"v_cndmask_b32 v96,v96,v27,s[86:87]\n"
"v_cndmask_b32 v97,v97,v27,s[88:89]\n"
"v_cndmask_b32 v98,v98,v27,s[90:91]\n"
"v_cndmask_b32 v99,v99,v27,s[22:23]\n"
"v_mov_b32 v100,v27\n"
"v_mov_b32 v101,v27\n"
"v_mov_b32 v102,v27\n"
"v_mov_b32 v103,v27\n"
"v_cndmask_b32 v116,v116,v27,s[86:87]\n"
"v_cndmask_b32 v117,v117,v27,s[88:89]\n"
"v_cndmask_b32 v118,v118,v27,s[90:91]\n"
"v_cndmask_b32 v119,v119,v27,s[22:23]\n"
"v_mov_b32 v104,v27\n"
"v_mov_b32 v105,v27\n"
"v_mov_b32 v106,v27\n"
"v_mov_b32 v107,v27\n"
"v_mov_b32 v120,v27\n"
"v_mov_b32 v121,v27\n"
"v_mov_b32 v122,v27\n"
"v_mov_b32 v123,v27\n"
"v_mov_b32 v108,v27\n"
"v_mov_b32 v109,v27\n"
"v_mov_b32 v110,v27\n"
"v_mov_b32 v111,v27\n"
"v_mov_b32 v124,v27\n"
"v_mov_b32 v125,v27\n"
"v_mov_b32 v126,v27\n"
"v_mov_b32 v127,v27\n"
"s_branch L_mdone_pro1b_%=\n"
"L_m2_pro1b_%=:\n"
"s_cmp_eq_i32 s53,-32\n"
"s_cbranch_scc0 L_m3_pro1b_%=\n"
"v_cndmask_b32 v104,v104,v27,s[86:87]\n"
"v_cndmask_b32 v105,v105,v27,s[88:89]\n"
"v_cndmask_b32 v106,v106,v27,s[90:91]\n"
"v_cndmask_b32 v107,v107,v27,s[22:23]\n"
"v_mov_b32 v108,v27\n"
"v_mov_b32 v109,v27\n"
"v_mov_b32 v110,v27\n"
"v_mov_b32 v111,v27\n"
"v_cndmask_b32 v124,v124,v27,s[86:87]\n"
"v_cndmask_b32 v125,v125,v27,s[88:89]\n"
"v_cndmask_b32 v126,v126,v27,s[90:91]\n"
"v_cndmask_b32 v127,v127,v27,s[22:23]\n"
"s_branch L_mdone_pro1b_%=\n"
"L_m3_pro1b_%=:\n"
"v_mov_b32 v96,v27\n"
"v_mov_b32 v97,v27\n"
"v_mov_b32 v98,v27\n"
"v_mov_b32 v99,v27\n"
"v_mov_b32 v112,v27\n"
"v_mov_b32 v113,v27\n"
"v_mov_b32 v114,v27\n"
"v_mov_b32 v115,v27\n"
"v_mov_b32 v100,v27\n"
"v_mov_b32 v101,v27\n"
"v_mov_b32 v102,v27\n"
"v_mov_b32 v103,v27\n"
"v_mov_b32 v116,v27\n"
"v_mov_b32 v117,v27\n"
"v_mov_b32 v118,v27\n"
"v_mov_b32 v119,v27\n"
"v_mov_b32 v104,v27\n"
"v_mov_b32 v105,v27\n"
"v_mov_b32 v106,v27\n"
"v_mov_b32 v107,v27\n"
"v_mov_b32 v120,v27\n"
"v_mov_b32 v121,v27\n"
"v_mov_b32 v122,v27\n"
"v_mov_b32 v123,v27\n"
"v_mov_b32 v108,v27\n"
"v_mov_b32 v109,v27\n"
"v_mov_b32 v110,v27\n"
"v_mov_b32 v111,v27\n"
"v_mov_b32 v124,v27\n"
"v_mov_b32 v125,v27\n"
"v_mov_b32 v126,v27\n"
"v_mov_b32 v127,v27\n"
"L_mdone_pro1b_%=:\n"
"L_nomask_pro1b_%=:\n"
"v_max3_f32 v56,v96,v97,v98\n"
"v_max3_f32 v58,v112,v113,v114\n"
"v_max3_f32 v56,v56,v99,v100\n"
"v_max3_f32 v58,v58,v115,v116\n"
"v_max3_f32 v56,v56,v101,v102\n"
"v_max3_f32 v58,v58,v117,v118\n"
"v_max3_f32 v56,v56,v103,v104\n"
"v_max3_f32 v58,v58,v119,v120\n"
"v_max3_f32 v56,v56,v105,v106\n"
"v_max3_f32 v58,v58,v121,v122\n"
"v_max3_f32 v56,v56,v107,v108\n"
"v_max3_f32 v58,v58,v123,v124\n"
"v_max3_f32 v56,v56,v109,v110\n"
"v_max3_f32 v58,v58,v125,v126\n"
"v_max_f32 v56,v56,v111\n"
"v_max_f32 v58,v58,v127\n"
"v_mov_b32 v57,v56\n"
"v_mov_b32 v59,v58\n"
"s_nop 0\n"
"v_permlane32_swap_b32 v56,v57\n"
"v_permlane32_swap_b32 v58,v59\n"
"v_max_f32 v56,v56,v57\n"
"v_max_f32 v58,v58,v59\n"
"v_mov_b32 v57,v56\n"
"v_mov_b32 v59,v58\n"
"s_nop 0\n"
"v_permlane16_swap_b32 v56,v57\n"
"v_permlane16_swap_b32 v58,v59\n"
"v_max_f32 v56,v56,v57\n"
"v_max_f32 v58,v58,v59\n"
"v_max_f32 v56,0xf149f2ca,v56\n"
"v_max_f32 v58,0xf149f2ca,v58\n"
"v_add_f32 v14,v14,v56\n"
"v_pk_add_f32 v[96:97],v[96:97],v[56:57] op_sel_hi:[1,0] neg_lo:[0,1] neg_hi:[0,1]\n"
"v_pk_add_f32 v[98:99],v[98:99],v[56:57] op_sel_hi:[1,0] neg_lo:[0,1] neg_hi:[0,1]\n"
"v_pk_add_f32 v[100:101],v[100:101],v[56:57] op_sel_hi:[1,0] neg_lo:[0,1] neg_hi:[0,1]\n"
"v_pk_add_f32 v[102:103],v[102:103],v[56:57] op_sel_hi:[1,0] neg_lo:[0,1] neg_hi:[0,1]\n"
"v_pk_add_f32 v[104:105],v[104:105],v[56:57] op_sel_hi:[1,0] neg_lo:[0,1] neg_hi:[0,1]\n"
"v_pk_add_f32 v[106:107],v[106:107],v[56:57] op_sel_hi:[1,0] neg_lo:[0,1] neg_hi:[0,1]\n"
"v_pk_add_f32 v[108:109],v[108:109],v[56:57] op_sel_hi:[1,0] neg_lo:[0,1] neg_hi:[0,1]\n"
"v_pk_add_f32 v[110:111],v[110:111],v[56:57] op_sel_hi:[1,0] neg_lo:[0,1] neg_hi:[0,1]\n"
"v_sub_f32 v16,0,v14\n"
"v_sub_f32 v17,0,v14\n"
"v_sub_f32 v18,0,v14\n"
"v_sub_f32 v19,0,v14\n"
"v_add_f32 v15,v15,v58\n"
"v_pk_add_f32 v[112:113],v[112:113],v[58:59] op_sel_hi:[1,0] neg_lo:[0,1] neg_hi:[0,1]\n"
"v_pk_add_f32 v[114:115],v[114:115],v[58:59] op_sel_hi:[1,0] neg_lo:[0,1] neg_hi:[0,1]\n"
"v_pk_add_f32 v[116:117],v[116:117],v[58:59] op_sel_hi:[1,0] neg_lo:[0,1] neg_hi:[0,1]\n"
"v_pk_add_f32 v[118:119],v[118:119],v[58:59] op_sel_hi:[1,0] neg_lo:[0,1] neg_hi:[0,1]\n"
"v_pk_add_f32 v[120:121],v[120:121],v[58:59] op_sel_hi:[1,0] neg_lo:[0,1] neg_hi:[0,1]\n"
"v_pk_add_f32 v[122:123],v[122:123],v[58:59] op_sel_hi:[1,0] neg_lo:[0,1] neg_hi:[0,1]\n"
"v_pk_add_f32 v[124:125],v[124:125],v[58:59] op_sel_hi:[1,0] neg_lo:[0,1] neg_hi:[0,1]\n"
"v_pk_add_f32 v[126:127],v[126:127],v[58:59] op_sel_hi:[1,0] neg_lo:[0,1] neg_hi:[0,1]\n"
"v_sub_f32 v20,0,v15\n"
"v_sub_f32 v21,0,v15\n"
"v_sub_f32 v22,0,v15\n"
"v_sub_f32 v23,0,v15\n"
"s_branch L_slowret_pro1b_%=\n"
"L_slow_odd_%=:\n"
"s_cmp_le_i32 s50,s52\n"
"s_cbranch_scc0 L_nomask_odd_%=\n"
"s_cmp_lt_i32 s53,-32\n"
"s_cbranch_scc1 L_mdone_odd_%=\n"
"s_cmp_eq_i32 s53,0\n"
"s_cbranch_scc0 L_m2_odd_%=\n"
"v_cndmask_b32 v96,v96,v27,s[86:87]\n"
"v_cndmask_b32 v97,v97,v27,s[88:89]\n"
"v_cndmask_b32 v98,v98,v27,s[90:91]\n"
"v_cndmask_b32 v99,v99,v27,s[22:23]\n"
"v_mov_b32 v100,v27\n"
"v_mov_b32 v101,v27\n"
"v_mov_b32 v102,v27\n"
"v_mov_b32 v103,v27\n"
"v_cndmask_b32 v116,v116,v27,s[86:87]\n"
"v_cndmask_b32 v117,v117,v27,s[88:89]\n"
"v_cndmask_b32 v118,v118,v27,s[90:91]\n"
"v_cndmask_b32 v119,v119,v27,s[22:23]\n"
"v_mov_b32 v104,v27\n"
"v_mov_b32 v105,v27\n"
"v_mov_b32 v106,v27\n"
"v_mov_b32 v107,v27\n"
"v_mov_b32 v120,v27\n"
"v_mov_b32 v121,v27\n"
"v_mov_b32 v122,v27\n"
"v_mov_b32 v123,v27\n"
"v_mov_b32 v108,v27\n"
"v_mov_b32 v109,v27\n"
"v_mov_b32 v110,v27\n"
"v_mov_b32 v111,v27\n"
"v_mov_b32 v124,v27\n"
"v_mov_b32 v125,v27\n"
"v_mov_b32 v126,v27\n"
"v_mov_b32 v127,v27\n"
"s_branch L_mdone_odd_%=\n"
"L_m2_odd_%=:\n"
"s_cmp_eq_i32 s53,-32\n"
"s_cbranch_scc0 L_m3_odd_%=\n"
"v_cndmask_b32 v104,v104,v27,s[86:87]\n"
"v_cndmask_b32 v105,v105,v27,s[88:89]\n"
"v_cndmask_b32 v106,v106,v27,s[90:91]\n"
"v_cndmask_b32 v107,v107,v27,s[22:23]\n"
"v_mov_b32 v108,v27\n"
"v_mov_b32 v109,v27\n"
"v_mov_b32 v110,v27\n"
"v_mov_b32 v111,v27\n"
"v_cndmask_b32 v124,v124,v27,s[86:87]\n"
"v_cndmask_b32 v125,v125,v27,s[88:89]\n"
"v_cndmask_b32 v126,v126,v27,s[90:91]\n"
"v_cndmask_b32 v127,v127,v27,s[22:23]\n"
"s_branch L_mdone_odd_%=\n"
"L_m3_odd_%=:\n"
"v_mov_b32 v96,v27\n"
"v_mov_b32 v97,v27\n"
"v_mov_b32 v98,v27\n"
"v_mov_b32 v99,v27\n"
"v_mov_b32 v112,v27\n"
"v_mov_b32 v113,v27\n"
"v_mov_b32 v114,v27\n"
"v_mov_b32 v115,v27\n"
"v_mov_b32 v100,v27\n"
"v_mov_b32 v101,v27\n"
"v_mov_b32 v102,v27\n"
"v_mov_b32 v103,v27\n"
"v_mov_b32 v116,v27\n"
"v_mov_b32 v117,v27\n"
"v_mov_b32 v118,v27\n"
"v_mov_b32 v119,v27\n"
"v_mov_b32 v104,v27\n"
"v_mov_b32 v105,v27\n"
"v_mov_b32 v106,v27\n"
"v_mov_b32 v107,v27\n"
"v_mov_b32 v120,v27\n"
"v_mov_b32 v121,v27\n"
"v_mov_b32 v122,v27\n"
"v_mov_b32 v123,v27\n"
"v_mov_b32 v108,v27\n"
"v_mov_b32 v109,v27\n"
"v_mov_b32 v110,v27\n"
"v_mov_b32 v111,v27\n"
"v_mov_b32 v124,v27\n"
"v_mov_b32 v125,v27\n"
"v_mov_b32 v126,v27\n"
"v_mov_b32 v127,v27\n"
"s_branch L_slowret_odd_%=\n"
"L_mdone_odd_%=:\n"
"v_cmp_nge_f32 vcc,s81,v11\n"
"s_nop 0\n"
"s_cbranch_vccz L_slowret_odd_%=\n"
"L_nomask_odd_%=:\n"
"v_max3_f32 v56,v96,v97,v98\n"
"v_max3_f32 v58,v112,v113,v114\n"
"v_max3_f32 v56,v56,v99,v100\n"
"v_max3_f32 v58,v58,v115,v116\n"
"v_max3_f32 v56,v56,v101,v102\n"
"v_max3_f32 v58,v58,v117,v118\n"
"v_max3_f32 v56,v56,v103,v104\n"
"v_max3_f32 v58,v58,v119,v120\n"
"v_max3_f32 v56,v56,v105,v106\n"
"v_max3_f32 v58,v58,v121,v122\n"
"v_max3_f32 v56,v56,v107,v108\n"
"v_max3_f32 v58,v58,v123,v124\n"
"v_max3_f32 v56,v56,v109,v110\n"
"v_max3_f32 v58,v58,v125,v126\n"
"v_max_f32 v56,v56,v111\n"
"v_max_f32 v58,v58,v127\n"
"v_mov_b32 v57,v56\n"
"v_mov_b32 v59,v58\n"
"s_nop 0\n"
"v_permlane32_swap_b32 v56,v57\n"
"s_nop 0\n"
"v_permlane32_swap_b32 v58,v59\n"
"v_max_f32 v56,v56,v57\n"
"v_max_f32 v58,v58,v59\n"
"v_mov_b32 v57,v56\n"
"v_mov_b32 v59,v58\n"
"s_nop 0\n"
"v_permlane16_swap_b32 v56,v57\n"
"v_permlane16_swap_b32 v58,v59\n"
"v_max_f32 v56,v56,v57\n"
"v_max_f32 v58,v58,v59\n"
"v_max_f32 v56,0,v56\n"
"v_max_f32 v58,0,v58\n"
"v_exp_f32 v24,-v56\n"
"v_exp_f32 v25,-v58\n"
"s_mov_b32 s76,1\n"
"v_mul_f32 v12,v12,v24\n"
"v_mul_f32 v13,v13,v25\n"
"v_add_f32 v14,v14,v56\n"
"v_sub_f32 v96,v96,v56\n"
"v_sub_f32 v97,v97,v56\n"
"v_sub_f32 v98,v98,v56\n"
"v_sub_f32 v99,v99,v56\n"
"v_sub_f32 v100,v100,v56\n"
"v_sub_f32 v101,v101,v56\n"
"v_sub_f32 v102,v102,v56\n"
"v_sub_f32 v103,v103,v56\n"
"v_sub_f32 v104,v104,v56\n"
"v_sub_f32 v105,v105,v56\n"
"v_sub_f32 v106,v106,v56\n"
"v_sub_f32 v107,v107,v56\n"
"v_sub_f32 v108,v108,v56\n"
"v_sub_f32 v109,v109,v56\n"
"v_sub_f32 v110,v110,v56\n"
"v_sub_f32 v111,v111,v56\n"
"v_sub_f32 v16,0,v14\n"
"v_sub_f32 v17,0,v14\n"
"v_sub_f32 v18,0,v14\n"
"v_sub_f32 v19,0,v14\n"
"v_add_f32 v15,v15,v58\n"
"v_sub_f32 v112,v112,v58\n"
"v_sub_f32 v113,v113,v58\n"
"v_sub_f32 v114,v114,v58\n"
"v_sub_f32 v115,v115,v58\n"
"v_sub_f32 v116,v116,v58\n"
"v_sub_f32 v117,v117,v58\n"
"v_sub_f32 v118,v118,v58\n"
"v_sub_f32 v119,v119,v58\n"
"v_sub_f32 v120,v120,v58\n"
"v_sub_f32 v121,v121,v58\n"
"v_sub_f32 v122,v122,v58\n"
"v_sub_f32 v123,v123,v58\n"
"v_sub_f32 v124,v124,v58\n"
"v_sub_f32 v125,v125,v58\n"
"v_sub_f32 v126,v126,v58\n"
"v_sub_f32 v127,v127,v58\n"
"v_sub_f32 v20,0,v15\n"
"v_sub_f32 v21,0,v15\n"
"v_sub_f32 v22,0,v15\n"
"v_sub_f32 v23,0,v15\n"
"s_branch L_slowret_odd_%=\n"
"L_slow_even_%=:\n"
"s_cmp_le_i32 s50,s52\n"
"s_cbranch_scc0 L_nomask_even_%=\n"
"s_cmp_lt_i32 s53,-32\n"
"s_cbranch_scc1 L_mdone_even_%=\n"
"s_cmp_eq_i32 s53,0\n"
"s_cbranch_scc0 L_m2_even_%=\n"
"v_cndmask_b32 v64,v64,v27,s[86:87]\n"
"v_cndmask_b32 v65,v65,v27,s[88:89]\n"
"v_cndmask_b32 v66,v66,v27,s[90:91]\n"
"v_cndmask_b32 v67,v67,v27,s[22:23]\n"
"v_mov_b32 v68,v27\n"
"v_mov_b32 v69,v27\n"
"v_mov_b32 v70,v27\n"
"v_mov_b32 v71,v27\n"
"v_cndmask_b32 v84,v84,v27,s[86:87]\n"
"v_cndmask_b32 v85,v85,v27,s[88:89]\n"
"v_cndmask_b32 v86,v86,v27,s[90:91]\n"
"v_cndmask_b32 v87,v87,v27,s[22:23]\n"
"v_mov_b32 v72,v27\n"
"v_mov_b32 v73,v27\n"
"v_mov_b32 v74,v27\n"
"v_mov_b32 v75,v27\n"
"v_mov_b32 v88,v27\n"
"v_mov_b32 v89,v27\n"
"v_mov_b32 v90,v27\n"
"v_mov_b32 v91,v27\n"
"v_mov_b32 v76,v27\n"
"v_mov_b32 v77,v27\n"
"v_mov_b32 v78,v27\n"
"v_mov_b32 v79,v27\n"
"v_mov_b32 v92,v27\n"
"v_mov_b32 v93,v27\n"
"v_mov_b32 v94,v27\n"
"v_mov_b32 v95,v27\n"
"s_branch L_mdone_even_%=\n"
"L_m2_even_%=:\n"
"s_cmp_eq_i32 s53,-32\n"
"s_cbranch_scc0 L_m3_even_%=\n"
"v_cndmask_b32 v72,v72,v27,s[86:87]\n"
"v_cndmask_b32 v73,v73,v27,s[88:89]\n"
"v_cndmask_b32 v74,v74,v27,s[90:91]\n"
"v_cndmask_b32 v75,v75,v27,s[22:23]\n"
"v_mov_b32 v76,v27\n"
"v_mov_b32 v77,v27\n"
"v_mov_b32 v78,v27\n"
"v_mov_b32 v79,v27\n"
"v_cndmask_b32 v92,v92,v27,s[86:87]\n"
"v_cndmask_b32 v93,v93,v27,s[88:89]\n"
"v_cndmask_b32 v94,v94,v27,s[90:91]\n"
"v_cndmask_b32 v95,v95,v27,s[22:23]\n"
"s_branch L_mdone_even_%=\n"
"L_m3_even_%=:\n"
"v_mov_b32 v64,v27\n"
"v_mov_b32 v65,v27\n"
"v_mov_b32 v66,v27\n"
"v_mov_b32 v67,v27\n"
"v_mov_b32 v80,v27\n"
"v_mov_b32 v81,v27\n"
"v_mov_b32 v82,v27\n"
"v_mov_b32 v83,v27\n"
"v_mov_b32 v68,v27\n"
"v_mov_b32 v69,v27\n"
"v_mov_b32 v70,v27\n"
"v_mov_b32 v71,v27\n"
"v_mov_b32 v84,v27\n"
"v_mov_b32 v85,v27\n"
"v_mov_b32 v86,v27\n"
"v_mov_b32 v87,v27\n"
"v_mov_b32 v72,v27\n"
"v_mov_b32 v73,v27\n"
"v_mov_b32 v74,v27\n"
"v_mov_b32 v75,v27\n"
"v_mov_b32 v88,v27\n"
"v_mov_b32 v89,v27\n"
"v_mov_b32 v90,v27\n"
"v_mov_b32 v91,v27\n"
"v_mov_b32 v76,v27\n"
"v_mov_b32 v77,v27\n"
"v_mov_b32 v78,v27\n"
"v_mov_b32 v79,v27\n"
"v_mov_b32 v92,v27\n"
"v_mov_b32 v93,v27\n"
"v_mov_b32 v94,v27\n"
"v_mov_b32 v95,v27\n"
"s_branch L_slowret_even_%=\n"
"L_mdone_even_%=:\n"
"v_cmp_nge_f32 vcc,s81,v11\n"
"s_nop 0\n"
"s_cbranch_vccz L_slowret_even_%=\n"
"L_nomask_even_%=:\n"
"v_max3_f32 v56,v64,v65,v66\n"
"v_max3_f32 v58,v80,v81,v82\n"
"v_max3_f32 v56,v56,v67,v68\n"
"v_max3_f32 v58,v58,v83,v84\n"
"v_max3_f32 v56,v56,v69,v70\n"
"v_max3_f32 v58,v58,v85,v86\n"
"v_max3_f32 v56,v56,v71,v72\n"
"v_max3_f32 v58,v58,v87,v88\n"
"v_max3_f32 v56,v56,v73,v74\n"
"v_max3_f32 v58,v58,v89,v90\n"
"v_max3_f32 v56,v56,v75,v76\n"
"v_max3_f32 v58,v58,v91,v92\n"
"v_max3_f32 v56,v56,v77,v78\n"
"v_max3_f32 v58,v58,v93,v94\n"
"v_max_f32 v56,v56,v79\n"
"v_max_f32 v58,v58,v95\n"
"v_mov_b32 v57,v56\n"
"v_mov_b32 v59,v58\n"
"s_nop 0\n"
"v_permlane32_swap_b32 v56,v57\n"
"s_nop 0\n"
"v_permlane32_swap_b32 v58,v59\n"
"v_max_f32 v56,v56,v57\n"
"v_max_f32 v58,v58,v59\n"
"v_mov_b32 v57,v56\n"
"v_mov_b32 v59,v58\n"
"s_nop 0\n"
"v_permlane16_swap_b32 v56,v57\n"
"v_permlane16_swap_b32 v58,v59\n"
"v_max_f32 v56,v56,v57\n"
"v_max_f32 v58,v58,v59\n"
"v_max_f32 v56,0,v56\n"
"v_max_f32 v58,0,v58\n"
"v_exp_f32 v24,-v56\n"
"v_exp_f32 v25,-v58\n"
"s_mov_b32 s76,1\n"
"v_mul_f32 v12,v12,v24\n"
"v_mul_f32 v13,v13,v25\n"
"v_add_f32 v14,v14,v56\n"
"v_sub_f32 v64,v64,v56\n"
"v_sub_f32 v65,v65,v56\n"
"v_sub_f32 v66,v66,v56\n"
"v_sub_f32 v67,v67,v56\n"
"v_sub_f32 v68,v68,v56\n"
"v_sub_f32 v69,v69,v56\n"
"v_sub_f32 v70,v70,v56\n"
"v_sub_f32 v71,v71,v56\n"
"v_sub_f32 v72,v72,v56\n"
"v_sub_f32 v73,v73,v56\n"
"v_sub_f32 v74,v74,v56\n"
"v_sub_f32 v75,v75,v56\n"
"v_sub_f32 v76,v76,v56\n"
"v_sub_f32 v77,v77,v56\n"
"v_sub_f32 v78,v78,v56\n"
"v_sub_f32 v79,v79,v56\n"
"v_sub_f32 v16,0,v14\n"
"v_sub_f32 v17,0,v14\n"
"v_sub_f32 v18,0,v14\n"
"v_sub_f32 v19,0,v14\n"
"v_add_f32 v15,v15,v58\n"
"v_sub_f32 v80,v80,v58\n"
"v_sub_f32 v81,v81,v58\n"
"v_sub_f32 v82,v82,v58\n"
"v_sub_f32 v83,v83,v58\n"
"v_sub_f32 v84,v84,v58\n"
"v_sub_f32 v85,v85,v58\n"
"v_sub_f32 v86,v86,v58\n"
"v_sub_f32 v87,v87,v58\n"
"v_sub_f32 v88,v88,v58\n"
"v_sub_f32 v89,v89,v58\n"
"v_sub_f32 v90,v90,v58\n"
"v_sub_f32 v91,v91,v58\n"
"v_sub_f32 v92,v92,v58\n"
"v_sub_f32 v93,v93,v58\n"
"v_sub_f32 v94,v94,v58\n"
"v_sub_f32 v95,v95,v58\n"
"v_sub_f32 v20,0,v15\n"
"v_sub_f32 v21,0,v15\n"
"v_sub_f32 v22,0,v15\n"
"v_sub_f32 v23,0,v15\n"
"s_branch L_slowret_even_%=\n"
"L_slow_lastf_%=:\n"
"s_cmp_le_i32 s50,s52\n"
"s_cbranch_scc0 L_nomask_lastf_%=\n"
"s_cmp_lt_i32 s53,-32\n"
"s_cbranch_scc1 L_mdone_lastf_%=\n"
"s_cmp_eq_i32 s53,0\n"
"s_cbranch_scc0 L_m2_lastf_%=\n"
"v_cndmask_b32 v96,v96,v27,s[86:87]\n"
"v_cndmask_b32 v97,v97,v27,s[88:89]\n"
"v_cndmask_b32 v98,v98,v27,s[90:91]\n"
"v_cndmask_b32 v99,v99,v27,s[22:23]\n"
"v_mov_b32 v100,v27\n"
"v_mov_b32 v101,v27\n"
"v_mov_b32 v102,v27\n"
"v_mov_b32 v103,v27\n"
"v_cndmask_b32 v116,v116,v27,s[86:87]\n"
"v_cndmask_b32 v117,v117,v27,s[88:89]\n"
"v_cndmask_b32 v118,v118,v27,s[90:91]\n"
"v_cndmask_b32 v119,v119,v27,s[22:23]\n"
"v_mov_b32 v104,v27\n"
"v_mov_b32 v105,v27\n"
"v_mov_b32 v106,v27\n"
"v_mov_b32 v107,v27\n"
"v_mov_b32 v120,v27\n"
"v_mov_b32 v121,v27\n"
"v_mov_b32 v122,v27\n"
"v_mov_b32 v123,v27\n"
"v_mov_b32 v108,v27\n"
"v_mov_b32 v109,v27\n"
"v_mov_b32 v110,v27\n"
"v_mov_b32 v111,v27\n"
"v_mov_b32 v124,v27\n"
"v_mov_b32 v125,v27\n"
"v_mov_b32 v126,v27\n"
"v_mov_b32 v127,v27\n"
"s_branch L_mdone_lastf_%=\n"
"L_m2_lastf_%=:\n"
"s_cmp_eq_i32 s53,-32\n"
"s_cbranch_scc0 L_m3_lastf_%=\n"
"v_cndmask_b32 v104,v104,v27,s[86:87]\n"
"v_cndmask_b32 v105,v105,v27,s[88:89]\n"
"v_cndmask_b32 v106,v106,v27,s[90:91]\n"
"v_cndmask_b32 v107,v107,v27,s[22:23]\n"
"v_mov_b32 v108,v27\n"
"v_mov_b32 v109,v27\n"
"v_mov_b32 v110,v27\n"
"v_mov_b32 v111,v27\n"
"v_cndmask_b32 v124,v124,v27,s[86:87]\n"
"v_cndmask_b32 v125,v125,v27,s[88:89]\n"
"v_cndmask_b32 v126,v126,v27,s[90:91]\n"
"v_cndmask_b32 v127,v127,v27,s[22:23]\n"
"s_branch L_mdone_lastf_%=\n"
"L_m3_lastf_%=:\n"
"v_mov_b32 v96,v27\n"
"v_mov_b32 v97,v27\n"
"v_mov_b32 v98,v27\n"
"v_mov_b32 v99,v27\n"
"v_mov_b32 v112,v27\n"
"v_mov_b32 v113,v27\n"
"v_mov_b32 v114,v27\n"
"v_mov_b32 v115,v27\n"
"v_mov_b32 v100,v27\n"
"v_mov_b32 v101,v27\n"
"v_mov_b32 v102,v27\n"
"v_mov_b32 v103,v27\n"
"v_mov_b32 v116,v27\n"
"v_mov_b32 v117,v27\n"
"v_mov_b32 v118,v27\n"
"v_mov_b32 v119,v27\n"
"v_mov_b32 v104,v27\n"
"v_mov_b32 v105,v27\n"
"v_mov_b32 v106,v27\n"
"v_mov_b32 v107,v27\n"
"v_mov_b32 v120,v27\n"
"v_mov_b32 v121,v27\n"
"v_mov_b32 v122,v27\n"
"v_mov_b32 v123,v27\n"
"v_mov_b32 v108,v27\n"
"v_mov_b32 v109,v27\n"
"v_mov_b32 v110,v27\n"
"v_mov_b32 v111,v27\n"
"v_mov_b32 v124,v27\n"
"v_mov_b32 v125,v27\n"
"v_mov_b32 v126,v27\n"
"v_mov_b32 v127,v27\n"
"s_branch L_slowret_lastf_%=\n"
"L_mdone_lastf_%=:\n"
"v_cmp_nge_f32 vcc,s81,v11\n"
"s_nop 0\n"
"s_cbranch_vccz L_slowret_lastf_%=\n"
"L_nomask_lastf_%=:\n"
"v_max3_f32 v56,v96,v97,v98\n"
"v_max3_f32 v58,v112,v113,v114\n"
"v_max3_f32 v56,v56,v99,v100\n"
"v_max3_f32 v58,v58,v115,v116\n"
"v_max3_f32 v56,v56,v101,v102\n"
"v_max3_f32 v58,v58,v117,v118\n"
"v_max3_f32 v56,v56,v103,v104\n"
"v_max3_f32 v58,v58,v119,v120\n"
"v_max3_f32 v56,v56,v105,v106\n"
"v_max3_f32 v58,v58,v121,v122\n"
"v_max3_f32 v56,v56,v107,v108\n"
"v_max3_f32 v58,v58,v123,v124\n"
"v_max3_f32 v56,v56,v109,v110\n"
"v_max3_f32 v58,v58,v125,v126\n"
"v_max_f32 v56,v56,v111\n"
"v_max_f32 v58,v58,v127\n"
"v_mov_b32 v57,v56\n"
"v_mov_b32 v59,v58\n"
"s_nop 0\n"
"v_permlane32_swap_b32 v56,v57\n"
"s_nop 0\n"
"v_permlane32_swap_b32 v58,v59\n"
"v_max_f32 v56,v56,v57\n"
"v_max_f32 v58,v58,v59\n"
"v_mov_b32 v57,v56\n"
"v_mov_b32 v59,v58\n"
"s_nop 0\n"
"v_permlane16_swap_b32 v56,v57\n"
"v_permlane16_swap_b32 v58,v59\n"
"v_max_f32 v56,v56,v57\n"
"v_max_f32 v58,v58,v59\n"
"v_max_f32 v56,0,v56\n"
"v_max_f32 v58,0,v58\n"
"v_exp_f32 v24,-v56\n"
"v_exp_f32 v25,-v58\n"
"s_mov_b32 s76,1\n"
"v_mul_f32 v12,v12,v24\n"
"v_mul_f32 v13,v13,v25\n"
"v_add_f32 v14,v14,v56\n"
"v_sub_f32 v96,v96,v56\n"
"v_sub_f32 v97,v97,v56\n"
"v_sub_f32 v98,v98,v56\n"
"v_sub_f32 v99,v99,v56\n"
"v_sub_f32 v100,v100,v56\n"
"v_sub_f32 v101,v101,v56\n"
"v_sub_f32 v102,v102,v56\n"
"v_sub_f32 v103,v103,v56\n"
"v_sub_f32 v104,v104,v56\n"
"v_sub_f32 v105,v105,v56\n"
"v_sub_f32 v106,v106,v56\n"
"v_sub_f32 v107,v107,v56\n"
"v_sub_f32 v108,v108,v56\n"
"v_sub_f32 v109,v109,v56\n"
"v_sub_f32 v110,v110,v56\n"
"v_sub_f32 v111,v111,v56\n"
"v_sub_f32 v16,0,v14\n"
"v_sub_f32 v17,0,v14\n"
"v_sub_f32 v18,0,v14\n"
"v_sub_f32 v19,0,v14\n"
"v_add_f32 v15,v15,v58\n"
"v_sub_f32 v112,v112,v58\n"
"v_sub_f32 v113,v113,v58\n"
"v_sub_f32 v114,v114,v58\n"
"v_sub_f32 v115,v115,v58\n"
"v_sub_f32 v116,v116,v58\n"
"v_sub_f32 v117,v117,v58\n"
"v_sub_f32 v118,v118,v58\n"
"v_sub_f32 v119,v119,v58\n"
"v_sub_f32 v120,v120,v58\n"
"v_sub_f32 v121,v121,v58\n"
"v_sub_f32 v122,v122,v58\n"
"v_sub_f32 v123,v123,v58\n"
"v_sub_f32 v124,v124,v58\n"
"v_sub_f32 v125,v125,v58\n"
"v_sub_f32 v126,v126,v58\n"
"v_sub_f32 v127,v127,v58\n"
"v_sub_f32 v20,0,v15\n"
"v_sub_f32 v21,0,v15\n"
"v_sub_f32 v22,0,v15\n"
"v_sub_f32 v23,0,v15\n"
"s_branch L_slowret_lastf_%=\n"
"L_slow_lastb_%=:\n"
"s_cmp_le_i32 s50,s52\n"
"s_cbranch_scc0 L_nomask_lastb_%=\n"
"s_cmp_lt_i32 s53,-32\n"
"s_cbranch_scc1 L_mdone_lastb_%=\n"
"s_cmp_eq_i32 s53,0\n"
"s_cbranch_scc0 L_m2_lastb_%=\n"
"v_cndmask_b32 v96,v96,v27,s[86:87]\n"
"v_cndmask_b32 v97,v97,v27,s[88:89]\n"
"v_cndmask_b32 v98,v98,v27,s[90:91]\n"
"v_cndmask_b32 v99,v99,v27,s[22:23]\n"
"v_mov_b32 v100,v27\n"
"v_mov_b32 v101,v27\n"
"v_mov_b32 v102,v27\n"
"v_mov_b32 v103,v27\n"
"v_cndmask_b32 v116,v116,v27,s[86:87]\n"
"v_cndmask_b32 v117,v117,v27,s[88:89]\n"
"v_cndmask_b32 v118,v118,v27,s[90:91]\n"
"v_cndmask_b32 v119,v119,v27,s[22:23]\n"
"v_mov_b32 v104,v27\n"
"v_mov_b32 v105,v27\n"
"v_mov_b32 v106,v27\n"
"v_mov_b32 v107,v27\n"
"v_mov_b32 v120,v27\n"
"v_mov_b32 v121,v27\n"
"v_mov_b32 v122,v27\n"
"v_mov_b32 v123,v27\n"
"v_mov_b32 v108,v27\n"
"v_mov_b32 v109,v27\n"
"v_mov_b32 v110,v27\n"
"v_mov_b32 v111,v27\n"
"v_mov_b32 v124,v27\n"
"v_mov_b32 v125,v27\n"
"v_mov_b32 v126,v27\n"
"v_mov_b32 v127,v27\n"
"s_branch L_mdone_lastb_%=\n"
"L_m2_lastb_%=:\n"
"s_cmp_eq_i32 s53,-32\n"
"s_cbranch_scc0 L_m3_lastb_%=\n"
"v_cndmask_b32 v104,v104,v27,s[86:87]\n"
"v_cndmask_b32 v105,v105,v27,s[88:89]\n"
"v_cndmask_b32 v106,v106,v27,s[90:91]\n"
"v_cndmask_b32 v107,v107,v27,s[22:23]\n"
"v_mov_b32 v108,v27\n"
"v_mov_b32 v109,v27\n"
"v_mov_b32 v110,v27\n"
"v_mov_b32 v111,v27\n"
"v_cndmask_b32 v124,v124,v27,s[86:87]\n"
"v_cndmask_b32 v125,v125,v27,s[88:89]\n"
"v_cndmask_b32 v126,v126,v27,s[90:91]\n"
"v_cndmask_b32 v127,v127,v27,s[22:23]\n"
"s_branch L_mdone_lastb_%=\n"
"L_m3_lastb_%=:\n"
"v_mov_b32 v96,v27\n"
"v_mov_b32 v97,v27\n"
"v_mov_b32 v98,v27\n"
"v_mov_b32 v99,v27\n"
"v_mov_b32 v112,v27\n"
"v_mov_b32 v113,v27\n"
"v_mov_b32 v114,v27\n"
"v_mov_b32 v115,v27\n"
"v_mov_b32 v100,v27\n"
"v_mov_b32 v101,v27\n"
"v_mov_b32 v102,v27\n"
"v_mov_b32 v103,v27\n"
"v_mov_b32 v116,v27\n"
"v_mov_b32 v117,v27\n"
"v_mov_b32 v118,v27\n"
"v_mov_b32 v119,v27\n"
"v_mov_b32 v104,v27\n"
"v_mov_b32 v105,v27\n"
"v_mov_b32 v106,v27\n"
"v_mov_b32 v107,v27\n"
"v_mov_b32 v120,v27\n"
"v_mov_b32 v121,v27\n"
"v_mov_b32 v122,v27\n"
"v_mov_b32 v123,v27\n"
"v_mov_b32 v108,v27\n"
"v_mov_b32 v109,v27\n"
"v_mov_b32 v110,v27\n"
"v_mov_b32 v111,v27\n"
"v_mov_b32 v124,v27\n"
"v_mov_b32 v125,v27\n"
"v_mov_b32 v126,v27\n"
"v_mov_b32 v127,v27\n"
"s_branch L_slowret_lastb_%=\n"
"L_mdone_lastb_%=:\n"
"v_cmp_nge_f32 vcc,s81,v11\n"
"s_nop 0\n"
"s_cbranch_vccz L_slowret_lastb_%=\n"
"L_nomask_lastb_%=:\n"
"v_max3_f32 v56,v96,v97,v98\n"
"v_max3_f32 v58,v112,v113,v114\n"
"v_max3_f32 v56,v56,v99,v100\n"
"v_max3_f32 v58,v58,v115,v116\n"
"v_max3_f32 v56,v56,v101,v102\n"
"v_max3_f32 v58,v58,v117,v118\n"
"v_max3_f32 v56,v56,v103,v104\n"
"v_max3_f32 v58,v58,v119,v120\n"
"v_max3_f32 v56,v56,v105,v106\n"
"v_max3_f32 v58,v58,v121,v122\n"
"v_max3_f32 v56,v56,v107,v108\n"
"v_max3_f32 v58,v58,v123,v124\n"
"v_max3_f32 v56,v56,v109,v110\n"
"v_max3_f32 v58,v58,v125,v126\n"
"v_max_f32 v56,v56,v111\n"
"v_max_f32 v58,v58,v127\n"
"v_mov_b32 v57,v56\n"
"v_mov_b32 v59,v58\n"
"s_nop 0\n"
"v_permlane32_swap_b32 v56,v57\n"
"s_nop 0\n"
"v_permlane32_swap_b32 v58,v59\n"
"v_max_f32 v56,v56,v57\n"
"v_max_f32 v58,v58,v59\n"
"v_mov_b32 v57,v56\n"
"v_mov_b32 v59,v58\n"
"s_nop 0\n"
"v_permlane16_swap_b32 v56,v57\n"
"v_permlane16_swap_b32 v58,v59\n"
"v_max_f32 v56,v56,v57\n"
"v_max_f32 v58,v58,v59\n"
"v_max_f32 v56,0,v56\n"
"v_max_f32 v58,0,v58\n"
"v_exp_f32 v24,-v56\n"
"v_exp_f32 v25,-v58\n"
"s_mov_b32 s76,1\n"
"v_mul_f32 v12,v12,v24\n"
"v_mul_f32 v13,v13,v25\n"
"v_add_f32 v14,v14,v56\n"
"v_sub_f32 v96,v96,v56\n"
"v_sub_f32 v97,v97,v56\n"
"v_sub_f32 v98,v98,v56\n"
"v_sub_f32 v99,v99,v56\n"
"v_sub_f32 v100,v100,v56\n"
"v_sub_f32 v101,v101,v56\n"
"v_sub_f32 v102,v102,v56\n"
"v_sub_f32 v103,v103,v56\n"
"v_sub_f32 v104,v104,v56\n"
"v_sub_f32 v105,v105,v56\n"
"v_sub_f32 v106,v106,v56\n"
"v_sub_f32 v107,v107,v56\n"
"v_sub_f32 v108,v108,v56\n"
"v_sub_f32 v109,v109,v56\n"
"v_sub_f32 v110,v110,v56\n"
"v_sub_f32 v111,v111,v56\n"
"v_sub_f32 v16,0,v14\n"
"v_sub_f32 v17,0,v14\n"
"v_sub_f32 v18,0,v14\n"
"v_sub_f32 v19,0,v14\n"
"v_add_f32 v15,v15,v58\n"
"v_sub_f32 v112,v112,v58\n"
"v_sub_f32 v113,v113,v58\n"
"v_sub_f32 v114,v114,v58\n"
"v_sub_f32 v115,v115,v58\n"
"v_sub_f32 v116,v116,v58\n"
"v_sub_f32 v117,v117,v58\n"
"v_sub_f32 v118,v118,v58\n"
"v_sub_f32 v119,v119,v58\n"
"v_sub_f32 v120,v120,v58\n"
"v_sub_f32 v121,v121,v58\n"
"v_sub_f32 v122,v122,v58\n"
"v_sub_f32 v123,v123,v58\n"
"v_sub_f32 v124,v124,v58\n"
"v_sub_f32 v125,v125,v58\n"
"v_sub_f32 v126,v126,v58\n"
"v_sub_f32 v127,v127,v58\n"
"v_sub_f32 v20,0,v15\n"
"v_sub_f32 v21,0,v15\n"
"v_sub_f32 v22,0,v15\n"
"v_sub_f32 v23,0,v15\n"
"s_branch L_slowret_lastb_%=\n"
"L_resc_odd_%=:\n"
"s_mov_b32 s76,0\n"
"v_mul_f32 v128,v128,v24\n"
"v_mul_f32 v129,v129,v24\n"
"v_mul_f32 v130,v130,v24\n"
"v_mul_f32 v131,v131,v24\n"
"v_mul_f32 v132,v132,v24\n"
"v_mul_f32 v133,v133,v24\n"
"v_mul_f32 v134,v134,v24\n"
"v_mul_f32 v135,v135,v24\n"
"v_mul_f32 v136,v136,v24\n"
"v_mul_f32 v137,v137,v24\n"
"v_mul_f32 v138,v138,v24\n"
"v_mul_f32 v139,v139,v24\n"
"v_mul_f32 v140,v140,v24\n"
"v_mul_f32 v141,v141,v24\n"
"v_mul_f32 v142,v142,v24\n"
"v_mul_f32 v143,v143,v24\n"
"v_mul_f32 v144,v144,v24\n"
"v_mul_f32 v145,v145,v24\n"
"v_mul_f32 v146,v146,v24\n"
"v_mul_f32 v147,v147,v24\n"
"v_mul_f32 v148,v148,v24\n"
"v_mul_f32 v149,v149,v24\n"
"v_mul_f32 v150,v150,v24\n"
"v_mul_f32 v151,v151,v24\n"
"v_mul_f32 v152,v152,v24\n"
"v_mul_f32 v153,v153,v24\n"
"v_mul_f32 v154,v154,v24\n"
"v_mul_f32 v155,v155,v24\n"
"v_mul_f32 v156,v156,v24\n"
"v_mul_f32 v157,v157,v24\n"
"v_mul_f32 v158,v158,v24\n"
"v_mul_f32 v159,v159,v24\n"
"v_mul_f32 v160,v160,v24\n"
"v_mul_f32 v161,v161,v24\n"
"v_mul_f32 v162,v162,v24\n"
"v_mul_f32 v163,v163,v24\n"
"v_mul_f32 v164,v164,v24\n"
"v_mul_f32 v165,v165,v24\n"
"v_mul_f32 v166,v166,v24\n"
"v_mul_f32 v167,v167,v24\n"
"v_mul_f32 v168,v168,v24\n"
"v_mul_f32 v169,v169,v24\n"
"v_mul_f32 v170,v170,v24\n"
"v_mul_f32 v171,v171,v24\n"
"v_mul_f32 v172,v172,v24\n"
"v_mul_f32 v173,v173,v24\n"
"v_mul_f32 v174,v174,v24\n"
"v_mul_f32 v175,v175,v24\n"
"v_mul_f32 v176,v176,v24\n"
"v_mul_f32 v177,v177,v24\n"
"v_mul_f32 v178,v178,v24\n"
"v_mul_f32 v179,v179,v24\n"
"v_mul_f32 v180,v180,v24\n"
"v_mul_f32 v181,v181,v24\n"
"v_mul_f32 v182,v182,v24\n"
"v_mul_f32 v183,v183,v24\n"
"v_mul_f32 v184,v184,v24\n"
"v_mul_f32 v185,v185,v24\n"
"v_mul_f32 v186,v186,v24\n"
"v_mul_f32 v187,v187,v24\n"
"v_mul_f32 v188,v188,v24\n"
"v_mul_f32 v189,v189,v24\n"
"v_mul_f32 v190,v190,v24\n"
"v_mul_f32 v191,v191,v24\n"
"v_mul_f32 v192,v192,v25\n"
"v_mul_f32 v193,v193,v25\n"
"v_mul_f32 v194,v194,v25\n"
"v_mul_f32 v195,v195,v25\n"
"v_mul_f32 v196,v196,v25\n"
"v_mul_f32 v197,v197,v25\n"
"v_mul_f32 v198,v198,v25\n"
"v_mul_f32 v199,v199,v25\n"
"v_mul_f32 v200,v200,v25\n"
"v_mul_f32 v201,v201,v25\n"
"v_mul_f32 v202,v202,v25\n"
"v_mul_f32 v203,v203,v25\n"
"v_mul_f32 v204,v204,v25\n"
"v_mul_f32 v205,v205,v25\n"
"v_mul_f32 v206,v206,v25\n"
"v_mul_f32 v207,v207,v25\n"
"v_mul_f32 v208,v208,v25\n"
"v_mul_f32 v209,v209,v25\n"
"v_mul_f32 v210,v210,v25\n"
"v_mul_f32 v211,v211,v25\n"
"v_mul_f32 v212,v212,v25\n"
"v_mul_f32 v213,v213,v25\n"
"v_mul_f32 v214,v214,v25\n"
"v_mul_f32 v215,v215,v25\n"
"v_mul_f32 v216,v216,v25\n"
"v_mul_f32 v217,v217,v25\n"
"v_mul_f32 v218,v218,v25\n"
"v_mul_f32 v219,v219,v25\n"
"v_mul_f32 v220,v220,v25\n"
"v_mul_f32 v221,v221,v25\n"
"v_mul_f32 v222,v222,v25\n"
"v_mul_f32 v223,v223,v25\n"
"v_mul_f32 v224,v224,v25\n"
"v_mul_f32 v225,v225,v25\n"
"v_mul_f32 v226,v226,v25\n"
"v_mul_f32 v227,v227,v25\n"
"v_mul_f32 v228,v228,v25\n"
"v_mul_f32 v229,v229,v25\n"
"v_mul_f32 v230,v230,v25\n"
"v_mul_f32 v231,v231,v25\n"
"v_mul_f32 v232,v232,v25\n"
"v_mul_f32 v233,v233,v25\n"
"v_mul_f32 v234,v234,v25\n"
"v_mul_f32 v235,v235,v25\n"
"v_mul_f32 v236,v236,v25\n"
"v_mul_f32 v237,v237,v25\n"
"v_mul_f32 v238,v238,v25\n"
"v_mul_f32 v239,v239,v25\n"
"v_mul_f32 v240,v240,v25\n"
"v_mul_f32 v241,v241,v25\n"
"v_mul_f32 v242,v242,v25\n"
"v_mul_f32 v243,v243,v25\n"
"v_mul_f32 v244,v244,v25\n"
"v_mul_f32 v245,v245,v25\n"
"v_mul_f32 v246,v246,v25\n"
"v_mul_f32 v247,v247,v25\n"
"v_mul_f32 v248,v248,v25\n"
"v_mul_f32 v249,v249,v25\n"
"v_mul_f32 v250,v250,v25\n"
"v_mul_f32 v251,v251,v25\n"
"v_mul_f32 v252,v252,v25\n"
"v_mul_f32 v253,v253,v25\n"
"v_mul_f32 v254,v254,v25\n"
"v_mul_f32 v255,v255,v25\n"
"s_branch L_rescret_odd_%=\n"
"L_resc_even_%=:\n"
"s_mov_b32 s76,0\n"
"v_mul_f32 v128,v128,v24\n"
"v_mul_f32 v129,v129,v24\n"
"v_mul_f32 v130,v130,v24\n"
"v_mul_f32 v131,v131,v24\n"
"v_mul_f32 v132,v132,v24\n"
"v_mul_f32 v133,v133,v24\n"
"v_mul_f32 v134,v134,v24\n"
"v_mul_f32 v135,v135,v24\n"
"v_mul_f32 v136,v136,v24\n"
"v_mul_f32 v137,v137,v24\n"
"v_mul_f32 v138,v138,v24\n"
"v_mul_f32 v139,v139,v24\n"
"v_mul_f32 v140,v140,v24\n"
"v_mul_f32 v141,v141,v24\n"
"v_mul_f32 v142,v142,v24\n"
"v_mul_f32 v143,v143,v24\n"
"v_mul_f32 v144,v144,v24\n"
"v_mul_f32 v145,v145,v24\n"
"v_mul_f32 v146,v146,v24\n"
"v_mul_f32 v147,v147,v24\n"
"v_mul_f32 v148,v148,v24\n"
"v_mul_f32 v149,v149,v24\n"
"v_mul_f32 v150,v150,v24\n"
"v_mul_f32 v151,v151,v24\n"
"v_mul_f32 v152,v152,v24\n"
"v_mul_f32 v153,v153,v24\n"
"v_mul_f32 v154,v154,v24\n"
"v_mul_f32 v155,v155,v24\n"
"v_mul_f32 v156,v156,v24\n"
"v_mul_f32 v157,v157,v24\n"
"v_mul_f32 v158,v158,v24\n"
"v_mul_f32 v159,v159,v24\n"
"v_mul_f32 v160,v160,v24\n"
"v_mul_f32 v161,v161,v24\n"
"v_mul_f32 v162,v162,v24\n"
"v_mul_f32 v163,v163,v24\n"
"v_mul_f32 v164,v164,v24\n"
"v_mul_f32 v165,v165,v24\n"
"v_mul_f32 v166,v166,v24\n"
"v_mul_f32 v167,v167,v24\n"
"v_mul_f32 v168,v168,v24\n"
"v_mul_f32 v169,v169,v24\n"
"v_mul_f32 v170,v170,v24\n"
"v_mul_f32 v171,v171,v24\n"
"v_mul_f32 v172,v172,v24\n"
"v_mul_f32 v173,v173,v24\n"
"v_mul_f32 v174,v174,v24\n"
"v_mul_f32 v175,v175,v24\n"
"v_mul_f32 v176,v176,v24\n"
"v_mul_f32 v177,v177,v24\n"
"v_mul_f32 v178,v178,v24\n"
"v_mul_f32 v179,v179,v24\n"
"v_mul_f32 v180,v180,v24\n"
"v_mul_f32 v181,v181,v24\n"
"v_mul_f32 v182,v182,v24\n"
"v_mul_f32 v183,v183,v24\n"
"v_mul_f32 v184,v184,v24\n"
"v_mul_f32 v185,v185,v24\n"
"v_mul_f32 v186,v186,v24\n"
"v_mul_f32 v187,v187,v24\n"
"v_mul_f32 v188,v188,v24\n"
"v_mul_f32 v189,v189,v24\n"
"v_mul_f32 v190,v190,v24\n"
"v_mul_f32 v191,v191,v24\n"
"v_mul_f32 v192,v192,v25\n"
"v_mul_f32 v193,v193,v25\n"
"v_mul_f32 v194,v194,v25\n"
"v_mul_f32 v195,v195,v25\n"
"v_mul_f32 v196,v196,v25\n"
"v_mul_f32 v197,v197,v25\n"
"v_mul_f32 v198,v198,v25\n"
"v_mul_f32 v199,v199,v25\n"
"v_mul_f32 v200,v200,v25\n"
"v_mul_f32 v201,v201,v25\n"
"v_mul_f32 v202,v202,v25\n"
"v_mul_f32 v203,v203,v25\n"
"v_mul_f32 v204,v204,v25\n"
"v_mul_f32 v205,v205,v25\n"
"v_mul_f32 v206,v206,v25\n"
"v_mul_f32 v207,v207,v25\n"
"v_mul_f32 v208,v208,v25\n"
"v_mul_f32 v209,v209,v25\n"
"v_mul_f32 v210,v210,v25\n"
"v_mul_f32 v211,v211,v25\n"
"v_mul_f32 v212,v212,v25\n"
"v_mul_f32 v213,v213,v25\n"
"v_mul_f32 v214,v214,v25\n"
"v_mul_f32 v215,v215,v25\n"
"v_mul_f32 v216,v216,v25\n"
"v_mul_f32 v217,v217,v25\n"
"v_mul_f32 v218,v218,v25\n"
"v_mul_f32 v219,v219,v25\n"
"v_mul_f32 v220,v220,v25\n"
"v_mul_f32 v221,v221,v25\n"
"v_mul_f32 v222,v222,v25\n"
"v_mul_f32 v223,v223,v25\n"
"v_mul_f32 v224,v224,v25\n"
"v_mul_f32 v225,v225,v25\n"
"v_mul_f32 v226,v226,v25\n"
"v_mul_f32 v227,v227,v25\n"
"v_mul_f32 v228,v228,v25\n"
"v_mul_f32 v229,v229,v25\n"
"v_mul_f32 v230,v230,v25\n"
"v_mul_f32 v231,v231,v25\n"
"v_mul_f32 v232,v232,v25\n"
"v_mul_f32 v233,v233,v25\n"
"v_mul_f32 v234,v234,v25\n"
"v_mul_f32 v235,v235,v25\n"
"v_mul_f32 v236,v236,v25\n"
"v_mul_f32 v237,v237,v25\n"
"v_mul_f32 v238,v238,v25\n"
"v_mul_f32 v239,v239,v25\n"
"v_mul_f32 v240,v240,v25\n"
"v_mul_f32 v241,v241,v25\n"
"v_mul_f32 v242,v242,v25\n"
"v_mul_f32 v243,v243,v25\n"
"v_mul_f32 v244,v244,v25\n"
"v_mul_f32 v245,v245,v25\n"
"v_mul_f32 v246,v246,v25\n"
"v_mul_f32 v247,v247,v25\n"
"v_mul_f32 v248,v248,v25\n"
"v_mul_f32 v249,v249,v25\n"
"v_mul_f32 v250,v250,v25\n"
"v_mul_f32 v251,v251,v25\n"
"v_mul_f32 v252,v252,v25\n"
"v_mul_f32 v253,v253,v25\n"
"v_mul_f32 v254,v254,v25\n"
"v_mul_f32 v255,v255,v25\n"
"s_branch L_rescret_even_%=\n"
"L_resc_lastf_%=:\n"
"s_mov_b32 s76,0\n"
"v_mul_f32 v128,v128,v24\n"
"v_mul_f32 v129,v129,v24\n"
"v_mul_f32 v130,v130,v24\n"
"v_mul_f32 v131,v131,v24\n"
"v_mul_f32 v132,v132,v24\n"
"v_mul_f32 v133,v133,v24\n"
"v_mul_f32 v134,v134,v24\n"
"v_mul_f32 v135,v135,v24\n"
"v_mul_f32 v136,v136,v24\n"
"v_mul_f32 v137,v137,v24\n"
"v_mul_f32 v138,v138,v24\n"
"v_mul_f32 v139,v139,v24\n"
"v_mul_f32 v140,v140,v24\n"
"v_mul_f32 v141,v141,v24\n"
"v_mul_f32 v142,v142,v24\n"
"v_mul_f32 v143,v143,v24\n"
"v_mul_f32 v144,v144,v24\n"
"v_mul_f32 v145,v145,v24\n"
"v_mul_f32 v146,v146,v24\n"
"v_mul_f32 v147,v147,v24\n"
"v_mul_f32 v148,v148,v24\n"
"v_mul_f32 v149,v149,v24\n"
"v_mul_f32 v150,v150,v24\n"
"v_mul_f32 v151,v151,v24\n"
"v_mul_f32 v152,v152,v24\n"
"v_mul_f32 v153,v153,v24\n"
"v_mul_f32 v154,v154,v24\n"
"v_mul_f32 v155,v155,v24\n"
"v_mul_f32 v156,v156,v24\n"
"v_mul_f32 v157,v157,v24\n"
"v_mul_f32 v158,v158,v24\n"
"v_mul_f32 v159,v159,v24\n"
"v_mul_f32 v160,v160,v24\n"
"v_mul_f32 v161,v161,v24\n"
"v_mul_f32 v162,v162,v24\n"
"v_mul_f32 v163,v163,v24\n"
"v_mul_f32 v164,v164,v24\n"
"v_mul_f32 v165,v165,v24\n"
"v_mul_f32 v166,v166,v24\n"
"v_mul_f32 v167,v167,v24\n"
"v_mul_f32 v168,v168,v24\n"
"v_mul_f32 v169,v169,v24\n"
"v_mul_f32 v170,v170,v24\n"
"v_mul_f32 v171,v171,v24\n"
"v_mul_f32 v172,v172,v24\n"
"v_mul_f32 v173,v173,v24\n"
"v_mul_f32 v174,v174,v24\n"
"v_mul_f32 v175,v175,v24\n"
"v_mul_f32 v176,v176,v24\n"
"v_mul_f32 v177,v177,v24\n"
"v_mul_f32 v178,v178,v24\n"
"v_mul_f32 v179,v179,v24\n"
"v_mul_f32 v180,v180,v24\n"
"v_mul_f32 v181,v181,v24\n"
"v_mul_f32 v182,v182,v24\n"
"v_mul_f32 v183,v183,v24\n"
"v_mul_f32 v184,v184,v24\n"
"v_mul_f32 v185,v185,v24\n"
"v_mul_f32 v186,v186,v24\n"
"v_mul_f32 v187,v187,v24\n"
"v_mul_f32 v188,v188,v24\n"
"v_mul_f32 v189,v189,v24\n"
"v_mul_f32 v190,v190,v24\n"
"v_mul_f32 v191,v191,v24\n"
"v_mul_f32 v192,v192,v25\n"
"v_mul_f32 v193,v193,v25\n"
"v_mul_f32 v194,v194,v25\n"
"v_mul_f32 v195,v195,v25\n"
"v_mul_f32 v196,v196,v25\n"
"v_mul_f32 v197,v197,v25\n"
"v_mul_f32 v198,v198,v25\n"
"v_mul_f32 v199,v199,v25\n"
"v_mul_f32 v200,v200,v25\n"
"v_mul_f32 v201,v201,v25\n"
"v_mul_f32 v202,v202,v25\n"
"v_mul_f32 v203,v203,v25\n"
"v_mul_f32 v204,v204,v25\n"
"v_mul_f32 v205,v205,v25\n"
"v_mul_f32 v206,v206,v25\n"
"v_mul_f32 v207,v207,v25\n"
"v_mul_f32 v208,v208,v25\n"
"v_mul_f32 v209,v209,v25\n"
"v_mul_f32 v210,v210,v25\n"
"v_mul_f32 v211,v211,v25\n"
"v_mul_f32 v212,v212,v25\n"
"v_mul_f32 v213,v213,v25\n"
"v_mul_f32 v214,v214,v25\n"
"v_mul_f32 v215,v215,v25\n"
"v_mul_f32 v216,v216,v25\n"
"v_mul_f32 v217,v217,v25\n"
"v_mul_f32 v218,v218,v25\n"
"v_mul_f32 v219,v219,v25\n"
"v_mul_f32 v220,v220,v25\n"
"v_mul_f32 v221,v221,v25\n"
"v_mul_f32 v222,v222,v25\n"
"v_mul_f32 v223,v223,v25\n"
"v_mul_f32 v224,v224,v25\n"
"v_mul_f32 v225,v225,v25\n"
"v_mul_f32 v226,v226,v25\n"
"v_mul_f32 v227,v227,v25\n"
"v_mul_f32 v228,v228,v25\n"
"v_mul_f32 v229,v229,v25\n"
"v_mul_f32 v230,v230,v25\n"
"v_mul_f32 v231,v231,v25\n"
"v_mul_f32 v232,v232,v25\n"
"v_mul_f32 v233,v233,v25\n"
"v_mul_f32 v234,v234,v25\n"
"v_mul_f32 v235,v235,v25\n"
"v_mul_f32 v236,v236,v25\n"
"v_mul_f32 v237,v237,v25\n"
"v_mul_f32 v238,v238,v25\n"
"v_mul_f32 v239,v239,v25\n"
"v_mul_f32 v240,v240,v25\n"
"v_mul_f32 v241,v241,v25\n"
"v_mul_f32 v242,v242,v25\n"
"v_mul_f32 v243,v243,v25\n"
"v_mul_f32 v244,v244,v25\n"
"v_mul_f32 v245,v245,v25\n"
"v_mul_f32 v246,v246,v25\n"
"v_mul_f32 v247,v247,v25\n"
"v_mul_f32 v248,v248,v25\n"
"v_mul_f32 v249,v249,v25\n"
"v_mul_f32 v250,v250,v25\n"
"v_mul_f32 v251,v251,v25\n"
"v_mul_f32 v252,v252,v25\n"
"v_mul_f32 v253,v253,v25\n"
"v_mul_f32 v254,v254,v25\n"
"v_mul_f32 v255,v255,v25\n"
"s_branch L_rescret_lastf_%=\n"
"L_resc_lastb_%=:\n"
"s_mov_b32 s76,0\n"
"v_mul_f32 v128,v128,v24\n"
"v_mul_f32 v129,v129,v24\n"
"v_mul_f32 v130,v130,v24\n"
"v_mul_f32 v131,v131,v24\n"
"v_mul_f32 v132,v132,v24\n"
"v_mul_f32 v133,v133,v24\n"
"v_mul_f32 v134,v134,v24\n"
"v_mul_f32 v135,v135,v24\n"
"v_mul_f32 v136,v136,v24\n"
"v_mul_f32 v137,v137,v24\n"
"v_mul_f32 v138,v138,v24\n"
"v_mul_f32 v139,v139,v24\n"
"v_mul_f32 v140,v140,v24\n"
"v_mul_f32 v141,v141,v24\n"
"v_mul_f32 v142,v142,v24\n"
"v_mul_f32 v143,v143,v24\n"
"v_mul_f32 v144,v144,v24\n"
"v_mul_f32 v145,v145,v24\n"
"v_mul_f32 v146,v146,v24\n"
"v_mul_f32 v147,v147,v24\n"
"v_mul_f32 v148,v148,v24\n"
"v_mul_f32 v149,v149,v24\n"
"v_mul_f32 v150,v150,v24\n"
"v_mul_f32 v151,v151,v24\n"
"v_mul_f32 v152,v152,v24\n"
"v_mul_f32 v153,v153,v24\n"
"v_mul_f32 v154,v154,v24\n"
"v_mul_f32 v155,v155,v24\n"
"v_mul_f32 v156,v156,v24\n"
"v_mul_f32 v157,v157,v24\n"
"v_mul_f32 v158,v158,v24\n"
"v_mul_f32 v159,v159,v24\n"
"v_mul_f32 v160,v160,v24\n"
"v_mul_f32 v161,v161,v24\n"
"v_mul_f32 v162,v162,v24\n"
"v_mul_f32 v163,v163,v24\n"
"v_mul_f32 v164,v164,v24\n"
"v_mul_f32 v165,v165,v24\n"
"v_mul_f32 v166,v166,v24\n"
"v_mul_f32 v167,v167,v24\n"
"v_mul_f32 v168,v168,v24\n"
"v_mul_f32 v169,v169,v24\n"
"v_mul_f32 v170,v170,v24\n"
"v_mul_f32 v171,v171,v24\n"
"v_mul_f32 v172,v172,v24\n"
"v_mul_f32 v173,v173,v24\n"
"v_mul_f32 v174,v174,v24\n"
"v_mul_f32 v175,v175,v24\n"
"v_mul_f32 v176,v176,v24\n"
"v_mul_f32 v177,v177,v24\n"
"v_mul_f32 v178,v178,v24\n"
"v_mul_f32 v179,v179,v24\n"
"v_mul_f32 v180,v180,v24\n"
"v_mul_f32 v181,v181,v24\n"
"v_mul_f32 v182,v182,v24\n"
"v_mul_f32 v183,v183,v24\n"
"v_mul_f32 v184,v184,v24\n"
"v_mul_f32 v185,v185,v24\n"
"v_mul_f32 v186,v186,v24\n"
"v_mul_f32 v187,v187,v24\n"
"v_mul_f32 v188,v188,v24\n"
"v_mul_f32 v189,v189,v24\n"
"v_mul_f32 v190,v190,v24\n"
"v_mul_f32 v191,v191,v24\n"
"v_mul_f32 v192,v192,v25\n"
"v_mul_f32 v193,v193,v25\n"
"v_mul_f32 v194,v194,v25\n"
"v_mul_f32 v195,v195,v25\n"
"v_mul_f32 v196,v196,v25\n"
"v_mul_f32 v197,v197,v25\n"
"v_mul_f32 v198,v198,v25\n"
"v_mul_f32 v199,v199,v25\n"
"v_mul_f32 v200,v200,v25\n"
"v_mul_f32 v201,v201,v25\n"
"v_mul_f32 v202,v202,v25\n"
"v_mul_f32 v203,v203,v25\n"
"v_mul_f32 v204,v204,v25\n"
"v_mul_f32 v205,v205,v25\n"
"v_mul_f32 v206,v206,v25\n"
"v_mul_f32 v207,v207,v25\n"
"v_mul_f32 v208,v208,v25\n"
"v_mul_f32 v209,v209,v25\n"
"v_mul_f32 v210,v210,v25\n"
"v_mul_f32 v211,v211,v25\n"
"v_mul_f32 v212,v212,v25\n"
"v_mul_f32 v213,v213,v25\n"
"v_mul_f32 v214,v214,v25\n"
"v_mul_f32 v215,v215,v25\n"
"v_mul_f32 v216,v216,v25\n"
"v_mul_f32 v217,v217,v25\n"
"v_mul_f32 v218,v218,v25\n"
"v_mul_f32 v219,v219,v25\n"
"v_mul_f32 v220,v220,v25\n"
"v_mul_f32 v221,v221,v25\n"
"v_mul_f32 v222,v222,v25\n"
"v_mul_f32 v223,v223,v25\n"
"v_mul_f32 v224,v224,v25\n"
"v_mul_f32 v225,v225,v25\n"
"v_mul_f32 v226,v226,v25\n"
"v_mul_f32 v227,v227,v25\n"
"v_mul_f32 v228,v228,v25\n"
"v_mul_f32 v229,v229,v25\n"
"v_mul_f32 v230,v230,v25\n"
"v_mul_f32 v231,v231,v25\n"
"v_mul_f32 v232,v232,v25\n"
"v_mul_f32 v233,v233,v25\n"
"v_mul_f32 v234,v234,v25\n"
"v_mul_f32 v235,v235,v25\n"
"v_mul_f32 v236,v236,v25\n"
"v_mul_f32 v237,v237,v25\n"
"v_mul_f32 v238,v238,v25\n"
"v_mul_f32 v239,v239,v25\n"
"v_mul_f32 v240,v240,v25\n"
"v_mul_f32 v241,v241,v25\n"
"v_mul_f32 v242,v242,v25\n"
"v_mul_f32 v243,v243,v25\n"
"v_mul_f32 v244,v244,v25\n"
"v_mul_f32 v245,v245,v25\n"
"v_mul_f32 v246,v246,v25\n"
"v_mul_f32 v247,v247,v25\n"
"v_mul_f32 v248,v248,v25\n"
"v_mul_f32 v249,v249,v25\n"
"v_mul_f32 v250,v250,v25\n"
"v_mul_f32 v251,v251,v25\n"
"v_mul_f32 v252,v252,v25\n"
"v_mul_f32 v253,v253,v25\n"
"v_mul_f32 v254,v254,v25\n"
"v_mul_f32 v255,v255,v25\n"
"s_branch L_rescret_lastb_%=\n"
"L_pubq_%=:\n"
"s_mov_b32 s58,0\n"
"s_cmp_eq_u32 s11,0\n"
"s_cbranch_scc0 L_np10_%=\n"
"v_mov_b32 v2,s34\n"
"v_mov_b32 v3,s35\n"
"v_mov_b32 v4,0\n"
"s_mov_b64 exec,1\n"
"global_store_dwordx2 v4,v[2:3],s[84:85] sc1\n"
"s_mov_b64 exec,-1\n"
"L_np10_%=:\n"
"s_branch L_pubq_ret_%=\n"
"L_qf_%=:\n"
"s_mov_b32 s96,0\n"
"L_qf_lp_%=:\n"
"global_load_dwordx2 v[2:3],v1,s[84:85] sc1\n"
"s_add_u32 s96,s96,1\n"
"s_waitcnt vmcnt(0)\n"
"v_cmp_ne_u64 vcc,s[34:35],v[2:3]\n"
"s_nop 0\n"
"s_cbranch_vccz L_qf_ret_%=\n"
"s_sleep 1\n"
"s_cmp_lt_u32 s96,0x20000\n"
"s_cbranch_scc1 L_qf_lp_%=\n"
"s_branch L_qf_ret_%=\n"
"L_pt_%=:\n"
"s_mov_b32 s96,0\n"
"L_pt_lp_%=:\n"
"global_load_dwordx2 v[2:3],v1,s[84:85] sc1\n"
"s_add_u32 s96,s96,1\n"
"s_waitcnt vmcnt(0)\n"
"v_cmp_ne_u64 vcc,s[34:35],v[2:3]\n"
"s_nop 0\n"
"s_cbranch_vccz L_pt_ret_%=\n"
"s_sleep 1\n"
"s_cmp_lt_u32 s96,0x20000\n"
"s_cbranch_scc1 L_pt_lp_%=\n"
"s_branch L_pt_ret_%=\n"
"L_vf0_%=:\n"
"s_mov_b32 s96,0\n"
"L_vf0_lp_%=:\n"
"global_load_dwordx2 v[2:3],v5,s[82:83] offset:64 sc1\n"
"s_add_u32 s96,s96,1\n"
"s_waitcnt vmcnt(0)\n"
"v_cmp_ne_u64 vcc,s[34:35],v[2:3]\n"
"s_nop 0\n"
"s_cbranch_vccz L_vf0_ret_%=\n"
"s_sleep 1\n"
"s_cmp_lt_u32 s96,0x20000\n"
"s_cbranch_scc1 L_vf0_lp_%=\n"
"s_branch L_vf0_ret_%=\n"
"L_vf1_%=:\n"
"s_mov_b32 s96,0\n"
"L_vf1_lp_%=:\n"
"global_load_dwordx2 v[2:3],v5,s[82:83] offset:64 sc1\n"
"s_add_u32 s96,s96,1\n"
"s_waitcnt vmcnt(0)\n"
"v_cmp_ne_u64 vcc,s[34:35],v[2:3]\n"
"s_nop 0\n"
"s_cbranch_vccz L_vf1_ret_%=\n"
"s_sleep 1\n"
"s_cmp_lt_u32 s96,0x20000\n"
"s_cbranch_scc1 L_vf1_lp_%=\n"
"s_branch L_vf1_ret_%=\n"
"L_fin_%=:\n"
    :: "s"(__builtin_amdgcn_kernarg_segment_ptr()), "s"(blockIdx.x), "s"(__builtin_amdgcn_readfirstlane(threadIdx.x >> 6))
    : "v0","v1","v2","v3","v4","v5","v6","v7","v8","v9","v10","v11","v12","v13","v14","v15","v16","v17","v18","v19","v20","v21","v22","v23","v24","v25","v26","v27","v28","v29","v30","v31","v32","v33","v34","v35","v36","v37","v38","v39","v40","v41","v42","v43","v44","v45","v46","v47","v48","v49","v50","v51","v52","v53","v54","v55","v56","v57","v58","v59","v60","v61","v62","v63","v64","v65","v66","v67","v68","v69","v70","v71","v72","v73","v74","v75","v76","v77","v78","v79","v80","v81","v82","v83","v84","v85","v86","v87","v88","v89","v90","v91","v92","v93","v94","v95","v96","v97","v98","v99","v100","v101","v102","v103","v104","v105","v106","v107","v108","v109","v110","v111","v112","v113","v114","v115","v116","v117","v118","v119","v120","v121","v122","v123","v124","v125","v126","v127","v128","v129","v130","v131","v132","v133","v134","v135","v136","v137","v138","v139","v140","v141","v142","v143","v144","v145","v146","v147","v148","v149","v150","v151","v152","v153","v154","v155","v156","v157","v158","v159","v160","v161","v162","v163","v164","v165","v166","v167","v168","v169","v170","v171","v172","v173","v174","v175","v176","v177","v178","v179","v180","v181","v182","v183","v184","v185","v186","v187","v188","v189","v190","v191","v192","v193","v194","v195","v196","v197","v198","v199","v200","v201","v202","v203","v204","v205","v206","v207","v208","v209","v210","v211","v212","v213","v214","v215","v216","v217","v218","v219","v220","v221","v222","v223","v224","v225","v226","v227","v228","v229","v230","v231","v232","v233","v234","v235","v236","v237","v238","v239","v240","v241","v242","v243","v244","v245","v246","v247","v248","v249","v250","v251","v252","v253","v254","v255","a0","a1","a2","a3","a4","a5","a6","a7","a8","a9","a10","a11","a12","a13","a14","a15","a16","a17","a18","a19","a20","a21","a22","a23","a24","a25","a26","a27","a28","a29","a30","a31","a32","a33","a34","a35","a36","a37","a38","a39","a40","a41","a42","a43","a44","a45","a46","a47","a48","a49","a50","a51","a52","a53","a54","a55","a56","a57","a58","a59","a60","a61","a62","a63","a64","a65","a66","a67","a68","a69","a70","a71","a72","a73","a74","a75","a76","a77","a78","a79","a80","a81","a82","a83","a84","a85","a86","a87","a88","a89","a90","a91","a92","a93","a94","a95","a96","a97","a98","a99","a100","a101","a102","a103","a104","a105","a106","a107","a108","a109","a110","a111","a112","a113","a114","a115","a116","a117","a118","a119","a120","a121","a122","a123","a124","a125","a126","a127","a128","a129","a130","a131","a132","a133","a134","a135","a136","a137","a138","a139","a140","a141","a142","a143","a144","a145","a146","a147","a148","a149","a150","a151","a152","a153","a154","a155","a156","a157","a158","a159","a160","a161","a162","a163","a164","a165","a166","a167","a168","a169","a170","a171","a172","a173","a174","a175","a176","a177","a178","a179","a180","a181","a182","a183","a184","a185","a186","a187","a188","a189","a190","a191","a192","a193","a194","a195","a196","a197","a198","a199","a200","a201","a202","a203","a204","a205","a206","a207","a208","a209","a210","a211","a212","a213","a214","a215","a216","a217","a218","a219","a220","a221","a222","a223","a224","a225","a226","a227","a228","a229","a230","a231","a232","a233","a234","a235","a236","a237","a238","a239","a240","a241","a242","a243","a244","a245","a246","a247","a248","a249","a250","a251","a252","a253","a254","a255","s8","s9","s10","s11","s12","s13","s14","s15","s16","s17","s18","s19","s20","s21","s22","s23","s24","s25","s26","s27","s28","s29","s30","s31","s32","s33","s34","s35","s36","s37","s38","s39","s40","s41","s42","s43","s44","s45","s46","s47","s48","s49","s50","s51","s52","s53","s54","s55","s56","s57","s58","s59","s60","s61","s62","s63","s64","s65","s66","s67","s68","s69","s70","s71","s72","s73","s74","s75","s76","s77","s78","s79","s80","s81","s82","s83","s84","s85","s86","s87","s88","s89","s90","s91","s92","s93","s94","s95","s96","s97","s98","s99","s100","s101","vcc","scc","memory");
}

extern "C" void kernel_launch(void* const* d_in, const int* in_sizes, int n_in, void* d_out, int out_size, void* d_ws, size_t ws_size, hipStream_t stream) {
  (void)hipFuncSetAttribute((const void*)fused_block, hipFuncAttributeMaxDynamicSharedMemorySize, 163840);
  fused_block<<<256, 256, 163840, stream>>>(d_in[0], d_in[1], d_in[2], d_in[3], d_in[4], d_in[5], d_in[7], d_in[8], d_in[9], d_out, d_ws);
}
```
